```python
import torch

def _softmax(x, axis=-1):
    return torch.softmax(x, dim=axis)
def _gelu(x):
    return torch.nn.functional.gelu(x, approximate="tanh")
def _silu(x):
    return torch.nn.functional.silu(x)
def _sigmoid(x):
    return torch.sigmoid(x)
def _layernorm(x, axis=-1, eps=1e-5):
    m = x.mean(dim=axis, keepdim=True)
    v = x.var(dim=axis, keepdim=True, unbiased=False)
    return (x - m) / torch.sqrt(v + eps)
def _scatter_add(tgt, idx, src, axis=0):
    return tgt.clone().index_add_(axis, idx, src)
def _segment_sum(src, seg, n):
    out = torch.zeros((n,) + src.shape[1:], dtype=src.dtype)
    return out.index_add_(0, seg, src)
def _pad(x, pw):
    flat = tuple(v for pair in reversed(pw) for v in pair)
    return torch.nn.functional.pad(x, flat)

kernel_name = 'parallel_sum__mha_block__glu_gated_glu__prompt'

def setup_inputs():

    _w = (lambda a: (lambda t: t.to(torch.bfloat16) if t.is_floating_point() else t)(torch.as_tensor(a)))
    def _A_setup():
        B, S, D, H = 1, 2048, 1024, 32
        return {
            "x": _w((lambda *s: torch.randn(*s, dtype=torch.bfloat16) * 0.05)(B, S, D)),
            "wqkv": _w((lambda *s: torch.randn(*s, dtype=torch.bfloat16) * 0.05)(D, 3 * D)),
            "wo": _w((lambda *s: torch.randn(*s, dtype=torch.bfloat16) * 0.05)(D, D)),
        }
    _A_in = _A_setup()

    def _B_setup():
        B, S, D, F = 1, 2048, 1024, 3584
        return {
            "x": _w((lambda *s: torch.randn(*s, dtype=torch.bfloat16) * 0.05)(B, S, D)),
            "w_gate": _w((lambda *s: torch.randn(*s, dtype=torch.bfloat16) * 0.05)(D, F)),
            "w_up": _w((lambda *s: torch.randn(*s, dtype=torch.bfloat16) * 0.05)(D, F)),
            "w_down": _w((lambda *s: torch.randn(*s, dtype=torch.bfloat16) * 0.05)(F, D)),
        }
    _B_in = _B_setup()

    return {
        "A_x": _A_in["x"],
        "A_wqkv": _A_in["wqkv"],
        "A_wo": _A_in["wo"],
        "B_x": _B_in["x"],
        "B_w_gate": _B_in["w_gate"],
        "B_w_up": _B_in["w_up"],
        "B_w_down": _B_in["w_down"],
    }

def reference(A_x, A_wqkv, A_wo, B_x, B_w_gate, B_w_up, B_w_down):
    def _A_ref(x, wqkv, wo):
        B, S, D = x.shape
        H = 32
        Dh = D // H
        qkv = (lambda x, *d: x.permute(*d))((x @ wqkv).reshape(B, S, 3, H, Dh), 2, 0, 3, 1, 4)
        q, k, v = qkv[0], qkv[1], qkv[2]
        scores = torch.einsum("bhqd,bhkd->bhqk", q, k) * (Dh ** -0.5)
        p = _softmax(scores, axis=-1)
        o = torch.einsum("bhqk,bhkd->bhqd", p, v)
        return (lambda x, *d: x.permute(*d))(o, 0, 2, 1, 3).reshape(B, S, D) @ wo

    def _B_ref(x, w_gate, w_up, w_down):
        g = torch.einsum("bsd,df->bsf", x, w_gate)
        u = torch.einsum("bsd,df->bsf", x, w_up)
        h = _sigmoid(g) * u
        return torch.einsum("bsf,fd->bsd", h, w_down)

    x = A_x
    xn = _layernorm(x, axis=-1)
    return x + _A_ref(xn, A_wqkv, A_wo) + _B_ref(xn, B_w_gate, B_w_up, B_w_down)
```

```cpp
#include <hip/hip_runtime.h>
#include <stdint.h>

typedef unsigned short bf16_t;
typedef short bf16x8 __attribute__((ext_vector_type(8)));
typedef short s16x4 __attribute__((ext_vector_type(4)));
typedef float f32x2 __attribute__((ext_vector_type(2)));
typedef float f32x4 __attribute__((ext_vector_type(4)));
typedef float f32x16 __attribute__((ext_vector_type(16)));
typedef unsigned u32x2 __attribute__((ext_vector_type(2)));
typedef unsigned u32x4 __attribute__((ext_vector_type(4)));
typedef __bf16 bf16x2_t __attribute__((ext_vector_type(2)));
#define LAS __attribute__((address_space(3)))
#define GAS __attribute__((address_space(1)))
typedef LAS unsigned char* lds_ptr;

constexpr int SEQ = 2048, DM = 1024, FF = 3584, NQKV = 3072, NH = 32, DH = 32;
constexpr int ACT_LD = FF + DM;
constexpr float LN_EPS = 1e-5f;
constexpr float LOG2E = 1.4426950408889634f;
constexpr float QSCALE = 0.17677669529663687f * LOG2E;

constexpr size_t WS_FLAGS = 0;
constexpr size_t WS_QKV = 256 * 1024;
constexpr size_t WS_ACT = WS_QKV + (size_t)3 * SEQ * DM * 2;
constexpr size_t WS_XCH = WS_ACT + (size_t)SEQ * ACT_LD * 2;
constexpr size_t WS_XN = WS_XCH + (size_t)256 * 32768;

#define STAMP(k, i) do {} while (0)
#define TS(v) do {} while (0)
#define SBAR() __builtin_amdgcn_sched_barrier(0)
#define BAR() __builtin_amdgcn_s_barrier()
#define WAIT_V(n) asm volatile("s_waitcnt vmcnt(" #n ")" ::: "memory")
#define WAIT_L0() asm volatile("s_waitcnt lgkmcnt(0)" ::: "memory")
#define MFMA16(a, b, c) __builtin_amdgcn_mfma_f32_16x16x32_bf16(a, b, c, 0, 0, 0)
#define MFMA16_ACC(acc, a, b) asm volatile("v_mfma_f32_16x16x32_bf16 %0, %1, %2, %0" : "+v"(acc) : "v"(a), "v"(b))
#define MFMA_DRAIN() asm volatile("s_nop 15" ::: "memory")
#define MFMA32(a, b, c) __builtin_amdgcn_mfma_f32_32x32x16_bf16(a, b, c, 0, 0, 0)

__device__ __forceinline__ void glds16(const void* sbase, unsigned voff, unsigned ldsdst) {
    asm volatile("s_mov_b32 m0, %2\n\ts_nop 0\n\tglobal_load_lds_dwordx4 %0, %1" ::"v"(voff), "s"(sbase), "s"(ldsdst) : "memory");
}
__device__ __forceinline__ bf16x8 lds_b128(lds_ptr p) { return *(const LAS bf16x8*)p; }
__device__ __forceinline__ s16x4 lds_tr(lds_ptr p) { return __builtin_amdgcn_ds_read_tr16_b64_v4i16((LAS s16x4*)p); }
__device__ __forceinline__ bf16x8 cat8(s16x4 a, s16x4 b) { return __builtin_shufflevector(a, b, 0, 1, 2, 3, 4, 5, 6, 7); }
__device__ __forceinline__ unsigned cvtpk(float lo, float hi) {
    f32x2 v = {lo, hi};
    return __builtin_bit_cast(unsigned, __builtin_convertvector(v, bf16x2_t));
}
__device__ __forceinline__ float dot2(unsigned a, unsigned b, float c) {
    return __builtin_amdgcn_fdot2_f32_bf16(__builtin_bit_cast(bf16x2_t, a), __builtin_bit_cast(bf16x2_t, b), c, false);
}
__device__ __forceinline__ float bf_lo(unsigned u) { return __builtin_bit_cast(float, u << 16); }
__device__ __forceinline__ float bf_hi(unsigned u) { return __builtin_bit_cast(float, u & 0xffff0000u); }
__device__ __forceinline__ void swap16(unsigned& a, unsigned& b) {
    auto r = __builtin_amdgcn_permlane16_swap(a, b, false, false);
    a = r[0]; b = r[1];
}
__device__ __forceinline__ void swap32(unsigned& a, unsigned& b) {
    auto r = __builtin_amdgcn_permlane32_swap(a, b, false, false);
    a = r[0]; b = r[1];
}

__device__ __forceinline__ float red4(float v) {
    unsigned a = __builtin_bit_cast(unsigned, v), b = a;
    swap16(a, b);
    v = __builtin_bit_cast(float, a) + __builtin_bit_cast(float, b);
    a = __builtin_bit_cast(unsigned, v); b = a;
    swap32(a, b);
    return __builtin_bit_cast(float, a) + __builtin_bit_cast(float, b);
}

__device__ __forceinline__ float wave_sum(float v) {
#pragma unroll
    for (int o = 1; o < 16; o <<= 1) v += __shfl_xor(v, o);
    return red4(v);
}
__global__ __launch_bounds__(256) void k_ln(const bf16_t* __restrict__ x, bf16_t* __restrict__ xn, unsigned* __restrict__ flags) {
    const int tid = threadIdx.x, lane = tid & 63, row = blockIdx.x * 4 + (tid >> 6);
    if (tid < 2) flags[(blockIdx.x * 2 + tid) * 32] = 0u;
    const u32x4* src = (const u32x4*)(x + (size_t)row * DM) + lane;
    const u32x4 a = src[0], b = src[64];
    float v[16];
#pragma unroll
    for (int i = 0; i < 4; ++i) { v[2 * i] = bf_lo(a[i]); v[2 * i + 1] = bf_hi(a[i]); v[8 + 2 * i] = bf_lo(b[i]); v[8 + 2 * i + 1] = bf_hi(b[i]); }
    float s = 0.f;
#pragma unroll
    for (int i = 0; i < 16; ++i) s += v[i];
    const float mu = wave_sum(s) * (1.0f / DM);
    float q = 0.f;
#pragma unroll
    for (int i = 0; i < 16; ++i) { v[i] -= mu; q += v[i] * v[i]; }
    const float rs = 1.0f / __builtin_sqrtf(wave_sum(q) * (1.0f / DM) + LN_EPS);
    u32x4 oa, ob;
#pragma unroll
    for (int i = 0; i < 4; ++i) { oa[i] = cvtpk(v[2 * i] * rs, v[2 * i + 1] * rs); ob[i] = cvtpk(v[8 + 2 * i] * rs, v[8 + 2 * i + 1] * rs); }
    u32x4* dst = (u32x4*)(xn + (size_t)row * DM) + lane;
    dst[0] = oa; dst[64] = ob;
}

constexpr int G1_A = 0, G1_W = 65536, G1_LDS = G1_W + 5 * 16384;

__global__ __launch_bounds__(512, 2) void k_gemm1(const bf16_t* __restrict__ x, const bf16_t* __restrict__ wqkv, const bf16_t* __restrict__ wg,
                                                  const bf16_t* __restrict__ wu, bf16_t* __restrict__ qkv, bf16_t* __restrict__ act) {
    __shared__ __attribute__((aligned(16))) unsigned char smem[G1_LDS];
    const lds_ptr lds = (lds_ptr)smem;
    const unsigned lds0 = (unsigned)(uintptr_t)lds;
    const int tid = threadIdx.x, lane = tid & 63;
    const int wid = __builtin_amdgcn_readfirstlane(tid >> 6), wr = wid >> 2, wc = wid & 3;
    const int bid = blockIdx.x, xcd = bid & 7, idx = bid >> 3, js = idx & 3, rb = idx >> 2;
    STAMP(0, 0);
    const bool typeY = (js == 3);
    const int f0 = 448 * xcd + 128 * js;
    const char* P0 = (const char*)(wg + f0);
    const char* P2 = (const char*)(wu + f0);
    const char* P1 = typeY ? (const char*)(wqkv + 128 * xcd + 64) : (const char*)(wg + f0 + 64);
    const char* P3 = typeY ? (const char*)(wqkv + 1024 + 128 * xcd + 64) : (const char*)(wu + f0 + 64);
    const char* P4 = typeY ? (const char*)(wqkv + 2048 + 128 * xcd + 64) : (const char*)(wqkv + js * 1024 + 128 * xcd);
    const unsigned stepF = 32u * FF * 2u, stepQ = 32u * NQKV * 2u, step13 = typeY ? stepQ : stepF;
    const char* XA = (const char*)(x + (size_t)rb * 256 * DM);

    const int r8 = lane >> 3, u8 = lane & 7;
    const int gw = (r8 & 2) | ((wc & 1) << 2);
    const unsigned voffF = (unsigned)(((wc * 8 + r8) * FF + (u8 ^ gw) * 8) * 2);
    const unsigned voffQ = (unsigned)(((wc * 8 + r8) * NQKV + (u8 ^ gw) * 8) * 2);
    const unsigned voff13 = typeY ? voffQ : voffF;
    const unsigned voffA0 = (unsigned)(((32 * wid + r8) * DM + (u8 ^ (r8 >> 1)) * 8) * 2);
    const unsigned voffA1 = (unsigned)(((32 * wid + 8 + r8) * DM + (u8 ^ (4 | (r8 >> 1))) * 8) * 2);
    const unsigned dstA = lds0 + G1_A + wid * 4096, dstW = lds0 + G1_W + wc * 1024;
#define G1_LOAD_A(t) do { const char* b_ = XA + (t) * 128; const unsigned d_ = dstA + ((t) & 1) * 32768; \
        glds16(b_, voffA0, d_); glds16(b_, voffA1, d_ + 1024); glds16(b_ + 32768, voffA0, d_ + 2048); glds16(b_ + 32768, voffA1, d_ + 3072); } while (0)
#define G1_LOAD_W(hs) do { const unsigned d_ = dstW + ((hs) & 3) * 4096; \
        glds16(P0 + (size_t)(hs) * stepF, voffF, d_); glds16(P1 + (size_t)(hs) * step13, voff13, d_ + 16384); glds16(P2 + (size_t)(hs) * stepF, voffF, d_ + 32768); \
        glds16(P3 + (size_t)(hs) * step13, voff13, d_ + 49152); glds16(P4 + (size_t)(hs) * stepQ, voffQ, d_ + 65536); } while (0)

    G1_LOAD_A(0);
    if (wr == 0) { G1_LOAD_W(0); G1_LOAD_W(1); } else { G1_LOAD_W(2); }

    int fr = lane & 15, fq = lane >> 4;
    const lds_ptr aB0 = lds + G1_A + (wr * 128 + fr) * 128 + ((fq ^ (fr >> 1)) * 16);
    const lds_ptr aB1 = lds + G1_A + (wr * 128 + fr) * 128 + (((4 | fq) ^ (fr >> 1)) * 16);
    const int tq = (lane >> 2) & 3, tp = lane & 3, tg = (tq & 2) | ((fq & 1) << 2);
    auto wbase = [&](int panel, int bp) { return lds + G1_W + panel * 16384 + (8 * fq + tq) * 128 + (((2 * bp + (tp >> 1)) ^ tg) * 16) + (tp & 1) * 8; };
    const int pX = wc >> 1, bX = 2 * (wc & 1);
    const lds_ptr wB0 = typeY ? wbase(0, wc) : wbase(pX, bX);
    const lds_ptr wB1 = typeY ? wbase(1, wc) : wbase(pX, bX + 1);
    const lds_ptr wB2 = typeY ? wbase(2, wc) : wbase(2 + pX, bX);
    const lds_ptr wB3 = typeY ? wbase(3, wc) : wbase(2 + pX, bX + 1);
    const lds_ptr wB4 = wbase(4, wc);

    f32x4 acc[8][5];
#pragma unroll
    for (int m = 0; m < 8; ++m)
#pragma unroll
        for (int b = 0; b < 5; ++b) acc[m][b] = (f32x4){0.f, 0.f, 0.f, 0.f};
    bf16x8 af[8], wf[5];
#define G1_RW(i, B, slot) wf[i] = cat8(lds_tr(B + (slot) * 4096), lds_tr(B + (slot) * 4096 + 512))
#define G1_RA(m, AB, abuf) af[m] = lds_b128(AB + (abuf) * 32768 + (m) * 2048)
#define G1_SEG(AB, abuf, slot, L0, L1, L2, L3, L4) do { \
        L0; G1_RW(0, wB0, slot); G1_RW(1, wB1, slot); L1; G1_RW(2, wB2, slot); G1_RW(3, wB3, slot); L2; G1_RW(4, wB4, slot); G1_RA(0, AB, abuf); G1_RA(1, AB, abuf); \
        L3; G1_RA(2, AB, abuf); G1_RA(3, AB, abuf); G1_RA(4, AB, abuf); L4; G1_RA(5, AB, abuf); G1_RA(6, AB, abuf); G1_RA(7, AB, abuf); \
        WAIT_L0(); SBAR(); BAR(); SBAR(); } while (0)
#define G1_SEG_A(abuf, slot, t) do { const char* b_ = XA + (t) * 128; const unsigned d_ = dstA + ((t) & 1) * 32768; \
        G1_SEG(aB0, abuf, slot, glds16(b_, voffA0, d_), glds16(b_, voffA1, d_ + 1024), glds16(b_ + 32768, voffA0, d_ + 2048), glds16(b_ + 32768, voffA1, d_ + 3072), (void)0); } while (0)
#define G1_SEG_W(abuf, slot, hs) do { const unsigned d_ = dstW + ((hs) & 3) * 4096; \
        G1_SEG(aB1, abuf, slot, glds16(P0 + (size_t)(hs) * stepF, voffF, d_), glds16(P1 + (size_t)(hs) * step13, voff13, d_ + 16384), glds16(P2 + (size_t)(hs) * stepF, voffF, d_ + 32768), \
               glds16(P3 + (size_t)(hs) * step13, voff13, d_ + 49152), glds16(P4 + (size_t)(hs) * stepQ, voffQ, d_ + 65536)); } while (0)
#define G1_SEG_N(AB, abuf, slot) G1_SEG(AB, abuf, slot, (void)0, (void)0, (void)0, (void)0, (void)0)
#define G1_MMA() do { __builtin_amdgcn_s_setprio(1); \
        _Pragma("unroll") for (int m = 0; m < 8; ++m) _Pragma("unroll") for (int b = 0; b < 5; ++b) MFMA16_ACC(acc[m][b], wf[b], af[m]); \
        __builtin_amdgcn_s_setprio(0); SBAR(); } while (0)

    if (wr == 1) BAR();
    WAIT_V(5);
    BAR();
    SBAR();
    STAMP(0, 1);
    for (int t = 0; t < 14; t += 2) {
        G1_SEG_A(0, 0, t + 1);          G1_MMA(); WAIT_V(4); BAR(); SBAR();
        G1_SEG_W(0, 1, 2 * t + 3 + wr); G1_MMA(); WAIT_V(5); BAR(); SBAR();
        G1_SEG_A(1, 2, t + 2);          G1_MMA(); WAIT_V(4); BAR(); SBAR();
        G1_SEG_W(1, 3, 2 * t + 5 + wr); G1_MMA(); WAIT_V(5); BAR(); SBAR();
    }
    G1_SEG_A(0, 0, 15); G1_MMA(); WAIT_V(4); BAR(); SBAR();
    if (wr == 0) G1_LOAD_W(31);
    G1_SEG_N(aB1, 0, 1); G1_MMA();
    if (wr == 0) WAIT_V(5); else WAIT_V(0);
    BAR(); SBAR();
    G1_SEG_N(aB0, 1, 2); G1_MMA(); WAIT_V(0); BAR(); SBAR();
    G1_SEG_N(aB1, 1, 3); G1_MMA();
    MFMA_DRAIN(); SBAR();
    if (wr == 0) BAR();
    SBAR();

    STAMP(0, 2);
    { int tz = tid; asm volatile("" : "+v"(tz)); fr = tz & 15; fq = (tz >> 4) & 3; }
    const int row0 = rb * 256 + wr * 128 + fr;
    auto glu = [&](const f32x4& g, const f32x4& u) {
        f32x4 h;
#pragma unroll
        for (int j = 0; j < 4; ++j) h[j] = u[j] * __builtin_amdgcn_rcpf(1.0f + __builtin_amdgcn_exp2f(g[j] * -LOG2E));
        return h;
    };
    auto qkv_store = [&](const f32x4& a0, const f32x4& a1, float sc, bf16_t* base, int m) {
        const f32x4 v0 = a0 * sc, v1 = a1 * sc;
        unsigned p0 = cvtpk(v0[0], v0[1]), p1 = cvtpk(v0[2], v0[3]), q0 = cvtpk(v1[0], v1[1]), q1 = cvtpk(v1[2], v1[3]);
        swap16(p0, q0); swap16(p1, q1);
        *(u32x4*)(base + (size_t)(row0 + (m + (fq & 1)) * 16) * DH + (fq >> 1) * 8) = (u32x4){p0, p1, q0, q1};
    };
    const int hd = 4 * xcd + (typeY ? 2 : 0) + (wc >> 1);
    bf16_t* const qb = qkv + (size_t)hd * SEQ * DH + (wc & 1) * 16;
    constexpr size_t KIND = (size_t)NH * SEQ * DH;
    if (!typeY) {
        bf16_t* const hb = act + f0 + 32 * wc + (fq & 1) * 16 + (fq >> 1) * 8;
        const float sc4 = (js == 0) ? QSCALE : 1.0f;
#pragma unroll
        for (int m = 0; m < 8; m += 2) qkv_store(acc[m][4], acc[m + 1][4], sc4, qb + js * KIND, m);
#pragma unroll
        for (int m = 0; m < 8; ++m) {
            const f32x4 h0 = glu(acc[m][0], acc[m][2]), h1 = glu(acc[m][1], acc[m][3]);
            unsigned p0 = cvtpk(h0[0], h0[1]), p1 = cvtpk(h0[2], h0[3]), q0 = cvtpk(h1[0], h1[1]), q1 = cvtpk(h1[2], h1[3]);
            swap16(p0, q0); swap16(p1, q1);
            *(u32x4*)(hb + (size_t)(row0 + m * 16) * ACT_LD) = (u32x4){p0, p1, q0, q1};
        }
    } else {
        bf16_t* const hb = act + f0 + 16 * wc + (fq >> 1) * 8;
#pragma unroll
        for (int m = 0; m < 8; m += 2) {
            qkv_store(acc[m][1], acc[m + 1][1], QSCALE, qb, m);
            qkv_store(acc[m][3], acc[m + 1][3], 1.0f, qb + KIND, m);
            qkv_store(acc[m][4], acc[m + 1][4], 1.0f, qb + 2 * KIND, m);
        }
#pragma unroll
        for (int m = 0; m < 8; m += 2) {
            const f32x4 h0 = glu(acc[m][0], acc[m][2]), h1 = glu(acc[m + 1][0], acc[m + 1][2]);
            unsigned p0 = cvtpk(h0[0], h0[1]), p1 = cvtpk(h0[2], h0[3]), q0 = cvtpk(h1[0], h1[1]), q1 = cvtpk(h1[2], h1[3]);
            swap16(p0, q0); swap16(p1, q1);
            *(u32x4*)(hb + (size_t)(row0 + (m + (fq & 1)) * 16) * ACT_LD) = (u32x4){p0, p1, q0, q1};
        }
    }
    STAMP(0, 3);
    STAMP(0, 4);
#undef G1_LOAD_A
#undef G1_LOAD_W
#undef G1_MMA
}

constexpr int AT_CH = 32768, AT_FLAG = 4 * AT_CH, AT_LDS = AT_FLAG + 64;

#define PIN(x) asm volatile("" : "+v"(x))
template <bool SAFE>
__device__ __forceinline__ void attn_pass(lds_ptr lds, unsigned lds0, const char* Kg, const char* Vg, bf16x8 q0, bf16x8 q1, int wid, int lane, f32x16& o, f32x4& l) {
    const int r32 = lane & 31, hi = lane >> 5;
    const unsigned voffK = (unsigned)(wid * 2048 + (lane >> 2) * 64 + (((lane & 3) ^ ((lane >> 4) & 3)) * 16));
    const unsigned voffV = (unsigned)(wid * 2048 + lane * 16);
    const unsigned dst = lds0 + wid * 2048;
#define AT_LOAD(c) do { const unsigned d_ = dst + ((c) & 3) * AT_CH; const char* k_ = Kg + ((c) & 7) * 16384; const char* v_ = Vg + ((c) & 7) * 16384; \
        glds16(k_, voffK, d_); glds16(k_ + 1024, voffK, d_ + 1024); glds16(v_, voffV, d_ + 16384); glds16(v_ + 1024, voffV, d_ + 16384 + 1024); } while (0)
    const int ksw = (r32 >> 2) & 3;
    const lds_ptr kb0 = lds + r32 * 64 + ((hi ^ ksw) * 16), kb1 = lds + r32 * 64 + (((2 | hi) ^ ksw) * 16);
    const lds_ptr vb = lds + 16384 + (4 * hi + ((lane >> 2) & 3)) * 64 + ((lane >> 4) & 1) * 32 + (lane & 3) * 8;
    bf16x8 ones;
    { const short v = (((lane >> 4) & 1) == ((lane >> 2) & 1)) ? (short)0x3f80 : (short)0; ones = (bf16x8){v, v, v, v, v, v, v, v}; }
    AT_LOAD(0); AT_LOAD(1);
    o = (f32x16){}; l = (f32x4){0.f, 0.f, 0.f, 0.f};
    f32x16 negm = (f32x16){}, sA0, sA1, sB0, sB1;
    bf16x8 kf0, kf1, kf2, kf3, vf, pf;
    float mref;
    WAIT_V(4); BAR(); SBAR();
    STAMP(1, 1);
    AT_LOAD(2);
#define AT_ROWMAX(S0, S1, RM) do { float a_ = __builtin_fmaxf(S0[0], S1[0]); _Pragma("unroll") for (int r = 1; r < 16; ++r) a_ = __builtin_fmaxf(__builtin_fmaxf(a_, S0[r]), S1[r]); \
        unsigned x_ = __builtin_bit_cast(unsigned, a_), y_ = x_; swap32(x_, y_); RM = __builtin_fmaxf(__builtin_bit_cast(float, x_), __builtin_bit_cast(float, y_)); } while (0)
    {
        kf0 = lds_b128(kb0); kf1 = lds_b128(kb0 + 2048); kf2 = lds_b128(kb1); kf3 = lds_b128(kb1 + 2048);
        sA0 = MFMA32(kf0, q0, negm); sA1 = MFMA32(kf1, q0, negm); sA0 = MFMA32(kf2, q1, sA0); sA1 = MFMA32(kf3, q1, sA1);
        kf0 = lds_b128(kb0 + 4096); kf1 = lds_b128(kb0 + 4096 + 2048); kf2 = lds_b128(kb1 + 4096); kf3 = lds_b128(kb1 + 4096 + 2048);
        AT_ROWMAX(sA0, sA1, mref);
#pragma unroll
        for (int r = 0; r < 16; ++r) { sA0[r] -= mref; sA1[r] -= mref; negm[r] = -mref; }
        PIN(negm);
        vf = (bf16x8){}; pf = (bf16x8){};
    }
#define EX(v) __builtin_amdgcn_exp2f(v)
#define ATV 0
#define AT_PACK(S, i) __builtin_bit_cast(bf16x8, (u32x4){cvtpk(S[i], S[i + 1]), cvtpk(S[i + 2], S[i + 3]), cvtpk(S[i + 4], S[i + 5]), cvtpk(S[i + 6], S[i + 7])})
#define AT_GROUP(QK, S, i, KF, KADDR, VADDR) do { \
        QK; SBAR(); \
        KF = lds_b128(KADDR); const s16x4 vl_ = lds_tr(VADDR), vh_ = lds_tr((VADDR) + 512); SBAR(); \
        S[i] = EX(S[i]); S[i + 1] = EX(S[i + 1]); S[i + 2] = EX(S[i + 2]); PIN(S); SBAR(); \
        o = MFMA32(vf, pf, o); SBAR(); \
        S[i + 3] = EX(S[i + 3]); S[i + 4] = EX(S[i + 4]); S[i + 5] = EX(S[i + 5]); PIN(S); SBAR(); \
        l = MFMA16(ones, pf, l); SBAR(); \
        S[i + 6] = EX(S[i + 6]); S[i + 7] = EX(S[i + 7]); \
        pf = AT_PACK(S, i); PIN(pf); \
        vf = cat8(vl_, vh_); SBAR(); } while (0)
#define AT_STEP(C0, C1, N0, N1, KO, VO) do { \
        AT_GROUP(N0 = MFMA32(kf0, q0, negm), C0, 0, kf0, kb0 + (KO), vb + (VO)); \
        AT_GROUP(N1 = MFMA32(kf1, q0, negm), C0, 8, kf1, kb0 + (KO) + 2048, vb + (VO) + 1024); \
        AT_GROUP(N0 = MFMA32(kf2, q1, N0), C1, 0, kf2, kb1 + (KO), vb + (VO) + 2048); \
        AT_GROUP(N1 = MFMA32(kf3, q1, N1), C1, 8, kf3, kb1 + (KO) + 2048, vb + (VO) + 3072); \
        if (SAFE) { o = MFMA32(vf, pf, o); l = MFMA16(ones, pf, l); pf = (bf16x8){}; \
            float rm_; AT_ROWMAX(N0, N1, rm_); const float d_ = __builtin_fmaxf(rm_, 0.f); mref += d_; const float f_ = EX(-d_); \
            _Pragma("unroll") for (int r = 0; r < 16; ++r) { N0[r] -= d_; N1[r] -= d_; negm[r] = -mref; o[r] *= f_; } l *= f_; SBAR(); } } while (0)
#pragma unroll 1
    for (int c = 0; c < 8; ++c) {
        const int so = (c & 3) * AT_CH, sn = ((c + 1) & 3) * AT_CH;
        AT_STEP(sA0, sA1, sB0, sB1, so + 8192, so);
        AT_STEP(sB0, sB1, sA0, sA1, so + 12288, so + 4096);
        WAIT_V(4); WAIT_L0(); BAR(); SBAR();
        AT_LOAD(c + 3);
        AT_STEP(sA0, sA1, sB0, sB1, sn, so + 8192);
        AT_STEP(sB0, sB1, sA0, sA1, sn + 4096, so + 12288);
    }
    o = MFMA32(vf, pf, o); l = MFMA16(ones, pf, l);
    WAIT_V(0);
#undef AT_LOAD
#undef AT_ROWMAX
#undef AT_GROUP
#undef AT_STEP
#undef EX
}

__global__ __launch_bounds__(512, 2) void k_attn(const bf16_t* __restrict__ qkv, bf16_t* __restrict__ act) {
    __shared__ __attribute__((aligned(16))) unsigned char smem[AT_LDS];
    const lds_ptr lds = (lds_ptr)smem;
    const unsigned lds0 = (unsigned)(uintptr_t)lds;
    const int tid = threadIdx.x, lane = tid & 63, r32 = lane & 31, hi = lane >> 5;
    const int wid = __builtin_amdgcn_readfirstlane(tid >> 6);
    const int bid = blockIdx.x, xcd = bid & 7, idx = bid >> 3, h = 4 * xcd + (idx & 3), qb = idx >> 2;
    STAMP(1, 0);
    const bf16_t* Qg = qkv + (size_t)h * SEQ * DH + (size_t)(qb * 256 + wid * 32 + r32) * DH + hi * 8;
    const char* Kg = (const char*)(qkv + (size_t)(NH + h) * SEQ * DH);
    const char* Vg = (const char*)(qkv + (size_t)(2 * NH + h) * SEQ * DH);
    const bf16x8 q0 = *(const bf16x8*)Qg, q1 = *(const bf16x8*)(Qg + 16);
    if (tid == 0) *(LAS unsigned*)(lds + AT_FLAG) = 0u;
    f32x16 o; f32x4 l;
    attn_pass<false>(lds, lds0, Kg, Vg, q0, q1, wid, lane, o, l);
    STAMP(1, 2);
    {
        bool ok = (l[0] > 0.f) && (l[0] < 0x1p96f);
#pragma unroll
        for (int r = 0; r < 16; ++r) ok = ok && (__builtin_fabsf(o[r]) <= 3.0e38f);
        if (!ok) *(LAS unsigned*)(lds + AT_FLAG) = 1u;
        WAIT_L0(); BAR(); asm volatile("" ::: "memory");
        if (__builtin_amdgcn_readfirstlane(*(volatile LAS unsigned*)(lds + AT_FLAG)) != 0u) attn_pass<true>(lds, lds0, Kg, Vg, q0, q1, wid, lane, o, l);
    }
    const float rl = 1.0f / l[0];
    unsigned p[8];
#pragma unroll
    for (int i = 0; i < 8; ++i) p[i] = cvtpk(o[2 * i] * rl, o[2 * i + 1] * rl);
    swap32(p[0], p[2]); swap32(p[1], p[3]); swap32(p[4], p[6]); swap32(p[5], p[7]);
    bf16_t* Og = act + (size_t)(qb * 256 + wid * 32 + r32) * ACT_LD + FF + h * DH + hi * 8;
    *(u32x4*)Og = (u32x4){p[0], p[1], p[2], p[3]};
    *(u32x4*)(Og + 16) = (u32x4){p[4], p[5], p[6], p[7]};
    STAMP(1, 3);
    STAMP(1, 4);
}

constexpr int G2_SLOT = 32768, G2_LDS = 5 * G2_SLOT;

__global__ __launch_bounds__(512, 2) void k_gemm2(const bf16_t* __restrict__ act, const bf16_t* __restrict__ wd, const bf16_t* __restrict__ wo,
                                                  const bf16_t* __restrict__ x, bf16_t* __restrict__ out, float* xch, unsigned* flags) {
    __shared__ __attribute__((aligned(16))) unsigned char smem[G2_LDS];
    const lds_ptr lds = (lds_ptr)smem;
    const unsigned lds0 = (unsigned)(uintptr_t)lds;
    const int tid = threadIdx.x, lane = tid & 63;
    const int wid = __builtin_amdgcn_readfirstlane(tid >> 6), g = wid >> 2, w4 = wid & 3, wm = w4 >> 1, wn = w4 & 1;
    const int bid = blockIdx.x, xcd = bid & 7, idx = bid >> 3;
    const int z = xcd & 1, rbk = ((xcd >> 1) & 1) * 8 + (idx & 7), cbk = (xcd >> 2) * 4 + (idx >> 3);
    STAMP(2, 0);
    const char* Ag = (const char*)(act + (size_t)rbk * 128 * ACT_LD + z * 2304);
    const char* Wd = (const char*)(wd + (size_t)z * 2304 * DM + cbk * 128);
    const char* Wo = (const char*)(wo + cbk * 128) - (size_t)20 * 64 * DM * 2;
    const int r8 = lane >> 3, u8 = lane & 7;
    const unsigned voffA0 = (unsigned)(((32 * w4 + r8) * ACT_LD + (u8 ^ (r8 >> 1)) * 8) * 2);
    const unsigned voffA1 = (unsigned)(((32 * w4 + 8 + r8) * ACT_LD + (u8 ^ (4 | (r8 >> 1))) * 8) * 2);
    const unsigned voffB0 = (unsigned)(((16 * w4 + r8) * DM + (u8 ^ (r8 & 2)) * 8) * 2);
    const unsigned voffB1 = (unsigned)(((16 * w4 + 8 + r8) * DM + (u8 ^ (4 | (r8 & 2))) * 8) * 2);
    const unsigned dstA = lds0 + w4 * 4096, dstB = lds0 + 16384 + w4 * 2048;
#define G2_LOAD(j, slotoff) do { const char* a_ = Ag + (j) * 128; const char* b_ = ((z == 1 && (j) >= 20) ? Wo : Wd) + (size_t)(j) * (64 * DM * 2); \
        const unsigned da_ = dstA + (slotoff), db_ = dstB + (slotoff); \
        glds16(b_, voffB0, db_); glds16(b_, voffB1, db_ + 1024); glds16(b_ + 128, voffB0, db_ + 8192); glds16(b_ + 128, voffB1, db_ + 8192 + 1024); \
        glds16(a_, voffA0, da_); glds16(a_, voffA1, da_ + 1024); glds16(a_ + 16 * ACT_LD * 2, voffA0, da_ + 2048); glds16(a_ + 16 * ACT_LD * 2, voffA1, da_ + 3072); } while (0)
    G2_LOAD(g, g * G2_SLOT);
    G2_LOAD(g + 2, (g + 2) * G2_SLOT);

    const int fr = lane & 15, fq = lane >> 4;
    const lds_ptr aB0 = lds + (wm * 64 + fr) * 128 + ((fq ^ (fr >> 1)) * 16);
    const lds_ptr aB1 = lds + (wm * 64 + fr) * 128 + (((4 | fq) ^ (fr >> 1)) * 16);
    const int tq = (lane >> 2) & 3, tp = lane & 3, tg = (tq & 2) | ((fq & 1) << 2);
    lds_ptr wB[4];
#pragma unroll
    for (int b = 0; b < 4; ++b) wB[b] = lds + 16384 + wn * 8192 + (8 * fq + tq) * 128 + (((2 * b + (tp >> 1)) ^ tg) * 16) + (tp & 1) * 8;
    f32x4 acc[4][4];
#pragma unroll
    for (int m = 0; m < 4; ++m)
#pragma unroll
        for (int b = 0; b < 4; ++b) acc[m][b] = (f32x4){0.f, 0.f, 0.f, 0.f};
    bf16x8 af[2][4], wf[2][4];

    if (g == 1) BAR();
    WAIT_V(8);
    BAR();
    SBAR();
    STAMP(2, 1);
#define G2_RW(kk, b) wf[kk][b] = cat8(lds_tr(wB[b] + so + (kk) * 4096), lds_tr(wB[b] + so + (kk) * 4096 + 512))
#define G2_RA(m) do { af[0][m] = lds_b128(aB0 + so + (m) * 2048); af[1][m] = lds_b128(aB1 + so + (m) * 2048); } while (0)
#define G2_MMA() do { __builtin_amdgcn_s_setprio(1); \
        _Pragma("unroll") for (int kk = 0; kk < 2; ++kk) _Pragma("unroll") for (int m = 0; m < 4; ++m) _Pragma("unroll") for (int b = 0; b < 4; ++b) MFMA16_ACC(acc[m][b], wf[kk][b], af[kk][m]); \
        __builtin_amdgcn_s_setprio(0); SBAR(); } while (0)
    int sl = g;
    for (int j = g; j < 32; j += 2) {
        const int so = sl * G2_SLOT;
        const int sp = ((sl == 0) ? 4 : sl - 1) * G2_SLOT;
        const char* a_ = Ag + (j + 4) * 128; const char* b_ = ((z == 1 && j + 4 >= 20) ? Wo : Wd) + (size_t)(j + 4) * (64 * DM * 2);
        const unsigned da_ = dstA + sp, db_ = dstB + sp;
        glds16(b_, voffB0, db_);                        G2_RW(0, 0); G2_RW(0, 1);
        glds16(b_, voffB1, db_ + 1024);                 G2_RW(0, 2); G2_RW(0, 3);
        glds16(b_ + 128, voffB0, db_ + 8192);           G2_RW(1, 0); G2_RW(1, 1);
        glds16(b_ + 128, voffB1, db_ + 8192 + 1024);    G2_RW(1, 2); G2_RW(1, 3);
        glds16(a_, voffA0, da_);                        G2_RA(0);
        glds16(a_, voffA1, da_ + 1024);                 G2_RA(1);
        glds16(a_ + 16 * ACT_LD * 2, voffA0, da_ + 2048); G2_RA(2); G2_RA(3);
        glds16(a_ + 16 * ACT_LD * 2, voffA1, da_ + 3072);
        WAIT_L0(); SBAR(); BAR(); SBAR();
        G2_MMA();
        WAIT_V(8);
        BAR(); SBAR();
        sl += 2; if (sl >= 5) sl -= 5;
    }
#pragma unroll
    for (int i = 0; i < 2; ++i) {
        const int so = sl * G2_SLOT;
#pragma unroll
        for (int b = 0; b < 4; ++b) { G2_RW(0, b); G2_RW(1, b); }
#pragma unroll
        for (int m = 0; m < 4; ++m) G2_RA(m);
        WAIT_L0(); SBAR(); BAR(); SBAR();
        G2_MMA();
        if (i == 0) { WAIT_V(0); BAR(); } else if (g == 0) BAR();
        SBAR();
        sl += 2; if (sl >= 5) sl -= 5;
    }
    MFMA_DRAIN(); SBAR();
    STAMP(2, 2);
    const int tile = rbk * 8 + cbk;
    int tz = tid; asm volatile("" : "+v"(tz));
    const int ln = tz & 63, fr2 = ln & 15, fq2 = ln >> 4;
    {
        const lds_ptr wp = lds + wid * 16384 + ln * 16;
#pragma unroll
        for (int i = 0; i < 16; ++i) *(LAS f32x4*)(wp + i * 1024) = acc[i >> 2][i & 3];
    }
    const int om = wid >> 1, on = wid & 1;
    const int row = rbk * 128 + z * 64 + om * 16 + fr2, col = cbk * 128 + on * 64 + (fq2 & 1) * 16 + (fq2 >> 1) * 8;
    const u32x4 xr0 = *(const u32x4*)(x + (size_t)row * DM + col), xr1 = *(const u32x4*)(x + (size_t)row * DM + col + 32);
    WAIT_L0(); BAR(); asm volatile("" ::: "memory");
    f32x4 vs[4], vk[4];
    {
        const lds_ptr rs_ = lds + (((1 - z) * 2 + on) * 16 + om * 4) * 1024 + ln * 16, rk_ = lds + ((z * 2 + on) * 16 + om * 4) * 1024 + ln * 16;
#pragma unroll
        for (int b = 0; b < 4; ++b) vs[b] = *(const LAS f32x4*)(rs_ + b * 1024) + *(const LAS f32x4*)(rs_ + 65536 + b * 1024);
        const __amdgpu_buffer_rsrc_t rs = __builtin_amdgcn_make_buffer_rsrc((void*)(xch + (size_t)(tile * 2 + 1 - z) * 8192), 0, 32768, 0x00020000);
#pragma unroll
        for (int b = 0; b < 4; ++b) __builtin_amdgcn_raw_buffer_store_b128(__builtin_bit_cast(u32x4, vs[b]), rs, wid * 4096 + b * 1024 + ln * 16, 0, 16);
#pragma unroll
        for (int b = 0; b < 4; ++b) vk[b] = *(const LAS f32x4*)(rk_ + b * 1024) + *(const LAS f32x4*)(rk_ + 65536 + b * 1024);
    }
    STAMP(2, 3);
    WAIT_V(0); BAR();
    if (wid == 0) {
        if (ln == 0) __hip_atomic_store((GAS unsigned*)(flags + (tile * 2 + 1 - z) * 32), 1u, __ATOMIC_RELAXED, __HIP_MEMORY_SCOPE_AGENT);
        for (unsigned spin = 0; spin < (1u << 22); ++spin) {
            if (__builtin_amdgcn_readfirstlane(__hip_atomic_load((GAS unsigned*)(flags + (tile * 2 + z) * 32), __ATOMIC_RELAXED, __HIP_MEMORY_SCOPE_AGENT)) != 0u) break;
            __builtin_amdgcn_s_sleep(1);
        }
    }
    asm volatile("" ::: "memory"); BAR(); asm volatile("" ::: "memory");
    {
        const __amdgpu_buffer_rsrc_t rr = __builtin_amdgcn_make_buffer_rsrc((void*)(xch + (size_t)(tile * 2 + z) * 8192), 0, 32768, 0x00020000);
#pragma unroll
        for (int b = 0; b < 4; ++b) vk[b] += __builtin_bit_cast(f32x4, __builtin_amdgcn_raw_buffer_load_b128(rr, wid * 4096 + b * 1024 + ln * 16, 0, 16));
#pragma unroll
        for (int i = 0; i < 2; ++i) {
            const u32x4 a = __builtin_bit_cast(u32x4, vk[2 * i]), b = __builtin_bit_cast(u32x4, vk[2 * i + 1]);
            unsigned a0 = a[0], a1 = a[1], a2 = a[2], a3 = a[3], b0 = b[0], b1 = b[1], b2 = b[2], b3 = b[3];
            swap16(a0, b0); swap16(a1, b1); swap16(a2, b2); swap16(a3, b3);
            const u32x4 xx = i ? xr1 : xr0;
            u32x4 w;
            w[0] = cvtpk(__builtin_bit_cast(float, a0) + bf_lo(xx[0]), __builtin_bit_cast(float, a1) + bf_hi(xx[0]));
            w[1] = cvtpk(__builtin_bit_cast(float, a2) + bf_lo(xx[1]), __builtin_bit_cast(float, a3) + bf_hi(xx[1]));
            w[2] = cvtpk(__builtin_bit_cast(float, b0) + bf_lo(xx[2]), __builtin_bit_cast(float, b1) + bf_hi(xx[2]));
            w[3] = cvtpk(__builtin_bit_cast(float, b2) + bf_lo(xx[3]), __builtin_bit_cast(float, b3) + bf_hi(xx[3]));
            *(u32x4*)(out + (size_t)row * DM + col + i * 32) = w;
        }
    }
    STAMP(2, 4);
#undef G2_LOAD
}

extern "C" void kernel_launch(void* const* d_in, const int* in_sizes, int n_in, void* d_out, int out_size, void* d_ws, size_t ws_size, hipStream_t stream) {
    const bf16_t* x = (const bf16_t*)d_in[0];
    const bf16_t* wqkv = (const bf16_t*)d_in[1];
    const bf16_t* wo = (const bf16_t*)d_in[2];
    const bf16_t* wg = (const bf16_t*)d_in[4];
    const bf16_t* wu = (const bf16_t*)d_in[5];
    const bf16_t* wd = (const bf16_t*)d_in[6];
    unsigned char* ws = (unsigned char*)d_ws;
    unsigned* flags = (unsigned*)(ws + WS_FLAGS);
    bf16_t* qkv = (bf16_t*)(ws + WS_QKV);
    bf16_t* act = (bf16_t*)(ws + WS_ACT);
    float* xch = (float*)(ws + WS_XCH);
    bf16_t* xn = (bf16_t*)(ws + WS_XN);
    k_ln<<<512, 256, 0, stream>>>(x, xn, flags);
    k_gemm1<<<256, 512, 0, stream>>>(xn, wqkv, wg, wu, qkv, act);
    k_attn<<<256, 512, 0, stream>>>(qkv, act);
    k_gemm2<<<256, 512, 0, stream>>>(act, wd, wo, x, (bf16_t*)d_out, xch, flags);
}
```

```cpp
#include <hip/hip_runtime.h>
#include <stdint.h>

typedef unsigned short bf16_t;
typedef short bf16x8 __attribute__((ext_vector_type(8)));
typedef short s16x4 __attribute__((ext_vector_type(4)));
typedef float f32x2 __attribute__((ext_vector_type(2)));
typedef float f32x4 __attribute__((ext_vector_type(4)));
typedef float f32x16 __attribute__((ext_vector_type(16)));
typedef unsigned u32x4 __attribute__((ext_vector_type(4)));
typedef __bf16 bf16x2_t __attribute__((ext_vector_type(2)));
#define LAS __attribute__((address_space(3)))
#define GAS __attribute__((address_space(1)))
typedef LAS unsigned char* lds_ptr;

constexpr int SEQ = 2048, DM = 1024, FF = 3584, NQKV = 3072, NH = 32;
constexpr float LN_EPS = 1e-5f;
constexpr float LOG2E = 1.4426950408889634f;
constexpr float QSCALE = 0.17677669529663687f * LOG2E;

constexpr unsigned WS_FLAGS = 0;
constexpr unsigned WS_QKV = 256 * 1024;
constexpr unsigned WS_ACT = WS_QKV + 3u * SEQ * DM * 2;
constexpr unsigned WS_XCH = WS_ACT + 144u * SEQ * 64;
constexpr unsigned WS_XN = WS_XCH + 256u * 32768;
constexpr unsigned WS_END = WS_XN + SEQ * DM * 2;
constexpr int F_QKV = 0, F_H = 256, F_O = 512, F_X = 768;
constexpr unsigned GRP = SEQ * 64;

#define SBAR() __builtin_amdgcn_sched_barrier(0)
#define BAR() __builtin_amdgcn_s_barrier()
#define WAIT_V(n) asm volatile("s_waitcnt vmcnt(" #n ")" ::: "memory")
#define WAIT_L0() do { __builtin_amdgcn_s_waitcnt(0xC07F); asm volatile("" ::: "memory"); } while (0)
#define CFENCE() asm volatile("" ::: "memory")
#define PIN(x) asm volatile("" : "+v"(x))
#define MFMA16(a, b, c) __builtin_amdgcn_mfma_f32_16x16x32_bf16(a, b, c, 0, 0, 0)
#define MFMA32(a, b, c) __builtin_amdgcn_mfma_f32_32x32x16_bf16(a, b, c, 0, 0, 0)
#define MFMA16_ACC(acc, a, b) asm volatile("v_mfma_f32_16x16x32_bf16 %0, %1, %2, %0" : "+v"(acc) : "v"(a), "v"(b))
#define MFMA_DRAIN() asm volatile("s_nop 15" ::: "memory")
#define RLX_AGENT __ATOMIC_RELAXED, __HIP_MEMORY_SCOPE_AGENT

#define STAMP(i) do {} while (0)
#define TS(v) do {} while (0)
#define PUT(i, v) do {} while (0)

__device__ __forceinline__ void glds16(const void* sbase, unsigned voff, unsigned ldsdst) {
    asm volatile("s_mov_b32 m0, %2\n\ts_nop 0\n\tglobal_load_lds_dwordx4 %0, %1" ::"v"(voff), "s"(sbase), "s"(ldsdst) : "memory");
}
__device__ __forceinline__ bf16x8 lds_b128(lds_ptr p) { return *(const LAS bf16x8*)p; }
__device__ __forceinline__ s16x4 lds_tr(lds_ptr p) { return __builtin_amdgcn_ds_read_tr16_b64_v4i16((LAS s16x4*)p); }
__device__ __forceinline__ bf16x8 cat8(s16x4 a, s16x4 b) { return __builtin_shufflevector(a, b, 0, 1, 2, 3, 4, 5, 6, 7); }
__device__ __forceinline__ unsigned cvtpk(float lo, float hi) {
    f32x2 v = {lo, hi};
    return __builtin_bit_cast(unsigned, __builtin_convertvector(v, bf16x2_t));
}
__device__ __forceinline__ float u2f(unsigned u) { return __builtin_bit_cast(float, u); }
__device__ __forceinline__ unsigned f2u(float f) { return __builtin_bit_cast(unsigned, f); }
__device__ __forceinline__ float bf_lo(unsigned u) { return u2f(u << 16); }
__device__ __forceinline__ float bf_hi(unsigned u) { return u2f(u & 0xffff0000u); }
__device__ __forceinline__ void swap16(unsigned& a, unsigned& b) {
    auto r = __builtin_amdgcn_permlane16_swap(a, b, false, false);
    a = r[0]; b = r[1];
}
__device__ __forceinline__ void swap32(unsigned& a, unsigned& b) {
    auto r = __builtin_amdgcn_permlane32_swap(a, b, false, false);
    a = r[0]; b = r[1];
}
__device__ __forceinline__ int opaque_tid() { int t = threadIdx.x; asm volatile("" : "+v"(t)); return t; }
__device__ __forceinline__ void signal(unsigned* flags, int word, int lane) {
    if (lane == 0) (void)__hip_atomic_fetch_add((GAS unsigned*)(flags + word * 32), 1u, RLX_AGENT);
}
__device__ __forceinline__ unsigned peek(unsigned* flags, int word) { return __hip_atomic_load((GAS unsigned*)(flags + word * 32), RLX_AGENT); }
__device__ __forceinline__ void wait_all(unsigned* flags, int word, unsigned want, unsigned v) {
    unsigned spin = 0;
    while (!__all(v >= want)) { if (++spin > (1u << 20)) break; __builtin_amdgcn_s_sleep(1); v = peek(flags, word); }
}
#define PEEK_AHEAD(v, ptr, sel) asm volatile("s_cmp_lg_u32 %2, 0\n\ts_cbranch_scc1 1f\n\tglobal_load_dword %0, %1, off sc1\n1:" : "+v"(v) : "v"(ptr), "s"(sel) : "memory", "scc")
#define PEEK_DONE(v) asm volatile("s_waitcnt vmcnt(0)" : "+v"(v) :: "memory")

__device__ __forceinline__ float wave_sum(float v) {
#pragma unroll
    for (int o = 1; o < 16; o <<= 1) v += __shfl_xor(v, o);
    unsigned a = f2u(v), b = a;
    swap16(a, b);
    v = u2f(a) + u2f(b);
    a = f2u(v); b = a;
    swap32(a, b);
    return u2f(a) + u2f(b);
}
__global__ __launch_bounds__(256) void k_ln(const bf16_t* __restrict__ x, unsigned char* __restrict__ ws) {
    const int tid = threadIdx.x, lane = tid & 63, row = blockIdx.x * 4 + (tid >> 6);
    if (tid < 2) ((unsigned*)(ws + WS_FLAGS))[(blockIdx.x * 2 + tid) * 32] = 0u;
    const u32x4* src = (const u32x4*)(x + (size_t)row * DM) + lane;
    const u32x4 a = src[0], b = src[64];
    float v[16];
#pragma unroll
    for (int i = 0; i < 4; ++i) { v[2 * i] = bf_lo(a[i]); v[2 * i + 1] = bf_hi(a[i]); v[8 + 2 * i] = bf_lo(b[i]); v[8 + 2 * i + 1] = bf_hi(b[i]); }
    float s = 0.f;
#pragma unroll
    for (int i = 0; i < 16; ++i) s += v[i];
    const float mu = wave_sum(s) * (1.0f / DM);
    float q = 0.f;
#pragma unroll
    for (int i = 0; i < 16; ++i) { v[i] -= mu; q += v[i] * v[i]; }
    const float rs = 1.0f / __builtin_sqrtf(wave_sum(q) * (1.0f / DM) + LN_EPS);
    u32x4 oa, ob;
#pragma unroll
    for (int i = 0; i < 4; ++i) { oa[i] = cvtpk(v[2 * i] * rs, v[2 * i + 1] * rs); ob[i] = cvtpk(v[8 + 2 * i] * rs, v[8 + 2 * i + 1] * rs); }
    u32x4* dst = (u32x4*)(ws + WS_XN + (size_t)row * DM * 2) + lane;
    dst[0] = oa; dst[64] = ob;
}

constexpr int SLOT = 32768, LDS_BYTES = 5 * SLOT;
constexpr int G1_A = 0, G1_W = 65536;

struct G2 { const char* A; const char* Wd; const char* Wo; int z; };
__device__ __forceinline__ void g2_load_b(const G2& g, int j, unsigned dst, int vw, unsigned voffB) {
    const char* b = ((g.z == 1 && j >= 20) ? g.Wo : g.Wd) + (size_t)(j * 64 + vw * 8) * (DM * 2);
    glds16(b, voffB, dst + 16384 + vw * 1024); glds16(b + 128, voffB, dst + 16384 + 8192 + vw * 1024);
}
__device__ __forceinline__ void g2_load_a(const G2& g, int j, unsigned dst, int vw, unsigned voffA0, unsigned voffA1) {
    const char* a = g.A + (size_t)j * (2 * GRP) + vw * 1024;
    glds16(a, voffA0, dst + vw * 2048); glds16(a, voffA1, dst + vw * 2048 + 1024);
}
__device__ __forceinline__ void g2_load(const G2& g, int j, unsigned dst, int vw, unsigned voffA0, unsigned voffA1, unsigned voffB) {
    g2_load_b(g, j, dst, vw, voffB); g2_load_a(g, j, dst, vw, voffA0, voffA1);
}
__device__ __forceinline__ void g2_voff(int lane, unsigned& voffA0, unsigned& voffA1, unsigned& voffB0, unsigned& voffB1) {
    const int r8 = lane >> 3, u8 = lane & 7, ua = u8 ^ (r8 >> 1), ub = ua ^ 4;
    voffA0 = (unsigned)(ua >> 2) * GRP + r8 * 64 + (ua & 3) * 16;
    voffA1 = (unsigned)(ub >> 2) * GRP + (r8 + 8) * 64 + (ub & 3) * 16;
    voffB0 = (unsigned)(r8 * (DM * 2) + (u8 ^ (r8 & 2)) * 16);
    voffB1 = (unsigned)(r8 * (DM * 2) + (u8 ^ (4 | (r8 & 2))) * 16);
}

struct AttnOut { f32x16 o0, o1; f32x4 l0, l1; float m0, m1; };
struct HPend { u32x4 v[8]; unsigned off; unsigned step; int n; };
template <bool SAFE>
__device__ __forceinline__ void attn_pass(lds_ptr lds, unsigned lds0, const char* Qg, const char* Kg, const char* Vg, const G2& g2, unsigned* flags, int hword, int hsig,
                                          const HPend& hp, __amdgpu_buffer_rsrc_t wsr, const char* wpf, int wid, AttnOut& R
                                          ) {
    const int lane = opaque_tid() & 63, r32 = lane & 31, hi = lane >> 5, kh = wid >> 2, qg = wid & 3;
    const int uk = (lane & 3) ^ ((lane >> 4) & 3), uv = lane & 3;
    const unsigned voffK = (unsigned)((uk >> 1) * 65536 + wid * 1024 + (lane >> 2) * 32 + (uk & 1) * 16);
    const unsigned voffV = (unsigned)((uv >> 1) * 65536 + wid * 1024 + (lane >> 2) * 32 + (uv & 1) * 16);
    const unsigned dst = lds0 + wid * 2048;
    unsigned gA0, gA1, gB0, gB1; g2_voff(lane, gA0, gA1, gB0, gB1);
    const unsigned gB = (wid & 1) ? gB1 : gB0;
#define AT_LOAD_K(c) do { const unsigned d_ = dst + ((c) & 3) * SLOT; const char* k_ = Kg + (c) * 8192; glds16(k_, voffK, d_); glds16(k_ + 512, voffK, d_ + 1024); } while (0)
#define AT_LOAD_V(c) do { const unsigned d_ = dst + ((c) & 3) * SLOT + 16384; const char* v_ = Vg + (c) * 8192; glds16(v_, voffV, d_); glds16(v_ + 512, voffV, d_ + 1024); } while (0)
    const char* qp = Qg + (qg * 64 + r32) * 32 + hi * 16;
    bf16x8 q00, q01, q10, q11;
    AT_LOAD_K(0);
    asm volatile("global_load_dwordx4 %0, %4, off\n\tglobal_load_dwordx4 %1, %4, off offset:1024\n\tglobal_load_dwordx4 %2, %5, off\n\tglobal_load_dwordx4 %3, %5, off offset:1024"
                 : "=&v"(q00), "=&v"(q10), "=&v"(q01), "=&v"(q11) : "v"(qp), "v"(qp + 65536) : "memory");
    AT_LOAD_V(0); AT_LOAD_K(1); AT_LOAD_V(1);
    const int ksw = (r32 >> 2) & 3;
    const lds_ptr kb0 = lds + kh * 8192 + r32 * 64 + ((hi ^ ksw) * 16), kb1 = lds + kh * 8192 + r32 * 64 + (((2 | hi) ^ ksw) * 16);
    const lds_ptr vb = lds + 16384 + kh * 8192 + (4 * hi + ((lane >> 2) & 3)) * 64 + ((lane >> 4) & 1) * 32 + (lane & 3) * 8;
    bf16x8 ones;
    { const short v = (((lane >> 4) & 1) == ((lane >> 2) & 1)) ? (short)0x3f80 : (short)0; ones = (bf16x8){v, v, v, v, v, v, v, v}; }
    f32x16 o0 = (f32x16){}, o1 = (f32x16){}, negm0 = (f32x16){}, negm1 = (f32x16){}, sA0, sA1, sB0, sB1;
    f32x4 l0 = (f32x4){0.f, 0.f, 0.f, 0.f}, l1 = l0;
    bf16x8 kfa, kfb, va, vc, pf;
    float m0, m1;
    asm volatile("s_waitcnt vmcnt(6)" : "+v"(q00), "+v"(q01), "+v"(q10), "+v"(q11) :: "memory");
    BAR(); SBAR();
    STAMP(15);
#define AT_ROWMAX(S, RM) do { float a_ = S[0]; _Pragma("unroll") for (int r = 1; r < 16; ++r) a_ = __builtin_fmaxf(a_, S[r]); \
        unsigned x_ = f2u(a_), y_ = x_; swap32(x_, y_); RM = __builtin_fmaxf(u2f(x_), u2f(y_)); } while (0)
    {
        kfa = lds_b128(kb0); kfb = lds_b128(kb1);
        sA0 = MFMA32(kfa, q00, negm0); sA1 = MFMA32(kfa, q10, negm1); sA0 = MFMA32(kfb, q01, sA0); sA1 = MFMA32(kfb, q11, sA1);
        kfa = lds_b128(kb0 + 2048); kfb = lds_b128(kb1 + 2048);
        AT_ROWMAX(sA0, m0); AT_ROWMAX(sA1, m1);
#pragma unroll
        for (int r = 0; r < 16; ++r) { sA0[r] -= m0; sA1[r] -= m1; negm0[r] = -m0; negm1[r] = -m1; }
        PIN(negm0); PIN(negm1);
        vc = (bf16x8){}; pf = (bf16x8){};
    }
    u32x4 h0 = hp.v[0], h1 = hp.v[1], h2 = hp.v[2], h3 = hp.v[3], h4 = hp.v[4], h5 = hp.v[5], h6 = hp.v[6], h7 = hp.v[7];
    unsigned hoff = hp.off;
#define AT_HSTORE() do { __builtin_amdgcn_raw_buffer_store_b128(h0, wsr, hoff, 0, 16); __builtin_amdgcn_raw_buffer_store_b128(h1, wsr, hoff + hp.step, 0, 16); \
        hoff += 2 * hp.step; h0 = h2; h1 = h3; h2 = h4; h3 = h5; h4 = h6; h5 = h7; CFENCE(); } while (0)
    WAIT_V(4); BAR(); SBAR();
    STAMP(5);
    if (!SAFE) AT_HSTORE();
    AT_LOAD_K(2); AT_LOAD_V(2);
#define EX(v) __builtin_amdgcn_exp2f(v)
#define AT_GROUP(QK, RD, S, i, O, L, VF) do { \
        QK; SBAR(); RD; SBAR(); \
        S[i] = EX(S[i]); S[i + 1] = EX(S[i + 1]); S[i + 2] = EX(S[i + 2]); PIN(S); SBAR(); \
        O = MFMA32(VF, pf, O); SBAR(); \
        S[i + 3] = EX(S[i + 3]); S[i + 4] = EX(S[i + 4]); S[i + 5] = EX(S[i + 5]); PIN(S); SBAR(); \
        L = MFMA16(ones, pf, L); SBAR(); \
        S[i + 6] = EX(S[i + 6]); S[i + 7] = EX(S[i + 7]); \
        pf = __builtin_bit_cast(bf16x8, (u32x4){cvtpk(S[i], S[i + 1]), cvtpk(S[i + 2], S[i + 3]), cvtpk(S[i + 4], S[i + 5]), cvtpk(S[i + 6], S[i + 7])}); PIN(pf); SBAR(); } while (0)
#define AT_FIX(N, M, NEGM, O, L) do { float rm_; AT_ROWMAX(N, rm_); const float d_ = __builtin_fmaxf(rm_, 0.f); M += d_; const float f_ = EX(-d_); \
        _Pragma("unroll") for (int r = 0; r < 16; ++r) { N[r] -= d_; NEGM[r] = -M; O[r] *= f_; } L *= f_; } while (0)
#define AT_STEP(C0, C1, N0, N1, KO, VO, LIVE) do { \
        AT_GROUP(N0 = MFMA32(kfa, q00, negm0), va = cat8(lds_tr(vb + (VO)), lds_tr(vb + (VO) + 512)), C0, 0, o1, l1, vc); \
        AT_GROUP(N1 = MFMA32(kfa, q10, negm1), { kfa = lds_b128(kb0 + (KO)); vc = cat8(lds_tr(vb + (VO) + 1024), lds_tr(vb + (VO) + 1536)); }, C1, 0, o0, l0, va); \
        AT_GROUP(N0 = MFMA32(kfb, q01, N0), (void)0, C0, 8, o1, l1, va); \
        AT_GROUP(N1 = MFMA32(kfb, q11, N1), kfb = lds_b128(kb1 + (KO)), C1, 8, o0, l0, vc); \
        if (SAFE) { o1 = MFMA32(vc, pf, o1); l1 = MFMA16(ones, pf, l1); pf = (bf16x8){}; \
            if (LIVE) { AT_FIX(N0, m0, negm0, o0, l0); AT_FIX(N1, m1, negm1, o1, l1); } SBAR(); } } while (0)
    unsigned hv = 0, pfd = 0;
    const unsigned pf0 = (unsigned)((lane >> 3) * (DM * 2) + (lane & 7) * 128), pf1 = (unsigned)(8 * (DM * 2) + (lane & 7) * 128);
    const unsigned* const hptr = flags + (hword + (lane & 31)) * 32;
#pragma unroll 1
    for (int c = 0; c < 8; ++c) {
        const int so = (c & 3) * SLOT, sn = (c == 7) ? so : ((c + 1) & 3) * SLOT;
        const int sel = (c ^ 5) | wid;
        PEEK_AHEAD(hv, hptr, sel);
        if (c == 7) g2_load_b(g2, 1, lds0, wid, gB);
        AT_STEP(sA0, sA1, sB0, sB1, so + 4096, so, true);
        if (c == 7) g2_load_a(g2, 1, lds0, wid, gA0, gA1);
        AT_STEP(sB0, sB1, sA0, sA1, so + 6144, so + 2048, true);
        STAMP(18 + 3 * c);
        if (sel == 0) { PEEK_DONE(hv); wait_all(flags, hword + (lane & 31), 8u, hv); __builtin_amdgcn_fence(__ATOMIC_ACQUIRE, "agent"); }
        if (c == 6) WAIT_V(0); else WAIT_V(4);
        if (!SAFE && c == 3) signal(flags, hsig, lane);
        STAMP(19 + 3 * c);
        WAIT_L0(); BAR(); SBAR();
        STAMP(20 + 3 * c);
        if (!SAFE) {
            if (c < hp.n) AT_HSTORE();
            const int psel = c ^ 3;
            asm volatile("s_cmp_lg_u32 %3, 0\n\ts_cbranch_scc1 1f\n\tglobal_load_dword %0, %1, %4\n\tglobal_load_dword %0, %2, %4\n1:"
                         : "+v"(pfd) : "v"(pf0), "v"(pf1), "s"(psel), "s"(wpf) : "memory", "scc");
        }
        if (c < 5) AT_LOAD_K(c + 3); else if (c > 5) g2_load_b(g2, 2 * (c - 6), lds0 + ((c == 6) ? 4 : 1) * SLOT, wid, gB);
        AT_STEP(sA0, sA1, sB0, sB1, sn, so + 4096, true);
        if (c < 5) AT_LOAD_V(c + 3); else if (c > 5) g2_load_a(g2, 2 * (c - 6), lds0 + ((c == 6) ? 4 : 1) * SLOT, wid, gA0, gA1);
        AT_STEP(sB0, sB1, sA0, sA1, sn + 2048, so + 6144, c < 7);
    }
    o1 = MFMA32(vc, pf, o1); l1 = MFMA16(ones, pf, l1);
    asm volatile("" :: "v"(pfd));
    R.o0 = o0; R.o1 = o1; R.l0 = l0; R.l1 = l1; R.m0 = m0; R.m1 = m1;
#undef AT_LOAD_K
#undef AT_LOAD_V
#undef AT_HSTORE
#undef AT_ROWMAX
#undef AT_GROUP
#undef AT_FIX
#undef AT_STEP
}

__global__ __launch_bounds__(512, 2) void k_main(const bf16_t* __restrict__ x, const bf16_t* __restrict__ wqkv, const bf16_t* __restrict__ wo, const bf16_t* __restrict__ wg,
                                                 const bf16_t* __restrict__ wu, const bf16_t* __restrict__ wd, bf16_t* __restrict__ out, unsigned char* ws) {
    __shared__ __attribute__((aligned(16))) unsigned char smem[LDS_BYTES];
    const lds_ptr lds = (lds_ptr)smem;
    const unsigned lds0 = (unsigned)(uintptr_t)lds;
    const int wid = __builtin_amdgcn_readfirstlane(threadIdx.x >> 6);
    const int bid = blockIdx.x, xcd = bid & 7, idx = bid >> 3;
    unsigned* const flags = (unsigned*)(ws + WS_FLAGS);
    const __amdgpu_buffer_rsrc_t wsr = __builtin_amdgcn_make_buffer_rsrc((void*)ws, 0, WS_END, 0x00020000);
#define ST16(v, off) __builtin_amdgcn_raw_buffer_store_b128(v, wsr, off, 0, 16)
    STAMP(0);
    HPend hp;
    {
        const int lane = threadIdx.x & 63, wr = wid >> 2, wc = wid & 3, js = idx & 3, rb = idx >> 2;
        const bool typeY = (js == 3);
        const int f0 = 448 * xcd + 128 * js;
        const char* P0 = (const char*)(wg + f0);
        const char* P2 = (const char*)(wu + f0);
        const char* P1 = typeY ? (const char*)(wqkv + 128 * xcd + 64) : (const char*)(wg + f0 + 64);
        const char* P3 = typeY ? (const char*)(wqkv + 1024 + 128 * xcd + 64) : (const char*)(wu + f0 + 64);
        const char* P4 = typeY ? (const char*)(wqkv + 2048 + 128 * xcd + 64) : (const char*)(wqkv + js * 1024 + 128 * xcd);
        const unsigned stepF = 32u * FF * 2u, stepQ = 32u * NQKV * 2u, step13 = typeY ? stepQ : stepF;
        const char* XA = (const char*)(ws + WS_XN) + (size_t)rb * 256 * DM * 2;
        const int r8 = lane >> 3, u8 = lane & 7;
        const int gw = (r8 & 2) | ((wc & 1) << 2);
        const unsigned voffF = (unsigned)(((wc * 8 + r8) * FF + (u8 ^ gw) * 8) * 2);
        const unsigned voffQ = (unsigned)(((wc * 8 + r8) * NQKV + (u8 ^ gw) * 8) * 2);
        const unsigned voff13 = typeY ? voffQ : voffF;
        const unsigned voffA0 = (unsigned)(((32 * wid + r8) * DM + (u8 ^ (r8 >> 1)) * 8) * 2);
        const unsigned voffA1 = (unsigned)(((32 * wid + 8 + r8) * DM + (u8 ^ (4 | (r8 >> 1))) * 8) * 2);
        const unsigned dstA = lds0 + G1_A + wid * 4096, dstW = lds0 + G1_W + wc * 1024;
#define G1_LOAD_A(t) do { const char* b_ = XA + (t) * 128; const unsigned d_ = dstA + ((t) & 1) * 32768; \
        glds16(b_, voffA0, d_); glds16(b_, voffA1, d_ + 1024); glds16(b_ + 32768, voffA0, d_ + 2048); glds16(b_ + 32768, voffA1, d_ + 3072); } while (0)
#define G1_LOAD_W(hs) do { const unsigned d_ = dstW + ((hs) & 3) * 4096; \
        glds16(P0 + (size_t)(hs) * stepF, voffF, d_); glds16(P1 + (size_t)(hs) * step13, voff13, d_ + 16384); glds16(P2 + (size_t)(hs) * stepF, voffF, d_ + 32768); \
        glds16(P3 + (size_t)(hs) * step13, voff13, d_ + 49152); glds16(P4 + (size_t)(hs) * stepQ, voffQ, d_ + 65536); } while (0)
        if (wr == 0) { G1_LOAD_W(0); G1_LOAD_A(0); G1_LOAD_W(1); } else { G1_LOAD_A(0); G1_LOAD_W(2); }

        int fr = lane & 15, fq = lane >> 4;
        const lds_ptr aB0 = lds + G1_A + (wr * 128 + fr) * 128 + ((fq ^ (fr >> 1)) * 16);
        const lds_ptr aB1 = lds + G1_A + (wr * 128 + fr) * 128 + (((4 | fq) ^ (fr >> 1)) * 16);
        const int tq = (lane >> 2) & 3, tp = lane & 3, tg = (tq & 2) | ((fq & 1) << 2);
        auto wbase = [&](int panel, int bp) { return lds + G1_W + panel * 16384 + (8 * fq + tq) * 128 + (((2 * bp + (tp >> 1)) ^ tg) * 16) + (tp & 1) * 8; };
        const int pX = wc >> 1, bX = 2 * (wc & 1);
        const lds_ptr wB0 = typeY ? wbase(0, wc) : wbase(pX, bX);
        const lds_ptr wB1 = typeY ? wbase(1, wc) : wbase(pX, bX + 1);
        const lds_ptr wB2 = typeY ? wbase(2, wc) : wbase(2 + pX, bX);
        const lds_ptr wB3 = typeY ? wbase(3, wc) : wbase(2 + pX, bX + 1);
        const lds_ptr wB4 = wbase(4, wc);
        f32x4 acc[8][5];
#pragma unroll
        for (int m = 0; m < 8; ++m)
#pragma unroll
            for (int b = 0; b < 5; ++b) acc[m][b] = (f32x4){0.f, 0.f, 0.f, 0.f};
        bf16x8 af[8], wf[5];
#define G1_RW(i, B, slot) wf[i] = cat8(lds_tr(B + (slot) * 4096), lds_tr(B + (slot) * 4096 + 512))
#define G1_RA(m, AB, abuf) af[m] = lds_b128(AB + (abuf) * 32768 + (m) * 2048)
#define G1_SEG(AB, abuf, slot, L0, L1, L2, L3, L4) do { \
        L0; G1_RW(0, wB0, slot); G1_RW(1, wB1, slot); L1; G1_RW(2, wB2, slot); G1_RW(3, wB3, slot); L2; G1_RW(4, wB4, slot); G1_RA(0, AB, abuf); G1_RA(1, AB, abuf); \
        L3; G1_RA(2, AB, abuf); G1_RA(3, AB, abuf); G1_RA(4, AB, abuf); L4; G1_RA(5, AB, abuf); G1_RA(6, AB, abuf); G1_RA(7, AB, abuf); \
        WAIT_L0(); SBAR(); BAR(); SBAR(); } while (0)
#define G1_SEG_A(abuf, slot, t) do { const char* b_ = XA + (t) * 128; const unsigned d_ = dstA + ((t) & 1) * 32768; \
        G1_SEG(aB0, abuf, slot, glds16(b_, voffA0, d_), glds16(b_, voffA1, d_ + 1024), glds16(b_ + 32768, voffA0, d_ + 2048), glds16(b_ + 32768, voffA1, d_ + 3072), (void)0); } while (0)
#define G1_SEG_W(abuf, slot, hs) do { const unsigned d_ = dstW + ((hs) & 3) * 4096; \
        G1_SEG(aB1, abuf, slot, glds16(P0 + (size_t)(hs) * stepF, voffF, d_), glds16(P1 + (size_t)(hs) * step13, voff13, d_ + 16384), glds16(P2 + (size_t)(hs) * stepF, voffF, d_ + 32768), \
               glds16(P3 + (size_t)(hs) * step13, voff13, d_ + 49152), glds16(P4 + (size_t)(hs) * stepQ, voffQ, d_ + 65536)); } while (0)
#define G1_SEG_N(AB, abuf, slot) G1_SEG(AB, abuf, slot, (void)0, (void)0, (void)0, (void)0, (void)0)
#define G1_MMA() do { __builtin_amdgcn_s_setprio(1); \
        _Pragma("unroll") for (int b = 0; b < 5; ++b) _Pragma("unroll") for (int m = 0; m < 8; ++m) MFMA16_ACC(acc[m][b], wf[b], af[m]); \
        __builtin_amdgcn_s_setprio(0); SBAR(); } while (0)

        if (wr == 1) BAR();
        WAIT_V(5);
        BAR();
        SBAR();
        STAMP(1);
        for (int t = 0; t < 14; t += 2) {
            G1_SEG_A(0, 0, t + 1);          G1_MMA(); WAIT_V(4); BAR(); SBAR();
            G1_SEG_W(0, 1, 2 * t + 3 + wr); G1_MMA(); WAIT_V(5); BAR(); SBAR();
            G1_SEG_A(1, 2, t + 2);          G1_MMA(); WAIT_V(4); BAR(); SBAR();
            G1_SEG_W(1, 3, 2 * t + 5 + wr); G1_MMA(); WAIT_V(5); BAR(); SBAR();
        }
        G1_SEG_A(0, 0, 15); G1_MMA(); WAIT_V(4); BAR(); SBAR();
        if (wr == 0) G1_LOAD_W(31);
        G1_SEG_N(aB1, 0, 1); G1_MMA();
        if (wr == 0) WAIT_V(5); else WAIT_V(0);
        BAR(); SBAR();
        G1_SEG_N(aB0, 1, 2); G1_MMA(); WAIT_V(0); BAR(); SBAR();
        G1_SEG_N(aB1, 1, 3); G1_MMA();
        MFMA_DRAIN(); SBAR();
        if (wr == 0) BAR();
        SBAR();
        STAMP(2);

        const int tz = opaque_tid(), ln = tz & 63;
        fr = tz & 15; fq = (tz >> 4) & 3;
        const int row0 = rb * 256 + wr * 128 + fr;
        auto glu = [&](const f32x4& g, const f32x4& u) {
            f32x4 h;
#pragma unroll
            for (int j = 0; j < 4; ++j) h[j] = u[j] * __builtin_amdgcn_rcpf(1.0f + __builtin_amdgcn_exp2f(g[j] * -LOG2E));
            return h;
        };
        auto pack2 = [&](const f32x4& v0, const f32x4& v1) {
            unsigned p0 = cvtpk(v0[0], v0[1]), p1 = cvtpk(v0[2], v0[3]), q0 = cvtpk(v1[0], v1[1]), q1 = cvtpk(v1[2], v1[3]);
            swap16(p0, q0); swap16(p1, q1);
            return (u32x4){p0, p1, q0, q1};
        };
        const int hd = 4 * xcd + (typeY ? 2 : 0) + (wc >> 1);
        const unsigned qoff = WS_QKV + (unsigned)(hd * 2 + (wc & 1)) * 65536u + (unsigned)(row0 + (fq & 1) * 16) * 32u + (fq >> 1) * 16;
        constexpr unsigned KIND = NH * 2 * 65536u;
        if (!typeY) {
            const float sc4 = (js == 0) ? QSCALE : 1.0f;
#pragma unroll
            for (int m = 0; m < 8; m += 2) ST16(pack2(acc[m][4] * sc4, acc[m + 1][4] * sc4), qoff + js * KIND + m * 512);
            CFENCE();
            hp.off = WS_ACT + (unsigned)(14 * xcd + 4 * js + wc) * GRP + (unsigned)row0 * 64u + (fq & 1) * 32 + (fq >> 1) * 16;
            hp.n = 3; hp.step = 1024;
#pragma unroll
            for (int m = 0; m < 8; ++m) {
                hp.v[m] = pack2(glu(acc[m][0], acc[m][2]), glu(acc[m][1], acc[m][3]));
                if (m == 5) { PIN(hp.v[5]); WAIT_V(0); signal(flags, F_QKV + bid, ln); }
            }
        } else {
#pragma unroll
            for (int m = 0; m < 8; m += 2) {
                ST16(pack2(acc[m][3], acc[m + 1][3]), qoff + KIND + m * 512);
                ST16(pack2(acc[m][4], acc[m + 1][4]), qoff + 2 * KIND + m * 512);
                ST16(pack2(acc[m][1] * QSCALE, acc[m + 1][1] * QSCALE), qoff + m * 512);
            }
            CFENCE();
            hp.off = WS_ACT + (unsigned)(14 * xcd + 12 + (wc >> 1)) * GRP + (unsigned)(row0 + (fq & 1) * 16) * 64u + (wc & 1) * 32 + (fq >> 1) * 16;
            hp.n = 1; hp.step = 2048;
#pragma unroll
            for (int m = 0; m < 8; m += 2) { hp.v[m >> 1] = pack2(glu(acc[m][0], acc[m][2]), glu(acc[m + 1][0], acc[m + 1][2])); hp.v[4 + (m >> 1)] = hp.v[m >> 1]; }
            PIN(hp.v[3]); WAIT_V(0); signal(flags, F_QKV + bid, ln);
        }
#pragma unroll
        for (int i = 0; i < 8; ++i) PIN(hp.v[i]);
        STAMP(3);
    }

    const int z = xcd & 1, rbk = ((xcd >> 1) & 1) * 8 + (idx & 7), cbk = (xcd >> 2) * 4 + (idx >> 3);
    G2 g2;
    g2.A = (const char*)ws + WS_ACT + (size_t)z * 72 * GRP + rbk * 128 * 64;
    g2.Wd = (const char*)(wd + (size_t)z * 2304 * DM + cbk * 128);
    g2.Wo = (const char*)(wo + cbk * 128) - (size_t)20 * 64 * DM * 2;
    g2.z = z;
    {
        const int hl = idx & 3, qb = idx >> 2, h = 4 * xcd + hl;
        if (wid == 0) {
            const int ln = opaque_tid() & 63;
            const int p = (hl >= 2) ? (ln & 7) * 4 + 3 : (ln < 8 ? ln * 4 + 1 : (ln < 16 ? (ln - 8) * 4 + 2 : qb * 4));
            const int word = F_QKV + p * 8 + xcd;
            wait_all(flags, word, 8u, peek(flags, word));
            STAMP(13);
            __builtin_amdgcn_fence(__ATOMIC_ACQUIRE, "agent");
            WAIT_V(0);
            STAMP(14);
        }
        CFENCE(); BAR(); CFENCE();
        STAMP(4);
        const char* Qg = (const char*)ws + WS_QKV + (size_t)h * 131072 + qb * 256 * 32;
        const char* Kg = (const char*)ws + WS_QKV + (size_t)(NH + h) * 131072;
        const char* Vg = (const char*)ws + WS_QKV + (size_t)(2 * NH + h) * 131072;
        const int hword = F_H + (rbk >> 1) * 32;
        const int prow = z * 2304 + idx * 72 + wid * 9;
        const int prow_c = (prow < FF && prow + 9 > FF) ? FF - 9 : prow;
        const char* wpf = (prow_c < FF ? (const char*)(wd + (size_t)prow_c * DM) : (const char*)(wo + (size_t)(prow_c - FF) * DM)) + (xcd >> 2) * 1024;
        AttnOut R;
#define DG
        attn_pass<false>(lds, lds0, Qg, Kg, Vg, g2, flags, hword, F_H + bid, hp, wsr, wpf, wid, R DG);
        STAMP(6);
        const int tz = opaque_tid(), ln = tz & 63, w4 = wid & 3, fr = ln & 15, fq = ln >> 4;
        const lds_ptr po = lds + 2 * SLOT + w4 * 8192 + ln * 16, pl = lds + 3 * SLOT + 4096 + w4 * 1024 + ln * 16;
        volatile LAS unsigned* const badw = (volatile LAS unsigned*)(lds + 3 * SLOT + 8192 + 4096);
#define AT_PUBLISH() do { if (wid < 4) { \
            _Pragma("unroll") for (int i = 0; i < 4; ++i) { *(LAS f32x4*)(po + i * 1024) = (f32x4){R.o0[4 * i], R.o0[4 * i + 1], R.o0[4 * i + 2], R.o0[4 * i + 3]}; \
                *(LAS f32x4*)(po + 4096 + i * 1024) = (f32x4){R.o1[4 * i], R.o1[4 * i + 1], R.o1[4 * i + 2], R.o1[4 * i + 3]}; } \
            *(LAS f32x4*)pl = (f32x4){R.l0[0], R.m0, R.l1[0], R.m1}; } } while (0)
        {
            AT_PUBLISH();
            bool ok = (R.l0[0] > 0.f) && (R.l0[0] < 0x1p96f) && (R.l1[0] > 0.f) && (R.l1[0] < 0x1p96f);
#pragma unroll
            for (int r = 0; r < 16; ++r) ok = ok && (__builtin_fabsf(R.o0[r]) <= 3.0e38f) && (__builtin_fabsf(R.o1[r]) <= 3.0e38f);
            const bool wave_bad = !__all(ok);
            if (ln == 0) badw[wid] = wave_bad ? 1u : 0u;
            STAMP(16);
            WAIT_V(0); STAMP(17); WAIT_L0(); BAR(); CFENCE();
            const unsigned anybad = badw[ln & 7];
            if (__any(anybad != 0u)) {
                WAIT_L0(); BAR(); CFENCE();
                attn_pass<true>(lds, lds0, Qg, Kg, Vg, g2, flags, hword, F_H + bid, hp, wsr, wpf, wid, R DG);
                AT_PUBLISH();
                WAIT_V(0); WAIT_L0(); BAR(); CFENCE();
            }
        }
        STAMP(7);

        const int tile = rbk * 8 + cbk;
        if (wid >= 4) {
            unsigned vA0, vA1, vB0, vB1; g2_voff(ln, vA0, vA1, vB0, vB1);
#define G2_LOAD(j, sl) do { g2_load(g2, j, lds0 + (sl) * SLOT, 2 * w4, vA0, vA1, vB0); g2_load(g2, j, lds0 + (sl) * SLOT, 2 * w4 + 1, vA0, vA1, vB1); } while (0)
            const f32x4 lm = *(const LAS f32x4*)pl;
            f32x4 t[8];
#pragma unroll
            for (int i = 0; i < 8; ++i) t[i] = *(const LAS f32x4*)(po + i * 1024);
            WAIT_L0(); BAR(); CFENCE();
            G2_LOAD(3, 2);
            const unsigned ooff = WS_ACT + (unsigned)(112 + h) * GRP + (unsigned)(qb * 256 + w4 * 64 + (ln & 31)) * 64u + (ln >> 5) * 16;
#pragma unroll
            for (int q = 0; q < 2; ++q) {
                const f32x16 ok_ = q ? R.o1 : R.o0;
                const float lk = q ? R.l1[0] : R.l0[0], mk = q ? R.m1 : R.m0, lo = lm[2 * q], mo = lm[2 * q + 1];
                const float mm = __builtin_fmaxf(mk, mo), fk = __builtin_amdgcn_exp2f(mk - mm), fo = __builtin_amdgcn_exp2f(mo - mm);
                const float rl = 1.0f / (lk * fk + lo * fo), ck = fk * rl, co = fo * rl;
                unsigned p[8];
#pragma unroll
                for (int i = 0; i < 4; ++i) {
                    const f32x4 u = t[4 * q + i];
                    p[2 * i] = cvtpk(ok_[4 * i] * ck + u[0] * co, ok_[4 * i + 1] * ck + u[1] * co);
                    p[2 * i + 1] = cvtpk(ok_[4 * i + 2] * ck + u[2] * co, ok_[4 * i + 3] * ck + u[3] * co);
                }
                swap32(p[0], p[2]); swap32(p[1], p[3]); swap32(p[4], p[6]); swap32(p[5], p[7]);
                ST16(((u32x4){p[0], p[1], p[2], p[3]}), ooff + q * 2048);
                ST16(((u32x4){p[4], p[5], p[6], p[7]}), ooff + q * 2048 + 32);
            }
            CFENCE(); BAR(); CFENCE();
#define LAG 3
            G2_LOAD(4, 3);
            if (LAG >= 3) G2_LOAD(5, 4);
            if (LAG >= 4) G2_LOAD(6, 0);
            int sl = (LAG + 2) % 5;
            unsigned t0 = 0, t1 = 0, t2 = 0, t3 = 0, a0 = 0, a1 = 0, a2 = 0;
#define WAIT_TILES(n) do { if ((n) == 0) WAIT_V(0); else if ((n) == 1) WAIT_V(8); else if ((n) == 2) WAIT_V(16); else WAIT_V(24); } while (0)
            if (LAG == 2) WAIT_V(12); else if (LAG == 3) WAIT_V(20); else WAIT_V(28);
            BAR();
            G2_LOAD(3 + LAG, sl);
            sl = (sl == 4) ? 0 : sl + 1;
#pragma unroll 1
            for (int j = 4; j < 36 - LAG; ++j) {
                TS(t0);
                WAIT_TILES(LAG - 1);
                if (j == 4) signal(flags, F_O + bid, ln);
                TS(t1);
                BAR();
                TS(t2);
                G2_LOAD(j + LAG, sl);
                TS(t3);
                a0 += t1 - t0; a1 += t2 - t1; a2 += t3 - t2;
                sl = (sl == 4) ? 0 : sl + 1;
            }
            PUT(50, a0); PUT(51, a1); PUT(52, a2);
#pragma unroll
            for (int i = LAG - 1; i >= 0; --i) { WAIT_TILES(i); BAR(); }
            if (wid == 4) wait_all(flags, F_X + tile * 2 + z, 4u, peek(flags, F_X + tile * 2 + z));
            CFENCE(); BAR();
        } else {
            const int wm = w4 >> 1, wn = w4 & 1;
            const unsigned aO0 = (wm * 64 + fr) * 128 + ((fq ^ (fr >> 1)) * 16), aO1 = aO0 ^ 64;
            const int tq = (ln >> 2) & 3, tp = ln & 3, tg = (tq & 2) | ((fq & 1) << 2);
            unsigned wO[4];
#pragma unroll
            for (int b = 0; b < 4; ++b) wO[b] = 16384 + wn * 8192 + (8 * fq + tq) * 128 + (((2 * b + (tp >> 1)) ^ tg) * 16) + (tp & 1) * 8;
            f32x4 acc[4][4];
#pragma unroll
            for (int m = 0; m < 4; ++m)
#pragma unroll
                for (int b = 0; b < 4; ++b) acc[m][b] = (f32x4){0.f, 0.f, 0.f, 0.f};
            bf16x8 xa[4], ya[4]; s16x4 xl[4], xh[4], yl[4], yh[4];
            lds_ptr pa0, pa1, pw0, pw1, pw2, pw3;
#define G2_BASES(so) do { pa0 = lds + aO0 + (so); pa1 = lds + aO1 + (so); pw0 = lds + wO[0] + (so); pw1 = lds + wO[1] + (so); pw2 = lds + wO[2] + (so); pw3 = lds + wO[3] + (so); } while (0)
#define G2_M(C, m, b) do { MFMA16_ACC(acc[m][b], cat8(C##l[b], C##h[b]), C##a[m]); SBAR(); } while (0)
#define G2_HALF(C, N, AB, WO, RD, TAIL) do { \
            G2_M(C, 0, 0); if (RD) { N##l[0] = lds_tr(pw0 + (WO)); N##h[0] = lds_tr(pw0 + (WO) + 512); } SBAR(); \
            G2_M(C, 1, 0); if (RD) { N##a[0] = lds_b128(AB); N##a[1] = lds_b128((AB) + 2048); } SBAR(); \
            G2_M(C, 2, 0); if (RD) { N##a[2] = lds_b128((AB) + 4096); N##a[3] = lds_b128((AB) + 6144); } SBAR(); \
            G2_M(C, 3, 0); if (RD) { N##l[1] = lds_tr(pw1 + (WO)); N##h[1] = lds_tr(pw1 + (WO) + 512); } SBAR(); \
            G2_M(C, 0, 1); if (RD) { N##l[2] = lds_tr(pw2 + (WO)); N##h[2] = lds_tr(pw2 + (WO) + 512); } SBAR(); \
            G2_M(C, 1, 1); if (RD) { N##l[3] = lds_tr(pw3 + (WO)); N##h[3] = lds_tr(pw3 + (WO) + 512); } SBAR(); \
            G2_M(C, 2, 1); G2_M(C, 3, 1); TAIL; SBAR(); G2_M(C, 0, 2); G2_M(C, 1, 2); G2_M(C, 2, 2); G2_M(C, 3, 2); \
            G2_M(C, 0, 3); G2_M(C, 1, 3); G2_M(C, 2, 3); G2_M(C, 3, 3); } while (0)
            int so = 4 * SLOT;
            G2_BASES(so);
            xl[0] = lds_tr(pw0); xh[0] = lds_tr(pw0 + 512); SBAR();
#pragma unroll
            for (int m = 0; m < 4; ++m) { xa[m] = lds_b128(pa0 + m * 2048); SBAR(); }
            xl[1] = lds_tr(pw1); xh[1] = lds_tr(pw1 + 512); xl[2] = lds_tr(pw2); xh[2] = lds_tr(pw2 + 512); xl[3] = lds_tr(pw3); xh[3] = lds_tr(pw3 + 512);
            const int oword = F_O + (rbk >> 1) * 32 + (ln & 31);
            const unsigned* const optr = flags + oword * 32;
            unsigned ov = 0, t0 = 0, t1 = 0, a0 = 0;
            const int osel = wid | (z ^ 1);
            STAMP(8);
#define G2_TILE() do { WAIT_L0(); SBAR(); \
                G2_HALF(x, y, pa1, 4096, true, { so = (so == 4 * SLOT) ? 0 : so + SLOT; G2_BASES(so); PIN(pa0); PIN(pa1); PIN(pw0); PIN(pw1); PIN(pw2); PIN(pw3); }); \
                WAIT_L0(); TS(t0); BAR(); TS(t1); SBAR(); a0 += t1 - t0;         \
                G2_HALF(y, x, pa0, 0, true, (void)0); } while (0)
#pragma unroll 1
            for (int ph = 0; ph < 4; ++ph) {
                const int n = (ph == 0) ? 12 : (ph == 1) ? 2 : (ph == 2) ? 1 : 20;
#pragma unroll 1
                for (int i = 0; i < n; ++i) G2_TILE();
                PEEK_AHEAD(ov, optr, osel | ph);
                if ((osel | (ph ^ 1)) == 0) { PEEK_DONE(ov); wait_all(flags, oword, 4u, ov); __builtin_amdgcn_fence(__ATOMIC_ACQUIRE, "agent"); }
                if (ph == 2) WAIT_V(0);
            }
            const int row = rbk * 128 + wm * 64 + z * 32 + fr, col = cbk * 128 + wn * 64 + (fq & 1) * 16 + (fq >> 1) * 8;
            u32x4 xr[4];
#pragma unroll
            for (int i = 0; i < 4; ++i) xr[i] = *(const u32x4*)(x + (size_t)(row + (i >> 1) * 16) * DM + col + (i & 1) * 32);
            WAIT_L0(); SBAR();
            G2_HALF(x, y, pa1, 4096, true, (void)0);
            WAIT_L0(); SBAR();
            G2_HALF(y, x, pa0, 0, false, (void)0);
            MFMA_DRAIN(); SBAR();
            STAMP(9); PUT(53, a0);
            f32x4 vk[8];
            const unsigned xo = WS_XCH + (unsigned)(tile * 2 + 1 - z) * 32768u + w4 * 8192 + ln * 16;
            if (z == 0) {
#pragma unroll
                for (int i = 0; i < 8; ++i) { ST16(__builtin_bit_cast(u32x4, acc[2 + (i >> 2)][i & 3]), xo + i * 1024); vk[i] = acc[i >> 2][i & 3]; }
            } else {
#pragma unroll
                for (int i = 0; i < 8; ++i) { ST16(__builtin_bit_cast(u32x4, acc[i >> 2][i & 3]), xo + i * 1024); vk[i] = acc[2 + (i >> 2)][i & 3]; }
            }
            WAIT_V(0);
            signal(flags, F_X + tile * 2 + 1 - z, ln);
            STAMP(10);
            CFENCE(); BAR(); CFENCE();
            STAMP(11);
            const unsigned xi = WS_XCH + (unsigned)(tile * 2 + z) * 32768u + w4 * 8192 + ln * 16;
#pragma unroll
            for (int i = 0; i < 8; ++i) vk[i] += __builtin_bit_cast(f32x4, __builtin_amdgcn_raw_buffer_load_b128(wsr, xi + i * 1024, 0, 16));
#pragma unroll
            for (int i = 0; i < 4; ++i) {
                const u32x4 a = __builtin_bit_cast(u32x4, vk[2 * i]), b = __builtin_bit_cast(u32x4, vk[2 * i + 1]);
                unsigned a0_ = a[0], a1 = a[1], a2 = a[2], a3 = a[3], b0 = b[0], b1 = b[1], b2 = b[2], b3 = b[3];
                swap16(a0_, b0); swap16(a1, b1); swap16(a2, b2); swap16(a3, b3);
                const u32x4 xx = xr[i];
                u32x4 w;
                w[0] = cvtpk(u2f(a0_) + bf_lo(xx[0]), u2f(a1) + bf_hi(xx[0]));
                w[1] = cvtpk(u2f(a2) + bf_lo(xx[1]), u2f(a3) + bf_hi(xx[1]));
                w[2] = cvtpk(u2f(b0) + bf_lo(xx[2]), u2f(b1) + bf_hi(xx[2]));
                w[3] = cvtpk(u2f(b2) + bf_lo(xx[3]), u2f(b3) + bf_hi(xx[3]));
                *(u32x4*)(out + (size_t)(row + (i >> 1) * 16) * DM + col + (i & 1) * 32) = w;
            }
        }
    }
}

extern "C" void kernel_launch(void* const* d_in, const int* in_sizes, int n_in, void* d_out, int out_size, void* d_ws, size_t ws_size, hipStream_t stream) {
    const bf16_t* x = (const bf16_t*)d_in[0];
    k_ln<<<512, 256, 0, stream>>>(x, (unsigned char*)d_ws);
    k_main<<<256, 512, 0, stream>>>(x, (const bf16_t*)d_in[1], (const bf16_t*)d_in[2], (const bf16_t*)d_in[4], (const bf16_t*)d_in[5], (const bf16_t*)d_in[6],
                                    (bf16_t*)d_out, (unsigned char*)d_ws);
}
```

```cpp
#include <hip/hip_runtime.h>
#include <stdint.h>

typedef unsigned short bf16_t;
typedef short bf16x8 __attribute__((ext_vector_type(8)));
typedef short s16x4 __attribute__((ext_vector_type(4)));
typedef float f32x2 __attribute__((ext_vector_type(2)));
typedef float f32x4 __attribute__((ext_vector_type(4)));
typedef float f32x16 __attribute__((ext_vector_type(16)));
typedef unsigned u32x4 __attribute__((ext_vector_type(4)));
typedef __bf16 bf16x2_t __attribute__((ext_vector_type(2)));
#define LAS __attribute__((address_space(3)))
#define GAS __attribute__((address_space(1)))
typedef LAS unsigned char* lds_ptr;

constexpr int SEQ = 2048, DM = 1024, FF = 3584, NQKV = 3072, NH = 32;
constexpr float LN_EPS = 1e-5f;
constexpr float LOG2E = 1.4426950408889634f;
constexpr float QSCALE = 0.17677669529663687f * LOG2E;

constexpr unsigned WS_FLAGS = 0;
constexpr unsigned WS_STAT = 192 * 1024;
constexpr unsigned WS_QKV = 256 * 1024;
constexpr unsigned WS_ACT = WS_QKV + 3u * SEQ * DM * 2;
constexpr unsigned WS_XCH = WS_ACT + 144u * SEQ * 64;
constexpr unsigned WS_END = WS_XCH + 256u * 32768;
constexpr int F_EPOCH = 0, F_QKV = 1, F_H = 257, F_O = 513, F_X = 769;
constexpr unsigned GRP = SEQ * 64;

#define SBAR() __builtin_amdgcn_sched_barrier(0)
#define BAR() __builtin_amdgcn_s_barrier()
#define WAIT_V(n) asm volatile("s_waitcnt vmcnt(" #n ")" ::: "memory")
#define WAIT_L0() do { __builtin_amdgcn_s_waitcnt(0xC07F); asm volatile("" ::: "memory"); } while (0)
#define CFENCE() asm volatile("" ::: "memory")
#define PIN(x) asm volatile("" : "+v"(x))
#define MFMA16(a, b, c) __builtin_amdgcn_mfma_f32_16x16x32_bf16(a, b, c, 0, 0, 0)
#define MFMA32(a, b, c) __builtin_amdgcn_mfma_f32_32x32x16_bf16(a, b, c, 0, 0, 0)
#define MFMA16_ACC(acc, a, b) asm volatile("v_mfma_f32_16x16x32_bf16 %0, %1, %2, %0" : "+v"(acc) : "v"(a), "v"(b))
#define MFMA_DRAIN() asm volatile("s_nop 15" ::: "memory")
#define RLX_AGENT __ATOMIC_RELAXED, __HIP_MEMORY_SCOPE_AGENT

#define STAMP(i) do {} while (0)
#define TS(v) do {} while (0)
#define PUT(i, v) do {} while (0)

__device__ __forceinline__ void glds16(const void* sbase, unsigned voff, unsigned ldsdst) {
    asm volatile("s_mov_b32 m0, %2\n\ts_nop 0\n\tglobal_load_lds_dwordx4 %0, %1" ::"v"(voff), "s"(sbase), "s"(ldsdst) : "memory");
}
__device__ __forceinline__ bf16x8 lds_b128(lds_ptr p) { return *(const LAS bf16x8*)p; }
__device__ __forceinline__ s16x4 lds_tr(lds_ptr p) { return __builtin_amdgcn_ds_read_tr16_b64_v4i16((LAS s16x4*)p); }
__device__ __forceinline__ bf16x8 cat8(s16x4 a, s16x4 b) { return __builtin_shufflevector(a, b, 0, 1, 2, 3, 4, 5, 6, 7); }
__device__ __forceinline__ unsigned cvtpk(float lo, float hi) {
    f32x2 v = {lo, hi};
    return __builtin_bit_cast(unsigned, __builtin_convertvector(v, bf16x2_t));
}
__device__ __forceinline__ float u2f(unsigned u) { return __builtin_bit_cast(float, u); }
__device__ __forceinline__ unsigned f2u(float f) { return __builtin_bit_cast(unsigned, f); }
__device__ __forceinline__ float bf_lo(unsigned u) { return u2f(u << 16); }
__device__ __forceinline__ float bf_hi(unsigned u) { return u2f(u & 0xffff0000u); }
__device__ __forceinline__ void swap16(unsigned& a, unsigned& b) {
    auto r = __builtin_amdgcn_permlane16_swap(a, b, false, false);
    a = r[0]; b = r[1];
}
__device__ __forceinline__ void swap32(unsigned& a, unsigned& b) {
    auto r = __builtin_amdgcn_permlane32_swap(a, b, false, false);
    a = r[0]; b = r[1];
}
__device__ __forceinline__ int opaque_tid() { int t = threadIdx.x; asm volatile("" : "+v"(t)); return t; }
typedef unsigned long long u64;
struct Flags { u64* p; u64 tag; };
__device__ __forceinline__ void signal(const Flags& f, int word, int lane) {
    if (lane == 0) __hip_atomic_store((GAS u64*)(f.p + word * 16), f.tag, RLX_AGENT);
}
__device__ __forceinline__ u64 peek(const Flags& f, int word) { return __hip_atomic_load((GAS u64*)(f.p + word * 16), RLX_AGENT); }
__device__ __forceinline__ void wait_all(const Flags& f, int word, u64 v) {
    unsigned spin = 0;
    while (!__all(v == f.tag)) { if (++spin > (1u << 20)) break; __builtin_amdgcn_s_sleep(1); v = peek(f, word); }
}
__device__ __forceinline__ void arrive(const Flags& f, int word, int lane, lds_ptr cnt, unsigned last) {
    if (lane == 0 && __hip_atomic_fetch_add((LAS unsigned*)cnt, 1u, __ATOMIC_RELAXED, __HIP_MEMORY_SCOPE_WORKGROUP) == last)
        __hip_atomic_store((GAS u64*)(f.p + word * 16), f.tag, RLX_AGENT);
}
#define PEEK_AHEAD(v, ptr, sel) asm volatile("s_cmp_lg_u32 %2, 0\n\ts_cbranch_scc1 1f\n\tglobal_load_dwordx2 %0, %1, off sc1\n1:" : "+v"(v) : "v"(ptr), "s"(sel) : "memory", "scc")
#define PEEK_DONE(v) asm volatile("s_waitcnt vmcnt(0)" : "+v"(v) :: "memory")

template <int CTRL> __device__ __forceinline__ float dpp_add(float v) { return v + u2f(__builtin_amdgcn_update_dpp(0u, f2u(v), CTRL, 0xf, 0xf, true)); }
__device__ __forceinline__ float red4(float v) {
    unsigned a = f2u(v), b = a;
    swap16(a, b);
    v = u2f(a) + u2f(b);
    a = f2u(v); b = a;
    swap32(a, b);
    return u2f(a) + u2f(b);
}
__device__ __forceinline__ float wave_sum(float v) {
    v = dpp_add<0xB1>(v); v = dpp_add<0x4E>(v); v = dpp_add<0x141>(v); v = dpp_add<0x140>(v);
    return red4(v);
}
__device__ __forceinline__ f32x2 row_stats(u32x4 a, u32x4 b) {
    float v[16];
#pragma unroll
    for (int i = 0; i < 4; ++i) { v[2 * i] = bf_lo(a[i]); v[2 * i + 1] = bf_hi(a[i]); v[8 + 2 * i] = bf_lo(b[i]); v[8 + 2 * i + 1] = bf_hi(b[i]); }
    float s = 0.f;
#pragma unroll
    for (int i = 0; i < 16; ++i) s += v[i];
    const float mu = wave_sum(s) * (1.0f / DM);
    float q = 0.f;
#pragma unroll
    for (int i = 0; i < 16; ++i) { const float d = v[i] - mu; q += d * d; }
    return (f32x2){mu, 1.0f / __builtin_sqrtf(wave_sum(q) * (1.0f / DM) + LN_EPS)};
}

constexpr int SLOT = 32768, LDS_BYTES = 5 * SLOT;
constexpr int G1_A = 0, G1_W = 65536, G1_STAT = G1_W + 5 * 16384, G1_CS = G1_STAT + 2048, G1_CNT = G1_CS + 4 * 6 * 64;

struct G2 { const char* Ah; const char* Ao; const char* Wd; const char* Wo; };
__device__ __forceinline__ void g2_load_b(const G2& g, int j, unsigned dst, int vw, unsigned voffB) {
    const char* b = (j >= 28 ? g.Wo : g.Wd) + (size_t)(j * 64 + vw * 8) * (DM * 2);
    glds16(b, voffB, dst + 16384 + vw * 1024); glds16(b + 128, voffB, dst + 16384 + 8192 + vw * 1024);
}
__device__ __forceinline__ void g2_load_a(const G2& g, int j, unsigned dst, int vw, unsigned voffA0, unsigned voffA1) {
    const char* a = (j >= 28 ? g.Ao : g.Ah) + (size_t)j * (2 * GRP) + vw * 1024;
    glds16(a, voffA0, dst + vw * 2048); glds16(a, voffA1, dst + vw * 2048 + 1024);
}
__device__ __forceinline__ void g2_voff(int lane, unsigned& voffA0, unsigned& voffA1, unsigned& voffB0, unsigned& voffB1) {
    const int r8 = lane >> 3, u8 = lane & 7, ua = u8 ^ (r8 >> 1), ub = ua ^ 4;
    voffA0 = (unsigned)(ua >> 2) * GRP + r8 * 64 + (ua & 3) * 16;
    voffA1 = (unsigned)(ub >> 2) * GRP + (r8 + 8) * 64 + (ub & 3) * 16;
    voffB0 = (unsigned)(r8 * (DM * 2) + (u8 ^ (r8 & 2)) * 16);
    voffB1 = (unsigned)(r8 * (DM * 2) + (u8 ^ (4 | (r8 & 2))) * 16);
}

struct AttnOut { f32x16 o0, o1; f32x4 l0, l1; float m0, m1; };
struct HPend { u32x4 v[8]; unsigned off, step; const void* base; };
struct AttnLane {
    unsigned voffK, voffV; lds_ptr kb0, kb1, vb; const char* qp; bf16x8 ones;
    __device__ __forceinline__ void init(lds_ptr lds, const char* Qg, int wid, int lane) {
        const int r32 = lane & 31, hi = lane >> 5, kh = wid >> 2, qg = wid & 3, ksw = (r32 >> 2) & 3;
        const int uk = (lane & 3) ^ ((lane >> 4) & 3), uv = lane & 3;
        voffK = (unsigned)((uk >> 1) * 65536 + wid * 1024 + (lane >> 2) * 32 + (uk & 1) * 16);
        voffV = (unsigned)((uv >> 1) * 65536 + wid * 1024 + (lane >> 2) * 32 + (uv & 1) * 16);
        kb0 = lds + kh * 8192 + r32 * 64 + ((hi ^ ksw) * 16); kb1 = lds + kh * 8192 + r32 * 64 + (((2 | hi) ^ ksw) * 16);
        vb = lds + 16384 + kh * 8192 + (4 * hi + ((lane >> 2) & 3)) * 64 + ((lane >> 4) & 1) * 32 + (lane & 3) * 8;
        qp = Qg + (qg * 64 + r32) * 32 + hi * 16;
        const short v = (((lane >> 4) & 1) == ((lane >> 2) & 1)) ? (short)0x3f80 : (short)0;
        ones = (bf16x8){v, v, v, v, v, v, v, v};
    }
};
#define EX(v) __builtin_amdgcn_exp2f(v)
#define AT_LOAD_K(c, slot) do { const unsigned d_ = lds0 + wid * 2048 + (slot) * SLOT; const char* k_ = Kg + (c) * 8192; glds16(k_, A.voffK, d_); glds16(k_ + 512, A.voffK, d_ + 1024); } while (0)
#define AT_LOAD_V(c, slot) do { const unsigned d_ = lds0 + wid * 2048 + (slot) * SLOT + 16384; const char* v_ = Vg + (c) * 8192; glds16(v_, A.voffV, d_); glds16(v_ + 512, A.voffV, d_ + 1024); } while (0)
#define AT_PACK(S, i) __builtin_bit_cast(bf16x8, (u32x4){cvtpk(S[i], S[i + 1]), cvtpk(S[i + 2], S[i + 3]), cvtpk(S[i + 4], S[i + 5]), cvtpk(S[i + 6], S[i + 7])})
__device__ __forceinline__ float rowmax16(const f32x16& s) {
    float a = s[0];
#pragma unroll
    for (int r = 1; r < 16; ++r) a = __builtin_fmaxf(a, s[r]);
    unsigned x = f2u(a), y = x; swap32(x, y);
    return __builtin_fmaxf(u2f(x), u2f(y));
}

__device__ __forceinline__ void attn_safe(lds_ptr lds, unsigned lds0, const char* Qg, const char* Kg, const char* Vg, int wid, AttnOut& R) {
    AttnLane A; A.init(lds, Qg, wid, opaque_tid() & 63);
    const bf16x8 q00 = *(const bf16x8*)A.qp, q01 = *(const bf16x8*)(A.qp + 65536), q10 = *(const bf16x8*)(A.qp + 1024), q11 = *(const bf16x8*)(A.qp + 65536 + 1024);
    f32x16 o0 = (f32x16){}, o1 = (f32x16){};
    f32x4 l0 = (f32x4){0.f, 0.f, 0.f, 0.f}, l1 = l0;
    float m0 = -__builtin_inff(), m1 = m0;
#pragma unroll 1
    for (int c = 0; c < 8; ++c) {
        AT_LOAD_K(c, 3); AT_LOAD_V(c, 3);
        WAIT_V(0); BAR(); CFENCE();
#pragma unroll 1
        for (int t = 0; t < 4; ++t) {
            const int to = 3 * SLOT + t * 2048;
            const bf16x8 ka = lds_b128(A.kb0 + to), kb = lds_b128(A.kb1 + to);
            const bf16x8 va = cat8(lds_tr(A.vb + to), lds_tr(A.vb + to + 512)), vc = cat8(lds_tr(A.vb + to + 1024), lds_tr(A.vb + to + 1536));
            f32x16 s0 = MFMA32(kb, q01, MFMA32(ka, q00, (f32x16){})), s1 = MFMA32(kb, q11, MFMA32(ka, q10, (f32x16){}));
            const float n0 = __builtin_fmaxf(m0, rowmax16(s0)), n1 = __builtin_fmaxf(m1, rowmax16(s1));
            const float f0 = EX(m0 - n0), f1 = EX(m1 - n1);
            m0 = n0; m1 = n1; o0 *= f0; o1 *= f1; l0 *= f0; l1 *= f1;
#pragma unroll
            for (int r = 0; r < 16; ++r) { s0[r] = EX(s0[r] - n0); s1[r] = EX(s1[r] - n1); }
            const bf16x8 p00 = AT_PACK(s0, 0), p01 = AT_PACK(s0, 8), p10 = AT_PACK(s1, 0), p11 = AT_PACK(s1, 8);
            o0 = MFMA32(vc, p01, MFMA32(va, p00, o0)); l0 = MFMA16(A.ones, p01, MFMA16(A.ones, p00, l0));
            o1 = MFMA32(vc, p11, MFMA32(va, p10, o1)); l1 = MFMA16(A.ones, p11, MFMA16(A.ones, p10, l1));
        }
        WAIT_L0(); BAR(); CFENCE();
    }
    R.o0 = o0; R.o1 = o1; R.l0 = l0; R.l1 = l1; R.m0 = m0; R.m1 = m1;
}

__device__ __forceinline__ void attn_fast(lds_ptr lds, unsigned lds0, const char* Qg, const char* Kg, const char* Vg, const G2& g2, const Flags& flags, int qkv0, int qkvword, int depword,
                                          int hsig, int c0, bool bump, const HPend& hp, const char* wpd, const char* wpo, int prow, int wid, AttnOut& R
                                          ) {
    const int lane = opaque_tid() & 63;
    AttnLane A; A.init(lds, Qg, wid, lane);
    const lds_ptr kb0 = A.kb0, kb1 = A.kb1, vb = A.vb;
    const bf16x8 ones = A.ones;
    const __amdgpu_buffer_rsrc_t hr = __builtin_amdgcn_make_buffer_rsrc((void*)hp.base, 0, 8192, 0x00020000);
    f32x16 o0 = (f32x16){}, o1 = (f32x16){}, negm0 = (f32x16){}, negm1 = (f32x16){}, sA0, sA1, sB0, sB1;
    f32x4 l0 = (f32x4){0.f, 0.f, 0.f, 0.f}, l1 = l0;
    bf16x8 kfa, kfb, va, vc = (bf16x8){}, pf = (bf16x8){};
    PIN(o0); PIN(o1); PIN(l0); PIN(l1); PIN(vc); PIN(pf); PIN(A.voffK); PIN(A.voffV); PIN(A.qp);
    unsigned gA0, gA1, gB0, gB1; g2_voff(lane, gA0, gA1, gB0, gB1);
    unsigned gB = (wid & 1) ? gB1 : gB0;
    unsigned pfo = (unsigned)(((lane >> 3) & 1) * (DM * 2) + (lane & 7) * 128);
    const u64* hptr = flags.p + (F_H + depword) * 16;
    const u64* qptr = flags.p + qkvword * 16;
    const char* qp2 = A.qp + 65536;
    lds_ptr stg = lds + wid * 2048 + lane * 16;
    PIN(gA0); PIN(gA1); PIN(gB); PIN(pfo); PIN(hptr); PIN(qptr); PIN(qp2); PIN(stg);
    if (wid == 0) {
        wait_all(flags, qkv0, peek(flags, qkv0));
        STAMP(13);
    }
    CFENCE(); BAR(); CFENCE();
    STAMP(4);
    bf16x8 q00, q01, q10, q11; u32x4 k0a, k0b, v0a, v0b;
    asm volatile("global_load_dwordx4 %0, %8, %9 sc1\n\tglobal_load_dwordx4 %1, %8, %9 offset:512 sc1\n\t"
                 "global_load_dwordx4 %2, %12, off sc1\n\tglobal_load_dwordx4 %3, %12, off offset:1024 sc1\n\tglobal_load_dwordx4 %4, %13, off sc1\n\tglobal_load_dwordx4 %5, %13, off offset:1024 sc1\n\t"
                 "global_load_dwordx4 %6, %10, %11 sc1\n\tglobal_load_dwordx4 %7, %10, %11 offset:512 sc1"
                 : "=&v"(k0a), "=&v"(k0b), "=&v"(q00), "=&v"(q10), "=&v"(q01), "=&v"(q11), "=&v"(v0a), "=&v"(v0b)
                 : "v"(A.voffK), "s"(Kg + c0 * 8192), "v"(A.voffV), "s"(Vg + c0 * 8192), "v"(A.qp), "v"(qp2) : "memory");
    u64 hv = 0;
    float m0, m1;
    asm volatile("s_waitcnt vmcnt(6)" : "+v"(k0a), "+v"(k0b) :: "memory");
    *(LAS u32x4*)stg = k0a; *(LAS u32x4*)(stg + 1024) = k0b;
    asm volatile("s_waitcnt vmcnt(2)" : "+v"(q00), "+v"(q01), "+v"(q10), "+v"(q11) :: "memory");
    PEEK_AHEAD(hv, qptr, wid);
    WAIT_L0(); BAR(); SBAR();
    STAMP(15);
    {
        kfa = lds_b128(kb0); kfb = lds_b128(kb1);
        sA0 = MFMA32(kfa, q00, negm0); sA1 = MFMA32(kfa, q10, negm1); sA0 = MFMA32(kfb, q01, sA0); sA1 = MFMA32(kfb, q11, sA1);
        kfa = lds_b128(kb0 + 2048); kfb = lds_b128(kb1 + 2048);
        m0 = rowmax16(sA0); m1 = rowmax16(sA1);
#pragma unroll
        for (int r = 0; r < 16; ++r) { sA0[r] -= m0; sA1[r] -= m1; negm0[r] = -m0; negm1[r] = -m1; }
        PIN(negm0); PIN(negm1);
    }
    u32x4 hA = hp.v[0], hB = hp.v[1], hC = hp.v[2], hD = hp.v[3];
    unsigned hoff = hp.off, pfd = 0;
#define AT_HST(v) do { __builtin_amdgcn_raw_buffer_store_b128(v, hr, hoff, 0, 16); hoff += hp.step; CFENCE(); } while (0)
    asm volatile("s_waitcnt vmcnt(0)" : "+v"(v0a), "+v"(v0b), "+v"(hv) :: "memory");
    *(LAS u32x4*)(stg + 16384) = v0a; *(LAS u32x4*)(stg + 16384 + 1024) = v0b;
    if (wid == 0) {
        wait_all(flags, qkvword, hv);
        __builtin_amdgcn_fence(__ATOMIC_ACQUIRE, "agent");
        if (bump) signal(flags, F_EPOCH, lane);
        WAIT_V(0);
        STAMP(14);
    }
    WAIT_L0(); BAR(); SBAR();
    STAMP(5);
    AT_LOAD_K((c0 + 1) & 7, 1); AT_LOAD_V((c0 + 1) & 7, 1);
#define HS 1
    hoff += 8192;
    __builtin_amdgcn_s_setprio(1);
    STAMP(19);
#define AT_GROUP(QK, RD, S, i, O, L, VF) do { \
        QK; SBAR(); RD; SBAR(); \
        S[i] = EX(S[i]); S[i + 1] = EX(S[i + 1]); S[i + 2] = EX(S[i + 2]); PIN(S); SBAR(); \
        O = MFMA32(VF, pf, O); SBAR(); \
        S[i + 3] = EX(S[i + 3]); S[i + 4] = EX(S[i + 4]); S[i + 5] = EX(S[i + 5]); PIN(S); SBAR(); \
        L = MFMA16(ones, pf, L); SBAR(); \
        S[i + 6] = EX(S[i + 6]); S[i + 7] = EX(S[i + 7]); \
        pf = AT_PACK(S, i); PIN(pf); SBAR(); } while (0)
#define AT_STEP(C0, C1, N0, N1, KO, VO) do { \
        AT_GROUP(N0 = MFMA32(kfa, q00, negm0), va = cat8(lds_tr(vb + (VO)), lds_tr(vb + (VO) + 512)), C0, 0, o1, l1, vc); \
        AT_GROUP(N1 = MFMA32(kfa, q10, negm1), { kfa = lds_b128(kb0 + (KO)); vc = cat8(lds_tr(vb + (VO) + 1024), lds_tr(vb + (VO) + 1536)); }, C1, 0, o0, l0, va); \
        AT_GROUP(N0 = MFMA32(kfb, q01, N0), (void)0, C0, 8, o1, l1, va); \
        AT_GROUP(N1 = MFMA32(kfb, q11, N1), kfb = lds_b128(kb1 + (KO)), C1, 8, o0, l0, vc); } while (0)
#define AT_YPRIO(p) asm volatile("s_cmp_lt_u32 %0, 4\n\ts_cbranch_scc1 1f\n\ts_setprio " #p "\n1:" :: "s"(wid) : "scc")
#pragma unroll 1
    for (int c = 0; c < 5; ++c) {
        const int so = (c & 3) * SLOT, sn = ((c + 1) & 3) * SLOT;
        if (c == HS) hoff = hp.off;
        AT_HST(hA);
        AT_STEP(sA0, sA1, sB0, sB1, so + 4096, so);
        AT_HST(hB); AT_YPRIO(0);
        AT_STEP(sB0, sB1, sA0, sA1, so + 6144, so + 2048);
        if (c == 0) WAIT_V(2); else WAIT_V(5);
        WAIT_L0(); BAR(); SBAR();
        STAMP(20 + c);
        AT_YPRIO(2);
        if (c == HS + 3 && wid == 0) signal(flags, hsig, lane);
        AT_LOAD_K((c0 + c + 2) & 7, (c + 2) & 3); AT_LOAD_V((c0 + c + 2) & 7, (c + 2) & 3); AT_HST(hC);
        {
            const int pi = (wid + 8 * c < 36) ? wid + 8 * c : 35, lr = prow + 2 * pi;
            const char* pp = (lr < 1792 ? wpd : wpo) + (size_t)lr * (DM * 2);
            asm volatile("global_load_dword %0, %1, %2" : "+v"(pfd) : "v"(pfo), "s"(pp) : "memory");
        }
        AT_STEP(sA0, sA1, sB0, sB1, sn, so + 4096);
        AT_HST(hD);
        AT_STEP(sB0, sB1, sA0, sA1, sn + 2048, so + 6144);
        if (c >= HS) { hA = hp.v[4]; hB = hp.v[5]; hC = hp.v[6]; hD = hp.v[7]; }
    }
#pragma unroll 1
    for (int c = 5; c < 8; ++c) {
        const int so = (c & 3) * SLOT, sn = (c == 7) ? so : ((c + 1) & 3) * SLOT;
        const int sel = (c ^ 6) | wid;
        if (sel == 0) { PEEK_DONE(hv); wait_all(flags, F_H + depword, hv); __builtin_amdgcn_fence(__ATOMIC_ACQUIRE, "agent"); }
        if (c == 5) g2_load_b(g2, 0, lds0 + 4 * SLOT, wid, gB);
        if (c == 6) g2_load_b(g2, 1, lds0, wid, gB);
        if (c == 7) g2_load_a(g2, 1, lds0, wid, gA0, gA1);
        AT_STEP(sA0, sA1, sB0, sB1, so + 4096, so);
        AT_YPRIO(0);
        AT_STEP(sB0, sB1, sA0, sA1, so + 6144, so + 2048);
        WAIT_V(0); WAIT_L0(); BAR(); SBAR();
        STAMP(20 + c);
        AT_YPRIO(2);
        if (c == 5) { AT_LOAD_K((c0 + 7) & 7, 3); AT_LOAD_V((c0 + 7) & 7, 3); }
        if (c == 6) { g2_load_a(g2, 0, lds0 + 4 * SLOT, wid, gA0, gA1); }
        if (c == 7) g2_load_a(g2, 2, lds0 + SLOT, wid, gA0, gA1);
        AT_STEP(sA0, sA1, sB0, sB1, sn, so + 4096);
        if (c == 6) g2_load_b(g2, 2, lds0 + SLOT, wid, gB);
        PEEK_AHEAD(hv, hptr, (c ^ 5) | wid);
        AT_STEP(sB0, sB1, sA0, sA1, sn + 2048, so + 6144);
    }
    STAMP(28);
    __builtin_amdgcn_s_setprio(0);
    o1 = MFMA32(vc, pf, o1); l1 = MFMA16(ones, pf, l1);
    asm volatile("" :: "v"(pfd));
    R.o0 = o0; R.o1 = o1; R.l0 = l0; R.l1 = l1; R.m0 = m0; R.m1 = m1;
}

__global__ __launch_bounds__(512, 2) void k_main(const bf16_t* __restrict__ x, const bf16_t* __restrict__ wqkv, const bf16_t* __restrict__ wo, const bf16_t* __restrict__ wg,
                                                 const bf16_t* __restrict__ wu, const bf16_t* __restrict__ wd, bf16_t* __restrict__ out, unsigned char* ws) {
    __shared__ __attribute__((aligned(16))) unsigned char smem[LDS_BYTES];
    const lds_ptr lds = (lds_ptr)smem;
    const unsigned lds0 = (unsigned)(uintptr_t)lds;
    const int wid = __builtin_amdgcn_readfirstlane(threadIdx.x >> 6);
    const int bid = blockIdx.x, xcd = bid & 7, idx = bid >> 3;
    Flags flags; flags.p = (u64*)(ws + WS_FLAGS);
    const __amdgpu_buffer_rsrc_t wsr = __builtin_amdgcn_make_buffer_rsrc((void*)ws, 0, WS_END, 0x00020000);
#define ST16(v, off) __builtin_amdgcn_raw_buffer_store_b128(v, wsr, off, 0, 16)
    STAMP(0);
    HPend hp;
    {
        const int lane = threadIdx.x & 63, wr = wid >> 2, wc = wid & 3, js = idx & 3, rb = idx >> 2;
        const bool typeY = (js == 3);
        const int f0 = 448 * xcd + 128 * js;
        const char* P0 = (const char*)(wg + f0);
        const char* P2 = (const char*)(wu + f0);
        const char* P1 = typeY ? (const char*)(wqkv + 128 * xcd + 64) : (const char*)(wg + f0 + 64);
        const char* P3 = typeY ? (const char*)(wqkv + 1024 + 128 * xcd + 64) : (const char*)(wu + f0 + 64);
        const char* P4 = typeY ? (const char*)(wqkv + 2048 + 128 * xcd + 64) : (const char*)(wqkv + js * 1024 + 128 * xcd);
        const unsigned stepF = 32u * FF * 2u, stepQ = 32u * NQKV * 2u, step13 = typeY ? stepQ : stepF;
        const char* XA = (const char*)x + (size_t)rb * 256 * DM * 2;
        u64 ep; u32x4 xa0, xa1;
        const int srow = rb * 256 + (js * 8 + xcd) * 8 + wid;
        asm volatile("global_load_dwordx4 %1, %5, %6\n\tglobal_load_dwordx4 %2, %5, %6 offset:1024\n\tglobal_load_dwordx2 %0, %3, %4 sc1"
                     : "=&v"(ep), "=&v"(xa0), "=&v"(xa1)
                     : "v"(0u), "s"(flags.p), "v"((unsigned)(lane * 16)), "s"((const char*)x + (size_t)srow * DM * 2) : "memory");
        const int r8 = lane >> 3, u8 = lane & 7;
        const int gw = (r8 & 2) | ((wc & 1) << 2);
        const unsigned voffF = (unsigned)(((wc * 8 + r8) * FF + (u8 ^ gw) * 8) * 2);
        const unsigned voffQ = (unsigned)(((wc * 8 + r8) * NQKV + (u8 ^ gw) * 8) * 2);
        const unsigned voff13 = typeY ? voffQ : voffF;
        const unsigned voffA0 = (unsigned)(((32 * wid + r8) * DM + (u8 ^ (r8 >> 1)) * 8) * 2);
        const unsigned voffA1 = (unsigned)(((32 * wid + 8 + r8) * DM + (u8 ^ (4 | (r8 >> 1))) * 8) * 2);
        const unsigned dstA = lds0 + G1_A + wid * 4096, dstW = lds0 + G1_W + wc * 1024;
#define G1_LOAD_A(t) do { const char* b_ = XA + (t) * 128; const unsigned d_ = dstA + ((t) & 1) * 32768; \
        glds16(b_, voffA0, d_); glds16(b_, voffA1, d_ + 1024); glds16(b_ + 32768, voffA0, d_ + 2048); glds16(b_ + 32768, voffA1, d_ + 3072); } while (0)
#define G1_LOAD_W(hs) do { const unsigned d_ = dstW + ((hs) & 3) * 4096; \
        glds16(P0 + (size_t)(hs) * stepF, voffF, d_); glds16(P1 + (size_t)(hs) * step13, voff13, d_ + 16384); glds16(P2 + (size_t)(hs) * stepF, voffF, d_ + 32768); \
        glds16(P3 + (size_t)(hs) * step13, voff13, d_ + 49152); glds16(P4 + (size_t)(hs) * stepQ, voffQ, d_ + 65536); } while (0)
        if (wr == 0) { G1_LOAD_W(0); G1_LOAD_A(0); G1_LOAD_W(1); } else { G1_LOAD_A(0); G1_LOAD_W(2); }

        int fr = lane & 15, fq = lane >> 4;
        const lds_ptr aB0 = lds + G1_A + (wr * 128 + fr) * 128 + ((fq ^ (fr >> 1)) * 16);
        const lds_ptr aB1 = lds + G1_A + (wr * 128 + fr) * 128 + (((4 | fq) ^ (fr >> 1)) * 16);
        const int tq = (lane >> 2) & 3, tp = lane & 3, tg = (tq & 2) | ((fq & 1) << 2);
        auto wbase = [&](int panel, int bp) { return lds + G1_W + panel * 16384 + (8 * fq + tq) * 128 + (((2 * bp + (tp >> 1)) ^ tg) * 16) + (tp & 1) * 8; };
        const int pX = wc >> 1, bX = 2 * (wc & 1);
        const lds_ptr wB0 = typeY ? wbase(0, wc) : wbase(pX, bX);
        const lds_ptr wB1 = typeY ? wbase(1, wc) : wbase(pX, bX + 1);
        const lds_ptr wB2 = typeY ? wbase(2, wc) : wbase(2 + pX, bX);
        const lds_ptr wB3 = typeY ? wbase(3, wc) : wbase(2 + pX, bX + 1);
        const lds_ptr wB4 = wbase(4, wc);
        if (threadIdx.x == 0) *(LAS unsigned*)(lds + G1_CNT) = 0u;
        asm volatile("s_cmp_eq_u32 %3, 0\n\ts_cbranch_scc1 1f\n\ts_waitcnt vmcnt(9)\n1:\n\ts_waitcnt vmcnt(14)" : "+v"(ep), "+v"(xa0), "+v"(xa1) : "s"(wr) : "memory", "scc");
        {
            const unsigned elo = __builtin_amdgcn_readfirstlane((unsigned)ep), ehi = __builtin_amdgcn_readfirstlane((unsigned)(ep >> 32));
            flags.tag = (((u64)ehi << 32) | elo) * 0x9E3779B97F4A7C15ull + 0x632BE59BD9B4E019ull;
        }
        const unsigned tag32 = (unsigned)flags.tag;
        {
            const f32x2 st = row_stats(xa0, xa1);
            if (lane == 0) ST16(((u32x4){f2u(st.x), tag32, f2u(st.y), tag32}), WS_STAT + (unsigned)srow * 16u);
            CFENCE();
        }
        f32x4 acc[8][5];
#pragma unroll
        for (int m = 0; m < 8; ++m)
#pragma unroll
            for (int b = 0; b < 5; ++b) acc[m][b] = (f32x4){0.f, 0.f, 0.f, 0.f};
        bf16x8 af[8], wf[5];
        float cs[3] = {0.f, 0.f, 0.f};
#define G1_CS_SRC(WR, r) __builtin_bit_cast(u32x4, wf[(r) >= 8 ? 4 : 2 * (WR) + ((r) >> 2)])[(r) >= 8 ? 2 * (WR) + ((r) & 1) : ((r) & 3)]
#define G1_RW(i, B, slot) wf[i] = cat8(lds_tr(B + (slot) * 4096), lds_tr(B + (slot) * 4096 + 512))
#define G1_RA(m, AB, abuf) af[m] = lds_b128(AB + (abuf) * 32768 + (m) * 2048)
#define G1_SEG(AB, abuf, slot, L0, L1, L2, L3, L4, CLOSE) do { \
        L0; G1_RW(0, wB0, slot); G1_RW(1, wB1, slot); L1; G1_RW(2, wB2, slot); G1_RW(3, wB3, slot); L2; G1_RW(4, wB4, slot); G1_RA(0, AB, abuf); G1_RA(1, AB, abuf); \
        L3; G1_RA(2, AB, abuf); G1_RA(3, AB, abuf); G1_RA(4, AB, abuf); L4; G1_RA(5, AB, abuf); G1_RA(6, AB, abuf); G1_RA(7, AB, abuf); \
        WAIT_L0(); SBAR(); if (CLOSE) { BAR(); SBAR(); } } while (0)
#define G1_SEG_A(abuf, slot, t) do { const char* b_ = XA + (t) * 128; const unsigned d_ = dstA + (1 - (abuf)) * 32768; \
        G1_SEG(aB0, abuf, slot, glds16(b_, voffA0, d_), glds16(b_, voffA1, d_ + 1024), glds16(b_ + 32768, voffA0, d_ + 2048), glds16(b_ + 32768, voffA1, d_ + 3072), (void)0, true); } while (0)
#define G1_SEG_W(abuf, slot, hs, dslot, CLOSE) do { const unsigned d_ = dstW + (dslot) * 4096; \
        G1_SEG(aB1, abuf, slot, glds16(P0 + (size_t)(hs) * stepF, voffF, d_), glds16(P1 + (size_t)(hs) * step13, voff13, d_ + 16384), glds16(P2 + (size_t)(hs) * stepF, voffF, d_ + 32768), \
               glds16(P3 + (size_t)(hs) * step13, voff13, d_ + 49152), glds16(P4 + (size_t)(hs) * stepQ, voffQ, d_ + 65536), CLOSE); } while (0)
#define G1_MMA8(WR, b) do { float ct_; \
        asm volatile("v_mfma_f32_16x16x32_bf16 %0, %10, %11, %0\n\tv_lshlrev_b32 %9, 16, %19\n\tv_mfma_f32_16x16x32_bf16 %1, %10, %12, %1\n\tv_add_f32 %8, %8, %9\n\t" \
                     "v_mfma_f32_16x16x32_bf16 %2, %10, %13, %2\n\tv_and_b32 %9, 0xffff0000, %19\n\tv_mfma_f32_16x16x32_bf16 %3, %10, %14, %3\n\tv_add_f32 %8, %8, %9\n\t" \
                     "v_mfma_f32_16x16x32_bf16 %4, %10, %15, %4\n\tv_lshlrev_b32 %9, 16, %20\n\tv_mfma_f32_16x16x32_bf16 %5, %10, %16, %5\n\tv_add_f32 %8, %8, %9\n\t" \
                     "v_mfma_f32_16x16x32_bf16 %6, %10, %17, %6\n\tv_and_b32 %9, 0xffff0000, %20\n\tv_mfma_f32_16x16x32_bf16 %7, %10, %18, %7\n\tv_add_f32 %8, %8, %9" \
            : "+v"(acc[0][b]), "+v"(acc[1][b]), "+v"(acc[2][b]), "+v"(acc[3][b]), "+v"(acc[4][b]), "+v"(acc[5][b]), "+v"(acc[6][b]), "+v"(acc[7][b]), "+v"(cs[(b) >> 1]), "=&v"(ct_) \
            : "v"(wf[b]), "v"(af[0]), "v"(af[1]), "v"(af[2]), "v"(af[3]), "v"(af[4]), "v"(af[5]), "v"(af[6]), "v"(af[7]), "v"(G1_CS_SRC(WR, 2 * (b))), "v"(G1_CS_SRC(WR, 2 * (b) + 1))); } while (0)
#define G1_MMA(WR) do { __builtin_amdgcn_s_setprio(1); G1_MMA8(WR, 0); G1_MMA8(WR, 2); G1_MMA8(WR, 1); G1_MMA8(WR, 3); G1_MMA8(WR, 4); \
        __builtin_amdgcn_s_setprio(0); SBAR(); } while (0)

        if (wr == 1) BAR();
        WAIT_V(6); WAIT_L0();
        BAR();
        SBAR();
        STAMP(1);
        u64 sg = 0;
        const u64* sgp = (const u64*)(ws + WS_STAT) + (rb * 256 + wid * 32 + (lane & 31)) * 2 + (lane >> 5);
#define G1_BODY(WR) do { \
        _Pragma("unroll 1") for (int t = 0; t < 16; t += 2) { \
            const int h1_ = (2 * t + 3 + (WR) < 31) ? 2 * t + 3 + (WR) : 31, h2_ = (2 * t + 5 + (WR) < 31) ? 2 * t + 5 + (WR) : 31, t2_ = (t < 14) ? t + 2 : 15; \
            PEEK_AHEAD(sg, sgp, t ^ 14); \
            G1_SEG_A(0, 0, t + 1); G1_MMA(WR); asm volatile("s_waitcnt vmcnt(4)" : "+v"(sg) :: "memory"); BAR(); SBAR(); \
            if (t == 14) { \
                _Pragma("unroll 1") for (unsigned spin = 0; spin < (1u << 20) && !__all((unsigned)(sg >> 32) == tag32); ++spin) \
                    asm volatile("s_sleep 1\n\tglobal_load_dwordx2 %0, %1, off sc1\n\ts_waitcnt vmcnt(0)" : "+v"(sg) : "v"(sgp) : "memory"); \
                const int tl = opaque_tid() & 63; \
                *(LAS unsigned*)(lds + G1_STAT + (wid * 32 + (tl & 31)) * 8 + (tl >> 5) * 4) = (unsigned)sg; } \
            G1_SEG_W(0, 1, h1_, (3 + (WR)) & 3, true); G1_MMA(WR); WAIT_V(5); BAR(); SBAR(); \
            G1_SEG_A(1, 2, t2_); G1_MMA(WR); WAIT_V(4); BAR(); SBAR(); \
            G1_SEG_W(1, 3, h2_, (1 + (WR)) & 3, false); \
            if (t == 14) { float c_[3] = {cs[0], cs[1], cs[2]}; \
                _Pragma("unroll") for (int r = 0; r < 10; ++r) { const unsigned w_ = G1_CS_SRC(WR, r); c_[r >> 2] += bf_lo(w_) + bf_hi(w_); } \
                const lds_ptr cp = lds + G1_CS + wc * 384 + (opaque_tid() & 15) * 4; \
                *(LAS float*)(cp + (WR) * 128) = red4(c_[0]); *(LAS float*)(cp + (WR) * 128 + 64) = red4(c_[1]); *(LAS float*)(cp + 256 + (WR) * 64) = red4(c_[2]); \
                WAIT_L0(); } \
            SBAR(); BAR(); SBAR(); G1_MMA(WR); WAIT_V(5); \
            if ((WR) == 0 || t < 14) BAR();       \
            SBAR(); \
        } } while (0)
        if (wr == 0) G1_BODY(0); else G1_BODY(1);
        MFMA_DRAIN(); SBAR();
        STAMP(2);

        const int tz = opaque_tid(), ln = tz & 63;
        fr = tz & 15; fq = (tz >> 4) & 3;
        const int row0 = rb * 256 + wr * 128 + fr;
        f32x4 csv[5]; f32x2 st[8];
#pragma unroll
        for (int b = 0; b < 5; ++b) csv[b] = *(const LAS f32x4*)(lds + G1_CS + wc * 384 + b * 64 + fq * 16);
        csv[4] += *(const LAS f32x4*)(lds + G1_CS + wc * 384 + 320 + fq * 16);
#pragma unroll
        for (int m = 0; m < 8; ++m) st[m] = *(const LAS f32x2*)(lds + G1_STAT + (wr * 128 + m * 16 + fr) * 8);
        auto lnv = [&](int m, int b, float sc) { return (acc[m][b] - st[m].x * csv[b]) * (st[m].y * sc); };
        auto glu = [&](int m, int bg, int bu) {
            const f32x4 g = lnv(m, bg, -LOG2E), u = lnv(m, bu, 1.0f);
            f32x4 h;
#pragma unroll
            for (int j = 0; j < 4; ++j) h[j] = u[j] * __builtin_amdgcn_rcpf(1.0f + __builtin_amdgcn_exp2f(g[j]));
            return h;
        };
        auto pack2 = [&](const f32x4& v0, const f32x4& v1) {
            unsigned p0 = cvtpk(v0[0], v0[1]), p1 = cvtpk(v0[2], v0[3]), q0 = cvtpk(v1[0], v1[1]), q1 = cvtpk(v1[2], v1[3]);
            swap16(p0, q0); swap16(p1, q1);
            return (u32x4){p0, p1, q0, q1};
        };
        const int hd = 4 * xcd + (typeY ? 2 : 0) + (wc >> 1);
        const unsigned qoff = WS_QKV + (unsigned)(hd * 2 + (wc & 1)) * 65536u + (unsigned)(row0 + (fq & 1) * 16) * 32u + (fq >> 1) * 16;
        constexpr unsigned KIND = NH * 2 * 65536u;
        if (!typeY) {
            const float sc4 = (js == 0) ? QSCALE : 1.0f;
#pragma unroll
            for (int m = 0; m < 8; m += 2) ST16(pack2(lnv(m, 4, sc4), lnv(m + 1, 4, sc4)), qoff + js * KIND + m * 512);
            CFENCE();
            hp.base = ws + WS_ACT + (unsigned)(14 * xcd + 4 * js + wc) * GRP + (unsigned)(rb * 256 + wr * 128) * 64u;
            hp.off = fr * 64 + (fq & 1) * 32 + (fq >> 1) * 16; hp.step = 1024;
#pragma unroll
            for (int m = 0; m < 8; ++m) {
                hp.v[m] = pack2(glu(m, 0, 2), glu(m, 1, 3));
                if (m == 5) { PIN(hp.v[5]); WAIT_V(0); arrive(flags, F_QKV + bid, ln, lds + G1_CNT, 7u); }
            }
        } else {
#pragma unroll
            for (int m = 0; m < 8; m += 2) {
                ST16(pack2(lnv(m, 3, 1.0f), lnv(m + 1, 3, 1.0f)), qoff + KIND + m * 512);
                ST16(pack2(lnv(m, 4, 1.0f), lnv(m + 1, 4, 1.0f)), qoff + 2 * KIND + m * 512);
                ST16(pack2(lnv(m, 1, QSCALE), lnv(m + 1, 1, QSCALE)), qoff + m * 512);
            }
            CFENCE();
            hp.base = ws + WS_ACT + (unsigned)(14 * xcd + 12 + (wc >> 1)) * GRP + (unsigned)(rb * 256 + wr * 128) * 64u;
            hp.off = (fr + (fq & 1) * 16) * 64 + (wc & 1) * 32 + (fq >> 1) * 16; hp.step = 2048;
#pragma unroll
            for (int m = 0; m < 8; m += 2) { hp.v[m >> 1] = pack2(glu(m, 0, 2), glu(m + 1, 0, 2)); hp.v[4 + (m >> 1)] = hp.v[m >> 1]; }
            PIN(hp.v[3]); WAIT_V(0); arrive(flags, F_QKV + bid, ln, lds + G1_CNT, 7u);
        }
#pragma unroll
        for (int i = 0; i < 8; ++i) PIN(hp.v[i]);
        STAMP(3);
    }

    const int z = xcd & 1, rbk = ((xcd >> 1) & 1) * 8 + (idx & 7), cbk = (xcd >> 2) * 4 + (idx >> 3);
    G2 g2;
    g2.Ah = (const char*)ws + WS_ACT + (size_t)z * 56 * GRP + rbk * 128 * 64;
    g2.Ao = (const char*)ws + WS_ACT + (size_t)(112 + z * 16 - 56) * GRP + rbk * 128 * 64;
    g2.Wd = (const char*)(wd + (size_t)z * 1792 * DM + cbk * 128);
    g2.Wo = (const char*)(wo + (size_t)z * 512 * DM + cbk * 128) - (size_t)1792 * DM * 2;
    {
        const int hl = idx & 3, qb = idx >> 2, h = 4 * xcd + hl;
        const int l0_ = opaque_tid() & 63;
        const int qkv0 = F_QKV + xcd + 8 * (qb * 4 + ((hl >= 2) ? 3 : l0_ % 3));
        const int qkvword = F_QKV + xcd + 8 * ((l0_ & 7) * 4 + ((hl >= 2) ? 3 : 1 + ((l0_ >> 3) & 1)));
        const int depword = (rbk >> 1) * 32 + ((l0_ >> 2) & 3) * 8 + 4 * z + (l0_ & 3);
        const char* Qg = (const char*)ws + WS_QKV + (size_t)h * 131072 + qb * 256 * 32;
        const char* Kg = (const char*)ws + WS_QKV + (size_t)(NH + h) * 131072;
        const char* Vg = (const char*)ws + WS_QKV + (size_t)(2 * NH + h) * 131072;
        const char* wpd = g2.Wd - (cbk & 3) * 256;
        const char* wpo = g2.Wo - (cbk & 3) * 256;
        AttnOut R;
#define DG
        attn_fast(lds, lds0, Qg, Kg, Vg, g2, flags, qkv0, qkvword, depword, F_H + bid, qb, bid == 0, hp, wpd, wpo, idx * 72, wid, R DG);
        STAMP(6);
        const int tz = opaque_tid(), ln = tz & 63, w4 = wid & 3, fr = ln & 15, fq = ln >> 4;
        const lds_ptr po = lds + 2 * SLOT + w4 * 8192 + ln * 16, pl = lds + 3 * SLOT + 4096 + w4 * 1024 + ln * 16;
        volatile LAS unsigned* const badw = (volatile LAS unsigned*)(lds + 3 * SLOT + 8192 + 4096);
#define AT_PUBLISH() do { if (wid < 4) { \
            _Pragma("unroll") for (int i = 0; i < 4; ++i) { *(LAS f32x4*)(po + i * 1024) = (f32x4){R.o0[4 * i], R.o0[4 * i + 1], R.o0[4 * i + 2], R.o0[4 * i + 3]}; \
                *(LAS f32x4*)(po + 4096 + i * 1024) = (f32x4){R.o1[4 * i], R.o1[4 * i + 1], R.o1[4 * i + 2], R.o1[4 * i + 3]}; } \
            *(LAS f32x4*)pl = (f32x4){R.l0[0], R.m0, R.l1[0], R.m1}; } } while (0)
        {
            AT_PUBLISH();
            const f32x16 z16 = R.o0 * 0.0f + R.o1 * 0.0f;
            float zs = 0.f;
#pragma unroll
            for (int r = 0; r < 16; ++r) zs += z16[r];
            const bool ok = (zs == 0.f) && (R.l0[0] > 0.f) && (R.l0[0] < 0x1p96f) && (R.l1[0] > 0.f) && (R.l1[0] < 0x1p96f);
            const bool wave_bad = !__all(ok);
            if (ln == 0) badw[wid] = wave_bad ? 1u : 0u;
            STAMP(16);
            WAIT_V(0); STAMP(17); WAIT_L0(); BAR(); CFENCE();
            const unsigned anybad = badw[ln & 7];
            if (__any(anybad != 0u)) {
                WAIT_L0(); BAR(); CFENCE();
                attn_safe(lds, lds0, Qg, Kg, Vg, wid, R);
                AT_PUBLISH();
                WAIT_L0(); BAR(); CFENCE();
            }
        }
        STAMP(7);

        const int tile = rbk * 8 + cbk;
        if (wid >= 4) {
            unsigned vA0, vA1, vB0, vB1; g2_voff(ln, vA0, vA1, vB0, vB1);
#define G2_LOAD(j, sl) do { const unsigned d_ = lds0 + (sl) * SLOT; g2_load_b(g2, j, d_, 2 * w4, vB0); g2_load_a(g2, j, d_, 2 * w4, vA0, vA1); \
                g2_load_b(g2, j, d_, 2 * w4 + 1, vB1); g2_load_a(g2, j, d_, 2 * w4 + 1, vA0, vA1); } while (0)
            const f32x4 lm = *(const LAS f32x4*)pl;
            f32x4 t[8];
#pragma unroll
            for (int i = 0; i < 8; ++i) t[i] = *(const LAS f32x4*)(po + i * 1024);
            WAIT_L0(); BAR(); CFENCE();
            G2_LOAD(3, 2);
            u32x4 ow[4];
            const unsigned ooff = WS_ACT + (unsigned)(112 + h) * GRP + (unsigned)(qb * 256 + w4 * 64 + (ln & 31)) * 64u + (ln >> 5) * 16;
#pragma unroll
            for (int q = 0; q < 2; ++q) {
                BAR();
                if (q == 0) G2_LOAD(4, 3); else G2_LOAD(5, 4);
                const f32x16 ok_ = q ? R.o1 : R.o0;
                const float lk = q ? R.l1[0] : R.l0[0], mk = q ? R.m1 : R.m0, lo = lm[2 * q], mo = lm[2 * q + 1];
                const float mm = __builtin_fmaxf(mk, mo), fk = __builtin_amdgcn_exp2f(mk - mm), fo = __builtin_amdgcn_exp2f(mo - mm);
                const float rl = 1.0f / (lk * fk + lo * fo), ck = fk * rl, co = fo * rl;
                unsigned p[8];
#pragma unroll
                for (int i = 0; i < 4; ++i) {
                    const f32x4 u = t[4 * q + i];
                    p[2 * i] = cvtpk(ok_[4 * i] * ck + u[0] * co, ok_[4 * i + 1] * ck + u[1] * co);
                    p[2 * i + 1] = cvtpk(ok_[4 * i + 2] * ck + u[2] * co, ok_[4 * i + 3] * ck + u[3] * co);
                }
                swap32(p[0], p[2]); swap32(p[1], p[3]); swap32(p[4], p[6]); swap32(p[5], p[7]);
                ow[2 * q] = (u32x4){p[0], p[1], p[2], p[3]}; ow[2 * q + 1] = (u32x4){p[4], p[5], p[6], p[7]};
                PIN(ow[2 * q]); PIN(ow[2 * q + 1]);
            }
#pragma unroll
            for (int i = 0; i < 4; ++i) ST16(ow[i], ooff + (i >> 1) * 2048 + (i & 1) * 32);
            CFENCE();
            unsigned t0 = 0, t1 = 0, t2 = 0, t3 = 0, a0 = 0, a1 = 0, a2 = 0;
            WAIT_V(20); BAR(); G2_LOAD(6, 0);
            WAIT_V(20); BAR(); G2_LOAD(7, 1);
            WAIT_V(20);
            int sl = 2;
#pragma unroll 1
            for (int j = 5; j < 33; ++j) {
                TS(t1);
                BAR();
                TS(t2);
                if (j == 6 && wid == 4) signal(flags, F_O + bid, ln);
                G2_LOAD(j + 3, sl);
                sl = (sl == 4) ? 0 : sl + 1;
                TS(t3);
                WAIT_V(16);
                TS(t0);
                a0 += t0 - t3; a1 += t2 - t1; a2 += t3 - t2;
            }
            PUT(50, a0); PUT(51, a1); PUT(52, a2);
            BAR();
            if (tz == 256) *(LAS unsigned*)lds = 0u;
            WAIT_V(8); WAIT_L0(); BAR(); WAIT_V(0); BAR();
                if (wid == 4) wait_all(flags, F_X + tile * 2 + z, peek(flags, F_X + tile * 2 + z));
            CFENCE(); BAR();
        } else {
            const int wm = w4 >> 1, wn = w4 & 1;
            const unsigned aO0 = (wm * 64 + fr) * 128 + ((fq ^ (fr >> 1)) * 16), aO1 = aO0 ^ 64;
            const int tq = (ln >> 2) & 3, tp = ln & 3, tg = (tq & 2) | ((fq & 1) << 2);
            unsigned wO[4];
#pragma unroll
            for (int b = 0; b < 4; ++b) wO[b] = 16384 + wn * 8192 + (8 * fq + tq) * 128 + (((2 * b + (tp >> 1)) ^ tg) * 16) + (tp & 1) * 8;
            f32x4 acc[4][4];
#pragma unroll
            for (int m = 0; m < 4; ++m)
#pragma unroll
                for (int b = 0; b < 4; ++b) acc[m][b] = (f32x4){0.f, 0.f, 0.f, 0.f};
            bf16x8 xa[4], ya[4] = {}; s16x4 xl[4], xh[4], yl[4] = {}, yh[4] = {};
            lds_ptr pa0, pa1, pw0, pw1, pw2, pw3;
#define G2_BASES(so) do { pa0 = lds + aO0 + (so); pa1 = lds + aO1 + (so); pw0 = lds + wO[0] + (so); pw1 = lds + wO[1] + (so); pw2 = lds + wO[2] + (so); pw3 = lds + wO[3] + (so); } while (0)
#define G2_M(C, m, b) do { MFMA16_ACC(acc[m][b], cat8(C##l[b], C##h[b]), C##a[m]); SBAR(); } while (0)
#define RDQ(x) (x)
#define G2_HALF(C, N, AB, WO, RD_, TAIL) do { const bool RD = RDQ(RD_); \
            G2_M(C, 0, 0); if (RD) { N##l[0] = lds_tr(pw0 + (WO)); N##h[0] = lds_tr(pw0 + (WO) + 512); } SBAR(); \
            G2_M(C, 1, 0); if (RD) N##a[0] = lds_b128(AB); SBAR(); \
            G2_M(C, 2, 0); if (RD) N##a[1] = lds_b128((AB) + 2048); SBAR(); \
            G2_M(C, 3, 0); if (RD) N##a[2] = lds_b128((AB) + 4096); SBAR(); \
            G2_M(C, 0, 1); if (RD) N##a[3] = lds_b128((AB) + 6144); SBAR(); \
            G2_M(C, 1, 1); if (RD) { N##l[1] = lds_tr(pw1 + (WO)); N##h[1] = lds_tr(pw1 + (WO) + 512); } SBAR(); \
            G2_M(C, 2, 1); if (RD) { N##l[2] = lds_tr(pw2 + (WO)); N##h[2] = lds_tr(pw2 + (WO) + 512); } SBAR(); \
            G2_M(C, 3, 1); if (RD) { N##l[3] = lds_tr(pw3 + (WO)); N##h[3] = lds_tr(pw3 + (WO) + 512); } SBAR(); \
            G2_M(C, 0, 2); TAIL(0); SBAR(); G2_M(C, 1, 2); TAIL(1); SBAR(); G2_M(C, 2, 2); TAIL(2); SBAR(); G2_M(C, 3, 2); TAIL(3); SBAR(); \
            G2_M(C, 0, 3); G2_M(C, 1, 3); G2_M(C, 2, 3); G2_M(C, 3, 3); } while (0)
            int so = 4 * SLOT;
            G2_BASES(so);
            xl[0] = lds_tr(pw0); xh[0] = lds_tr(pw0 + 512); SBAR();
#pragma unroll
            for (int m = 0; m < 4; ++m) { xa[m] = lds_b128(pa0 + m * 2048); SBAR(); }
            xl[1] = lds_tr(pw1); xh[1] = lds_tr(pw1 + 512); xl[2] = lds_tr(pw2); xh[2] = lds_tr(pw2 + 512); xl[3] = lds_tr(pw3); xh[3] = lds_tr(pw3 + 512);
            const u64* const optr = flags.p + (F_O + depword) * 16;
            u64 ov = 0; unsigned t0 = 0, t1 = 0, a0 = 0;
            STAMP(8);
#define G2_NEXT(i) do { if ((i) == 0) so = (so == 4 * SLOT) ? 0 : so + SLOT; \
                if ((i) == 1) { pa0 = lds + aO0 + so; pa1 = lds + aO1 + so; PIN(pa0); PIN(pa1); } \
                if ((i) == 2) { pw0 = lds + wO[0] + so; pw1 = lds + wO[1] + so; PIN(pw0); PIN(pw1); } \
                if ((i) == 3) { pw2 = lds + wO[2] + so; pw3 = lds + wO[3] + so; PIN(pw2); PIN(pw3); } } while (0)
#define G2_NONE(i) do {} while (0)
            WAIT_L0(); BAR(); SBAR();
#pragma unroll 1
            for (int ph = 0; ph < 4; ++ph) {
                const int n = (ph == 0) ? 9 : (ph == 3) ? 6 : 1;
#pragma unroll 1
                for (int i = 0; i < n; ++i) {
                    WAIT_L0(); SBAR(); G2_HALF(x, y, pa1, 4096, true, G2_NEXT);
                    WAIT_L0(); BAR(); SBAR(); G2_HALF(y, x, pa0, 0, true, G2_NONE);
                    WAIT_L0(); SBAR(); G2_HALF(x, y, pa1, 4096, true, G2_NEXT);
                    WAIT_L0(); TS(t0); BAR(); TS(t1); SBAR(); a0 += t1 - t0;
                    G2_HALF(y, x, pa0, 0, true, G2_NONE);
                }
                PEEK_AHEAD(ov, optr, wid | ph);
                if ((wid | (ph ^ 1)) == 0) { PEEK_DONE(ov); wait_all(flags, F_O + depword, ov); __builtin_amdgcn_fence(__ATOMIC_ACQUIRE, "agent"); }
                if (ph == 2) WAIT_V(0);
            }
            const int row = rbk * 128 + wm * 64 + z * 32 + fr, col = cbk * 128 + wn * 64 + (fq & 1) * 16 + (fq >> 1) * 8;
            u32x4 xr[4];
#pragma unroll
            for (int i = 0; i < 4; ++i) xr[i] = *(const u32x4*)(x + (size_t)(row + (i >> 1) * 16) * DM + col + (i & 1) * 32);
            WAIT_L0(); SBAR(); G2_HALF(x, y, pa1, 4096, true, G2_NEXT);
            WAIT_L0(); BAR(); SBAR(); G2_HALF(y, x, pa0, 0, true, G2_NONE);
            WAIT_L0(); SBAR(); G2_HALF(x, y, pa1, 4096, true, G2_NONE);
            WAIT_L0(); SBAR(); G2_HALF(y, x, pa0, 0, false, G2_NONE);
            MFMA_DRAIN(); SBAR();
            STAMP(9); PUT(53, a0);
            f32x4 vk[8];
            const unsigned xo = WS_XCH + (unsigned)(tile * 2 + 1 - z) * 32768u + w4 * 8192 + ln * 16;
            if (z == 0) {
#pragma unroll
                for (int i = 0; i < 8; ++i) { ST16(__builtin_bit_cast(u32x4, acc[2 + (i >> 2)][i & 3]), xo + i * 1024); vk[i] = acc[i >> 2][i & 3]; }
            } else {
#pragma unroll
                for (int i = 0; i < 8; ++i) { ST16(__builtin_bit_cast(u32x4, acc[i >> 2][i & 3]), xo + i * 1024); vk[i] = acc[2 + (i >> 2)][i & 3]; }
            }
            WAIT_V(0);
            arrive(flags, F_X + tile * 2 + 1 - z, ln, lds, 3u);
            STAMP(10);
            CFENCE(); BAR(); CFENCE();
            STAMP(11);
            const unsigned xi = WS_XCH + (unsigned)(tile * 2 + z) * 32768u + w4 * 8192 + ln * 16;
#pragma unroll
            for (int i = 0; i < 8; ++i) vk[i] += __builtin_bit_cast(f32x4, __builtin_amdgcn_raw_buffer_load_b128(wsr, xi + i * 1024, 0, 16));
#pragma unroll
            for (int i = 0; i < 4; ++i) {
                const u32x4 a = __builtin_bit_cast(u32x4, vk[2 * i]), b = __builtin_bit_cast(u32x4, vk[2 * i + 1]);
                unsigned a0_ = a[0], a1 = a[1], a2 = a[2], a3 = a[3], b0 = b[0], b1 = b[1], b2 = b[2], b3 = b[3];
                swap16(a0_, b0); swap16(a1, b1); swap16(a2, b2); swap16(a3, b3);
                const u32x4 xx = xr[i];
                u32x4 w;
                w[0] = cvtpk(u2f(a0_) + bf_lo(xx[0]), u2f(a1) + bf_hi(xx[0]));
                w[1] = cvtpk(u2f(a2) + bf_lo(xx[1]), u2f(a3) + bf_hi(xx[1]));
                w[2] = cvtpk(u2f(b0) + bf_lo(xx[2]), u2f(b1) + bf_hi(xx[2]));
                w[3] = cvtpk(u2f(b2) + bf_lo(xx[3]), u2f(b3) + bf_hi(xx[3]));
                *(u32x4*)(out + (size_t)(row + (i >> 1) * 16) * DM + col + (i & 1) * 32) = w;
            }
        }
    }
}

extern "C" void kernel_launch(void* const* d_in, const int* in_sizes, int n_in, void* d_out, int out_size, void* d_ws, size_t ws_size, hipStream_t stream) {
    const bf16_t* x = (const bf16_t*)d_in[0];
    k_main<<<256, 512, 0, stream>>>(x, (const bf16_t*)d_in[1], (const bf16_t*)d_in[2], (const bf16_t*)d_in[4], (const bf16_t*)d_in[5], (const bf16_t*)d_in[6],
                                    (bf16_t*)d_out, (unsigned char*)d_ws);
}
```

```cpp
#include <hip/hip_runtime.h>
#include <stdint.h>

typedef unsigned short bf16_t;
typedef short bf16x8 __attribute__((ext_vector_type(8)));
typedef short s16x4 __attribute__((ext_vector_type(4)));
typedef float f32x2 __attribute__((ext_vector_type(2)));
typedef float f32x4 __attribute__((ext_vector_type(4)));
typedef float f32x16 __attribute__((ext_vector_type(16)));
typedef unsigned u32x4 __attribute__((ext_vector_type(4)));
typedef __bf16 bf16x2_t __attribute__((ext_vector_type(2)));
#define LAS __attribute__((address_space(3)))
#define GAS __attribute__((address_space(1)))
typedef LAS unsigned char* lds_ptr;

constexpr int SEQ = 2048, DM = 1024, FF = 3584, NQKV = 3072, NH = 32;
constexpr float LN_EPS = 1e-5f;
constexpr float LOG2E = 1.4426950408889634f;
constexpr float QSCALE = 0.17677669529663687f * LOG2E;

constexpr unsigned WS_FLAGS = 0;
constexpr unsigned WS_STAT = 192 * 1024;
constexpr unsigned WS_QKV = 256 * 1024;
constexpr unsigned WS_ACT = WS_QKV + 3u * SEQ * DM * 2;
constexpr unsigned WS_XCH = WS_ACT + 144u * SEQ * 64;
constexpr unsigned WS_XC = WS_XCH + 256u * 32768;
constexpr unsigned WS_END = WS_XC + SEQ * DM * 2;
constexpr int F_EPOCH = 0, F_QKV = 1, F_H = 257, F_O = 513, F_X = 769, F_XC = 1025;
constexpr unsigned GRP = SEQ * 64;

#define SBAR() __builtin_amdgcn_sched_barrier(0)
#define BAR() __builtin_amdgcn_s_barrier()
#define WAIT_V(n) asm volatile("s_waitcnt vmcnt(" #n ")" ::: "memory")
#define WAIT_L0() do { __builtin_amdgcn_s_waitcnt(0xC07F); asm volatile("" ::: "memory"); } while (0)
#define CFENCE() asm volatile("" ::: "memory")
#define PIN(x) asm volatile("" : "+v"(x))
#define MFMA16(a, b, c) __builtin_amdgcn_mfma_f32_16x16x32_bf16(a, b, c, 0, 0, 0)
#define MFMA32(a, b, c) __builtin_amdgcn_mfma_f32_32x32x16_bf16(a, b, c, 0, 0, 0)
#define MFMA16_ACC(acc, a, b) asm volatile("v_mfma_f32_16x16x32_bf16 %0, %1, %2, %0" : "+v"(acc) : "v"(a), "v"(b))
#define MFMA_DRAIN() asm volatile("s_nop 15" ::: "memory")
#define RLX_AGENT __ATOMIC_RELAXED, __HIP_MEMORY_SCOPE_AGENT

#define STAMP(i) do {} while (0)
#define TS(v) do {} while (0)
#define PUT(i, v) do {} while (0)

__device__ __forceinline__ void glds16(const void* sbase, unsigned voff, unsigned ldsdst) {
    asm volatile("s_mov_b32 m0, %2\n\ts_nop 0\n\tglobal_load_lds_dwordx4 %0, %1" ::"v"(voff), "s"(sbase), "s"(ldsdst) : "memory");
}
__device__ __forceinline__ void glds16c(const void* sbase, unsigned voff, unsigned ldsdst) {
    asm volatile("s_mov_b32 m0, %2\n\ts_nop 0\n\tglobal_load_lds_dwordx4 %0, %1 sc1" ::"v"(voff), "s"(sbase), "s"(ldsdst) : "memory");
}
__device__ __forceinline__ bf16x8 lds_b128(lds_ptr p) { return *(const LAS bf16x8*)p; }
__device__ __forceinline__ s16x4 lds_tr(lds_ptr p) { return __builtin_amdgcn_ds_read_tr16_b64_v4i16((LAS s16x4*)p); }
__device__ __forceinline__ bf16x8 cat8(s16x4 a, s16x4 b) { return __builtin_shufflevector(a, b, 0, 1, 2, 3, 4, 5, 6, 7); }
__device__ __forceinline__ unsigned cvtpk(float lo, float hi) {
    f32x2 v = {lo, hi};
    return __builtin_bit_cast(unsigned, __builtin_convertvector(v, bf16x2_t));
}
__device__ __forceinline__ float u2f(unsigned u) { return __builtin_bit_cast(float, u); }
__device__ __forceinline__ unsigned f2u(float f) { return __builtin_bit_cast(unsigned, f); }
__device__ __forceinline__ float bf_lo(unsigned u) { return u2f(u << 16); }
__device__ __forceinline__ float bf_hi(unsigned u) { return u2f(u & 0xffff0000u); }
__device__ __forceinline__ void swap16(unsigned& a, unsigned& b) {
    auto r = __builtin_amdgcn_permlane16_swap(a, b, false, false);
    a = r[0]; b = r[1];
}
__device__ __forceinline__ void swap32(unsigned& a, unsigned& b) {
    auto r = __builtin_amdgcn_permlane32_swap(a, b, false, false);
    a = r[0]; b = r[1];
}
__device__ __forceinline__ int opaque_tid() { int t = threadIdx.x; asm volatile("" : "+v"(t)); return t; }
typedef unsigned long long u64;
struct Flags { u64* p; u64 tag; };
__device__ __forceinline__ void signal(const Flags& f, int word, int lane) {
    if (lane == 0) __hip_atomic_store((GAS u64*)(f.p + word * 16), f.tag, RLX_AGENT);
}
__device__ __forceinline__ u64 peek(const Flags& f, int word) { return __hip_atomic_load((GAS u64*)(f.p + word * 16), RLX_AGENT); }
__device__ __forceinline__ void wait_all(const Flags& f, int word, u64 v) {
    unsigned spin = 0;
    while (!__all(v == f.tag)) { if (++spin > (1u << 20)) break; __builtin_amdgcn_s_sleep(1); v = peek(f, word); }
}
__device__ __forceinline__ void arrive(const Flags& f, int word, int lane, lds_ptr cnt, unsigned last) {
    if (lane == 0 && __hip_atomic_fetch_add((LAS unsigned*)cnt, 1u, __ATOMIC_RELAXED, __HIP_MEMORY_SCOPE_WORKGROUP) == last)
        __hip_atomic_store((GAS u64*)(f.p + word * 16), f.tag, RLX_AGENT);
}
#define PEEK_AHEAD(v, ptr, sel) asm volatile("s_cmp_lg_u32 %2, 0\n\ts_cbranch_scc1 1f\n\tglobal_load_dwordx2 %0, %1, off sc1\n1:" : "+v"(v) : "v"(ptr), "s"(sel) : "memory", "scc")
#define PEEK_DONE(v) asm volatile("s_waitcnt vmcnt(0)" : "+v"(v) :: "memory")

template <int CTRL> __device__ __forceinline__ float dpp_add(float v) { return v + u2f(__builtin_amdgcn_update_dpp(0u, f2u(v), CTRL, 0xf, 0xf, true)); }
__device__ __forceinline__ float red4(float v) {
    unsigned a = f2u(v), b = a;
    swap16(a, b);
    v = u2f(a) + u2f(b);
    a = f2u(v); b = a;
    swap32(a, b);
    return u2f(a) + u2f(b);
}
__device__ __forceinline__ float wave_sum(float v) {
    v = dpp_add<0xB1>(v); v = dpp_add<0x4E>(v); v = dpp_add<0x141>(v); v = dpp_add<0x140>(v);
    return red4(v);
}
__device__ __forceinline__ f32x2 row_stats(u32x4& a, u32x4& b) {
    float v[16];
#pragma unroll
    for (int i = 0; i < 4; ++i) { v[2 * i] = bf_lo(a[i]); v[2 * i + 1] = bf_hi(a[i]); v[8 + 2 * i] = bf_lo(b[i]); v[8 + 2 * i + 1] = bf_hi(b[i]); }
    float s = 0.f;
#pragma unroll
    for (int i = 0; i < 16; ++i) s += v[i];
    const float mu = wave_sum(s) * (1.0f / DM);
    float q = 0.f;
#pragma unroll
    for (int i = 0; i < 16; ++i) { v[i] -= mu; q += v[i] * v[i]; }
#pragma unroll
    for (int i = 0; i < 4; ++i) { a[i] = cvtpk(v[2 * i], v[2 * i + 1]); b[i] = cvtpk(v[8 + 2 * i], v[8 + 2 * i + 1]); }
    return (f32x2){mu, 1.0f / __builtin_sqrtf(wave_sum(q) * (1.0f / DM) + LN_EPS)};
}

constexpr int SLOT = 32768, LDS_BYTES = 5 * SLOT;
constexpr int G1_A = 0, G1_W = 65536, G1_STAT = G1_W + 5 * 16384, G1_CS = G1_STAT + 2048, G1_CNT = G1_CS + 4 * 6 * 64;

struct G2 { const char* Ah; const char* Ao; const char* Wd; const char* Wo; };
__device__ __forceinline__ void g2_load_b(const G2& g, int j, unsigned dst, int vw, unsigned voffB) {
    const char* b = (j >= 28 ? g.Wo : g.Wd) + (size_t)(j * 64 + vw * 8) * (DM * 2);
    glds16(b, voffB, dst + 16384 + vw * 1024); glds16(b + 128, voffB, dst + 16384 + 8192 + vw * 1024);
}
__device__ __forceinline__ void g2_load_a(const G2& g, int j, unsigned dst, int vw, unsigned voffA0, unsigned voffA1) {
    const char* a = (j >= 28 ? g.Ao : g.Ah) + (size_t)j * (2 * GRP) + vw * 1024;
    glds16c(a, voffA0, dst + vw * 2048); glds16c(a, voffA1, dst + vw * 2048 + 1024);
}
__device__ __forceinline__ void g2_voff(int lane, unsigned& voffA0, unsigned& voffA1, unsigned& voffB0, unsigned& voffB1) {
    const int r8 = lane >> 3, u8 = lane & 7, ua = u8 ^ (r8 >> 1), ub = ua ^ 4;
    voffA0 = (unsigned)(ua >> 2) * GRP + r8 * 64 + (ua & 3) * 16;
    voffA1 = (unsigned)(ub >> 2) * GRP + (r8 + 8) * 64 + (ub & 3) * 16;
    voffB0 = (unsigned)(r8 * (DM * 2) + (u8 ^ (r8 & 2)) * 16);
    voffB1 = (unsigned)(r8 * (DM * 2) + (u8 ^ (4 | (r8 & 2))) * 16);
}

struct AttnOut { f32x16 o0, o1; f32x4 l0, l1; float m0, m1; };
struct HPend { u32x4 v[8]; unsigned off, step; const void* base; };
struct AttnLane {
    unsigned voffK, voffV; lds_ptr kb0, kb1, vb; const char* qp; bf16x8 ones;
    __device__ __forceinline__ void init(lds_ptr lds, const char* Qg, int wid, int lane) {
        const int r32 = lane & 31, hi = lane >> 5, kh = wid >> 2, qg = wid & 3, ksw = (r32 >> 2) & 3;
        const int uk = (lane & 3) ^ ((lane >> 4) & 3), uv = lane & 3;
        voffK = (unsigned)((uk >> 1) * 65536 + wid * 1024 + (lane >> 2) * 32 + (uk & 1) * 16);
        voffV = (unsigned)((uv >> 1) * 65536 + wid * 1024 + (lane >> 2) * 32 + (uv & 1) * 16);
        kb0 = lds + kh * 8192 + r32 * 64 + ((hi ^ ksw) * 16); kb1 = lds + kh * 8192 + r32 * 64 + (((2 | hi) ^ ksw) * 16);
        vb = lds + 16384 + kh * 8192 + (4 * hi + ((lane >> 2) & 3)) * 64 + ((lane >> 4) & 1) * 32 + (lane & 3) * 8;
        qp = Qg + (qg * 64 + r32) * 32 + hi * 16;
        const short v = (((lane >> 4) & 1) == ((lane >> 2) & 1)) ? (short)0x3f80 : (short)0;
        ones = (bf16x8){v, v, v, v, v, v, v, v};
    }
};
#define EX(v) __builtin_amdgcn_exp2f(v)
#define AT_LOAD_K(c, slot) do { const unsigned d_ = lds0 + wid * 2048 + (slot) * SLOT; const char* k_ = Kg + (c) * 8192; glds16c(k_, A.voffK, d_); glds16c(k_ + 512, A.voffK, d_ + 1024); } while (0)
#define AT_LOAD_V(c, slot) do { const unsigned d_ = lds0 + wid * 2048 + (slot) * SLOT + 16384; const char* v_ = Vg + (c) * 8192; glds16c(v_, A.voffV, d_); glds16c(v_ + 512, A.voffV, d_ + 1024); } while (0)
#define AT_PACK(S, i) __builtin_bit_cast(bf16x8, (u32x4){cvtpk(S[i], S[i + 1]), cvtpk(S[i + 2], S[i + 3]), cvtpk(S[i + 4], S[i + 5]), cvtpk(S[i + 6], S[i + 7])})
#define AT_Q_LOAD() asm volatile("global_load_dwordx4 %0, %4, off sc1\n\tglobal_load_dwordx4 %1, %4, off offset:1024 sc1\n\tglobal_load_dwordx4 %2, %5, off sc1\n\tglobal_load_dwordx4 %3, %5, off offset:1024 sc1" \
        : "=&v"(q00), "=&v"(q10), "=&v"(q01), "=&v"(q11) : "v"(A.qp), "v"(qp2) : "memory")
#define AT_Q_WAIT(n) asm volatile("s_waitcnt vmcnt(" #n ")" : "+v"(q00), "+v"(q01), "+v"(q10), "+v"(q11) :: "memory")
__device__ __forceinline__ float rowmax16(const f32x16& s) {
    float a = s[0];
#pragma unroll
    for (int r = 1; r < 16; ++r) a = __builtin_fmaxf(a, s[r]);
    unsigned x = f2u(a), y = x; swap32(x, y);
    return __builtin_fmaxf(u2f(x), u2f(y));
}

__device__ __forceinline__ void attn_safe(lds_ptr lds, unsigned lds0, const char* Qg, const char* Kg, const char* Vg, int wid, AttnOut& R) {
    AttnLane A; A.init(lds, Qg, wid, opaque_tid() & 63);
    bf16x8 q00, q01, q10, q11; const char* qp2 = A.qp + 65536;
    AT_Q_LOAD(); AT_Q_WAIT(0);
    f32x16 o0 = (f32x16){}, o1 = (f32x16){};
    f32x4 l0 = (f32x4){0.f, 0.f, 0.f, 0.f}, l1 = l0;
    float m0 = -__builtin_inff(), m1 = m0;
#pragma unroll 1
    for (int c = 0; c < 8; ++c) {
        AT_LOAD_K(c, 3); AT_LOAD_V(c, 3);
        WAIT_V(0); BAR(); CFENCE();
#pragma unroll 1
        for (int t = 0; t < 4; ++t) {
            const int to = 3 * SLOT + t * 2048;
            const bf16x8 ka = lds_b128(A.kb0 + to), kb = lds_b128(A.kb1 + to);
            const bf16x8 va = cat8(lds_tr(A.vb + to), lds_tr(A.vb + to + 512)), vc = cat8(lds_tr(A.vb + to + 1024), lds_tr(A.vb + to + 1536));
            f32x16 s0 = MFMA32(kb, q01, MFMA32(ka, q00, (f32x16){})), s1 = MFMA32(kb, q11, MFMA32(ka, q10, (f32x16){}));
            const float n0 = __builtin_fmaxf(m0, rowmax16(s0)), n1 = __builtin_fmaxf(m1, rowmax16(s1));
            const float f0 = EX(m0 - n0), f1 = EX(m1 - n1);
            m0 = n0; m1 = n1; o0 *= f0; o1 *= f1; l0 *= f0; l1 *= f1;
#pragma unroll
            for (int r = 0; r < 16; ++r) { s0[r] = EX(s0[r] - n0); s1[r] = EX(s1[r] - n1); }
            const bf16x8 p00 = AT_PACK(s0, 0), p01 = AT_PACK(s0, 8), p10 = AT_PACK(s1, 0), p11 = AT_PACK(s1, 8);
            o0 = MFMA32(vc, p01, MFMA32(va, p00, o0)); l0 = MFMA16(A.ones, p01, MFMA16(A.ones, p00, l0));
            o1 = MFMA32(vc, p11, MFMA32(va, p10, o1)); l1 = MFMA16(A.ones, p11, MFMA16(A.ones, p10, l1));
        }
        WAIT_L0(); BAR(); CFENCE();
    }
    R.o0 = o0; R.o1 = o1; R.l0 = l0; R.l1 = l1; R.m0 = m0; R.m1 = m1;
}

__device__ __forceinline__ void attn_fast(lds_ptr lds, unsigned lds0, const char* Qg, const char* Kg, const char* Vg, const G2& g2, const Flags& flags, int qkv0, int qkvword, int depword,
                                          int hsig, int c0, bool bump, const HPend& hp, const char* wpd, const char* wpo, int prow, int wid, AttnOut& R
                                          ) {
    const int lane = opaque_tid() & 63;
    AttnLane A; A.init(lds, Qg, wid, lane);
    const lds_ptr kb0 = A.kb0, kb1 = A.kb1, vb = A.vb;
    const bf16x8 ones = A.ones;
    const __amdgpu_buffer_rsrc_t hr = __builtin_amdgcn_make_buffer_rsrc((void*)hp.base, 0, 8192, 0x00020000);
    f32x16 o0 = (f32x16){}, o1 = (f32x16){}, negm0 = (f32x16){}, negm1 = (f32x16){}, sA0, sA1, sB0, sB1;
    f32x4 l0 = (f32x4){0.f, 0.f, 0.f, 0.f}, l1 = l0;
    bf16x8 kfa, kfb, va, vc = (bf16x8){}, pf = (bf16x8){};
    PIN(o0); PIN(o1); PIN(l0); PIN(l1); PIN(vc); PIN(pf); PIN(A.voffK); PIN(A.voffV); PIN(A.qp);
    unsigned gA0, gA1, gB0, gB1; g2_voff(lane, gA0, gA1, gB0, gB1);
    unsigned gB = (wid & 1) ? gB1 : gB0;
    unsigned pfo = (unsigned)(((lane >> 3) & 1) * (DM * 2) + (lane & 7) * 128);
    const u64* hptr = flags.p + (F_H + depword) * 16;
    const u64* qptr = flags.p + qkvword * 16;
    const char* qp2 = A.qp + 65536;
    PIN(gA0); PIN(gA1); PIN(gB); PIN(pfo); PIN(hptr); PIN(qptr); PIN(qp2);
    if (wid == 0) {
        wait_all(flags, qkv0, peek(flags, qkv0));
        STAMP(13);
    }
    CFENCE(); BAR(); CFENCE();
    STAMP(4);
    bf16x8 q00, q01, q10, q11;
    AT_LOAD_K(c0, 0); AT_Q_LOAD(); AT_LOAD_V(c0, 0);
    u64 hv = 0;
    float m0, m1;
    AT_Q_WAIT(2);
    PEEK_AHEAD(hv, qptr, wid);
    BAR(); SBAR();
    STAMP(15);
    {
        kfa = lds_b128(kb0); kfb = lds_b128(kb1);
        sA0 = MFMA32(kfa, q00, negm0); sA1 = MFMA32(kfa, q10, negm1); sA0 = MFMA32(kfb, q01, sA0); sA1 = MFMA32(kfb, q11, sA1);
        kfa = lds_b128(kb0 + 2048); kfb = lds_b128(kb1 + 2048);
        m0 = rowmax16(sA0); m1 = rowmax16(sA1);
#pragma unroll
        for (int r = 0; r < 16; ++r) { sA0[r] -= m0; sA1[r] -= m1; negm0[r] = -m0; negm1[r] = -m1; }
        PIN(negm0); PIN(negm1);
    }
    u32x4 hA = hp.v[0], hB = hp.v[1], hC = hp.v[2], hD = hp.v[3];
    unsigned hoff = hp.off, pfd = 0;
#define AT_HST(v) do { __builtin_amdgcn_raw_buffer_store_b128(v, hr, hoff, 0, 16); hoff += hp.step; CFENCE(); } while (0)
    PEEK_DONE(hv);
    if (wid == 0) {
        wait_all(flags, qkvword, hv);
        if (bump) signal(flags, F_EPOCH, lane);
        STAMP(14);
    }
    CFENCE(); BAR(); SBAR();
    STAMP(5);
    AT_LOAD_K((c0 + 1) & 7, 1); AT_LOAD_V((c0 + 1) & 7, 1);
#define HS 1
    hoff += 8192;
    __builtin_amdgcn_s_setprio(1);
    STAMP(19);
#define AT_GROUP(QK, RD, S, i, O, L, VF) do { \
        QK; SBAR(); RD; SBAR(); \
        S[i] = EX(S[i]); S[i + 1] = EX(S[i + 1]); S[i + 2] = EX(S[i + 2]); PIN(S); SBAR(); \
        O = MFMA32(VF, pf, O); SBAR(); \
        S[i + 3] = EX(S[i + 3]); S[i + 4] = EX(S[i + 4]); S[i + 5] = EX(S[i + 5]); PIN(S); SBAR(); \
        L = MFMA16(ones, pf, L); SBAR(); \
        S[i + 6] = EX(S[i + 6]); S[i + 7] = EX(S[i + 7]); \
        pf = AT_PACK(S, i); PIN(pf); SBAR(); } while (0)
#define AT_STEP(C0, C1, N0, N1, KO, VO) do { \
        AT_GROUP(N0 = MFMA32(kfa, q00, negm0), va = cat8(lds_tr(vb + (VO)), lds_tr(vb + (VO) + 512)), C0, 0, o1, l1, vc); \
        AT_GROUP(N1 = MFMA32(kfa, q10, negm1), { kfa = lds_b128(kb0 + (KO)); vc = cat8(lds_tr(vb + (VO) + 1024), lds_tr(vb + (VO) + 1536)); }, C1, 0, o0, l0, va); \
        AT_GROUP(N0 = MFMA32(kfb, q01, N0), (void)0, C0, 8, o1, l1, va); \
        AT_GROUP(N1 = MFMA32(kfb, q11, N1), kfb = lds_b128(kb1 + (KO)), C1, 8, o0, l0, vc); } while (0)
#define AT_YPRIO(p) asm volatile("s_cmp_lt_u32 %0, 4\n\ts_cbranch_scc1 1f\n\ts_setprio " #p "\n1:" :: "s"(wid) : "scc")
#pragma unroll 1
    for (int c = 0; c < 5; ++c) {
        const int so = (c & 3) * SLOT, sn = ((c + 1) & 3) * SLOT;
        if (c == HS) hoff = hp.off;
        AT_HST(hA);
        AT_STEP(sA0, sA1, sB0, sB1, so + 4096, so);
        AT_HST(hB); AT_YPRIO(0);
        AT_STEP(sB0, sB1, sA0, sA1, so + 6144, so + 2048);
        if (c == 0) WAIT_V(2); else WAIT_V(5);
        WAIT_L0(); BAR(); SBAR();
        STAMP(20 + c);
        AT_YPRIO(2);
        if (c == HS + 3 && wid == 0) signal(flags, hsig, lane);
        AT_LOAD_K((c0 + c + 2) & 7, (c + 2) & 3); AT_LOAD_V((c0 + c + 2) & 7, (c + 2) & 3); AT_HST(hC);
        {
            const int pi = (wid + 8 * c < 36) ? wid + 8 * c : 35, lr = prow + 2 * pi;
            const char* pp = (lr < 1792 ? wpd : wpo) + (size_t)lr * (DM * 2);
            asm volatile("global_load_dword %0, %1, %2" : "+v"(pfd) : "v"(pfo), "s"(pp) : "memory");
        }
        AT_STEP(sA0, sA1, sB0, sB1, sn, so + 4096);
        AT_HST(hD);
        AT_STEP(sB0, sB1, sA0, sA1, sn + 2048, so + 6144);
        if (c >= HS) { hA = hp.v[4]; hB = hp.v[5]; hC = hp.v[6]; hD = hp.v[7]; }
    }
#pragma unroll 1
    for (int c = 5; c < 8; ++c) {
        const int so = (c & 3) * SLOT, sn = (c == 7) ? so : ((c + 1) & 3) * SLOT;
        const int sel = (c ^ 6) | wid;
        if (sel == 0) { PEEK_DONE(hv); wait_all(flags, F_H + depword, hv); }
        if (c == 5) g2_load_b(g2, 0, lds0 + 4 * SLOT, wid, gB);
        if (c == 6) g2_load_b(g2, 1, lds0, wid, gB);
        if (c == 7) g2_load_a(g2, 1, lds0, wid, gA0, gA1);
        AT_STEP(sA0, sA1, sB0, sB1, so + 4096, so);
        AT_YPRIO(0);
        AT_STEP(sB0, sB1, sA0, sA1, so + 6144, so + 2048);
        WAIT_V(0); WAIT_L0(); BAR(); SBAR();
        STAMP(20 + c);
        AT_YPRIO(2);
        if (c == 5) { AT_LOAD_K((c0 + 7) & 7, 3); AT_LOAD_V((c0 + 7) & 7, 3); }
        if (c == 6) { g2_load_a(g2, 0, lds0 + 4 * SLOT, wid, gA0, gA1); }
        if (c == 7) g2_load_a(g2, 2, lds0 + SLOT, wid, gA0, gA1);
        AT_STEP(sA0, sA1, sB0, sB1, sn, so + 4096);
        if (c == 6) g2_load_b(g2, 2, lds0 + SLOT, wid, gB);
        PEEK_AHEAD(hv, hptr, (c ^ 5) | wid);
        AT_STEP(sB0, sB1, sA0, sA1, sn + 2048, so + 6144);
    }
    STAMP(28);
    __builtin_amdgcn_s_setprio(0);
    o1 = MFMA32(vc, pf, o1); l1 = MFMA16(ones, pf, l1);
    asm volatile("" :: "v"(pfd));
    R.o0 = o0; R.o1 = o1; R.l0 = l0; R.l1 = l1; R.m0 = m0; R.m1 = m1;
}

__global__ __launch_bounds__(512, 2) void k_main(const bf16_t* __restrict__ x, const bf16_t* __restrict__ wqkv, const bf16_t* __restrict__ wo, const bf16_t* __restrict__ wg,
                                                 const bf16_t* __restrict__ wu, const bf16_t* __restrict__ wd, bf16_t* __restrict__ out, unsigned char* ws) {
    __shared__ __attribute__((aligned(16))) unsigned char smem[LDS_BYTES];
    const lds_ptr lds = (lds_ptr)smem;
    const unsigned lds0 = (unsigned)(uintptr_t)lds;
    const int wid = __builtin_amdgcn_readfirstlane(threadIdx.x >> 6);
    const int bid = blockIdx.x, xcd = bid & 7, idx = bid >> 3;
    Flags flags; flags.p = (u64*)(ws + WS_FLAGS);
    const __amdgpu_buffer_rsrc_t wsr = __builtin_amdgcn_make_buffer_rsrc((void*)ws, 0, WS_END, 0x00020000);
#define ST16(v, off) __builtin_amdgcn_raw_buffer_store_b128(v, wsr, off, 0, 16)
    STAMP(0);
    unsigned cpd = 0;
    {
        const char* cbase; unsigned clen;
        asm volatile("s_getpc_b64 %0\n.Lk_begin:\n\ts_mov_b32 %1, .Lk_end-.Lk_begin" : "=s"(cbase), "=s"(clen));
        unsigned cpo = (unsigned)(idx * 16 + (threadIdx.x & 15)) * 128u;
        cpo = (cpo < clen) ? cpo : 0u;
        asm volatile("global_load_dword %0, %1, %2" : "+v"(cpd) : "v"(cpo), "s"(cbase) : "memory");
    }
    HPend hp;
    {
        const int lane = threadIdx.x & 63, wr = wid >> 2, wc = wid & 3, js = idx & 3, rb = idx >> 2;
        const bool typeY = (js == 3);
        const int f0 = 448 * xcd + 128 * js;
        const char* P0 = (const char*)(wg + f0);
        const char* P2 = (const char*)(wu + f0);
        const char* P1 = typeY ? (const char*)(wqkv + 128 * xcd + 64) : (const char*)(wg + f0 + 64);
        const char* P3 = typeY ? (const char*)(wqkv + 1024 + 128 * xcd + 64) : (const char*)(wu + f0 + 64);
        const char* P4 = typeY ? (const char*)(wqkv + 2048 + 128 * xcd + 64) : (const char*)(wqkv + js * 1024 + 128 * xcd);
        const unsigned stepF = 32u * FF * 2u, stepQ = 32u * NQKV * 2u, step13 = typeY ? stepQ : stepF;
        const char* XA = (const char*)x + (size_t)rb * 256 * DM * 2;
        const char* XC = (const char*)ws + WS_XC + (size_t)rb * 256 * DM * 2;
        u64 ep; u32x4 xa0, xa1;
        const int srow = rb * 256 + (js * 8 + xcd) * 8 + wid;
        asm volatile("global_load_dwordx4 %1, %5, %6\n\tglobal_load_dwordx4 %2, %5, %6 offset:1024\n\tglobal_load_dwordx2 %0, %3, %4 sc1"
                     : "=&v"(ep), "=&v"(xa0), "=&v"(xa1)
                     : "v"(0u), "s"(flags.p), "v"((unsigned)(lane * 16)), "s"((const char*)x + (size_t)srow * DM * 2) : "memory");
        const int r8 = lane >> 3, u8 = lane & 7;
        const int gw = (r8 & 2) | ((wc & 1) << 2);
        const unsigned voffF = (unsigned)(((wc * 8 + r8) * FF + (u8 ^ gw) * 8) * 2);
        const unsigned voffQ = (unsigned)(((wc * 8 + r8) * NQKV + (u8 ^ gw) * 8) * 2);
        const unsigned voff13 = typeY ? voffQ : voffF;
        const unsigned voffA0 = (unsigned)(((32 * wid + r8) * DM + (u8 ^ (r8 >> 1)) * 8) * 2);
        const unsigned voffA1 = (unsigned)(((32 * wid + 8 + r8) * DM + (u8 ^ (4 | (r8 >> 1))) * 8) * 2);
        const unsigned dstA = lds0 + G1_A + wid * 4096, dstW = lds0 + G1_W + wc * 1024;
#define G1_LOAD_A(t) do { const char* b_ = XA + (t) * 128; const unsigned d_ = dstA + ((t) & 1) * 32768; \
        glds16(b_, voffA0, d_); glds16(b_, voffA1, d_ + 1024); glds16(b_ + 32768, voffA0, d_ + 2048); glds16(b_ + 32768, voffA1, d_ + 3072); } while (0)
#define G1_LOAD_W(hs) do { const unsigned d_ = dstW + ((hs) & 3) * 4096; \
        glds16(P0 + (size_t)(hs) * stepF, voffF, d_); glds16(P1 + (size_t)(hs) * step13, voff13, d_ + 16384); glds16(P2 + (size_t)(hs) * stepF, voffF, d_ + 32768); \
        glds16(P3 + (size_t)(hs) * step13, voff13, d_ + 49152); glds16(P4 + (size_t)(hs) * stepQ, voffQ, d_ + 65536); } while (0)
        STAMP(29);
        if (wr == 1) G1_LOAD_W(0);
        G1_LOAD_A(0);
        STAMP(30);

        int fr = lane & 15, fq = lane >> 4;
        const lds_ptr aB0 = lds + G1_A + (wr * 128 + fr) * 128 + ((fq ^ (fr >> 1)) * 16);
        const lds_ptr aB1 = lds + G1_A + (wr * 128 + fr) * 128 + (((4 | fq) ^ (fr >> 1)) * 16);
        const int tq = (lane >> 2) & 3, tp = lane & 3, tg = (tq & 2) | ((fq & 1) << 2);
        auto wbase = [&](int panel, int bp) { return lds + G1_W + panel * 16384 + (8 * fq + tq) * 128 + (((2 * bp + (tp >> 1)) ^ tg) * 16) + (tp & 1) * 8; };
        const int pX = wc >> 1, bX = 2 * (wc & 1);
        const lds_ptr wB0 = typeY ? wbase(0, wc) : wbase(pX, bX);
        const lds_ptr wB1 = typeY ? wbase(1, wc) : wbase(pX, bX + 1);
        const lds_ptr wB2 = typeY ? wbase(2, wc) : wbase(2 + pX, bX);
        const lds_ptr wB3 = typeY ? wbase(3, wc) : wbase(2 + pX, bX + 1);
        const lds_ptr wB4 = wbase(4, wc);
        if (threadIdx.x == 0) *(LAS unsigned*)(lds + G1_CNT) = 0u;
        f32x4 acc[8][5];
#pragma unroll
        for (int m = 0; m < 8; ++m)
#pragma unroll
            for (int b = 0; b < 5; ++b) { acc[m][b] = (f32x4){0.f, 0.f, 0.f, 0.f}; PIN(acc[m][b]); }
        asm volatile("s_cmp_lg_u32 %3, 0\n\ts_cbranch_scc1 1f\n\ts_waitcnt vmcnt(4)\n1:\n\ts_waitcnt vmcnt(9)" : "+v"(ep), "+v"(xa0), "+v"(xa1) : "s"(wr), "v"(cpd) : "memory", "scc");
        STAMP(31);
        {
            const unsigned elo = __builtin_amdgcn_readfirstlane((unsigned)ep), ehi = __builtin_amdgcn_readfirstlane((unsigned)(ep >> 32));
            flags.tag = (((u64)ehi << 32) | elo) * 0x9E3779B97F4A7C15ull + 0x632BE59BD9B4E019ull;
        }
        const unsigned tag32 = (unsigned)flags.tag;
        {
            const f32x2 st = row_stats(xa0, xa1);
            G1_LOAD_W(1 + wr);
            ST16(xa0, (lane >= 32) ? WS_XC + (unsigned)srow * 2048u + lane * 16 : WS_END);
            ST16(xa1, WS_XC + (unsigned)srow * 2048u + 1024 + lane * 16);
            ST16(((u32x4){f2u(st.x), tag32, f2u(st.y), tag32}), (lane == 0) ? WS_STAT + (unsigned)srow * 16u : WS_END);
            CFENCE();
        }
        STAMP(32);
        bf16x8 af[8], wf[5];
        float cs0 = 0.f, cs1 = 0.f, cs2 = 0.f;
#define CS1(c, w) "v_dot2c_f32_bf16 %" #c ", %25, %" #w "\n\t"
#define G1_CSUM(t, lim) do { const u32x4 w0_ = __builtin_bit_cast(u32x4, wf[0]), w1_ = __builtin_bit_cast(u32x4, wf[1]), w2_ = __builtin_bit_cast(u32x4, wf[2]), \
            w3_ = __builtin_bit_cast(u32x4, wf[3]), w4_ = __builtin_bit_cast(u32x4, wf[4]); \
        asm volatile("s_cmp_gt_u32 %24, %26\n\ts_cbranch_scc1 2f\n\ts_cmp_lg_u32 %23, 0\n\ts_cbranch_scc1 1f\n\t" \
            CS1(0, 3) CS1(1, 7) CS1(2, 19) CS1(0, 4) CS1(1, 8) CS1(2, 20) CS1(0, 5) CS1(1, 9) CS1(0, 6) CS1(1, 10) "s_nop 1\n\ts_branch 2f\n1:\n\t" \
            CS1(0, 11) CS1(1, 15) CS1(2, 21) CS1(0, 12) CS1(1, 16) CS1(2, 22) CS1(0, 13) CS1(1, 17) CS1(0, 14) CS1(1, 18) "s_nop 2\n2:" \
            : "+v"(cs0), "+v"(cs1), "+v"(cs2) \
            : "v"(w0_[0]), "v"(w0_[1]), "v"(w0_[2]), "v"(w0_[3]), "v"(w1_[0]), "v"(w1_[1]), "v"(w1_[2]), "v"(w1_[3]), "v"(w2_[0]), "v"(w2_[1]), "v"(w2_[2]), "v"(w2_[3]), \
              "v"(w3_[0]), "v"(w3_[1]), "v"(w3_[2]), "v"(w3_[3]), "v"(w4_[0]), "v"(w4_[1]), "v"(w4_[2]), "v"(w4_[3]), "s"(wr), "s"(t), "s"(0x3f803f80u), "i"(lim) : "scc"); } while (0)
#define G1_RW(i, B, slot) wf[i] = cat8(lds_tr(B + (slot) * 4096), lds_tr(B + (slot) * 4096 + 512))
#define G1_RA(m, AB, abuf) af[m] = lds_b128(AB + (abuf) * 32768 + (m) * 2048)
#define G1_SEG(AB, abuf, slot, L0, L1, L2, L3, L4, CLOSE) do { \
        L0; G1_RW(0, wB0, slot); G1_RW(1, wB1, slot); L1; G1_RW(2, wB2, slot); G1_RW(3, wB3, slot); L2; G1_RW(4, wB4, slot); G1_RA(0, AB, abuf); G1_RA(1, AB, abuf); \
        L3; G1_RA(2, AB, abuf); G1_RA(3, AB, abuf); G1_RA(4, AB, abuf); L4; G1_RA(5, AB, abuf); G1_RA(6, AB, abuf); G1_RA(7, AB, abuf); \
        SBAR(); G1_CSUM(t, 3); WAIT_L0(); SBAR(); if (CLOSE) { BAR(); SBAR(); } } while (0)
#define G1_SEG_A(abuf, slot, tt) do { const char* b_ = ((tt) < 4 ? XA : XC) + (tt) * 128; const unsigned d_ = dstA + (1 - (abuf)) * 32768; \
        G1_SEG(aB0, abuf, slot, glds16c(b_, voffA0, d_), glds16c(b_, voffA1, d_ + 1024), glds16c(b_ + 32768, voffA0, d_ + 2048), glds16c(b_ + 32768, voffA1, d_ + 3072), (void)0, true); } while (0)
#define G1_SEG_W(abuf, slot, hs, dslot, CLOSE) do { const unsigned d_ = dstW + (dslot) * 4096; \
        G1_SEG(aB1, abuf, slot, glds16(P0 + (size_t)(hs) * stepF, voffF, d_), glds16(P1 + (size_t)(hs) * step13, voff13, d_ + 16384), glds16(P2 + (size_t)(hs) * stepF, voffF, d_ + 32768), \
               glds16(P3 + (size_t)(hs) * step13, voff13, d_ + 49152), glds16(P4 + (size_t)(hs) * stepQ, voffQ, d_ + 65536), CLOSE); } while (0)
#define G1_MMA() do { __builtin_amdgcn_s_setprio(1); \
        _Pragma("unroll") for (int b = 0; b < 5; ++b) _Pragma("unroll") for (int m = 0; m < 8; ++m) MFMA16_ACC(acc[m][b], wf[b], af[m]); \
        __builtin_amdgcn_s_setprio(0); SBAR(); } while (0)

        STAMP(33);
        if (wr == 1) { WAIT_V(12); BAR(); }
        WAIT_V(8); WAIT_L0();
        STAMP(34);
        BAR();
        SBAR();
        STAMP(1);
        u64 sg = 0;
        const u64* sgp = flags.p + (F_XC + rb * 32 + (lane & 31)) * 16;
#pragma unroll 1
        for (int t = 0; t < 16; t += 2) {
            const int h1 = (2 * t + 3 + wr < 31) ? 2 * t + 3 + wr : 31, h2 = (2 * t + 5 + wr < 31) ? 2 * t + 5 + wr : 31, t2 = (t < 14) ? t + 2 : 15;
            const int sel = (t == 12) ? 0 : ((t ^ 2) | wid);
            STAMP(35 + (t >> 1));
            PEEK_AHEAD(sg, sgp, sel);
            G1_SEG_A(0, 0, t + 1); G1_MMA(); asm volatile("s_waitcnt vmcnt(4)" : "+v"(sg) :: "memory"); BAR(); SBAR();
            if (t == 0 && wid == 4) signal(flags, F_XC + bid, lane);
            if (sel == 0) {
                const u64 want = (t == 12) ? ((u64)tag32 << 32) : flags.tag, mask = (t == 12) ? 0xffffffff00000000ull : ~0ull;
#pragma unroll 1
                for (unsigned spin = 0; spin < (1u << 20) && !__all((sg & mask) == want); ++spin)
                    asm volatile("s_sleep 1\n\tglobal_load_dwordx2 %0, %1, off sc1\n\ts_waitcnt vmcnt(0)" : "+v"(sg) : "v"(sgp) : "memory");
                const int tl = opaque_tid() & 63;
                if (t == 12) *(LAS unsigned*)(lds + G1_STAT + (wid * 32 + (tl & 31)) * 8 + (tl >> 5) * 4) = (unsigned)sg;
            }
            G1_SEG_W(0, 1, h1, (3 + wr) & 3, true); G1_MMA(); WAIT_V(5); BAR(); SBAR();
            G1_SEG_A(1, 2, t2); G1_MMA(); WAIT_V(4); BAR(); SBAR();
            G1_SEG_W(1, 3, h2, (1 + wr) & 3, false);
            if (t == 2) {
                const int tl = opaque_tid() & 63;
                const lds_ptr cp = lds + G1_CS + wc * 384 + (tl & 15) * 4;
                *(LAS float*)(cp + wr * 128) = red4(cs0); *(LAS float*)(cp + wr * 128 + 64) = red4(cs1); *(LAS float*)(cp + 256 + wr * 64) = red4(cs2);
                sgp = (const u64*)(ws + WS_STAT) + (rb * 256 + wid * 32 + (tl & 31)) * 2 + (tl >> 5);
                WAIT_L0();
            }
            SBAR(); BAR(); SBAR(); G1_MMA(); WAIT_V(5);
            if (wr == 0 || t < 14) BAR();
            SBAR();
        }
        MFMA_DRAIN(); SBAR();
        STAMP(2);

        const int tz = opaque_tid(), ln = tz & 63;
        fr = tz & 15; fq = (tz >> 4) & 3;
        const int row0 = rb * 256 + wr * 128 + fr;
        f32x4 csv[5]; f32x2 st[8];
#pragma unroll
        for (int b = 0; b < 5; ++b) csv[b] = *(const LAS f32x4*)(lds + G1_CS + wc * 384 + b * 64 + fq * 16);
        csv[4] += *(const LAS f32x4*)(lds + G1_CS + wc * 384 + 320 + fq * 16);
#pragma unroll
        for (int m = 0; m < 8; ++m) st[m] = *(const LAS f32x2*)(lds + G1_STAT + (wr * 128 + m * 16 + fr) * 8);
        auto lnv = [&](int m, int b, float sc) { return (acc[m][b] - st[m].x * csv[b]) * (st[m].y * sc); };
        auto glu = [&](int m, int bg, int bu) {
            const f32x4 g = lnv(m, bg, -LOG2E), u = lnv(m, bu, 1.0f);
            f32x4 h;
#pragma unroll
            for (int j = 0; j < 4; ++j) h[j] = u[j] * __builtin_amdgcn_rcpf(1.0f + __builtin_amdgcn_exp2f(g[j]));
            return h;
        };
        auto pack2 = [&](const f32x4& v0, const f32x4& v1) {
            unsigned p0 = cvtpk(v0[0], v0[1]), p1 = cvtpk(v0[2], v0[3]), q0 = cvtpk(v1[0], v1[1]), q1 = cvtpk(v1[2], v1[3]);
            swap16(p0, q0); swap16(p1, q1);
            return (u32x4){p0, p1, q0, q1};
        };
        const int hd = 4 * xcd + (typeY ? 2 : 0) + (wc >> 1);
        const unsigned qoff = WS_QKV + (unsigned)(hd * 2 + (wc & 1)) * 65536u + (unsigned)(row0 + (fq & 1) * 16) * 32u + (fq >> 1) * 16;
        constexpr unsigned KIND = NH * 2 * 65536u;
        if (!typeY) {
            const float sc4 = (js == 0) ? QSCALE : 1.0f;
#pragma unroll
            for (int m = 0; m < 8; m += 2) ST16(pack2(lnv(m, 4, sc4), lnv(m + 1, 4, sc4)), qoff + js * KIND + m * 512);
            CFENCE();
            hp.base = ws + WS_ACT + (unsigned)(14 * xcd + 4 * js + wc) * GRP + (unsigned)(rb * 256 + wr * 128) * 64u;
            hp.off = fr * 64 + (fq & 1) * 32 + (fq >> 1) * 16; hp.step = 1024;
#pragma unroll
            for (int m = 0; m < 8; ++m) {
                hp.v[m] = pack2(glu(m, 0, 2), glu(m, 1, 3));
                if (m == 5) { PIN(hp.v[5]); WAIT_V(0); arrive(flags, F_QKV + bid, ln, lds + G1_CNT, 7u); }
            }
        } else {
#pragma unroll
            for (int m = 0; m < 8; m += 2) {
                ST16(pack2(lnv(m, 3, 1.0f), lnv(m + 1, 3, 1.0f)), qoff + KIND + m * 512);
                ST16(pack2(lnv(m, 4, 1.0f), lnv(m + 1, 4, 1.0f)), qoff + 2 * KIND + m * 512);
                ST16(pack2(lnv(m, 1, QSCALE), lnv(m + 1, 1, QSCALE)), qoff + m * 512);
            }
            CFENCE();
            hp.base = ws + WS_ACT + (unsigned)(14 * xcd + 12 + (wc >> 1)) * GRP + (unsigned)(rb * 256 + wr * 128) * 64u;
            hp.off = (fr + (fq & 1) * 16) * 64 + (wc & 1) * 32 + (fq >> 1) * 16; hp.step = 2048;
#pragma unroll
            for (int m = 0; m < 8; m += 2) { hp.v[m >> 1] = pack2(glu(m, 0, 2), glu(m + 1, 0, 2)); hp.v[4 + (m >> 1)] = hp.v[m >> 1]; }
            PIN(hp.v[3]); WAIT_V(0); arrive(flags, F_QKV + bid, ln, lds + G1_CNT, 7u);
        }
#pragma unroll
        for (int i = 0; i < 8; ++i) PIN(hp.v[i]);
        STAMP(3);
    }

    const int z = xcd & 1, rbk = ((xcd >> 1) & 1) * 8 + (idx & 7), cbk = (xcd >> 2) * 4 + (idx >> 3);
    G2 g2;
    g2.Ah = (const char*)ws + WS_ACT + (size_t)z * 56 * GRP + rbk * 128 * 64;
    g2.Ao = (const char*)ws + WS_ACT + (size_t)(112 + z * 16 - 56) * GRP + rbk * 128 * 64;
    g2.Wd = (const char*)(wd + (size_t)z * 1792 * DM + cbk * 128);
    g2.Wo = (const char*)(wo + (size_t)z * 512 * DM + cbk * 128) - (size_t)1792 * DM * 2;
    {
        const int hl = idx & 3, qb = idx >> 2, h = 4 * xcd + hl;
        const int l0_ = opaque_tid() & 63;
        const int qkv0 = F_QKV + xcd + 8 * (qb * 4 + ((hl >= 2) ? 3 : l0_ % 3));
        const int qkvword = F_QKV + xcd + 8 * ((l0_ & 7) * 4 + ((hl >= 2) ? 3 : 1 + ((l0_ >> 3) & 1)));
        const int depword = (rbk >> 1) * 32 + ((l0_ >> 2) & 3) * 8 + 4 * z + (l0_ & 3);
        const char* Qg = (const char*)ws + WS_QKV + (size_t)h * 131072 + qb * 256 * 32;
        const char* Kg = (const char*)ws + WS_QKV + (size_t)(NH + h) * 131072;
        const char* Vg = (const char*)ws + WS_QKV + (size_t)(2 * NH + h) * 131072;
        const char* wpd = g2.Wd - (cbk & 3) * 256;
        const char* wpo = g2.Wo - (cbk & 3) * 256;
        AttnOut R;
#define DG
        attn_fast(lds, lds0, Qg, Kg, Vg, g2, flags, qkv0, qkvword, depword, F_H + bid, qb, bid == 0, hp, wpd, wpo, idx * 72, wid, R DG);
        STAMP(6);
        const int tz = opaque_tid(), ln = tz & 63, w4 = wid & 3, fr = ln & 15, fq = ln >> 4;
        const lds_ptr po = lds + 2 * SLOT + w4 * 8192 + ln * 16, pl = lds + 3 * SLOT + 4096 + w4 * 1024 + ln * 16;
        volatile LAS unsigned* const badw = (volatile LAS unsigned*)(lds + 3 * SLOT + 8192 + 4096);
#define AT_PUBLISH() do { if (wid < 4) { \
            _Pragma("unroll") for (int i = 0; i < 4; ++i) { *(LAS f32x4*)(po + i * 1024) = (f32x4){R.o0[4 * i], R.o0[4 * i + 1], R.o0[4 * i + 2], R.o0[4 * i + 3]}; \
                *(LAS f32x4*)(po + 4096 + i * 1024) = (f32x4){R.o1[4 * i], R.o1[4 * i + 1], R.o1[4 * i + 2], R.o1[4 * i + 3]}; } \
            *(LAS f32x4*)pl = (f32x4){R.l0[0], R.m0, R.l1[0], R.m1}; } } while (0)
        {
            AT_PUBLISH();
            const f32x16 z16 = R.o0 * 0.0f + R.o1 * 0.0f;
            float zs = 0.f;
#pragma unroll
            for (int r = 0; r < 16; ++r) zs += z16[r];
            const bool ok = (zs == 0.f) && (R.l0[0] > 0.f) && (R.l0[0] < 0x1p96f) && (R.l1[0] > 0.f) && (R.l1[0] < 0x1p96f);
            const bool wave_bad = !__all(ok);
            if (ln == 0) badw[wid] = wave_bad ? 1u : 0u;
            STAMP(16);
            WAIT_V(0); STAMP(17); WAIT_L0(); BAR(); CFENCE();
            const unsigned anybad = badw[ln & 7];
            if (__any(anybad != 0u)) {
                WAIT_L0(); BAR(); CFENCE();
                attn_safe(lds, lds0, Qg, Kg, Vg, wid, R);
                AT_PUBLISH();
                WAIT_L0(); BAR(); CFENCE();
            }
        }
        STAMP(7);

        const int tile = rbk * 8 + cbk;
        if (wid >= 4) {
            unsigned vA0, vA1, vB0, vB1; g2_voff(ln, vA0, vA1, vB0, vB1);
#define G2_LOAD(j, sl) do { const unsigned d_ = lds0 + (sl) * SLOT; g2_load_b(g2, j, d_, 2 * w4, vB0); g2_load_a(g2, j, d_, 2 * w4, vA0, vA1); \
                g2_load_b(g2, j, d_, 2 * w4 + 1, vB1); g2_load_a(g2, j, d_, 2 * w4 + 1, vA0, vA1); } while (0)
            const f32x4 lm = *(const LAS f32x4*)pl;
            f32x4 t[8];
#pragma unroll
            for (int i = 0; i < 8; ++i) t[i] = *(const LAS f32x4*)(po + i * 1024);
            WAIT_L0(); BAR(); CFENCE();
            G2_LOAD(3, 2);
            u32x4 ow[4];
            const unsigned ooff = WS_ACT + (unsigned)(112 + h) * GRP + (unsigned)(qb * 256 + w4 * 64 + (ln & 31)) * 64u + (ln >> 5) * 16;
#pragma unroll
            for (int q = 0; q < 2; ++q) {
                BAR();
                if (q == 0) G2_LOAD(4, 3); else G2_LOAD(5, 4);
                const f32x16 ok_ = q ? R.o1 : R.o0;
                const float lk = q ? R.l1[0] : R.l0[0], mk = q ? R.m1 : R.m0, lo = lm[2 * q], mo = lm[2 * q + 1];
                const float mm = __builtin_fmaxf(mk, mo), fk = __builtin_amdgcn_exp2f(mk - mm), fo = __builtin_amdgcn_exp2f(mo - mm);
                const float rl = 1.0f / (lk * fk + lo * fo), ck = fk * rl, co = fo * rl;
                unsigned p[8];
#pragma unroll
                for (int i = 0; i < 4; ++i) {
                    const f32x4 u = t[4 * q + i];
                    p[2 * i] = cvtpk(ok_[4 * i] * ck + u[0] * co, ok_[4 * i + 1] * ck + u[1] * co);
                    p[2 * i + 1] = cvtpk(ok_[4 * i + 2] * ck + u[2] * co, ok_[4 * i + 3] * ck + u[3] * co);
                }
                swap32(p[0], p[2]); swap32(p[1], p[3]); swap32(p[4], p[6]); swap32(p[5], p[7]);
                ow[2 * q] = (u32x4){p[0], p[1], p[2], p[3]}; ow[2 * q + 1] = (u32x4){p[4], p[5], p[6], p[7]};
                PIN(ow[2 * q]); PIN(ow[2 * q + 1]);
            }
#pragma unroll
            for (int i = 0; i < 4; ++i) ST16(ow[i], ooff + (i >> 1) * 2048 + (i & 1) * 32);
            CFENCE();
            unsigned t0 = 0, t1 = 0, t2 = 0, t3 = 0, a0 = 0, a1 = 0, a2 = 0;
            WAIT_V(20); BAR(); G2_LOAD(6, 0);
            WAIT_V(20); BAR(); G2_LOAD(7, 1);
            WAIT_V(20);
            int sl = 2;
#pragma unroll 1
            for (int j = 5; j < 33; ++j) {
                TS(t1);
                BAR();
                TS(t2);
                if (j == 6 && wid == 4) signal(flags, F_O + bid, ln);
                G2_LOAD(j + 3, sl);
                sl = (sl == 4) ? 0 : sl + 1;
                TS(t3);
                WAIT_V(16);
                TS(t0);
                a0 += t0 - t3; a1 += t2 - t1; a2 += t3 - t2;
            }
            PUT(50, a0); PUT(51, a1); PUT(52, a2);
            BAR();
            if (tz == 256) *(LAS unsigned*)lds = 0u;
            WAIT_V(8); WAIT_L0(); BAR(); WAIT_V(0); BAR();
                if (wid == 4) wait_all(flags, F_X + tile * 2 + z, peek(flags, F_X + tile * 2 + z));
            CFENCE(); BAR();
            asm volatile(".Lk_end:");
        } else {
            const int wm = w4 >> 1, wn = w4 & 1;
            const unsigned aO0 = (wm * 64 + fr) * 128 + ((fq ^ (fr >> 1)) * 16), aO1 = aO0 ^ 64;
            const int tq = (ln >> 2) & 3, tp = ln & 3, tg = (tq & 2) | ((fq & 1) << 2);
            unsigned wO[4];
#pragma unroll
            for (int b = 0; b < 4; ++b) wO[b] = 16384 + wn * 8192 + (8 * fq + tq) * 128 + (((2 * b + (tp >> 1)) ^ tg) * 16) + (tp & 1) * 8;
            f32x4 acc[4][4];
#pragma unroll
            for (int m = 0; m < 4; ++m)
#pragma unroll
                for (int b = 0; b < 4; ++b) acc[m][b] = (f32x4){0.f, 0.f, 0.f, 0.f};
            bf16x8 xa[4], ya[4] = {}; s16x4 xl[4], xh[4], yl[4] = {}, yh[4] = {};
            lds_ptr pa0, pa1, pw0, pw1, pw2, pw3;
#define G2_BASES(so) do { pa0 = lds + aO0 + (so); pa1 = lds + aO1 + (so); pw0 = lds + wO[0] + (so); pw1 = lds + wO[1] + (so); pw2 = lds + wO[2] + (so); pw3 = lds + wO[3] + (so); } while (0)
#define G2_M(C, m, b) do { MFMA16_ACC(acc[m][b], cat8(C##l[b], C##h[b]), C##a[m]); SBAR(); } while (0)
#define RDQ(x) (x)
#define G2_HALF(C, N, AB, WO, RD_, TAIL) do { const bool RD = RDQ(RD_); \
            G2_M(C, 0, 0); if (RD) { N##l[0] = lds_tr(pw0 + (WO)); N##h[0] = lds_tr(pw0 + (WO) + 512); } SBAR(); \
            G2_M(C, 1, 0); if (RD) N##a[0] = lds_b128(AB); SBAR(); \
            G2_M(C, 2, 0); if (RD) N##a[1] = lds_b128((AB) + 2048); SBAR(); \
            G2_M(C, 3, 0); if (RD) N##a[2] = lds_b128((AB) + 4096); SBAR(); \
            G2_M(C, 0, 1); if (RD) N##a[3] = lds_b128((AB) + 6144); SBAR(); \
            G2_M(C, 1, 1); if (RD) { N##l[1] = lds_tr(pw1 + (WO)); N##h[1] = lds_tr(pw1 + (WO) + 512); } SBAR(); \
            G2_M(C, 2, 1); if (RD) { N##l[2] = lds_tr(pw2 + (WO)); N##h[2] = lds_tr(pw2 + (WO) + 512); } SBAR(); \
            G2_M(C, 3, 1); if (RD) { N##l[3] = lds_tr(pw3 + (WO)); N##h[3] = lds_tr(pw3 + (WO) + 512); } SBAR(); \
            G2_M(C, 0, 2); TAIL(0); SBAR(); G2_M(C, 1, 2); TAIL(1); SBAR(); G2_M(C, 2, 2); TAIL(2); SBAR(); G2_M(C, 3, 2); TAIL(3); SBAR(); \
            G2_M(C, 0, 3); G2_M(C, 1, 3); G2_M(C, 2, 3); G2_M(C, 3, 3); } while (0)
            int so = 4 * SLOT;
            G2_BASES(so);
            xl[0] = lds_tr(pw0); xh[0] = lds_tr(pw0 + 512); SBAR();
#pragma unroll
            for (int m = 0; m < 4; ++m) { xa[m] = lds_b128(pa0 + m * 2048); SBAR(); }
            xl[1] = lds_tr(pw1); xh[1] = lds_tr(pw1 + 512); xl[2] = lds_tr(pw2); xh[2] = lds_tr(pw2 + 512); xl[3] = lds_tr(pw3); xh[3] = lds_tr(pw3 + 512);
            const u64* const optr = flags.p + (F_O + depword) * 16;
            u64 ov = 0; unsigned t0 = 0, t1 = 0, a0 = 0;
            STAMP(8);
#define G2_NEXT(i) do { if ((i) == 0) so = (so == 4 * SLOT) ? 0 : so + SLOT; \
                if ((i) == 1) { pa0 = lds + aO0 + so; pa1 = lds + aO1 + so; PIN(pa0); PIN(pa1); } \
                if ((i) == 2) { pw0 = lds + wO[0] + so; pw1 = lds + wO[1] + so; PIN(pw0); PIN(pw1); } \
                if ((i) == 3) { pw2 = lds + wO[2] + so; pw3 = lds + wO[3] + so; PIN(pw2); PIN(pw3); } } while (0)
#define G2_NONE(i) do {} while (0)
            WAIT_L0(); BAR(); SBAR();
#pragma unroll 1
            for (int ph = 0; ph < 3; ++ph) {
                const int n = (ph == 0) ? 11 : (ph == 1) ? 1 : 5;
#pragma unroll 1
                for (int i = 0; i < n; ++i) {
                    WAIT_L0(); SBAR(); G2_HALF(x, y, pa1, 4096, true, G2_NEXT);
                    WAIT_L0(); BAR(); SBAR(); G2_HALF(y, x, pa0, 0, true, G2_NONE);
                    WAIT_L0(); SBAR(); G2_HALF(x, y, pa1, 4096, true, G2_NEXT);
                    WAIT_L0(); TS(t0); BAR(); TS(t1); SBAR(); a0 += t1 - t0;
                    G2_HALF(y, x, pa0, 0, true, G2_NONE);
                }
                PEEK_AHEAD(ov, optr, wid | ph);
                if ((wid | (ph ^ 1)) == 0) { PEEK_DONE(ov); wait_all(flags, F_O + depword, ov); }
            }
            const int row = rbk * 128 + wm * 64 + z * 32 + fr, col = cbk * 128 + wn * 64 + (fq & 1) * 16 + (fq >> 1) * 8;
            u32x4 xr[4];
#pragma unroll
            for (int i = 0; i < 4; ++i) xr[i] = *(const u32x4*)(x + (size_t)(row + (i >> 1) * 16) * DM + col + (i & 1) * 32);
            WAIT_L0(); SBAR(); G2_HALF(x, y, pa1, 4096, true, G2_NEXT);
            WAIT_L0(); BAR(); SBAR(); G2_HALF(y, x, pa0, 0, true, G2_NONE);
            WAIT_L0(); SBAR(); G2_HALF(x, y, pa1, 4096, true, G2_NONE);
            WAIT_L0(); SBAR(); G2_HALF(y, x, pa0, 0, false, G2_NONE);
            MFMA_DRAIN(); SBAR();
            STAMP(9); PUT(53, a0);
            f32x4 vk[8];
            const unsigned xo = WS_XCH + (unsigned)(tile * 2 + 1 - z) * 32768u + w4 * 8192 + ln * 16;
            if (z == 0) {
#pragma unroll
                for (int i = 0; i < 8; ++i) { ST16(__builtin_bit_cast(u32x4, acc[2 + (i >> 2)][i & 3]), xo + i * 1024); vk[i] = acc[i >> 2][i & 3]; }
            } else {
#pragma unroll
                for (int i = 0; i < 8; ++i) { ST16(__builtin_bit_cast(u32x4, acc[i >> 2][i & 3]), xo + i * 1024); vk[i] = acc[2 + (i >> 2)][i & 3]; }
            }
            WAIT_V(0);
            arrive(flags, F_X + tile * 2 + 1 - z, ln, lds, 3u);
            STAMP(10);
            CFENCE(); BAR(); CFENCE();
            STAMP(11);
            const unsigned xi = WS_XCH + (unsigned)(tile * 2 + z) * 32768u + w4 * 8192 + ln * 16;
#pragma unroll
            for (int i = 0; i < 8; ++i) vk[i] += __builtin_bit_cast(f32x4, __builtin_amdgcn_raw_buffer_load_b128(wsr, xi + i * 1024, 0, 16));
#pragma unroll
            for (int i = 0; i < 4; ++i) {
                const u32x4 a = __builtin_bit_cast(u32x4, vk[2 * i]), b = __builtin_bit_cast(u32x4, vk[2 * i + 1]);
                unsigned a0_ = a[0], a1 = a[1], a2 = a[2], a3 = a[3], b0 = b[0], b1 = b[1], b2 = b[2], b3 = b[3];
                swap16(a0_, b0); swap16(a1, b1); swap16(a2, b2); swap16(a3, b3);
                const u32x4 xx = xr[i];
                u32x4 w;
                w[0] = cvtpk(u2f(a0_) + bf_lo(xx[0]), u2f(a1) + bf_hi(xx[0]));
                w[1] = cvtpk(u2f(a2) + bf_lo(xx[1]), u2f(a3) + bf_hi(xx[1]));
                w[2] = cvtpk(u2f(b0) + bf_lo(xx[2]), u2f(b1) + bf_hi(xx[2]));
                w[3] = cvtpk(u2f(b2) + bf_lo(xx[3]), u2f(b3) + bf_hi(xx[3]));
                *(u32x4*)(out + (size_t)(row + (i >> 1) * 16) * DM + col + (i & 1) * 32) = w;
            }
        }
    }
}

extern "C" void kernel_launch(void* const* d_in, const int* in_sizes, int n_in, void* d_out, int out_size, void* d_ws, size_t ws_size, hipStream_t stream) {
    const bf16_t* x = (const bf16_t*)d_in[0];
    k_main<<<256, 512, 0, stream>>>(x, (const bf16_t*)d_in[1], (const bf16_t*)d_in[2], (const bf16_t*)d_in[4], (const bf16_t*)d_in[5], (const bf16_t*)d_in[6],
                                    (bf16_t*)d_out, (unsigned char*)d_ws);
}
```

```cpp
#include <hip/hip_runtime.h>
#include <stdint.h>
#define TLBW 32768u

typedef unsigned short bf16_t;
typedef short bf16x8 __attribute__((ext_vector_type(8)));
typedef short s16x4 __attribute__((ext_vector_type(4)));
typedef float f32x2 __attribute__((ext_vector_type(2)));
typedef float f32x4 __attribute__((ext_vector_type(4)));
typedef float f32x16 __attribute__((ext_vector_type(16)));
typedef unsigned u32x4 __attribute__((ext_vector_type(4)));
typedef __bf16 bf16x2_t __attribute__((ext_vector_type(2)));
#define LAS __attribute__((address_space(3)))
#define GAS __attribute__((address_space(1)))
typedef LAS unsigned char* lds_ptr;

constexpr int SEQ = 2048, DM = 1024, FF = 3584, NQKV = 3072, NH = 32;
constexpr float LN_EPS = 1e-5f;
constexpr float LOG2E = 1.4426950408889634f;
constexpr float QSCALE = 0.17677669529663687f * LOG2E;

constexpr unsigned WS_FLAGS = 0;
constexpr unsigned WS_STAT = 192 * 1024;
constexpr unsigned WS_QKV = 256 * 1024;
constexpr unsigned WS_ACT = WS_QKV + 3u * SEQ * DM * 2;
constexpr unsigned WS_XCH = WS_ACT + 144u * SEQ * 64;
constexpr unsigned WS_XC = WS_XCH + 256u * 32768;
constexpr unsigned WS_END = WS_XC + SEQ * DM * 2;
constexpr int F_EPOCH = 0, F_QKV = 1, F_H = 257, F_O = 513, F_X = 769, F_XC = 1025;
constexpr unsigned GRP = SEQ * 64;

#define SBAR() __builtin_amdgcn_sched_barrier(0)
#define BAR() __builtin_amdgcn_s_barrier()
#define WAIT_V(n) asm volatile("s_waitcnt vmcnt(" #n ")" ::: "memory")
#define WAIT_L0() do { __builtin_amdgcn_s_waitcnt(0xC07F); asm volatile("" ::: "memory"); } while (0)
#define CFENCE() asm volatile("" ::: "memory")
#define PIN(x) asm volatile("" : "+v"(x))
#define MFMA16(a, b, c) __builtin_amdgcn_mfma_f32_16x16x32_bf16(a, b, c, 0, 0, 0)
#define MFMA32(a, b, c) __builtin_amdgcn_mfma_f32_32x32x16_bf16(a, b, c, 0, 0, 0)
#define MFMA16_ACC(acc, a, b) asm volatile("v_mfma_f32_16x16x32_bf16 %0, %1, %2, %0" : "+v"(acc) : "v"(a), "v"(b))
#define MFMA_DRAIN() asm volatile("s_nop 15" ::: "memory")
#define RLX_AGENT __ATOMIC_RELAXED, __HIP_MEMORY_SCOPE_AGENT

#define STAMP(i) do {} while (0)
#define TS(v) do {} while (0)
#define PUT(i, v) do {} while (0)

__device__ __forceinline__ void glds16(const void* sbase, unsigned voff, unsigned ldsdst) {
    asm volatile("s_mov_b32 m0, %2\n\ts_nop 0\n\tglobal_load_lds_dwordx4 %0, %1" ::"v"(voff), "s"(sbase), "s"(ldsdst) : "memory");
}
__device__ __forceinline__ void glds16c(const void* sbase, unsigned voff, unsigned ldsdst) {
    asm volatile("s_mov_b32 m0, %2\n\ts_nop 0\n\tglobal_load_lds_dwordx4 %0, %1 sc1" ::"v"(voff), "s"(sbase), "s"(ldsdst) : "memory");
}
__device__ __forceinline__ bf16x8 lds_b128(lds_ptr p) { return *(const LAS bf16x8*)p; }
__device__ __forceinline__ s16x4 lds_tr(lds_ptr p) { return __builtin_amdgcn_ds_read_tr16_b64_v4i16((LAS s16x4*)p); }
__device__ __forceinline__ bf16x8 cat8(s16x4 a, s16x4 b) { return __builtin_shufflevector(a, b, 0, 1, 2, 3, 4, 5, 6, 7); }
__device__ __forceinline__ unsigned cvtpk(float lo, float hi) {
    f32x2 v = {lo, hi};
    return __builtin_bit_cast(unsigned, __builtin_convertvector(v, bf16x2_t));
}
__device__ __forceinline__ float u2f(unsigned u) { return __builtin_bit_cast(float, u); }
__device__ __forceinline__ unsigned f2u(float f) { return __builtin_bit_cast(unsigned, f); }
__device__ __forceinline__ float bf_lo(unsigned u) { return u2f(u << 16); }
__device__ __forceinline__ float bf_hi(unsigned u) { return u2f(u & 0xffff0000u); }
__device__ __forceinline__ void swap16(unsigned& a, unsigned& b) {
    auto r = __builtin_amdgcn_permlane16_swap(a, b, false, false);
    a = r[0]; b = r[1];
}
__device__ __forceinline__ void swap32(unsigned& a, unsigned& b) {
    auto r = __builtin_amdgcn_permlane32_swap(a, b, false, false);
    a = r[0]; b = r[1];
}
__device__ __forceinline__ int opaque_tid() { int t = threadIdx.x; asm volatile("" : "+v"(t)); return t; }
typedef unsigned long long u64;
struct Flags { u64* p; u64 tag; };
__device__ __forceinline__ void signal(const Flags& f, int word, int lane) {
    if (lane == 0) __hip_atomic_store((GAS u64*)(f.p + word * 16), f.tag, RLX_AGENT);
}
__device__ __forceinline__ u64 peek(const Flags& f, int word) { return __hip_atomic_load((GAS u64*)(f.p + word * 16), RLX_AGENT); }
__device__ __forceinline__ void wait_all(const Flags& f, int word, u64 v) {
    unsigned spin = 0;
    while (!__all(v == f.tag)) { if (++spin > (1u << 20)) break; __builtin_amdgcn_s_sleep(1); v = peek(f, word); }
}
__device__ __forceinline__ void arrive(const Flags& f, int word, int lane, lds_ptr cnt, unsigned last) {
    if (lane == 0 && __hip_atomic_fetch_add((LAS unsigned*)cnt, 1u, __ATOMIC_RELAXED, __HIP_MEMORY_SCOPE_WORKGROUP) == last)
        __hip_atomic_store((GAS u64*)(f.p + word * 16), f.tag, RLX_AGENT);
}
#define PEEK_AHEAD(v, ptr, sel) asm volatile("s_cmp_lg_u32 %2, 0\n\ts_cbranch_scc1 1f\n\tglobal_load_dwordx2 %0, %1, off sc1\n1:" : "+v"(v) : "v"(ptr), "s"(sel) : "memory", "scc")
#define PEEK_DONE(v) asm volatile("s_waitcnt vmcnt(0)" : "+v"(v) :: "memory")

template <int CTRL> __device__ __forceinline__ float dpp_add(float v) { return v + u2f(__builtin_amdgcn_update_dpp(0u, f2u(v), CTRL, 0xf, 0xf, true)); }
__device__ __forceinline__ float red4(float v) {
    unsigned a = f2u(v), b = a;
    swap16(a, b);
    v = u2f(a) + u2f(b);
    a = f2u(v); b = a;
    swap32(a, b);
    return u2f(a) + u2f(b);
}
__device__ __forceinline__ float wave_sum(float v) {
    v = dpp_add<0xB1>(v); v = dpp_add<0x4E>(v); v = dpp_add<0x141>(v); v = dpp_add<0x140>(v);
    return red4(v);
}
__device__ __forceinline__ f32x2 row_stats(u32x4& a, u32x4& b) {
    float v[16];
#pragma unroll
    for (int i = 0; i < 4; ++i) { v[2 * i] = bf_lo(a[i]); v[2 * i + 1] = bf_hi(a[i]); v[8 + 2 * i] = bf_lo(b[i]); v[8 + 2 * i + 1] = bf_hi(b[i]); }
    float s = 0.f;
#pragma unroll
    for (int i = 0; i < 16; ++i) s += v[i];
    const float mu = wave_sum(s) * (1.0f / DM);
    float q = 0.f;
#pragma unroll
    for (int i = 0; i < 16; ++i) { v[i] -= mu; q += v[i] * v[i]; }
#pragma unroll
    for (int i = 0; i < 4; ++i) { a[i] = cvtpk(v[2 * i], v[2 * i + 1]); b[i] = cvtpk(v[8 + 2 * i], v[8 + 2 * i + 1]); }
    return (f32x2){mu, 1.0f / __builtin_sqrtf(wave_sum(q) * (1.0f / DM) + LN_EPS)};
}

constexpr int SLOT = 32768, LDS_BYTES = 5 * SLOT;
constexpr int G1_A = 0, G1_W = 65536, G1_STAT = G1_W + 5 * 16384, G1_CS = G1_STAT + 2048, G1_CNT = G1_CS + 4 * 6 * 64;

struct G2 { const char* Ah; const char* Ao; const char* Wd; const char* Wo; };
__device__ __forceinline__ void g2_load_b(const G2& g, int j, unsigned dst, int vw, unsigned voffB) {
    const char* b = (j >= 28 ? g.Wo : g.Wd) + (size_t)(j * 64 + vw * 8) * (DM * 2);
    glds16(b, voffB, dst + 16384 + vw * 1024); glds16(b + 128, voffB, dst + 16384 + 8192 + vw * 1024);
}
__device__ __forceinline__ void g2_load_a(const G2& g, int j, unsigned dst, int vw, unsigned voffA0, unsigned voffA1) {
    const char* a = (j >= 28 ? g.Ao : g.Ah) + (size_t)j * (2 * GRP) + vw * 1024;
    glds16c(a, voffA0, dst + vw * 2048); glds16c(a, voffA1, dst + vw * 2048 + 1024);
}
__device__ __forceinline__ void g2_voff(int lane, unsigned& voffA0, unsigned& voffA1, unsigned& voffB0, unsigned& voffB1) {
    const int r8 = lane >> 3, u8 = lane & 7, ua = u8 ^ (r8 >> 1), ub = ua ^ 4;
    voffA0 = (unsigned)(ua >> 2) * GRP + r8 * 64 + (ua & 3) * 16;
    voffA1 = (unsigned)(ub >> 2) * GRP + (r8 + 8) * 64 + (ub & 3) * 16;
    voffB0 = (unsigned)(r8 * (DM * 2) + (u8 ^ (r8 & 2)) * 16);
    voffB1 = (unsigned)(r8 * (DM * 2) + (u8 ^ (4 | (r8 & 2))) * 16);
}

struct AttnOut { f32x16 o0, o1; f32x4 l0, l1; float m0, m1; };
struct HPend { u32x4 v[8]; unsigned off, step; const void* base; };
struct AttnLane {
    unsigned voffK, voffV; lds_ptr kb0, kb1, vb; const char* qp; bf16x8 ones;
    __device__ __forceinline__ void init(lds_ptr lds, const char* Qg, int wid, int lane) {
        const int r32 = lane & 31, hi = lane >> 5, kh = wid >> 2, qg = wid & 3, ksw = (r32 >> 2) & 3;
        const int uk = (lane & 3) ^ ((lane >> 4) & 3), uv = lane & 3;
        voffK = (unsigned)((uk >> 1) * 65536 + wid * 1024 + (lane >> 2) * 32 + (uk & 1) * 16);
        voffV = (unsigned)((uv >> 1) * 65536 + wid * 1024 + (lane >> 2) * 32 + (uv & 1) * 16);
        kb0 = lds + kh * 8192 + r32 * 64 + ((hi ^ ksw) * 16); kb1 = lds + kh * 8192 + r32 * 64 + (((2 | hi) ^ ksw) * 16);
        vb = lds + 16384 + kh * 8192 + (4 * hi + ((lane >> 2) & 3)) * 64 + ((lane >> 4) & 1) * 32 + (lane & 3) * 8;
        qp = Qg + (qg * 64 + r32) * 32 + hi * 16;
        const short v = (((lane >> 4) & 1) == ((lane >> 2) & 1)) ? (short)0x3f80 : (short)0;
        ones = (bf16x8){v, v, v, v, v, v, v, v};
    }
};
#define EX(v) __builtin_amdgcn_exp2f(v)
#define AT_LOAD_K(c, slot) do { const unsigned d_ = lds0 + wid * 2048 + (slot) * SLOT; const char* k_ = Kg + (c) * 8192; glds16c(k_, A.voffK, d_); glds16c(k_ + 512, A.voffK, d_ + 1024); } while (0)
#define AT_LOAD_V(c, slot) do { const unsigned d_ = lds0 + wid * 2048 + (slot) * SLOT + 16384; const char* v_ = Vg + (c) * 8192; glds16c(v_, A.voffV, d_); glds16c(v_ + 512, A.voffV, d_ + 1024); } while (0)
#define AT_PACK(S, i) __builtin_bit_cast(bf16x8, (u32x4){cvtpk(S[i], S[i + 1]), cvtpk(S[i + 2], S[i + 3]), cvtpk(S[i + 4], S[i + 5]), cvtpk(S[i + 6], S[i + 7])})
#define AT_Q_LOAD() asm volatile("global_load_dwordx4 %0, %4, off sc1\n\tglobal_load_dwordx4 %1, %4, off offset:1024 sc1\n\tglobal_load_dwordx4 %2, %5, off sc1\n\tglobal_load_dwordx4 %3, %5, off offset:1024 sc1" \
        : "=&v"(q00), "=&v"(q10), "=&v"(q01), "=&v"(q11) : "v"(A.qp), "v"(qp2) : "memory")
#define AT_Q_WAIT(n) asm volatile("s_waitcnt vmcnt(" #n ")" : "+v"(q00), "+v"(q01), "+v"(q10), "+v"(q11) :: "memory")
__device__ __forceinline__ float rowmax16(const f32x16& s) {
    float a = s[0];
#pragma unroll
    for (int r = 1; r < 16; ++r) a = __builtin_fmaxf(a, s[r]);
    unsigned x = f2u(a), y = x; swap32(x, y);
    return __builtin_fmaxf(u2f(x), u2f(y));
}

__device__ __forceinline__ void attn_safe(lds_ptr lds, unsigned lds0, const char* Qg, const char* Kg, const char* Vg, int wid, AttnOut& R) {
    AttnLane A; A.init(lds, Qg, wid, opaque_tid() & 63);
    bf16x8 q00, q01, q10, q11; const char* qp2 = A.qp + 65536;
    AT_Q_LOAD(); AT_Q_WAIT(0);
    f32x16 o0 = (f32x16){}, o1 = (f32x16){};
    f32x4 l0 = (f32x4){0.f, 0.f, 0.f, 0.f}, l1 = l0;
    float m0 = -__builtin_inff(), m1 = m0;
#pragma unroll 1
    for (int c = 0; c < 8; ++c) {
        AT_LOAD_K(c, 3); AT_LOAD_V(c, 3);
        WAIT_V(0); BAR(); CFENCE();
#pragma unroll 1
        for (int t = 0; t < 4; ++t) {
            const int to = 3 * SLOT + t * 2048;
            const bf16x8 ka = lds_b128(A.kb0 + to), kb = lds_b128(A.kb1 + to);
            const bf16x8 va = cat8(lds_tr(A.vb + to), lds_tr(A.vb + to + 512)), vc = cat8(lds_tr(A.vb + to + 1024), lds_tr(A.vb + to + 1536));
            f32x16 s0 = MFMA32(kb, q01, MFMA32(ka, q00, (f32x16){})), s1 = MFMA32(kb, q11, MFMA32(ka, q10, (f32x16){}));
            const float n0 = __builtin_fmaxf(m0, rowmax16(s0)), n1 = __builtin_fmaxf(m1, rowmax16(s1));
            const float f0 = EX(m0 - n0), f1 = EX(m1 - n1);
            m0 = n0; m1 = n1; o0 *= f0; o1 *= f1; l0 *= f0; l1 *= f1;
#pragma unroll
            for (int r = 0; r < 16; ++r) { s0[r] = EX(s0[r] - n0); s1[r] = EX(s1[r] - n1); }
            const bf16x8 p00 = AT_PACK(s0, 0), p01 = AT_PACK(s0, 8), p10 = AT_PACK(s1, 0), p11 = AT_PACK(s1, 8);
            o0 = MFMA32(vc, p01, MFMA32(va, p00, o0)); l0 = MFMA16(A.ones, p01, MFMA16(A.ones, p00, l0));
            o1 = MFMA32(vc, p11, MFMA32(va, p10, o1)); l1 = MFMA16(A.ones, p11, MFMA16(A.ones, p10, l1));
        }
        WAIT_L0(); BAR(); CFENCE();
    }
    R.o0 = o0; R.o1 = o1; R.l0 = l0; R.l1 = l1; R.m0 = m0; R.m1 = m1;
}

__device__ __forceinline__ void attn_fast(lds_ptr lds, unsigned lds0, const char* Qg, const char* Kg, const char* Vg, const G2& g2, const Flags& flags, int qkv0, int qkvword, int depword,
                                          int hsig, int c0, bool bump, const HPend& hp, const char* wpd, const char* wpo, int prow, int wid, AttnOut& R
                                          ) {
    const int lane = opaque_tid() & 63;
    AttnLane A; A.init(lds, Qg, wid, lane);
    const lds_ptr kb0 = A.kb0, kb1 = A.kb1, vb = A.vb;
    const bf16x8 ones = A.ones;
    const __amdgpu_buffer_rsrc_t hr = __builtin_amdgcn_make_buffer_rsrc((void*)hp.base, 0, 8192, 0x00020000);
    f32x16 o0 = (f32x16){}, o1 = (f32x16){}, negm0 = (f32x16){}, negm1 = (f32x16){}, sA0, sA1, sB0, sB1;
    f32x4 l0 = (f32x4){0.f, 0.f, 0.f, 0.f}, l1 = l0;
    bf16x8 kfa, kfb, va, vc = (bf16x8){}, pf = (bf16x8){};
    PIN(o0); PIN(o1); PIN(l0); PIN(l1); PIN(vc); PIN(pf); PIN(A.voffK); PIN(A.voffV); PIN(A.qp);
    unsigned gA0, gA1, gB0, gB1; g2_voff(lane, gA0, gA1, gB0, gB1);
    unsigned gB = (wid & 1) ? gB1 : gB0;
    unsigned pfo = (unsigned)(((lane >> 3) & 1) * (DM * 2) + (lane & 7) * 128);
    const u64* hptr = flags.p + (F_H + depword) * 16;
    const u64* qptr = flags.p + qkvword * 16;
    const char* qp2 = A.qp + 65536;
    PIN(gA0); PIN(gA1); PIN(gB); PIN(pfo); PIN(hptr); PIN(qptr); PIN(qp2);
    if (wid == 0) {
        wait_all(flags, qkv0, peek(flags, qkv0));
        STAMP(13);
    }
    CFENCE(); BAR(); CFENCE();
    STAMP(4);
    bf16x8 q00, q01, q10, q11;
    AT_LOAD_K(c0, 0); AT_Q_LOAD(); AT_LOAD_V(c0, 0);
    u64 hv = 0;
    float m0, m1;
    AT_Q_WAIT(2);
    PEEK_AHEAD(hv, qptr, wid);
    BAR(); SBAR();
    STAMP(15);
    {
        kfa = lds_b128(kb0); kfb = lds_b128(kb1);
        sA0 = MFMA32(kfa, q00, negm0); sA1 = MFMA32(kfa, q10, negm1); sA0 = MFMA32(kfb, q01, sA0); sA1 = MFMA32(kfb, q11, sA1);
        kfa = lds_b128(kb0 + 2048); kfb = lds_b128(kb1 + 2048);
        m0 = rowmax16(sA0); m1 = rowmax16(sA1);
#pragma unroll
        for (int r = 0; r < 16; ++r) { sA0[r] -= m0; sA1[r] -= m1; negm0[r] = -m0; negm1[r] = -m1; }
        PIN(negm0); PIN(negm1);
    }
    u32x4 hA = hp.v[0], hB = hp.v[1], hC = hp.v[2], hD = hp.v[3];
    unsigned hoff = hp.off, pfd = 0;
#define AT_HST(v) do { __builtin_amdgcn_raw_buffer_store_b128(v, hr, hoff, 0, 16); hoff += hp.step; CFENCE(); } while (0)
    PEEK_DONE(hv);
    if (wid == 0) {
        wait_all(flags, qkvword, hv);
        if (bump) signal(flags, F_EPOCH, lane);
        STAMP(14);
    }
    CFENCE(); BAR(); SBAR();
    STAMP(5);
    AT_LOAD_K((c0 + 1) & 7, 1); AT_LOAD_V((c0 + 1) & 7, 1);
#define HS 1
    hoff += 8192;
    STAMP(19);
#define AT_GROUP(QK, RD, S, i, O, L, VF) do { \
        QK; SBAR(); RD; SBAR(); \
        S[i] = EX(S[i]); S[i + 1] = EX(S[i + 1]); S[i + 2] = EX(S[i + 2]); PIN(S); SBAR(); \
        O = MFMA32(VF, pf, O); SBAR(); \
        S[i + 3] = EX(S[i + 3]); S[i + 4] = EX(S[i + 4]); S[i + 5] = EX(S[i + 5]); PIN(S); SBAR(); \
        L = MFMA16(ones, pf, L); SBAR(); \
        S[i + 6] = EX(S[i + 6]); S[i + 7] = EX(S[i + 7]); \
        pf = AT_PACK(S, i); PIN(pf); SBAR(); } while (0)
#define AT_STEP(C0, C1, N0, N1, KO, VO) do { \
        AT_GROUP(N0 = MFMA32(kfa, q00, negm0), va = cat8(lds_tr(vb + (VO)), lds_tr(vb + (VO) + 512)), C0, 0, o1, l1, vc); \
        AT_GROUP(N1 = MFMA32(kfa, q10, negm1), { kfa = lds_b128(kb0 + (KO)); vc = cat8(lds_tr(vb + (VO) + 1024), lds_tr(vb + (VO) + 1536)); }, C1, 0, o0, l0, va); \
        AT_GROUP(N0 = MFMA32(kfb, q01, N0), (void)0, C0, 8, o1, l1, va); \
        AT_GROUP(N1 = MFMA32(kfb, q11, N1), kfb = lds_b128(kb1 + (KO)), C1, 8, o0, l0, vc); } while (0)
#pragma unroll 1
    for (int c = 0; c < 5; ++c) {
        const int so = (c & 3) * SLOT, sn = ((c + 1) & 3) * SLOT;
        if (c == HS) hoff = hp.off;
        AT_HST(hA);
        AT_STEP(sA0, sA1, sB0, sB1, so + 4096, so);
        AT_HST(hB);
        AT_STEP(sB0, sB1, sA0, sA1, so + 6144, so + 2048);
        if (c == 0) WAIT_V(2); else WAIT_V(5);
        WAIT_L0(); BAR(); SBAR();
        STAMP(20 + c);
        if (c == HS + 3 && wid == 0) signal(flags, hsig, lane);
        AT_LOAD_K((c0 + c + 2) & 7, (c + 2) & 3); AT_LOAD_V((c0 + c + 2) & 7, (c + 2) & 3); AT_HST(hC);
        {
            const int pi = (wid + 8 * c < 36) ? wid + 8 * c : 35, lr = prow + 2 * pi;
            const char* pp = (lr < 1792 ? wpd : wpo) + (size_t)lr * (DM * 2);
            asm volatile("global_load_dword %0, %1, %2" : "+v"(pfd) : "v"(pfo), "s"(pp) : "memory");
        }
        AT_STEP(sA0, sA1, sB0, sB1, sn, so + 4096);
        AT_HST(hD);
        AT_STEP(sB0, sB1, sA0, sA1, sn + 2048, so + 6144);
        if (c >= HS) { hA = hp.v[4]; hB = hp.v[5]; hC = hp.v[6]; hD = hp.v[7]; }
    }
#pragma unroll 1
    for (int c = 5; c < 8; ++c) {
        const int so = (c & 3) * SLOT, sn = (c == 7) ? so : ((c + 1) & 3) * SLOT;
        const int sel = (c ^ 6) | wid;
        if (sel == 0) { PEEK_DONE(hv); wait_all(flags, F_H + depword, hv); }
        if (c == 5) g2_load_b(g2, 0, lds0 + 4 * SLOT, wid, gB);
        if (c == 6) g2_load_b(g2, 1, lds0, wid, gB);
        if (c == 7) g2_load_a(g2, 1, lds0, wid, gA0, gA1);
        AT_STEP(sA0, sA1, sB0, sB1, so + 4096, so);

        AT_STEP(sB0, sB1, sA0, sA1, so + 6144, so + 2048);
        WAIT_V(0); WAIT_L0(); BAR(); SBAR();
        STAMP(20 + c);
        if (c == 5) { AT_LOAD_K((c0 + 7) & 7, 3); AT_LOAD_V((c0 + 7) & 7, 3); }
        if (c == 6) { g2_load_a(g2, 0, lds0 + 4 * SLOT, wid, gA0, gA1); }
        if (c == 7) g2_load_a(g2, 2, lds0 + SLOT, wid, gA0, gA1);
        AT_STEP(sA0, sA1, sB0, sB1, sn, so + 4096);
        if (c == 6) g2_load_b(g2, 2, lds0 + SLOT, wid, gB);
        PEEK_AHEAD(hv, hptr, (c ^ 5) | wid);
        AT_STEP(sB0, sB1, sA0, sA1, sn + 2048, so + 6144);
    }
    STAMP(28);
    o1 = MFMA32(vc, pf, o1); l1 = MFMA16(ones, pf, l1);
    asm volatile("" :: "v"(pfd));
    R.o0 = o0; R.o1 = o1; R.l0 = l0; R.l1 = l1; R.m0 = m0; R.m1 = m1;
}

__global__ __launch_bounds__(512, 2) void k_main(const bf16_t* __restrict__ x, const bf16_t* __restrict__ wqkv, const bf16_t* __restrict__ wo, const bf16_t* __restrict__ wg,
                                                 const bf16_t* __restrict__ wu, const bf16_t* __restrict__ wd, bf16_t* __restrict__ out, unsigned char* ws) {
    __shared__ __attribute__((aligned(16))) unsigned char smem[LDS_BYTES];
    const lds_ptr lds = (lds_ptr)smem;
    const unsigned lds0 = (unsigned)(uintptr_t)lds;
    const int wid = __builtin_amdgcn_readfirstlane(threadIdx.x >> 6);
    const int bid = blockIdx.x, xcd = bid & 7, idx = bid >> 3;
    Flags flags; flags.p = (u64*)(ws + WS_FLAGS);
    const __amdgpu_buffer_rsrc_t wsr = __builtin_amdgcn_make_buffer_rsrc((void*)ws, 0, WS_END, 0x00020000);
#define ST16(v, off) __builtin_amdgcn_raw_buffer_store_b128(v, wsr, off, 0, 16)
    STAMP(0);
    unsigned cpd = 0;
    {
        const char* cbase; unsigned clen;
        asm volatile("s_getpc_b64 %0\n.Lk_begin:\n\ts_mov_b32 %1, .Lk_end-.Lk_begin" : "=s"(cbase), "=s"(clen));
        unsigned cpo = (unsigned)(idx * 16 + (threadIdx.x & 15)) * 128u;
        cpo = (cpo < clen) ? cpo : 0u;
        asm volatile("global_load_dword %0, %1, %2" : "+v"(cpd) : "v"(cpo), "s"(cbase) : "memory");
    }
    HPend hp;
    {
        const int lane = threadIdx.x & 63, wr = wid >> 2, wc = wid & 3, js = idx & 3, rb = idx >> 2;
        const bool typeY = (js == 3);
        const int f0 = 448 * xcd + 128 * js;
        const char* P0 = (const char*)(wg + f0);
        const char* P2 = (const char*)(wu + f0);
        const char* P1 = typeY ? (const char*)(wqkv + 128 * xcd + 64) : (const char*)(wg + f0 + 64);
        const char* P3 = typeY ? (const char*)(wqkv + 1024 + 128 * xcd + 64) : (const char*)(wu + f0 + 64);
        const char* P4 = typeY ? (const char*)(wqkv + 2048 + 128 * xcd + 64) : (const char*)(wqkv + js * 1024 + 128 * xcd);
        const unsigned stepF = 32u * FF * 2u, stepQ = 32u * NQKV * 2u, step13 = typeY ? stepQ : stepF;
        const char* XA = (const char*)x + (size_t)rb * 256 * DM * 2;
        const char* XC = (const char*)ws + WS_XC + (size_t)rb * 256 * DM * 2;
        u64 ep; u32x4 xa0, xa1;
        const int srow = rb * 256 + (js * 8 + xcd) * 8 + wid;
        asm volatile("global_load_dwordx4 %1, %5, %6\n\tglobal_load_dwordx4 %2, %5, %6 offset:1024\n\tglobal_load_dwordx2 %0, %3, %4 sc1"
                     : "=&v"(ep), "=&v"(xa0), "=&v"(xa1)
                     : "v"(0u), "s"(flags.p), "v"((unsigned)(lane * 16)), "s"((const char*)x + (size_t)srow * DM * 2) : "memory");
        const int r8 = lane >> 3, u8 = lane & 7;
        const int gw = (r8 & 2) | ((wc & 1) << 2);
        const unsigned voffF = (unsigned)(((wc * 8 + r8) * FF + (u8 ^ gw) * 8) * 2);
        const unsigned voffQ = (unsigned)(((wc * 8 + r8) * NQKV + (u8 ^ gw) * 8) * 2);
        const unsigned voff13 = typeY ? voffQ : voffF;
        const unsigned voffA0 = (unsigned)(((32 * wid + r8) * DM + (u8 ^ (r8 >> 1)) * 8) * 2);
        const unsigned voffA1 = (unsigned)(((32 * wid + 8 + r8) * DM + (u8 ^ (4 | (r8 >> 1))) * 8) * 2);
        const unsigned dstA = lds0 + G1_A + wid * 4096, dstW = lds0 + G1_W + wc * 1024;
#define G1_LOAD_A(t) do { const char* b_ = XA + (t) * 128; const unsigned d_ = dstA + ((t) & 1) * 32768; \
        glds16(b_, voffA0, d_); glds16(b_, voffA1, d_ + 1024); glds16(b_ + 32768, voffA0, d_ + 2048); glds16(b_ + 32768, voffA1, d_ + 3072); } while (0)
#define G1_LOAD_W(hs) do { const unsigned d_ = dstW + ((hs) & 3) * 4096; \
        glds16(P0 + (size_t)(hs) * stepF, voffF, d_); glds16(P1 + (size_t)(hs) * step13, voff13, d_ + 16384); glds16(P2 + (size_t)(hs) * stepF, voffF, d_ + 32768); \
        glds16(P3 + (size_t)(hs) * step13, voff13, d_ + 49152); glds16(P4 + (size_t)(hs) * stepQ, voffQ, d_ + 65536); } while (0)
        STAMP(29);
        if (wr == 1) G1_LOAD_W(0);
        G1_LOAD_A(0);
        STAMP(30);

        int fr = lane & 15, fq = lane >> 4;
        const lds_ptr aB0 = lds + G1_A + (wr * 128 + fr) * 128 + ((fq ^ (fr >> 1)) * 16);
        const lds_ptr aB1 = lds + G1_A + (wr * 128 + fr) * 128 + (((4 | fq) ^ (fr >> 1)) * 16);
        const int tq = (lane >> 2) & 3, tp = lane & 3, tg = (tq & 2) | ((fq & 1) << 2);
        auto wbase = [&](int panel, int bp) { return lds + G1_W + panel * 16384 + (8 * fq + tq) * 128 + (((2 * bp + (tp >> 1)) ^ tg) * 16) + (tp & 1) * 8; };
        const int pX = wc >> 1, bX = 2 * (wc & 1);
        const lds_ptr wB0 = typeY ? wbase(0, wc) : wbase(pX, bX);
        const lds_ptr wB1 = typeY ? wbase(1, wc) : wbase(pX, bX + 1);
        const lds_ptr wB2 = typeY ? wbase(2, wc) : wbase(2 + pX, bX);
        const lds_ptr wB3 = typeY ? wbase(3, wc) : wbase(2 + pX, bX + 1);
        const lds_ptr wB4 = wbase(4, wc);
        if (threadIdx.x == 0) *(LAS unsigned*)(lds + G1_CNT) = 0u;
        f32x4 acc[8][5];
#pragma unroll
        for (int m = 0; m < 8; ++m)
#pragma unroll
            for (int b = 0; b < 5; ++b) { acc[m][b] = (f32x4){0.f, 0.f, 0.f, 0.f}; PIN(acc[m][b]); }
        asm volatile("s_cmp_lg_u32 %3, 0\n\ts_cbranch_scc1 1f\n\ts_waitcnt vmcnt(4)\n1:\n\ts_waitcnt vmcnt(9)" : "+v"(ep), "+v"(xa0), "+v"(xa1) : "s"(wr), "v"(cpd) : "memory", "scc");
        STAMP(31);
        {
            const unsigned elo = __builtin_amdgcn_readfirstlane((unsigned)ep), ehi = __builtin_amdgcn_readfirstlane((unsigned)(ep >> 32));
            flags.tag = (((u64)ehi << 32) | elo) * 0x9E3779B97F4A7C15ull + 0x632BE59BD9B4E019ull;
        }
        const unsigned tag32 = (unsigned)flags.tag;
        {
            const f32x2 st = row_stats(xa0, xa1);
            G1_LOAD_W(1 + wr);
            {
                const char* tb = wid == 0 ? (const char*)ws : wid == 1 ? (const char*)wqkv : wid == 2 ? (const char*)wg : wid == 3 ? (const char*)wu : wid == 4 ? (const char*)wd
                               : wid == 5 ? (const char*)wo : wid == 6 ? (const char*)out : (const char*)x;
                const unsigned tn = wid == 0 ? WS_END : wid == 1 ? DM * NQKV * 2 : wid < 5 ? DM * FF * 2 : wid == 5 ? DM * DM * 2 : SEQ * DM * 2;
                unsigned to = (unsigned)(lane * 32 + idx) * TLBW;
                to = to < tn ? to : 0u;
                asm volatile("global_load_dword %0, %1, %2" : "+v"(cpd) : "v"(to), "s"(tb) : "memory");
            }
            ST16(xa0, (lane >= 32) ? WS_XC + (unsigned)srow * 2048u + lane * 16 : WS_END);
            ST16(xa1, WS_XC + (unsigned)srow * 2048u + 1024 + lane * 16);
            ST16(((u32x4){f2u(st.x), tag32, f2u(st.y), tag32}), (lane == 0) ? WS_STAT + (unsigned)srow * 16u : WS_END);
            CFENCE();
        }
        STAMP(32);
        bf16x8 af[8], wf[5];
        float cs0 = 0.f, cs1 = 0.f, cs2 = 0.f;
#define CS1(c, w) "v_dot2c_f32_bf16 %" #c ", %25, %" #w "\n\t"
#define G1_CSUM(t, lim) do { const u32x4 w0_ = __builtin_bit_cast(u32x4, wf[0]), w1_ = __builtin_bit_cast(u32x4, wf[1]), w2_ = __builtin_bit_cast(u32x4, wf[2]), \
            w3_ = __builtin_bit_cast(u32x4, wf[3]), w4_ = __builtin_bit_cast(u32x4, wf[4]); \
        asm volatile("s_cmp_gt_u32 %24, %26\n\ts_cbranch_scc1 2f\n\ts_cmp_lg_u32 %23, 0\n\ts_cbranch_scc1 1f\n\t" \
            CS1(0, 3) CS1(1, 7) CS1(2, 19) CS1(0, 4) CS1(1, 8) CS1(2, 20) CS1(0, 5) CS1(1, 9) CS1(0, 6) CS1(1, 10) "s_nop 1\n\ts_branch 2f\n1:\n\t" \
            CS1(0, 11) CS1(1, 15) CS1(2, 21) CS1(0, 12) CS1(1, 16) CS1(2, 22) CS1(0, 13) CS1(1, 17) CS1(0, 14) CS1(1, 18) "s_nop 2\n2:" \
            : "+v"(cs0), "+v"(cs1), "+v"(cs2) \
            : "v"(w0_[0]), "v"(w0_[1]), "v"(w0_[2]), "v"(w0_[3]), "v"(w1_[0]), "v"(w1_[1]), "v"(w1_[2]), "v"(w1_[3]), "v"(w2_[0]), "v"(w2_[1]), "v"(w2_[2]), "v"(w2_[3]), \
              "v"(w3_[0]), "v"(w3_[1]), "v"(w3_[2]), "v"(w3_[3]), "v"(w4_[0]), "v"(w4_[1]), "v"(w4_[2]), "v"(w4_[3]), "s"(wr), "s"(t), "s"(0x3f803f80u), "i"(lim) : "scc"); } while (0)
#define G1_RW(i, B, slot) wf[i] = cat8(lds_tr(B + (slot) * 4096), lds_tr(B + (slot) * 4096 + 512))
#define G1_RA(m, AB, abuf) af[m] = lds_b128(AB + (abuf) * 32768 + (m) * 2048)
#define G1_SEG(AB, abuf, slot, L0, L1, L2, L3, L4, CLOSE) do { \
        L0; G1_RW(0, wB0, slot); G1_RW(1, wB1, slot); L1; G1_RW(2, wB2, slot); G1_RW(3, wB3, slot); L2; G1_RW(4, wB4, slot); G1_RA(0, AB, abuf); G1_RA(1, AB, abuf); \
        L3; G1_RA(2, AB, abuf); G1_RA(3, AB, abuf); G1_RA(4, AB, abuf); L4; G1_RA(5, AB, abuf); G1_RA(6, AB, abuf); G1_RA(7, AB, abuf); \
        SBAR(); G1_CSUM(t, 3); WAIT_L0(); SBAR(); if (CLOSE) { BAR(); SBAR(); } } while (0)
#define G1_SEG_A(abuf, slot, tt) do { const char* b_ = ((tt) < 4 ? XA : XC) + (tt) * 128; const unsigned d_ = dstA + (1 - (abuf)) * 32768; \
        G1_SEG(aB0, abuf, slot, glds16c(b_, voffA0, d_), glds16c(b_, voffA1, d_ + 1024), glds16c(b_ + 32768, voffA0, d_ + 2048), glds16c(b_ + 32768, voffA1, d_ + 3072), (void)0, true); } while (0)
#define G1_SEG_W(abuf, slot, hs, dslot, CLOSE) do { const unsigned d_ = dstW + (dslot) * 4096; \
        G1_SEG(aB1, abuf, slot, glds16(P0 + (size_t)(hs) * stepF, voffF, d_), glds16(P1 + (size_t)(hs) * step13, voff13, d_ + 16384), glds16(P2 + (size_t)(hs) * stepF, voffF, d_ + 32768), \
               glds16(P3 + (size_t)(hs) * step13, voff13, d_ + 49152), glds16(P4 + (size_t)(hs) * stepQ, voffQ, d_ + 65536), CLOSE); } while (0)
#define G1_MMA() do { __builtin_amdgcn_s_setprio(1); \
        _Pragma("unroll") for (int b = 0; b < 5; ++b) _Pragma("unroll") for (int m = 0; m < 8; ++m) MFMA16_ACC(acc[m][b], wf[b], af[m]); \
        __builtin_amdgcn_s_setprio(0); SBAR(); } while (0)

        STAMP(33);
        if (wr == 1) { WAIT_V(13); BAR(); }
        WAIT_V(9); WAIT_L0();
        STAMP(34);
        BAR();
        SBAR();
        STAMP(1);
        u64 sg = 0;
        const u64* sgp = flags.p + (F_XC + rb * 32 + (lane & 31)) * 16;
#pragma unroll 1
        for (int t = 0; t < 16; t += 2) {
            const int h1 = (2 * t + 3 + wr < 31) ? 2 * t + 3 + wr : 31, h2 = (2 * t + 5 + wr < 31) ? 2 * t + 5 + wr : 31, t2 = (t < 14) ? t + 2 : 15;
            const int sel = (t == 12) ? 0 : ((t ^ 2) | wid);
            STAMP(35 + (t >> 1));
            PEEK_AHEAD(sg, sgp, sel);
            G1_SEG_A(0, 0, t + 1); G1_MMA(); asm volatile("s_waitcnt vmcnt(4)" : "+v"(sg), "+v"(cpd) :: "memory"); BAR(); SBAR();
            if (t == 0 && wid == 4) signal(flags, F_XC + bid, lane);
            if (sel == 0) {
                const u64 want = (t == 12) ? ((u64)tag32 << 32) : flags.tag, mask = (t == 12) ? 0xffffffff00000000ull : ~0ull;
#pragma unroll 1
                for (unsigned spin = 0; spin < (1u << 20) && !__all((sg & mask) == want); ++spin)
                    asm volatile("s_sleep 1\n\tglobal_load_dwordx2 %0, %1, off sc1\n\ts_waitcnt vmcnt(0)" : "+v"(sg) : "v"(sgp) : "memory");
                const int tl = opaque_tid() & 63;
                if (t == 12) *(LAS unsigned*)(lds + G1_STAT + (wid * 32 + (tl & 31)) * 8 + (tl >> 5) * 4) = (unsigned)sg;
            }
            G1_SEG_W(0, 1, h1, (3 + wr) & 3, true); G1_MMA(); WAIT_V(5); BAR(); SBAR();
            G1_SEG_A(1, 2, t2); G1_MMA(); WAIT_V(4); BAR(); SBAR();
            G1_SEG_W(1, 3, h2, (1 + wr) & 3, false);
            if (t == 2) {
                const int tl = opaque_tid() & 63;
                const lds_ptr cp = lds + G1_CS + wc * 384 + (tl & 15) * 4;
                *(LAS float*)(cp + wr * 128) = red4(cs0); *(LAS float*)(cp + wr * 128 + 64) = red4(cs1); *(LAS float*)(cp + 256 + wr * 64) = red4(cs2);
                sgp = (const u64*)(ws + WS_STAT) + (rb * 256 + wid * 32 + (tl & 31)) * 2 + (tl >> 5);
                WAIT_L0();
            }
            SBAR(); BAR(); SBAR(); G1_MMA(); WAIT_V(5);
            if (wr == 0 || t < 14) BAR();
            SBAR();
        }
        MFMA_DRAIN(); SBAR();
        STAMP(2);

        const int tz = opaque_tid(), ln = tz & 63;
        fr = tz & 15; fq = (tz >> 4) & 3;
        const int row0 = rb * 256 + wr * 128 + fr;
        f32x4 csv[5]; f32x2 st[8];
#pragma unroll
        for (int b = 0; b < 5; ++b) csv[b] = *(const LAS f32x4*)(lds + G1_CS + wc * 384 + b * 64 + fq * 16);
        csv[4] += *(const LAS f32x4*)(lds + G1_CS + wc * 384 + 320 + fq * 16);
#pragma unroll
        for (int m = 0; m < 8; ++m) st[m] = *(const LAS f32x2*)(lds + G1_STAT + (wr * 128 + m * 16 + fr) * 8);
        auto lnv = [&](int m, int b, float sc) { return (acc[m][b] - st[m].x * csv[b]) * (st[m].y * sc); };
        auto glu = [&](int m, int bg, int bu) {
            const f32x4 g = lnv(m, bg, -LOG2E), u = lnv(m, bu, 1.0f);
            f32x4 h;
#pragma unroll
            for (int j = 0; j < 4; ++j) h[j] = u[j] * __builtin_amdgcn_rcpf(1.0f + __builtin_amdgcn_exp2f(g[j]));
            return h;
        };
        auto pack2 = [&](const f32x4& v0, const f32x4& v1) {
            unsigned p0 = cvtpk(v0[0], v0[1]), p1 = cvtpk(v0[2], v0[3]), q0 = cvtpk(v1[0], v1[1]), q1 = cvtpk(v1[2], v1[3]);
            swap16(p0, q0); swap16(p1, q1);
            return (u32x4){p0, p1, q0, q1};
        };
        const int hd = 4 * xcd + (typeY ? 2 : 0) + (wc >> 1);
        const unsigned qoff = WS_QKV + (unsigned)(hd * 2 + (wc & 1)) * 65536u + (unsigned)(row0 + (fq & 1) * 16) * 32u + (fq >> 1) * 16;
        constexpr unsigned KIND = NH * 2 * 65536u;
        if (!typeY) {
            const float sc4 = (js == 0) ? QSCALE : 1.0f;
#pragma unroll
            for (int m = 0; m < 8; m += 2) ST16(pack2(lnv(m, 4, sc4), lnv(m + 1, 4, sc4)), qoff + js * KIND + m * 512);
            CFENCE();
            hp.base = ws + WS_ACT + (unsigned)(14 * xcd + 4 * js + wc) * GRP + (unsigned)(rb * 256 + wr * 128) * 64u;
            hp.off = fr * 64 + (fq & 1) * 32 + (fq >> 1) * 16; hp.step = 1024;
#pragma unroll
            for (int m = 0; m < 8; ++m) {
                hp.v[m] = pack2(glu(m, 0, 2), glu(m, 1, 3));
                if (m == 5) { PIN(hp.v[5]); WAIT_V(0); arrive(flags, F_QKV + bid, ln, lds + G1_CNT, 7u); }
            }
        } else {
#pragma unroll
            for (int m = 0; m < 8; m += 2) {
                ST16(pack2(lnv(m, 3, 1.0f), lnv(m + 1, 3, 1.0f)), qoff + KIND + m * 512);
                ST16(pack2(lnv(m, 4, 1.0f), lnv(m + 1, 4, 1.0f)), qoff + 2 * KIND + m * 512);
                ST16(pack2(lnv(m, 1, QSCALE), lnv(m + 1, 1, QSCALE)), qoff + m * 512);
            }
            CFENCE();
            hp.base = ws + WS_ACT + (unsigned)(14 * xcd + 12 + (wc >> 1)) * GRP + (unsigned)(rb * 256 + wr * 128) * 64u;
            hp.off = (fr + (fq & 1) * 16) * 64 + (wc & 1) * 32 + (fq >> 1) * 16; hp.step = 2048;
#pragma unroll
            for (int m = 0; m < 8; m += 2) { hp.v[m >> 1] = pack2(glu(m, 0, 2), glu(m + 1, 0, 2)); hp.v[4 + (m >> 1)] = hp.v[m >> 1]; }
            PIN(hp.v[3]); WAIT_V(0); arrive(flags, F_QKV + bid, ln, lds + G1_CNT, 7u);
        }
#pragma unroll
        for (int i = 0; i < 8; ++i) PIN(hp.v[i]);
        STAMP(3);
    }

    const int z = xcd & 1, rbk = ((xcd >> 1) & 1) * 8 + (idx & 7), cbk = (xcd >> 2) * 4 + (idx >> 3);
    G2 g2;
    g2.Ah = (const char*)ws + WS_ACT + (size_t)z * 56 * GRP + rbk * 128 * 64;
    g2.Ao = (const char*)ws + WS_ACT + (size_t)(112 + z * 16 - 56) * GRP + rbk * 128 * 64;
    g2.Wd = (const char*)(wd + (size_t)z * 1792 * DM + cbk * 128);
    g2.Wo = (const char*)(wo + (size_t)z * 512 * DM + cbk * 128) - (size_t)1792 * DM * 2;
    {
        const int hl = idx & 3, qb = idx >> 2, h = 4 * xcd + hl;
        const int l0_ = opaque_tid() & 63;
        const int qkv0 = F_QKV + xcd + 8 * (qb * 4 + ((hl >= 2) ? 3 : l0_ % 3));
        const int qkvword = F_QKV + xcd + 8 * ((l0_ & 7) * 4 + ((hl >= 2) ? 3 : 1 + ((l0_ >> 3) & 1)));
        const int depword = (rbk >> 1) * 32 + ((l0_ >> 2) & 3) * 8 + 4 * z + (l0_ & 3);
        const char* Qg = (const char*)ws + WS_QKV + (size_t)h * 131072 + qb * 256 * 32;
        const char* Kg = (const char*)ws + WS_QKV + (size_t)(NH + h) * 131072;
        const char* Vg = (const char*)ws + WS_QKV + (size_t)(2 * NH + h) * 131072;
        const char* wpd = g2.Wd - (cbk & 3) * 256;
        const char* wpo = g2.Wo - (cbk & 3) * 256;
        AttnOut R;
#define DG
        attn_fast(lds, lds0, Qg, Kg, Vg, g2, flags, qkv0, qkvword, depword, F_H + bid, qb, bid == 0, hp, wpd, wpo, idx * 72, wid, R DG);
        STAMP(6);
        const int tz = opaque_tid(), ln = tz & 63, w4 = wid & 3, fr = ln & 15, fq = ln >> 4;
        const lds_ptr po = lds + 2 * SLOT + w4 * 8192 + ln * 16, pl = lds + 3 * SLOT + 4096 + w4 * 1024 + ln * 16;
        volatile LAS unsigned* const badw = (volatile LAS unsigned*)(lds + 3 * SLOT + 8192 + 4096);
#define AT_PUBLISH() do { if (wid < 4) { \
            _Pragma("unroll") for (int i = 0; i < 4; ++i) { *(LAS f32x4*)(po + i * 1024) = (f32x4){R.o0[4 * i], R.o0[4 * i + 1], R.o0[4 * i + 2], R.o0[4 * i + 3]}; \
                *(LAS f32x4*)(po + 4096 + i * 1024) = (f32x4){R.o1[4 * i], R.o1[4 * i + 1], R.o1[4 * i + 2], R.o1[4 * i + 3]}; } \
            *(LAS f32x4*)pl = (f32x4){R.l0[0], R.m0, R.l1[0], R.m1}; } } while (0)
        {
            AT_PUBLISH();
            const f32x16 z16 = R.o0 * 0.0f + R.o1 * 0.0f;
            float zs = 0.f;
#pragma unroll
            for (int r = 0; r < 16; ++r) zs += z16[r];
            const bool ok = (zs == 0.f) && (R.l0[0] > 0.f) && (R.l0[0] < 0x1p96f) && (R.l1[0] > 0.f) && (R.l1[0] < 0x1p96f);
            const bool wave_bad = !__all(ok);
            if (ln == 0) badw[wid] = wave_bad ? 1u : 0u;
            STAMP(16);
            WAIT_V(0); STAMP(17); WAIT_L0(); BAR(); CFENCE();
            const unsigned anybad = badw[ln & 7];
            if (__any(anybad != 0u)) {
                WAIT_L0(); BAR(); CFENCE();
                attn_safe(lds, lds0, Qg, Kg, Vg, wid, R);
                AT_PUBLISH();
                WAIT_L0(); BAR(); CFENCE();
            }
        }
        STAMP(7);

        const int tile = rbk * 8 + cbk;
        if (wid >= 4) {
            unsigned vA0, vA1, vB0, vB1; g2_voff(ln, vA0, vA1, vB0, vB1);
#define G2_LOAD(j, sl) do { const unsigned d_ = lds0 + (sl) * SLOT; g2_load_b(g2, j, d_, 2 * w4, vB0); g2_load_a(g2, j, d_, 2 * w4, vA0, vA1); \
                g2_load_b(g2, j, d_, 2 * w4 + 1, vB1); g2_load_a(g2, j, d_, 2 * w4 + 1, vA0, vA1); } while (0)
            const f32x4 lm = *(const LAS f32x4*)pl;
            f32x4 t[8];
#pragma unroll
            for (int i = 0; i < 8; ++i) t[i] = *(const LAS f32x4*)(po + i * 1024);
            WAIT_L0(); BAR(); CFENCE();
            G2_LOAD(3, 2);
            u32x4 ow[4];
            const unsigned ooff = WS_ACT + (unsigned)(112 + h) * GRP + (unsigned)(qb * 256 + w4 * 64 + (ln & 31)) * 64u + (ln >> 5) * 16;
#pragma unroll
            for (int q = 0; q < 2; ++q) {
                BAR();
                if (q == 0) G2_LOAD(4, 3); else G2_LOAD(5, 4);
                const f32x16 ok_ = q ? R.o1 : R.o0;
                const float lk = q ? R.l1[0] : R.l0[0], mk = q ? R.m1 : R.m0, lo = lm[2 * q], mo = lm[2 * q + 1];
                const float mm = __builtin_fmaxf(mk, mo), fk = __builtin_amdgcn_exp2f(mk - mm), fo = __builtin_amdgcn_exp2f(mo - mm);
                const float rl = 1.0f / (lk * fk + lo * fo), ck = fk * rl, co = fo * rl;
                unsigned p[8];
#pragma unroll
                for (int i = 0; i < 4; ++i) {
                    const f32x4 u = t[4 * q + i];
                    p[2 * i] = cvtpk(ok_[4 * i] * ck + u[0] * co, ok_[4 * i + 1] * ck + u[1] * co);
                    p[2 * i + 1] = cvtpk(ok_[4 * i + 2] * ck + u[2] * co, ok_[4 * i + 3] * ck + u[3] * co);
                }
                swap32(p[0], p[2]); swap32(p[1], p[3]); swap32(p[4], p[6]); swap32(p[5], p[7]);
                ow[2 * q] = (u32x4){p[0], p[1], p[2], p[3]}; ow[2 * q + 1] = (u32x4){p[4], p[5], p[6], p[7]};
                PIN(ow[2 * q]); PIN(ow[2 * q + 1]);
            }
#pragma unroll
            for (int i = 0; i < 4; ++i) ST16(ow[i], ooff + (i >> 1) * 2048 + (i & 1) * 32);
            CFENCE();
            unsigned t0 = 0, t1 = 0, t2 = 0, t3 = 0, a0 = 0, a1 = 0, a2 = 0;
            WAIT_V(20); BAR(); G2_LOAD(6, 0);
            WAIT_V(20); BAR(); G2_LOAD(7, 1);
            WAIT_V(20);
            int sl = 2;
#pragma unroll 1
            for (int j = 5; j < 33; ++j) {
                TS(t1);
                BAR();
                TS(t2);
                if (j == 6 && wid == 4) signal(flags, F_O + bid, ln);
                G2_LOAD(j + 3, sl);
                sl = (sl == 4) ? 0 : sl + 1;
                TS(t3);
                WAIT_V(16);
                TS(t0);
                a0 += t0 - t3; a1 += t2 - t1; a2 += t3 - t2;
            }
            PUT(50, a0); PUT(51, a1); PUT(52, a2);
            BAR();
            if (tz == 256) *(LAS unsigned*)lds = 0u;
            WAIT_V(8); WAIT_L0(); BAR(); WAIT_V(0); BAR();
                if (wid == 4) wait_all(flags, F_X + tile * 2 + z, peek(flags, F_X + tile * 2 + z));
            CFENCE(); BAR();
            asm volatile(".Lk_end:");
        } else {
            const int wm = w4 >> 1, wn = w4 & 1;
            const unsigned aO0 = (wm * 64 + fr) * 128 + ((fq ^ (fr >> 1)) * 16), aO1 = aO0 ^ 64;
            const int tq = (ln >> 2) & 3, tp = ln & 3, tg = (tq & 2) | ((fq & 1) << 2);
            unsigned wO[4];
#pragma unroll
            for (int b = 0; b < 4; ++b) wO[b] = 16384 + wn * 8192 + (8 * fq + tq) * 128 + (((2 * b + (tp >> 1)) ^ tg) * 16) + (tp & 1) * 8;
            f32x4 acc[4][4];
#pragma unroll
            for (int m = 0; m < 4; ++m)
#pragma unroll
                for (int b = 0; b < 4; ++b) acc[m][b] = (f32x4){0.f, 0.f, 0.f, 0.f};
            bf16x8 xa[4], ya[4] = {}; s16x4 xl[4], xh[4], yl[4] = {}, yh[4] = {};
            lds_ptr pa0, pa1, pw0, pw1, pw2, pw3;
#define G2_BASES(so) do { pa0 = lds + aO0 + (so); pa1 = lds + aO1 + (so); pw0 = lds + wO[0] + (so); pw1 = lds + wO[1] + (so); pw2 = lds + wO[2] + (so); pw3 = lds + wO[3] + (so); } while (0)
#define G2_M(C, m, b) do { MFMA16_ACC(acc[m][b], cat8(C##l[b], C##h[b]), C##a[m]); SBAR(); } while (0)
#define RDQ(x) (x)
#define G2_HALF(C, N, AB, WO, RD_, TAIL) do { const bool RD = RDQ(RD_); \
            G2_M(C, 0, 0); if (RD) { N##l[0] = lds_tr(pw0 + (WO)); N##h[0] = lds_tr(pw0 + (WO) + 512); } SBAR(); \
            G2_M(C, 1, 0); if (RD) N##a[0] = lds_b128(AB); SBAR(); \
            G2_M(C, 2, 0); if (RD) N##a[1] = lds_b128((AB) + 2048); SBAR(); \
            G2_M(C, 3, 0); if (RD) N##a[2] = lds_b128((AB) + 4096); SBAR(); \
            G2_M(C, 0, 1); if (RD) N##a[3] = lds_b128((AB) + 6144); SBAR(); \
            G2_M(C, 1, 1); if (RD) { N##l[1] = lds_tr(pw1 + (WO)); N##h[1] = lds_tr(pw1 + (WO) + 512); } SBAR(); \
            G2_M(C, 2, 1); if (RD) { N##l[2] = lds_tr(pw2 + (WO)); N##h[2] = lds_tr(pw2 + (WO) + 512); } SBAR(); \
            G2_M(C, 3, 1); if (RD) { N##l[3] = lds_tr(pw3 + (WO)); N##h[3] = lds_tr(pw3 + (WO) + 512); } SBAR(); \
            G2_M(C, 0, 2); TAIL(0); SBAR(); G2_M(C, 1, 2); TAIL(1); SBAR(); G2_M(C, 2, 2); TAIL(2); SBAR(); G2_M(C, 3, 2); TAIL(3); SBAR(); \
            G2_M(C, 0, 3); G2_M(C, 1, 3); G2_M(C, 2, 3); G2_M(C, 3, 3); } while (0)
            int so = 4 * SLOT;
            G2_BASES(so);
            xl[0] = lds_tr(pw0); xh[0] = lds_tr(pw0 + 512); SBAR();
#pragma unroll
            for (int m = 0; m < 4; ++m) { xa[m] = lds_b128(pa0 + m * 2048); SBAR(); }
            xl[1] = lds_tr(pw1); xh[1] = lds_tr(pw1 + 512); xl[2] = lds_tr(pw2); xh[2] = lds_tr(pw2 + 512); xl[3] = lds_tr(pw3); xh[3] = lds_tr(pw3 + 512);
            const u64* const optr = flags.p + (F_O + depword) * 16;
            u64 ov = 0; unsigned t0 = 0, t1 = 0, a0 = 0;
            STAMP(8);
#define G2_NEXT(i) do { if ((i) == 0) so = (so == 4 * SLOT) ? 0 : so + SLOT; \
                if ((i) == 1) { pa0 = lds + aO0 + so; pa1 = lds + aO1 + so; PIN(pa0); PIN(pa1); } \
                if ((i) == 2) { pw0 = lds + wO[0] + so; pw1 = lds + wO[1] + so; PIN(pw0); PIN(pw1); } \
                if ((i) == 3) { pw2 = lds + wO[2] + so; pw3 = lds + wO[3] + so; PIN(pw2); PIN(pw3); } } while (0)
#define G2_NONE(i) do {} while (0)
            WAIT_L0(); BAR(); SBAR();
#pragma unroll 1
            for (int ph = 0; ph < 3; ++ph) {
                const int n = (ph == 0) ? 11 : (ph == 1) ? 1 : 5;
#pragma unroll 1
                for (int i = 0; i < n; ++i) {
                    WAIT_L0(); SBAR(); G2_HALF(x, y, pa1, 4096, true, G2_NEXT);
                    WAIT_L0(); BAR(); SBAR(); G2_HALF(y, x, pa0, 0, true, G2_NONE);
                    WAIT_L0(); SBAR(); G2_HALF(x, y, pa1, 4096, true, G2_NEXT);
                    WAIT_L0(); TS(t0); BAR(); TS(t1); SBAR(); a0 += t1 - t0;
                    G2_HALF(y, x, pa0, 0, true, G2_NONE);
                }
                PEEK_AHEAD(ov, optr, wid | ph);
                if ((wid | (ph ^ 1)) == 0) { PEEK_DONE(ov); wait_all(flags, F_O + depword, ov); }
            }
            const int row = rbk * 128 + wm * 64 + z * 32 + fr, col = cbk * 128 + wn * 64 + (fq & 1) * 16 + (fq >> 1) * 8;
            u32x4 xr[4];
#pragma unroll
            for (int i = 0; i < 4; ++i) xr[i] = *(const u32x4*)(x + (size_t)(row + (i >> 1) * 16) * DM + col + (i & 1) * 32);
            WAIT_L0(); SBAR(); G2_HALF(x, y, pa1, 4096, true, G2_NEXT);
            WAIT_L0(); BAR(); SBAR(); G2_HALF(y, x, pa0, 0, true, G2_NONE);
            WAIT_L0(); SBAR(); G2_HALF(x, y, pa1, 4096, true, G2_NONE);
            WAIT_L0(); SBAR(); G2_HALF(y, x, pa0, 0, false, G2_NONE);
            MFMA_DRAIN(); SBAR();
            STAMP(9); PUT(53, a0);
            f32x4 vk[8];
            const unsigned xo = WS_XCH + (unsigned)(tile * 2 + 1 - z) * 32768u + w4 * 8192 + ln * 16;
            if (z == 0) {
#pragma unroll
                for (int i = 0; i < 8; ++i) { ST16(__builtin_bit_cast(u32x4, acc[2 + (i >> 2)][i & 3]), xo + i * 1024); vk[i] = acc[i >> 2][i & 3]; }
            } else {
#pragma unroll
                for (int i = 0; i < 8; ++i) { ST16(__builtin_bit_cast(u32x4, acc[i >> 2][i & 3]), xo + i * 1024); vk[i] = acc[2 + (i >> 2)][i & 3]; }
            }
            WAIT_V(0);
            arrive(flags, F_X + tile * 2 + 1 - z, ln, lds, 3u);
            STAMP(10);
            CFENCE(); BAR(); CFENCE();
            STAMP(11);
            const unsigned xi = WS_XCH + (unsigned)(tile * 2 + z) * 32768u + w4 * 8192 + ln * 16;
#pragma unroll
            for (int i = 0; i < 8; ++i) vk[i] += __builtin_bit_cast(f32x4, __builtin_amdgcn_raw_buffer_load_b128(wsr, xi + i * 1024, 0, 16));
#pragma unroll
            for (int i = 0; i < 4; ++i) {
                const u32x4 a = __builtin_bit_cast(u32x4, vk[2 * i]), b = __builtin_bit_cast(u32x4, vk[2 * i + 1]);
                unsigned a0_ = a[0], a1 = a[1], a2 = a[2], a3 = a[3], b0 = b[0], b1 = b[1], b2 = b[2], b3 = b[3];
                swap16(a0_, b0); swap16(a1, b1); swap16(a2, b2); swap16(a3, b3);
                const u32x4 xx = xr[i];
                u32x4 w;
                w[0] = cvtpk(u2f(a0_) + bf_lo(xx[0]), u2f(a1) + bf_hi(xx[0]));
                w[1] = cvtpk(u2f(a2) + bf_lo(xx[1]), u2f(a3) + bf_hi(xx[1]));
                w[2] = cvtpk(u2f(b0) + bf_lo(xx[2]), u2f(b1) + bf_hi(xx[2]));
                w[3] = cvtpk(u2f(b2) + bf_lo(xx[3]), u2f(b3) + bf_hi(xx[3]));
                *(u32x4*)(out + (size_t)(row + (i >> 1) * 16) * DM + col + (i & 1) * 32) = w;
            }
        }
    }
}

extern "C" void kernel_launch(void* const* d_in, const int* in_sizes, int n_in, void* d_out, int out_size, void* d_ws, size_t ws_size, hipStream_t stream) {
    const bf16_t* x = (const bf16_t*)d_in[0];
    k_main<<<256, 512, 0, stream>>>(x, (const bf16_t*)d_in[1], (const bf16_t*)d_in[2], (const bf16_t*)d_in[4], (const bf16_t*)d_in[5], (const bf16_t*)d_in[6],
                                    (bf16_t*)d_out, (unsigned char*)d_ws);
}
```

```cpp
#include <hip/hip_runtime.h>
#include <stdint.h>

typedef unsigned short bf16_t;
typedef short bf16x8 __attribute__((ext_vector_type(8)));
typedef short s16x4 __attribute__((ext_vector_type(4)));
typedef float f32x2 __attribute__((ext_vector_type(2)));
typedef float f32x4 __attribute__((ext_vector_type(4)));
typedef float f32x16 __attribute__((ext_vector_type(16)));
typedef unsigned u32x4 __attribute__((ext_vector_type(4)));
typedef __bf16 bf16x2_t __attribute__((ext_vector_type(2)));
#define LAS __attribute__((address_space(3)))
#define GAS __attribute__((address_space(1)))
typedef LAS unsigned char* lds_ptr;

constexpr int SEQ = 2048, DM = 1024, FF = 3584, NQKV = 3072, NH = 32;
constexpr float LN_EPS = 1e-5f;
constexpr float LOG2E = 1.4426950408889634f;
constexpr float QSCALE = 0.17677669529663687f * LOG2E;

constexpr unsigned WS_FLAGS = 0;
constexpr unsigned WS_STAT = 192 * 1024;
constexpr unsigned WS_QKV = 256 * 1024;
constexpr unsigned WS_ACT = WS_QKV + 3u * SEQ * DM * 2;
constexpr unsigned WS_XCH = WS_ACT + 144u * SEQ * 64;
constexpr unsigned WS_XC = WS_XCH + 256u * 32768;
constexpr unsigned WS_END = WS_XC + SEQ * DM * 2;
constexpr int F_EPOCH = 0, F_QKV = 1, F_H = 257, F_O = 513, F_X = 769, F_XC = 1025;
constexpr unsigned GRP = SEQ * 64;

#define SBAR() __builtin_amdgcn_sched_barrier(0)
#define BAR() __builtin_amdgcn_s_barrier()
#define WAIT_V(n) asm volatile("s_waitcnt vmcnt(" #n ")" ::: "memory")
#define WAIT_L0() do { __builtin_amdgcn_s_waitcnt(0xC07F); asm volatile("" ::: "memory"); } while (0)
#define CFENCE() asm volatile("" ::: "memory")
#define PIN(x) asm volatile("" : "+v"(x))
#define MFMA16(a, b, c) __builtin_amdgcn_mfma_f32_16x16x32_bf16(a, b, c, 0, 0, 0)
#define MFMA32(a, b, c) __builtin_amdgcn_mfma_f32_32x32x16_bf16(a, b, c, 0, 0, 0)
#define MFMA16_ACC(acc, a, b) asm volatile("v_mfma_f32_16x16x32_bf16 %0, %1, %2, %0" : "+v"(acc) : "v"(a), "v"(b))
#define MFMA_DRAIN() asm volatile("s_nop 15" ::: "memory")
#define RLX_AGENT __ATOMIC_RELAXED, __HIP_MEMORY_SCOPE_AGENT

#define STAMP(i) do {} while (0)
#define TS(v) do {} while (0)
#define PUT(i, v) do {} while (0)

__device__ __forceinline__ void glds16(const void* sbase, unsigned voff, unsigned ldsdst) {
    asm volatile("s_mov_b32 m0, %2\n\ts_nop 0\n\tglobal_load_lds_dwordx4 %0, %1" ::"v"(voff), "s"(sbase), "s"(ldsdst) : "memory");
}
__device__ __forceinline__ void glds16c(const void* sbase, unsigned voff, unsigned ldsdst) {
    asm volatile("s_mov_b32 m0, %2\n\ts_nop 0\n\tglobal_load_lds_dwordx4 %0, %1 sc1" ::"v"(voff), "s"(sbase), "s"(ldsdst) : "memory");
}
__device__ __forceinline__ bf16x8 lds_b128(lds_ptr p) { return *(const LAS bf16x8*)p; }
__device__ __forceinline__ s16x4 lds_tr(lds_ptr p) { return __builtin_amdgcn_ds_read_tr16_b64_v4i16((LAS s16x4*)p); }
__device__ __forceinline__ bf16x8 cat8(s16x4 a, s16x4 b) { return __builtin_shufflevector(a, b, 0, 1, 2, 3, 4, 5, 6, 7); }
__device__ __forceinline__ unsigned cvtpk(float lo, float hi) {
    f32x2 v = {lo, hi};
    return __builtin_bit_cast(unsigned, __builtin_convertvector(v, bf16x2_t));
}
__device__ __forceinline__ float u2f(unsigned u) { return __builtin_bit_cast(float, u); }
__device__ __forceinline__ unsigned f2u(float f) { return __builtin_bit_cast(unsigned, f); }
__device__ __forceinline__ float bf_lo(unsigned u) { return u2f(u << 16); }
__device__ __forceinline__ float bf_hi(unsigned u) { return u2f(u & 0xffff0000u); }
__device__ __forceinline__ void swap16(unsigned& a, unsigned& b) {
    auto r = __builtin_amdgcn_permlane16_swap(a, b, false, false);
    a = r[0]; b = r[1];
}
__device__ __forceinline__ void swap32(unsigned& a, unsigned& b) {
    auto r = __builtin_amdgcn_permlane32_swap(a, b, false, false);
    a = r[0]; b = r[1];
}
__device__ __forceinline__ int opaque_tid() { int t = threadIdx.x; asm volatile("" : "+v"(t)); return t; }
typedef unsigned long long u64;
struct Flags { u64* p; u64 tag; };
__device__ __forceinline__ void signal(const Flags& f, int word, int lane) {
    if (lane == 0) __hip_atomic_store((GAS u64*)(f.p + word * 16), f.tag, RLX_AGENT);
}
__device__ __forceinline__ u64 peek(const Flags& f, int word) { return __hip_atomic_load((GAS u64*)(f.p + word * 16), RLX_AGENT); }
__device__ __forceinline__ void wait_all(const Flags& f, int word, u64 v) {
    unsigned spin = 0;
    while (!__all(v == f.tag)) { if (++spin > (1u << 20)) break; __builtin_amdgcn_s_sleep(1); v = peek(f, word); }
}
__device__ __forceinline__ void arrive(const Flags& f, int word, int lane, lds_ptr cnt, unsigned last) {
    if (lane == 0 && __hip_atomic_fetch_add((LAS unsigned*)cnt, 1u, __ATOMIC_RELAXED, __HIP_MEMORY_SCOPE_WORKGROUP) == last)
        __hip_atomic_store((GAS u64*)(f.p + word * 16), f.tag, RLX_AGENT);
}
#define PEEK_AHEAD(v, ptr, sel) asm volatile("s_cmp_lg_u32 %2, 0\n\ts_cbranch_scc1 1f\n\tglobal_load_dwordx2 %0, %1, off sc1\n1:" : "+v"(v) : "v"(ptr), "s"(sel) : "memory", "scc")
#define PEEK_DONE(v) asm volatile("s_waitcnt vmcnt(0)" : "+v"(v) :: "memory")

template <int CTRL> __device__ __forceinline__ float dpp_add(float v) { return v + u2f(__builtin_amdgcn_update_dpp(0u, f2u(v), CTRL, 0xf, 0xf, true)); }
__device__ __forceinline__ float red4(float v) {
    unsigned a = f2u(v), b = a;
    swap16(a, b);
    v = u2f(a) + u2f(b);
    a = f2u(v); b = a;
    swap32(a, b);
    return u2f(a) + u2f(b);
}
__device__ __forceinline__ float wave_sum(float v) {
    v = dpp_add<0xB1>(v); v = dpp_add<0x4E>(v); v = dpp_add<0x141>(v); v = dpp_add<0x140>(v);
    return red4(v);
}
__device__ __forceinline__ f32x2 row_stats(u32x4& a, u32x4& b) {
    float v[16];
#pragma unroll
    for (int i = 0; i < 4; ++i) { v[2 * i] = bf_lo(a[i]); v[2 * i + 1] = bf_hi(a[i]); v[8 + 2 * i] = bf_lo(b[i]); v[8 + 2 * i + 1] = bf_hi(b[i]); }
    float s = 0.f;
#pragma unroll
    for (int i = 0; i < 16; ++i) s += v[i];
    const float mu = wave_sum(s) * (1.0f / DM);
    float q = 0.f;
#pragma unroll
    for (int i = 0; i < 16; ++i) { v[i] -= mu; q += v[i] * v[i]; }
#pragma unroll
    for (int i = 0; i < 4; ++i) { a[i] = cvtpk(v[2 * i], v[2 * i + 1]); b[i] = cvtpk(v[8 + 2 * i], v[8 + 2 * i + 1]); }
    return (f32x2){mu, 1.0f / __builtin_sqrtf(wave_sum(q) * (1.0f / DM) + LN_EPS)};
}

constexpr int SLOT = 32768, LDS_BYTES = 5 * SLOT;
constexpr int G1_A = 0, G1_W = 65536, G1_STAT = G1_W + 5 * 16384, G1_CS = G1_STAT + 2048, G1_CNT = G1_CS + 4 * 6 * 64;

struct G2 { const char* Ah; const char* Ao; const char* Wd; const char* Wo; };
__device__ __forceinline__ void g2_load_b(const G2& g, int j, unsigned dst, int vw, unsigned voffB) {
    const char* b = (j >= 28 ? g.Wo : g.Wd) + (size_t)(j * 64 + vw * 8) * (DM * 2);
    glds16(b, voffB, dst + 16384 + vw * 1024); glds16(b + 128, voffB, dst + 16384 + 8192 + vw * 1024);
}
__device__ __forceinline__ void g2_load_a(const G2& g, int j, unsigned dst, int vw, unsigned voffA0, unsigned voffA1) {
    const char* a = (j >= 28 ? g.Ao : g.Ah) + (size_t)j * (2 * GRP) + vw * 1024;
    glds16c(a, voffA0, dst + vw * 2048); glds16c(a, voffA1, dst + vw * 2048 + 1024);
}
__device__ __forceinline__ void g2_voff(int lane, unsigned& voffA0, unsigned& voffA1, unsigned& voffB0, unsigned& voffB1) {
    const int r8 = lane >> 3, u8 = lane & 7, ua = u8 ^ (r8 >> 1), ub = ua ^ 4;
    voffA0 = (unsigned)(ua >> 2) * GRP + r8 * 64 + (ua & 3) * 16;
    voffA1 = (unsigned)(ub >> 2) * GRP + (r8 + 8) * 64 + (ub & 3) * 16;
    voffB0 = (unsigned)(r8 * (DM * 2) + (u8 ^ (r8 & 2)) * 16);
    voffB1 = (unsigned)(r8 * (DM * 2) + (u8 ^ (4 | (r8 & 2))) * 16);
}

struct AttnOut { f32x16 o0, o1; f32x4 l0, l1; float m0, m1; };
struct HPend { u32x4 v[8]; unsigned off, step; const void* base; };
struct AttnLane {
    unsigned voffK, voffV; lds_ptr kb0, kb1, vb; const char* qp; bf16x8 ones;
    __device__ __forceinline__ void init(lds_ptr lds, const char* Qg, int wid, int lane) {
        const int r32 = lane & 31, hi = lane >> 5, kh = wid >> 2, qg = wid & 3, ksw = (r32 >> 2) & 3;
        const int uk = (lane & 3) ^ ((lane >> 4) & 3), uv = lane & 3;
        voffK = (unsigned)((uk >> 1) * 65536 + wid * 1024 + (lane >> 2) * 32 + (uk & 1) * 16);
        voffV = (unsigned)((uv >> 1) * 65536 + wid * 1024 + (lane >> 2) * 32 + (uv & 1) * 16);
        kb0 = lds + kh * 8192 + r32 * 64 + ((hi ^ ksw) * 16); kb1 = lds + kh * 8192 + r32 * 64 + (((2 | hi) ^ ksw) * 16);
        vb = lds + 16384 + kh * 8192 + (4 * hi + ((lane >> 2) & 3)) * 64 + ((lane >> 4) & 1) * 32 + (lane & 3) * 8;
        qp = Qg + (qg * 64 + r32) * 32 + hi * 16;
        const short v = (((lane >> 4) & 1) == ((lane >> 2) & 1)) ? (short)0x3f80 : (short)0;
        ones = (bf16x8){v, v, v, v, v, v, v, v};
    }
};
#define EX(v) __builtin_amdgcn_exp2f(v)
#define AT_LOAD_K(c, slot) do { const unsigned d_ = lds0 + wid * 2048 + (slot) * SLOT; const char* k_ = Kg + (c) * 8192; glds16c(k_, A.voffK, d_); glds16c(k_ + 512, A.voffK, d_ + 1024); } while (0)
#define AT_LOAD_V(c, slot) do { const unsigned d_ = lds0 + wid * 2048 + (slot) * SLOT + 16384; const char* v_ = Vg + (c) * 8192; glds16c(v_, A.voffV, d_); glds16c(v_ + 512, A.voffV, d_ + 1024); } while (0)
#define AT_PACK(S, i) __builtin_bit_cast(bf16x8, (u32x4){cvtpk(S[i], S[i + 1]), cvtpk(S[i + 2], S[i + 3]), cvtpk(S[i + 4], S[i + 5]), cvtpk(S[i + 6], S[i + 7])})
#define AT_Q_LOAD() asm volatile("global_load_dwordx4 %0, %4, off sc1\n\tglobal_load_dwordx4 %1, %4, off offset:1024 sc1\n\tglobal_load_dwordx4 %2, %5, off sc1\n\tglobal_load_dwordx4 %3, %5, off offset:1024 sc1" \
        : "=&v"(q00), "=&v"(q10), "=&v"(q01), "=&v"(q11) : "v"(A.qp), "v"(qp2) : "memory")
#define AT_Q_WAIT(n) asm volatile("s_waitcnt vmcnt(" #n ")" : "+v"(q00), "+v"(q01), "+v"(q10), "+v"(q11) :: "memory")
__device__ __forceinline__ float rowmax16(const f32x16& s) {
    float a = s[0];
#pragma unroll
    for (int r = 1; r < 16; ++r) a = __builtin_fmaxf(a, s[r]);
    unsigned x = f2u(a), y = x; swap32(x, y);
    return __builtin_fmaxf(u2f(x), u2f(y));
}

__device__ __forceinline__ void attn_safe(lds_ptr lds, unsigned lds0, const char* Qg, const char* Kg, const char* Vg, int wid, AttnOut& R) {
    AttnLane A; A.init(lds, Qg, wid, opaque_tid() & 63);
    bf16x8 q00, q01, q10, q11; const char* qp2 = A.qp + 65536;
    AT_Q_LOAD(); AT_Q_WAIT(0);
    f32x16 o0 = (f32x16){}, o1 = (f32x16){};
    f32x4 l0 = (f32x4){0.f, 0.f, 0.f, 0.f}, l1 = l0;
    float m0 = -__builtin_inff(), m1 = m0;
#pragma unroll 1
    for (int c = 0; c < 8; ++c) {
        AT_LOAD_K(c, 3); AT_LOAD_V(c, 3);
        WAIT_V(0); BAR(); CFENCE();
#pragma unroll 1
        for (int t = 0; t < 4; ++t) {
            const int to = 3 * SLOT + t * 2048;
            const bf16x8 ka = lds_b128(A.kb0 + to), kb = lds_b128(A.kb1 + to);
            const bf16x8 va = cat8(lds_tr(A.vb + to), lds_tr(A.vb + to + 512)), vc = cat8(lds_tr(A.vb + to + 1024), lds_tr(A.vb + to + 1536));
            f32x16 s0 = MFMA32(kb, q01, MFMA32(ka, q00, (f32x16){})), s1 = MFMA32(kb, q11, MFMA32(ka, q10, (f32x16){}));
            const float n0 = __builtin_fmaxf(m0, rowmax16(s0)), n1 = __builtin_fmaxf(m1, rowmax16(s1));
            const float f0 = EX(m0 - n0), f1 = EX(m1 - n1);
            m0 = n0; m1 = n1; o0 *= f0; o1 *= f1; l0 *= f0; l1 *= f1;
#pragma unroll
            for (int r = 0; r < 16; ++r) { s0[r] = EX(s0[r] - n0); s1[r] = EX(s1[r] - n1); }
            const bf16x8 p00 = AT_PACK(s0, 0), p01 = AT_PACK(s0, 8), p10 = AT_PACK(s1, 0), p11 = AT_PACK(s1, 8);
            o0 = MFMA32(vc, p01, MFMA32(va, p00, o0)); l0 = MFMA16(A.ones, p01, MFMA16(A.ones, p00, l0));
            o1 = MFMA32(vc, p11, MFMA32(va, p10, o1)); l1 = MFMA16(A.ones, p11, MFMA16(A.ones, p10, l1));
        }
        WAIT_L0(); BAR(); CFENCE();
    }
    R.o0 = o0; R.o1 = o1; R.l0 = l0; R.l1 = l1; R.m0 = m0; R.m1 = m1;
}

__device__ __forceinline__ void attn_fast(lds_ptr lds, unsigned lds0, const char* Qg, const char* Kg, const char* Vg, const G2& g2, const Flags& flags, int qkv0, int qkvword, int depword,
                                          int hsig, int c0, bool bump, const HPend& hp, const char* wpd, const char* wpo, int prow, int wid, AttnOut& R
                                          ) {
    const int lane = opaque_tid() & 63;
    AttnLane A; A.init(lds, Qg, wid, lane);
    const lds_ptr kb0 = A.kb0, kb1 = A.kb1, vb = A.vb;
    const bf16x8 ones = A.ones;
    const __amdgpu_buffer_rsrc_t hr = __builtin_amdgcn_make_buffer_rsrc((void*)hp.base, 0, 8192, 0x00020000);
    f32x16 o0 = (f32x16){}, o1 = (f32x16){}, negm0 = (f32x16){}, negm1 = (f32x16){}, sA0, sA1, sB0, sB1;
    f32x4 l0 = (f32x4){0.f, 0.f, 0.f, 0.f}, l1 = l0;
    bf16x8 kfa, kfb, va, vc = (bf16x8){}, pf = (bf16x8){};
    PIN(o0); PIN(o1); PIN(l0); PIN(l1); PIN(vc); PIN(pf); PIN(A.voffK); PIN(A.voffV); PIN(A.qp);
    unsigned gA0, gA1, gB0, gB1; g2_voff(lane, gA0, gA1, gB0, gB1);
    unsigned gB = (wid & 1) ? gB1 : gB0;
    unsigned pfo = (unsigned)(((lane >> 3) & 1) * (DM * 2) + (lane & 7) * 128);
    const u64* hptr = flags.p + (F_H + depword) * 16;
    const u64* qptr = flags.p + qkvword * 16;
    const char* qp2 = A.qp + 65536;
    PIN(gA0); PIN(gA1); PIN(gB); PIN(pfo); PIN(hptr); PIN(qptr); PIN(qp2);
    if (wid == 0) {
        wait_all(flags, qkv0, peek(flags, qkv0));
        STAMP(13);
    }
    CFENCE(); BAR(); CFENCE();
    STAMP(4);
    bf16x8 q00, q01, q10, q11;
    AT_LOAD_K(c0, 0); AT_Q_LOAD(); AT_LOAD_V(c0, 0);
    u64 hv = 0;
    float m0, m1;
    AT_Q_WAIT(2);
    PEEK_AHEAD(hv, qptr, wid);
    BAR(); SBAR();
    STAMP(15);
    {
        kfa = lds_b128(kb0); kfb = lds_b128(kb1);
        sA0 = MFMA32(kfa, q00, negm0); sA1 = MFMA32(kfa, q10, negm1); sA0 = MFMA32(kfb, q01, sA0); sA1 = MFMA32(kfb, q11, sA1);
        kfa = lds_b128(kb0 + 2048); kfb = lds_b128(kb1 + 2048);
        m0 = rowmax16(sA0); m1 = rowmax16(sA1);
#pragma unroll
        for (int r = 0; r < 16; ++r) { sA0[r] -= m0; sA1[r] -= m1; negm0[r] = -m0; negm1[r] = -m1; }
        PIN(negm0); PIN(negm1);
    }
    u32x4 hA = hp.v[0], hB = hp.v[1], hC = hp.v[2], hD = hp.v[3];
    unsigned hoff = hp.off, pfd = 0;
#define AT_HST(v) do { __builtin_amdgcn_raw_buffer_store_b128(v, hr, hoff, 0, 16); hoff += hp.step; CFENCE(); } while (0)
    PEEK_DONE(hv);
    if (wid == 0) {
        wait_all(flags, qkvword, hv);
        if (bump) signal(flags, F_EPOCH, lane);
        STAMP(14);
    }
    CFENCE(); BAR(); SBAR();
    STAMP(5);
    AT_LOAD_K((c0 + 1) & 7, 1); AT_LOAD_V((c0 + 1) & 7, 1);
#define HS 1
    hoff += 8192;
    STAMP(19);
#define AT_GROUP(QK, RD, S, i, O, L, VF) do { \
        QK; SBAR(); RD; SBAR(); \
        S[i] = EX(S[i]); S[i + 1] = EX(S[i + 1]); S[i + 2] = EX(S[i + 2]); PIN(S); SBAR(); \
        O = MFMA32(VF, pf, O); SBAR(); \
        S[i + 3] = EX(S[i + 3]); S[i + 4] = EX(S[i + 4]); S[i + 5] = EX(S[i + 5]); PIN(S); SBAR(); \
        L = MFMA16(ones, pf, L); SBAR(); \
        S[i + 6] = EX(S[i + 6]); S[i + 7] = EX(S[i + 7]); \
        pf = AT_PACK(S, i); PIN(pf); SBAR(); } while (0)
#define AT_STEP(C0, C1, N0, N1, KO, VO) do { \
        AT_GROUP(N0 = MFMA32(kfa, q00, negm0), va = cat8(lds_tr(vb + (VO)), lds_tr(vb + (VO) + 512)), C0, 0, o1, l1, vc); \
        AT_GROUP(N1 = MFMA32(kfa, q10, negm1), { kfa = lds_b128(kb0 + (KO)); vc = cat8(lds_tr(vb + (VO) + 1024), lds_tr(vb + (VO) + 1536)); }, C1, 0, o0, l0, va); \
        AT_GROUP(N0 = MFMA32(kfb, q01, N0), (void)0, C0, 8, o1, l1, va); \
        AT_GROUP(N1 = MFMA32(kfb, q11, N1), kfb = lds_b128(kb1 + (KO)), C1, 8, o0, l0, vc); } while (0)
#pragma unroll 1
    for (int c = 0; c < 5; ++c) {
        const int so = (c & 3) * SLOT, sn = ((c + 1) & 3) * SLOT;
        if (c == HS) hoff = hp.off;
        AT_HST(hA);
        AT_STEP(sA0, sA1, sB0, sB1, so + 4096, so);
        AT_HST(hB);
        AT_STEP(sB0, sB1, sA0, sA1, so + 6144, so + 2048);
        if (c == 0) WAIT_V(2); else WAIT_V(5);
        WAIT_L0(); BAR(); SBAR();
        STAMP(20 + c);
        if (c == HS + 3 && wid == 0) signal(flags, hsig, lane);
        AT_LOAD_K((c0 + c + 2) & 7, (c + 2) & 3); AT_LOAD_V((c0 + c + 2) & 7, (c + 2) & 3); AT_HST(hC);
        {
            const int pi = (wid + 8 * c < 36) ? wid + 8 * c : 35, lr = prow + 2 * pi;
            const char* pp = (lr < 1792 ? wpd : wpo) + (size_t)lr * (DM * 2);
            asm volatile("global_load_dword %0, %1, %2" : "+v"(pfd) : "v"(pfo), "s"(pp) : "memory");
        }
        AT_STEP(sA0, sA1, sB0, sB1, sn, so + 4096);
        AT_HST(hD);
        AT_STEP(sB0, sB1, sA0, sA1, sn + 2048, so + 6144);
        if (c >= HS) { hA = hp.v[4]; hB = hp.v[5]; hC = hp.v[6]; hD = hp.v[7]; }
    }
#pragma unroll 1
    for (int c = 5; c < 8; ++c) {
        const int so = (c & 3) * SLOT, sn = (c == 7) ? so : ((c + 1) & 3) * SLOT;
        const int sel = (c ^ 6) | wid;
        if (sel == 0) { PEEK_DONE(hv); wait_all(flags, F_H + depword, hv); }
        if (c == 5) g2_load_b(g2, 0, lds0 + 4 * SLOT, wid, gB);
        if (c == 6) g2_load_b(g2, 1, lds0, wid, gB);
        if (c == 7) g2_load_a(g2, 1, lds0, wid, gA0, gA1);
        AT_STEP(sA0, sA1, sB0, sB1, so + 4096, so);

        AT_STEP(sB0, sB1, sA0, sA1, so + 6144, so + 2048);
        WAIT_V(0); WAIT_L0(); BAR(); SBAR();
        STAMP(20 + c);
        if (c == 5) { AT_LOAD_K((c0 + 7) & 7, 3); AT_LOAD_V((c0 + 7) & 7, 3); }
        if (c == 6) { g2_load_a(g2, 0, lds0 + 4 * SLOT, wid, gA0, gA1); }
        if (c == 7) g2_load_a(g2, 2, lds0 + SLOT, wid, gA0, gA1);
        AT_STEP(sA0, sA1, sB0, sB1, sn, so + 4096);
        if (c == 6) g2_load_b(g2, 2, lds0 + SLOT, wid, gB);
        PEEK_AHEAD(hv, hptr, (c ^ 5) | wid);
        AT_STEP(sB0, sB1, sA0, sA1, sn + 2048, so + 6144);
    }
    STAMP(28);
    o1 = MFMA32(vc, pf, o1); l1 = MFMA16(ones, pf, l1);
    asm volatile("" :: "v"(pfd));
    R.o0 = o0; R.o1 = o1; R.l0 = l0; R.l1 = l1; R.m0 = m0; R.m1 = m1;
}

__global__ __launch_bounds__(512, 2) void k_main(const bf16_t* __restrict__ x, const bf16_t* __restrict__ wqkv, const bf16_t* __restrict__ wo, const bf16_t* __restrict__ wg,
                                                 const bf16_t* __restrict__ wu, const bf16_t* __restrict__ wd, bf16_t* __restrict__ out, unsigned char* ws) {
    __shared__ __attribute__((aligned(16))) unsigned char smem[LDS_BYTES];
    const lds_ptr lds = (lds_ptr)smem;
    const unsigned lds0 = (unsigned)(uintptr_t)lds;
    const int wid = __builtin_amdgcn_readfirstlane(threadIdx.x >> 6);
    const int bid = blockIdx.x, xcd = bid & 7, idx = bid >> 3;
    Flags flags; flags.p = (u64*)(ws + WS_FLAGS);
    const __amdgpu_buffer_rsrc_t wsr = __builtin_amdgcn_make_buffer_rsrc((void*)ws, 0, WS_END, 0x00020000);
#define ST16(v, off) __builtin_amdgcn_raw_buffer_store_b128(v, wsr, off, 0, 16)
    STAMP(0);
    unsigned cpd = 0;
    {
        const char* cbase; unsigned clen;
        asm volatile("s_getpc_b64 %0\n.Lk_begin:\n\ts_mov_b32 %1, .Lk_end-.Lk_begin" : "=s"(cbase), "=s"(clen));
        unsigned cpo = (unsigned)(idx * 16 + (threadIdx.x & 15)) * 128u;
        cpo = (cpo < clen) ? cpo : 0u;
        asm volatile("global_load_dword %0, %1, %2" : "+v"(cpd) : "v"(cpo), "s"(cbase) : "memory");
    }
    HPend hp;
    {
        const int lane = threadIdx.x & 63, wr = wid >> 2, wc = wid & 3, js = idx & 3, rb = idx >> 2;
        const bool typeY = (js == 3);
        const int f0 = 448 * xcd + 128 * js;
        const char* P0 = (const char*)(wg + f0);
        const char* P2 = (const char*)(wu + f0);
        const char* P1 = typeY ? (const char*)(wqkv + 128 * xcd + 64) : (const char*)(wg + f0 + 64);
        const char* P3 = typeY ? (const char*)(wqkv + 1024 + 128 * xcd + 64) : (const char*)(wu + f0 + 64);
        const char* P4 = typeY ? (const char*)(wqkv + 2048 + 128 * xcd + 64) : (const char*)(wqkv + js * 1024 + 128 * xcd);
        const unsigned stepF = 32u * FF * 2u, stepQ = 32u * NQKV * 2u, step13 = typeY ? stepQ : stepF;
        const char* XA = (const char*)x + (size_t)rb * 256 * DM * 2;
        const char* XC = (const char*)ws + WS_XC + (size_t)rb * 256 * DM * 2;
        u64 ep; u32x4 xa0, xa1;
        const int srow = rb * 256 + (js * 8 + xcd) * 8 + wid;
        asm volatile("global_load_dwordx4 %1, %5, %6\n\tglobal_load_dwordx4 %2, %5, %6 offset:1024\n\tglobal_load_dwordx2 %0, %3, %4 sc1"
                     : "=&v"(ep), "=&v"(xa0), "=&v"(xa1)
                     : "v"(0u), "s"(flags.p), "v"((unsigned)(lane * 16)), "s"((const char*)x + (size_t)srow * DM * 2) : "memory");
        const int r8 = lane >> 3, u8 = lane & 7;
        const int gw = (r8 & 2) | ((wc & 1) << 2);
        const unsigned voffF = (unsigned)(((wc * 8 + r8) * FF + (u8 ^ gw) * 8) * 2);
        const unsigned voffQ = (unsigned)(((wc * 8 + r8) * NQKV + (u8 ^ gw) * 8) * 2);
        const unsigned voff13 = typeY ? voffQ : voffF;
        const unsigned voffA0 = (unsigned)(((32 * wid + r8) * DM + (u8 ^ (r8 >> 1)) * 8) * 2);
        const unsigned voffA1 = (unsigned)(((32 * wid + 8 + r8) * DM + (u8 ^ (4 | (r8 >> 1))) * 8) * 2);
        const unsigned dstA = lds0 + G1_A + wid * 4096, dstW = lds0 + G1_W + wc * 1024;
#define G1_LOAD_A(t) do { const char* b_ = XA + (t) * 128; const unsigned d_ = dstA + ((t) & 1) * 32768; \
        glds16(b_, voffA0, d_); glds16(b_, voffA1, d_ + 1024); glds16(b_ + 32768, voffA0, d_ + 2048); glds16(b_ + 32768, voffA1, d_ + 3072); } while (0)
#define G1_LOAD_W(hs) do { const unsigned d_ = dstW + ((hs) & 3) * 4096; \
        glds16(P0 + (size_t)(hs) * stepF, voffF, d_); glds16(P1 + (size_t)(hs) * step13, voff13, d_ + 16384); glds16(P2 + (size_t)(hs) * stepF, voffF, d_ + 32768); \
        glds16(P3 + (size_t)(hs) * step13, voff13, d_ + 49152); glds16(P4 + (size_t)(hs) * stepQ, voffQ, d_ + 65536); } while (0)
        STAMP(29);
        if (wr == 1) G1_LOAD_W(0);
        G1_LOAD_A(0);
        STAMP(30);

        int fr = lane & 15, fq = lane >> 4;
        const lds_ptr aB0 = lds + G1_A + (wr * 128 + fr) * 128 + ((fq ^ (fr >> 1)) * 16);
        const lds_ptr aB1 = lds + G1_A + (wr * 128 + fr) * 128 + (((4 | fq) ^ (fr >> 1)) * 16);
        const int tq = (lane >> 2) & 3, tp = lane & 3, tg = (tq & 2) | ((fq & 1) << 2);
        auto wbase = [&](int panel, int bp) { return lds + G1_W + panel * 16384 + (8 * fq + tq) * 128 + (((2 * bp + (tp >> 1)) ^ tg) * 16) + (tp & 1) * 8; };
        const int pX = wc >> 1, bX = 2 * (wc & 1);
        const lds_ptr wB0 = typeY ? wbase(0, wc) : wbase(pX, bX);
        const lds_ptr wB1 = typeY ? wbase(1, wc) : wbase(pX, bX + 1);
        const lds_ptr wB2 = typeY ? wbase(2, wc) : wbase(2 + pX, bX);
        const lds_ptr wB3 = typeY ? wbase(3, wc) : wbase(2 + pX, bX + 1);
        const lds_ptr wB4 = wbase(4, wc);
        if (threadIdx.x == 0) *(LAS unsigned*)(lds + G1_CNT) = 0u;
        f32x4 acc[8][5];
#pragma unroll
        for (int m = 0; m < 8; ++m)
#pragma unroll
            for (int b = 0; b < 5; ++b) { acc[m][b] = (f32x4){0.f, 0.f, 0.f, 0.f}; PIN(acc[m][b]); }
        asm volatile("s_cmp_lg_u32 %3, 0\n\ts_cbranch_scc1 1f\n\ts_waitcnt vmcnt(4)\n1:\n\ts_waitcnt vmcnt(9)" : "+v"(ep), "+v"(xa0), "+v"(xa1) : "s"(wr), "v"(cpd) : "memory", "scc");
        STAMP(31);
        {
            const unsigned elo = __builtin_amdgcn_readfirstlane((unsigned)ep), ehi = __builtin_amdgcn_readfirstlane((unsigned)(ep >> 32));
            flags.tag = (((u64)ehi << 32) | elo) * 0x9E3779B97F4A7C15ull + 0x632BE59BD9B4E019ull;
        }
        const unsigned tag32 = (unsigned)flags.tag;
        {
            const f32x2 st = row_stats(xa0, xa1);
            G1_LOAD_W(1 + wr);
            ST16(xa0, (lane >= 32) ? WS_XC + (unsigned)srow * 2048u + lane * 16 : WS_END);
            ST16(xa1, WS_XC + (unsigned)srow * 2048u + 1024 + lane * 16);
            ST16(((u32x4){f2u(st.x), tag32, f2u(st.y), tag32}), (lane == 0) ? WS_STAT + (unsigned)srow * 16u : WS_END);
            CFENCE();
        }
        STAMP(32);
        bf16x8 af[8], wf[5];
        float cs0 = 0.f, cs1 = 0.f, cs2 = 0.f;
#define CS1(c, w) "v_dot2c_f32_bf16 %" #c ", %25, %" #w "\n\t"
#define G1_CSUM(t, lim) do { const u32x4 w0_ = __builtin_bit_cast(u32x4, wf[0]), w1_ = __builtin_bit_cast(u32x4, wf[1]), w2_ = __builtin_bit_cast(u32x4, wf[2]), \
            w3_ = __builtin_bit_cast(u32x4, wf[3]), w4_ = __builtin_bit_cast(u32x4, wf[4]); \
        asm volatile("s_cmp_gt_u32 %24, %26\n\ts_cbranch_scc1 2f\n\ts_cmp_lg_u32 %23, 0\n\ts_cbranch_scc1 1f\n\t" \
            CS1(0, 3) CS1(1, 7) CS1(2, 19) CS1(0, 4) CS1(1, 8) CS1(2, 20) CS1(0, 5) CS1(1, 9) CS1(0, 6) CS1(1, 10) "s_nop 1\n\ts_branch 2f\n1:\n\t" \
            CS1(0, 11) CS1(1, 15) CS1(2, 21) CS1(0, 12) CS1(1, 16) CS1(2, 22) CS1(0, 13) CS1(1, 17) CS1(0, 14) CS1(1, 18) "s_nop 2\n2:" \
            : "+v"(cs0), "+v"(cs1), "+v"(cs2) \
            : "v"(w0_[0]), "v"(w0_[1]), "v"(w0_[2]), "v"(w0_[3]), "v"(w1_[0]), "v"(w1_[1]), "v"(w1_[2]), "v"(w1_[3]), "v"(w2_[0]), "v"(w2_[1]), "v"(w2_[2]), "v"(w2_[3]), \
              "v"(w3_[0]), "v"(w3_[1]), "v"(w3_[2]), "v"(w3_[3]), "v"(w4_[0]), "v"(w4_[1]), "v"(w4_[2]), "v"(w4_[3]), "s"(wr), "s"(t), "s"(0x3f803f80u), "i"(lim) : "scc"); } while (0)
#define G1_RW(i, B, slot) wf[i] = cat8(lds_tr(B + (slot) * 4096), lds_tr(B + (slot) * 4096 + 512))
#define G1_RA(m, AB, abuf) af[m] = lds_b128(AB + (abuf) * 32768 + (m) * 2048)
#define G1_SEG(AB, abuf, slot, L0, L1, L2, L3, L4, CLOSE) do { \
        L0; G1_RW(0, wB0, slot); G1_RW(1, wB1, slot); L1; G1_RW(2, wB2, slot); G1_RW(3, wB3, slot); L2; G1_RW(4, wB4, slot); G1_RA(0, AB, abuf); G1_RA(1, AB, abuf); \
        L3; G1_RA(2, AB, abuf); G1_RA(3, AB, abuf); G1_RA(4, AB, abuf); L4; G1_RA(5, AB, abuf); G1_RA(6, AB, abuf); G1_RA(7, AB, abuf); \
        SBAR(); G1_CSUM(t, 3); WAIT_L0(); SBAR(); if (CLOSE) { BAR(); SBAR(); } } while (0)
#define G1_SEG_A(abuf, slot, tt) do { const char* b_ = ((tt) < 4 ? XA : XC) + (tt) * 128; const unsigned d_ = dstA + (1 - (abuf)) * 32768; \
        G1_SEG(aB0, abuf, slot, glds16c(b_, voffA0, d_), glds16c(b_, voffA1, d_ + 1024), glds16c(b_ + 32768, voffA0, d_ + 2048), glds16c(b_ + 32768, voffA1, d_ + 3072), (void)0, true); } while (0)
#define G1_SEG_W(abuf, slot, hs, dslot, CLOSE) do { const unsigned d_ = dstW + (dslot) * 4096; \
        G1_SEG(aB1, abuf, slot, glds16(P0 + (size_t)(hs) * stepF, voffF, d_), glds16(P1 + (size_t)(hs) * step13, voff13, d_ + 16384), glds16(P2 + (size_t)(hs) * stepF, voffF, d_ + 32768), \
               glds16(P3 + (size_t)(hs) * step13, voff13, d_ + 49152), glds16(P4 + (size_t)(hs) * stepQ, voffQ, d_ + 65536), CLOSE); } while (0)
#define G1_MMA() do { __builtin_amdgcn_s_setprio(1); \
        _Pragma("unroll") for (int b = 0; b < 5; ++b) _Pragma("unroll") for (int m = 0; m < 8; ++m) MFMA16_ACC(acc[m][b], wf[b], af[m]); \
        __builtin_amdgcn_s_setprio(0); SBAR(); } while (0)

        STAMP(33);
        if (wr == 1) { WAIT_V(12); BAR(); }
        WAIT_V(8); WAIT_L0();
        STAMP(34);
        BAR();
        SBAR();
        STAMP(1);
        u64 sg = 0;
        const u64* sgp = flags.p + (F_XC + rb * 32 + (lane & 31)) * 16;
#pragma unroll 1
        for (int t = 0; t < 16; t += 2) {
            const int h1 = (2 * t + 3 + wr < 31) ? 2 * t + 3 + wr : 31, h2 = (2 * t + 5 + wr < 31) ? 2 * t + 5 + wr : 31, t2 = (t < 14) ? t + 2 : 15;
            const int sel = (t == 12) ? 0 : ((t ^ 2) | wid);
            STAMP(35 + (t >> 1));
            PEEK_AHEAD(sg, sgp, sel);
            G1_SEG_A(0, 0, t + 1); G1_MMA(); asm volatile("s_waitcnt vmcnt(4)" : "+v"(sg), "+v"(cpd) :: "memory"); BAR(); SBAR();
            if (t == 0 && wid == 4) signal(flags, F_XC + bid, lane);
            if (sel == 0) {
                const u64 want = (t == 12) ? ((u64)tag32 << 32) : flags.tag, mask = (t == 12) ? 0xffffffff00000000ull : ~0ull;
#pragma unroll 1
                for (unsigned spin = 0; spin < (1u << 20) && !__all((sg & mask) == want); ++spin)
                    asm volatile("s_sleep 1\n\tglobal_load_dwordx2 %0, %1, off sc1\n\ts_waitcnt vmcnt(0)" : "+v"(sg) : "v"(sgp) : "memory");
                const int tl = opaque_tid() & 63;
                if (t == 12) *(LAS unsigned*)(lds + G1_STAT + (wid * 32 + (tl & 31)) * 8 + (tl >> 5) * 4) = (unsigned)sg;
            }
            G1_SEG_W(0, 1, h1, (3 + wr) & 3, true); G1_MMA(); WAIT_V(5); BAR(); SBAR();
            G1_SEG_A(1, 2, t2); G1_MMA(); WAIT_V(4); BAR(); SBAR();
            G1_SEG_W(1, 3, h2, (1 + wr) & 3, false);
            if (t == 2) {
                const int tl = opaque_tid() & 63;
                const lds_ptr cp = lds + G1_CS + wc * 384 + (tl & 15) * 4;
                *(LAS float*)(cp + wr * 128) = red4(cs0); *(LAS float*)(cp + wr * 128 + 64) = red4(cs1); *(LAS float*)(cp + 256 + wr * 64) = red4(cs2);
                sgp = (const u64*)(ws + WS_STAT) + (rb * 256 + wid * 32 + (tl & 31)) * 2 + (tl >> 5);
                WAIT_L0();
            }
            SBAR(); BAR(); SBAR(); G1_MMA(); WAIT_V(5);
            if (wr == 0 || t < 14) BAR();
            SBAR();
        }
        MFMA_DRAIN(); SBAR();
        STAMP(2);

        const int tz = opaque_tid(), ln = tz & 63;
        fr = tz & 15; fq = (tz >> 4) & 3;
        const int row0 = rb * 256 + wr * 128 + fr;
        f32x4 csv[5]; f32x2 st[8];
#pragma unroll
        for (int b = 0; b < 5; ++b) csv[b] = *(const LAS f32x4*)(lds + G1_CS + wc * 384 + b * 64 + fq * 16);
        csv[4] += *(const LAS f32x4*)(lds + G1_CS + wc * 384 + 320 + fq * 16);
#pragma unroll
        for (int m = 0; m < 8; ++m) st[m] = *(const LAS f32x2*)(lds + G1_STAT + (wr * 128 + m * 16 + fr) * 8);
        auto lnv = [&](int m, int b, float sc) { return (acc[m][b] - st[m].x * csv[b]) * (st[m].y * sc); };
        auto glu = [&](int m, int bg, int bu) {
            const f32x4 g = lnv(m, bg, -LOG2E), u = lnv(m, bu, 1.0f);
            f32x4 h;
#pragma unroll
            for (int j = 0; j < 4; ++j) h[j] = u[j] * __builtin_amdgcn_rcpf(1.0f + __builtin_amdgcn_exp2f(g[j]));
            return h;
        };
        auto pack2 = [&](const f32x4& v0, const f32x4& v1) {
            unsigned p0 = cvtpk(v0[0], v0[1]), p1 = cvtpk(v0[2], v0[3]), q0 = cvtpk(v1[0], v1[1]), q1 = cvtpk(v1[2], v1[3]);
            swap16(p0, q0); swap16(p1, q1);
            return (u32x4){p0, p1, q0, q1};
        };
        const int hd = 4 * xcd + (typeY ? 2 : 0) + (wc >> 1);
        const unsigned qoff = WS_QKV + (unsigned)(hd * 2 + (wc & 1)) * 65536u + (unsigned)(row0 + (fq & 1) * 16) * 32u + (fq >> 1) * 16;
        constexpr unsigned KIND = NH * 2 * 65536u;
        if (!typeY) {
            const float sc4 = (js == 0) ? QSCALE : 1.0f;
#pragma unroll
            for (int m = 0; m < 8; m += 2) ST16(pack2(lnv(m, 4, sc4), lnv(m + 1, 4, sc4)), qoff + js * KIND + m * 512);
            CFENCE();
            hp.base = ws + WS_ACT + (unsigned)(14 * xcd + 4 * js + wc) * GRP + (unsigned)(rb * 256 + wr * 128) * 64u;
            hp.off = fr * 64 + (fq & 1) * 32 + (fq >> 1) * 16; hp.step = 1024;
#pragma unroll
            for (int m = 0; m < 8; ++m) {
                hp.v[m] = pack2(glu(m, 0, 2), glu(m, 1, 3));
                if (m == 5) { PIN(hp.v[5]); WAIT_V(0); arrive(flags, F_QKV + bid, ln, lds + G1_CNT, 7u); }
            }
        } else {
#pragma unroll
            for (int m = 0; m < 8; m += 2) {
                ST16(pack2(lnv(m, 3, 1.0f), lnv(m + 1, 3, 1.0f)), qoff + KIND + m * 512);
                ST16(pack2(lnv(m, 4, 1.0f), lnv(m + 1, 4, 1.0f)), qoff + 2 * KIND + m * 512);
                ST16(pack2(lnv(m, 1, QSCALE), lnv(m + 1, 1, QSCALE)), qoff + m * 512);
            }
            CFENCE();
            hp.base = ws + WS_ACT + (unsigned)(14 * xcd + 12 + (wc >> 1)) * GRP + (unsigned)(rb * 256 + wr * 128) * 64u;
            hp.off = (fr + (fq & 1) * 16) * 64 + (wc & 1) * 32 + (fq >> 1) * 16; hp.step = 2048;
#pragma unroll
            for (int m = 0; m < 8; m += 2) { hp.v[m >> 1] = pack2(glu(m, 0, 2), glu(m + 1, 0, 2)); hp.v[4 + (m >> 1)] = hp.v[m >> 1]; }
            PIN(hp.v[3]); WAIT_V(0); arrive(flags, F_QKV + bid, ln, lds + G1_CNT, 7u);
        }
#pragma unroll
        for (int i = 0; i < 8; ++i) PIN(hp.v[i]);
        STAMP(3);
    }

    const int z = xcd & 1, rbk = ((xcd >> 1) & 1) * 8 + (idx & 7), cbk = (xcd >> 2) * 4 + (idx >> 3);
    G2 g2;
    g2.Ah = (const char*)ws + WS_ACT + (size_t)z * 56 * GRP + rbk * 128 * 64;
    g2.Ao = (const char*)ws + WS_ACT + (size_t)(112 + z * 16 - 56) * GRP + rbk * 128 * 64;
    g2.Wd = (const char*)(wd + (size_t)z * 1792 * DM + cbk * 128);
    g2.Wo = (const char*)(wo + (size_t)z * 512 * DM + cbk * 128) - (size_t)1792 * DM * 2;
    {
        const int hl = idx & 3, qb = idx >> 2, h = 4 * xcd + hl;
        const int l0_ = opaque_tid() & 63;
        const int qkv0 = F_QKV + xcd + 8 * (qb * 4 + ((hl >= 2) ? 3 : l0_ % 3));
        const int qkvword = F_QKV + xcd + 8 * ((l0_ & 7) * 4 + ((hl >= 2) ? 3 : 1 + ((l0_ >> 3) & 1)));
        const int depword = (rbk >> 1) * 32 + ((l0_ >> 2) & 3) * 8 + 4 * z + (l0_ & 3);
        const char* Qg = (const char*)ws + WS_QKV + (size_t)h * 131072 + qb * 256 * 32;
        const char* Kg = (const char*)ws + WS_QKV + (size_t)(NH + h) * 131072;
        const char* Vg = (const char*)ws + WS_QKV + (size_t)(2 * NH + h) * 131072;
        const char* wpd = g2.Wd - (cbk & 3) * 256;
        const char* wpo = g2.Wo - (cbk & 3) * 256;
        AttnOut R;
#define DG
        attn_fast(lds, lds0, Qg, Kg, Vg, g2, flags, qkv0, qkvword, depword, F_H + bid, qb, bid == 0, hp, wpd, wpo, idx * 72, wid, R DG);
        STAMP(6);
        const int tz = opaque_tid(), ln = tz & 63, w4 = wid & 3, fr = ln & 15, fq = ln >> 4;
        const lds_ptr po = lds + 2 * SLOT + w4 * 8192 + ln * 16, pl = lds + 3 * SLOT + 4096 + w4 * 1024 + ln * 16;
        volatile LAS unsigned* const badw = (volatile LAS unsigned*)(lds + 3 * SLOT + 8192 + 4096);
#define AT_PUBLISH() do { if (wid < 4) { \
            _Pragma("unroll") for (int i = 0; i < 4; ++i) { *(LAS f32x4*)(po + i * 1024) = (f32x4){R.o0[4 * i], R.o0[4 * i + 1], R.o0[4 * i + 2], R.o0[4 * i + 3]}; \
                *(LAS f32x4*)(po + 4096 + i * 1024) = (f32x4){R.o1[4 * i], R.o1[4 * i + 1], R.o1[4 * i + 2], R.o1[4 * i + 3]}; } \
            *(LAS f32x4*)pl = (f32x4){R.l0[0], R.m0, R.l1[0], R.m1}; } } while (0)
        {
            AT_PUBLISH();
            const f32x16 z16 = R.o0 * 0.0f + R.o1 * 0.0f;
            float zs = 0.f;
#pragma unroll
            for (int r = 0; r < 16; ++r) zs += z16[r];
            const bool ok = (zs == 0.f) && (R.l0[0] > 0.f) && (R.l0[0] < 0x1p96f) && (R.l1[0] > 0.f) && (R.l1[0] < 0x1p96f);
            const bool wave_bad = !__all(ok);
            if (ln == 0) badw[wid] = wave_bad ? 1u : 0u;
            STAMP(16);
            WAIT_V(0); STAMP(17); WAIT_L0(); BAR(); CFENCE();
            const unsigned anybad = badw[ln & 7];
            if (__any(anybad != 0u)) {
                WAIT_L0(); BAR(); CFENCE();
                attn_safe(lds, lds0, Qg, Kg, Vg, wid, R);
                AT_PUBLISH();
                WAIT_L0(); BAR(); CFENCE();
            }
        }
        STAMP(7);

        const int tile = rbk * 8 + cbk;
        if (wid >= 4) {
            unsigned vA0, vA1, vB0, vB1; g2_voff(ln, vA0, vA1, vB0, vB1);
#define G2_LOAD(j, sl) do { const unsigned d_ = lds0 + (sl) * SLOT; g2_load_b(g2, j, d_, 2 * w4, vB0); g2_load_a(g2, j, d_, 2 * w4, vA0, vA1); \
                g2_load_b(g2, j, d_, 2 * w4 + 1, vB1); g2_load_a(g2, j, d_, 2 * w4 + 1, vA0, vA1); } while (0)
            const f32x4 lm = *(const LAS f32x4*)pl;
            f32x4 t[8];
#pragma unroll
            for (int i = 0; i < 8; ++i) t[i] = *(const LAS f32x4*)(po + i * 1024);
            WAIT_L0(); BAR(); CFENCE();
            G2_LOAD(3, 2);
            u32x4 ow[4];
            const unsigned ooff = WS_ACT + (unsigned)(112 + h) * GRP + (unsigned)(qb * 256 + w4 * 64 + (ln & 31)) * 64u + (ln >> 5) * 16;
#pragma unroll
            for (int q = 0; q < 2; ++q) {
                BAR();
                if (q == 0) G2_LOAD(4, 3); else G2_LOAD(5, 4);
                const f32x16 ok_ = q ? R.o1 : R.o0;
                const float lk = q ? R.l1[0] : R.l0[0], mk = q ? R.m1 : R.m0, lo = lm[2 * q], mo = lm[2 * q + 1];
                const float mm = __builtin_fmaxf(mk, mo), fk = __builtin_amdgcn_exp2f(mk - mm), fo = __builtin_amdgcn_exp2f(mo - mm);
                const float rl = 1.0f / (lk * fk + lo * fo), ck = fk * rl, co = fo * rl;
                unsigned p[8];
#pragma unroll
                for (int i = 0; i < 4; ++i) {
                    const f32x4 u = t[4 * q + i];
                    p[2 * i] = cvtpk(ok_[4 * i] * ck + u[0] * co, ok_[4 * i + 1] * ck + u[1] * co);
                    p[2 * i + 1] = cvtpk(ok_[4 * i + 2] * ck + u[2] * co, ok_[4 * i + 3] * ck + u[3] * co);
                }
                swap32(p[0], p[2]); swap32(p[1], p[3]); swap32(p[4], p[6]); swap32(p[5], p[7]);
                ow[2 * q] = (u32x4){p[0], p[1], p[2], p[3]}; ow[2 * q + 1] = (u32x4){p[4], p[5], p[6], p[7]};
                PIN(ow[2 * q]); PIN(ow[2 * q + 1]);
            }
#pragma unroll
            for (int i = 0; i < 4; ++i) ST16(ow[i], ooff + (i >> 1) * 2048 + (i & 1) * 32);
            CFENCE();
            unsigned t0 = 0, t1 = 0, t2 = 0, t3 = 0, a0 = 0, a1 = 0, a2 = 0;
            WAIT_V(20); BAR(); G2_LOAD(6, 0);
            WAIT_V(20); BAR(); G2_LOAD(7, 1);
            WAIT_V(20);
            int sl = 2;
#pragma unroll 1
            for (int j = 5; j < 33; ++j) {
                TS(t1);
                BAR();
                TS(t2);
                if (j == 6 && wid == 4) signal(flags, F_O + bid, ln);
                G2_LOAD(j + 3, sl);
                sl = (sl == 4) ? 0 : sl + 1;
                TS(t3);
                WAIT_V(16);
                TS(t0);
                a0 += t0 - t3; a1 += t2 - t1; a2 += t3 - t2;
            }
            PUT(50, a0); PUT(51, a1); PUT(52, a2);
            BAR();
            if (tz == 256) *(LAS unsigned*)lds = 0u;
            WAIT_V(8); WAIT_L0(); BAR(); WAIT_V(0); BAR();
                if (wid == 4) wait_all(flags, F_X + tile * 2 + z, peek(flags, F_X + tile * 2 + z));
            CFENCE(); BAR();
            asm volatile(".Lk_end:");
        } else {
            const int wm = w4 >> 1, wn = w4 & 1;
            const unsigned aO0 = (wm * 64 + fr) * 128 + ((fq ^ (fr >> 1)) * 16), aO1 = aO0 ^ 64;
            const int tq = (ln >> 2) & 3, tp = ln & 3, tg = (tq & 2) | ((fq & 1) << 2);
            unsigned wO[4];
#pragma unroll
            for (int b = 0; b < 4; ++b) wO[b] = 16384 + wn * 8192 + (8 * fq + tq) * 128 + (((2 * b + (tp >> 1)) ^ tg) * 16) + (tp & 1) * 8;
            f32x4 acc[4][4];
#pragma unroll
            for (int m = 0; m < 4; ++m)
#pragma unroll
                for (int b = 0; b < 4; ++b) acc[m][b] = (f32x4){0.f, 0.f, 0.f, 0.f};
            bf16x8 xa[4], ya[4] = {}; s16x4 xl[4], xh[4], yl[4] = {}, yh[4] = {};
            lds_ptr pa0, pa1, pw0, pw1, pw2, pw3;
#define G2_BASES(so) do { pa0 = lds + aO0 + (so); pa1 = lds + aO1 + (so); pw0 = lds + wO[0] + (so); pw1 = lds + wO[1] + (so); pw2 = lds + wO[2] + (so); pw3 = lds + wO[3] + (so); } while (0)
#define G2_M(C, m, b) do { MFMA16_ACC(acc[m][b], cat8(C##l[b], C##h[b]), C##a[m]); SBAR(); } while (0)
#define RDQ(x) (x)
#define G2_HALF(C, N, AB, WO, RD_, TAIL) do { const bool RD = RDQ(RD_); \
            G2_M(C, 0, 0); if (RD) { N##l[0] = lds_tr(pw0 + (WO)); N##h[0] = lds_tr(pw0 + (WO) + 512); } SBAR(); \
            G2_M(C, 1, 0); if (RD) N##a[0] = lds_b128(AB); SBAR(); \
            G2_M(C, 2, 0); if (RD) N##a[1] = lds_b128((AB) + 2048); SBAR(); \
            G2_M(C, 3, 0); if (RD) N##a[2] = lds_b128((AB) + 4096); SBAR(); \
            G2_M(C, 0, 1); if (RD) N##a[3] = lds_b128((AB) + 6144); SBAR(); \
            G2_M(C, 1, 1); if (RD) { N##l[1] = lds_tr(pw1 + (WO)); N##h[1] = lds_tr(pw1 + (WO) + 512); } SBAR(); \
            G2_M(C, 2, 1); if (RD) { N##l[2] = lds_tr(pw2 + (WO)); N##h[2] = lds_tr(pw2 + (WO) + 512); } SBAR(); \
            G2_M(C, 3, 1); if (RD) { N##l[3] = lds_tr(pw3 + (WO)); N##h[3] = lds_tr(pw3 + (WO) + 512); } SBAR(); \
            G2_M(C, 0, 2); TAIL(0); SBAR(); G2_M(C, 1, 2); TAIL(1); SBAR(); G2_M(C, 2, 2); TAIL(2); SBAR(); G2_M(C, 3, 2); TAIL(3); SBAR(); \
            G2_M(C, 0, 3); G2_M(C, 1, 3); G2_M(C, 2, 3); G2_M(C, 3, 3); } while (0)
            int so = 4 * SLOT;
            G2_BASES(so);
            xl[0] = lds_tr(pw0); xh[0] = lds_tr(pw0 + 512); SBAR();
#pragma unroll
            for (int m = 0; m < 4; ++m) { xa[m] = lds_b128(pa0 + m * 2048); SBAR(); }
            xl[1] = lds_tr(pw1); xh[1] = lds_tr(pw1 + 512); xl[2] = lds_tr(pw2); xh[2] = lds_tr(pw2 + 512); xl[3] = lds_tr(pw3); xh[3] = lds_tr(pw3 + 512);
            const u64* const optr = flags.p + (F_O + depword) * 16;
            u64 ov = 0; unsigned t0 = 0, t1 = 0, a0 = 0;
            STAMP(8);
#define G2_NEXT(i) do { if ((i) == 0) so = (so == 4 * SLOT) ? 0 : so + SLOT; \
                if ((i) == 1) { pa0 = lds + aO0 + so; pa1 = lds + aO1 + so; PIN(pa0); PIN(pa1); } \
                if ((i) == 2) { pw0 = lds + wO[0] + so; pw1 = lds + wO[1] + so; PIN(pw0); PIN(pw1); } \
                if ((i) == 3) { pw2 = lds + wO[2] + so; pw3 = lds + wO[3] + so; PIN(pw2); PIN(pw3); } } while (0)
#define G2_NONE(i) do {} while (0)
            WAIT_L0(); BAR(); SBAR();
#pragma unroll 1
            for (int ph = 0; ph < 3; ++ph) {
                const int n = (ph == 0) ? 11 : (ph == 1) ? 1 : 5;
#pragma unroll 1
                for (int i = 0; i < n; ++i) {
                    WAIT_L0(); SBAR(); G2_HALF(x, y, pa1, 4096, true, G2_NEXT);
                    WAIT_L0(); BAR(); SBAR(); G2_HALF(y, x, pa0, 0, true, G2_NONE);
                    WAIT_L0(); SBAR(); G2_HALF(x, y, pa1, 4096, true, G2_NEXT);
                    WAIT_L0(); TS(t0); BAR(); TS(t1); SBAR(); a0 += t1 - t0;
                    G2_HALF(y, x, pa0, 0, true, G2_NONE);
                }
                PEEK_AHEAD(ov, optr, wid | ph);
                if ((wid | (ph ^ 1)) == 0) { PEEK_DONE(ov); wait_all(flags, F_O + depword, ov); }
            }
            const int row = rbk * 128 + wm * 64 + z * 32 + fr, col = cbk * 128 + wn * 64 + (fq & 1) * 16 + (fq >> 1) * 8;
            u32x4 xr[4];
#pragma unroll
            for (int i = 0; i < 4; ++i) xr[i] = *(const u32x4*)(x + (size_t)(row + (i >> 1) * 16) * DM + col + (i & 1) * 32);
            WAIT_L0(); SBAR(); G2_HALF(x, y, pa1, 4096, true, G2_NEXT);
            WAIT_L0(); BAR(); SBAR(); G2_HALF(y, x, pa0, 0, true, G2_NONE);
            WAIT_L0(); SBAR(); G2_HALF(x, y, pa1, 4096, true, G2_NONE);
            WAIT_L0(); SBAR(); G2_HALF(y, x, pa0, 0, false, G2_NONE);
            MFMA_DRAIN(); SBAR();
            STAMP(9); PUT(53, a0);
            f32x4 vk[8];
            const unsigned xo = WS_XCH + (unsigned)(tile * 2 + 1 - z) * 32768u + w4 * 8192 + ln * 16;
            if (z == 0) {
#pragma unroll
                for (int i = 0; i < 8; ++i) { ST16(__builtin_bit_cast(u32x4, acc[2 + (i >> 2)][i & 3]), xo + i * 1024); vk[i] = acc[i >> 2][i & 3]; }
            } else {
#pragma unroll
                for (int i = 0; i < 8; ++i) { ST16(__builtin_bit_cast(u32x4, acc[i >> 2][i & 3]), xo + i * 1024); vk[i] = acc[2 + (i >> 2)][i & 3]; }
            }
            WAIT_V(0);
            arrive(flags, F_X + tile * 2 + 1 - z, ln, lds, 3u);
            STAMP(10);
            CFENCE(); BAR(); CFENCE();
            STAMP(11);
            const unsigned xi = WS_XCH + (unsigned)(tile * 2 + z) * 32768u + w4 * 8192 + ln * 16;
#pragma unroll
            for (int i = 0; i < 8; ++i) vk[i] += __builtin_bit_cast(f32x4, __builtin_amdgcn_raw_buffer_load_b128(wsr, xi + i * 1024, 0, 16));
#pragma unroll
            for (int i = 0; i < 4; ++i) {
                const u32x4 a = __builtin_bit_cast(u32x4, vk[2 * i]), b = __builtin_bit_cast(u32x4, vk[2 * i + 1]);
                unsigned a0_ = a[0], a1 = a[1], a2 = a[2], a3 = a[3], b0 = b[0], b1 = b[1], b2 = b[2], b3 = b[3];
                swap16(a0_, b0); swap16(a1, b1); swap16(a2, b2); swap16(a3, b3);
                const u32x4 xx = xr[i];
                u32x4 w;
                w[0] = cvtpk(u2f(a0_) + bf_lo(xx[0]), u2f(a1) + bf_hi(xx[0]));
                w[1] = cvtpk(u2f(a2) + bf_lo(xx[1]), u2f(a3) + bf_hi(xx[1]));
                w[2] = cvtpk(u2f(b0) + bf_lo(xx[2]), u2f(b1) + bf_hi(xx[2]));
                w[3] = cvtpk(u2f(b2) + bf_lo(xx[3]), u2f(b3) + bf_hi(xx[3]));
                __builtin_amdgcn_raw_buffer_store_b128(w, __builtin_amdgcn_make_buffer_rsrc((void*)out, 0, SEQ * DM * 2, 0x00020000), (unsigned)((row + (i >> 1) * 16) * DM + col + (i & 1) * 32) * 2u, 0, 2);
            }
        }
    }
}

extern "C" void kernel_launch(void* const* d_in, const int* in_sizes, int n_in, void* d_out, int out_size, void* d_ws, size_t ws_size, hipStream_t stream) {
    const bf16_t* x = (const bf16_t*)d_in[0];
    k_main<<<256, 512, 0, stream>>>(x, (const bf16_t*)d_in[1], (const bf16_t*)d_in[2], (const bf16_t*)d_in[4], (const bf16_t*)d_in[5], (const bf16_t*)d_in[6],
                                    (bf16_t*)d_out, (unsigned char*)d_ws);
}
```

```cpp
#include <hip/hip_runtime.h>
#include <stdint.h>

typedef unsigned short bf16_t;
typedef short bf16x8 __attribute__((ext_vector_type(8)));
typedef short s16x4 __attribute__((ext_vector_type(4)));
typedef float f32x2 __attribute__((ext_vector_type(2)));
typedef float f32x4 __attribute__((ext_vector_type(4)));
typedef float f32x16 __attribute__((ext_vector_type(16)));
typedef unsigned u32x4 __attribute__((ext_vector_type(4)));
typedef __bf16 bf16x2_t __attribute__((ext_vector_type(2)));
#define LAS __attribute__((address_space(3)))
#define GAS __attribute__((address_space(1)))
typedef LAS unsigned char* lds_ptr;

constexpr int SEQ = 2048, DM = 1024, FF = 3584, NQKV = 3072, NH = 32;
constexpr float LN_EPS = 1e-5f;
constexpr float LOG2E = 1.4426950408889634f;
constexpr float QSCALE = 0.17677669529663687f * LOG2E;

constexpr unsigned WS_FLAGS = 0;
constexpr unsigned WS_STAT = 192 * 1024;
constexpr unsigned WS_QKV = 256 * 1024;
constexpr unsigned WS_ACT = WS_QKV + 3u * SEQ * DM * 2;
constexpr unsigned WS_XCH = WS_ACT + 144u * SEQ * 64;
constexpr unsigned WS_XC = WS_XCH + 256u * 32768;
constexpr unsigned WS_END = WS_XC + SEQ * DM * 2;
constexpr int F_EPOCH = 0, F_QKV = 1, F_H = 257, F_O = 513, F_X = 769, F_XC = 1025;
constexpr unsigned GRP = SEQ * 64;

#define SBAR() __builtin_amdgcn_sched_barrier(0)
#define BAR() __builtin_amdgcn_s_barrier()
#define WAIT_V(n) asm volatile("s_waitcnt vmcnt(" #n ")" ::: "memory")
#define WAIT_L0() do { __builtin_amdgcn_s_waitcnt(0xC07F); asm volatile("" ::: "memory"); } while (0)
#define CFENCE() asm volatile("" ::: "memory")
#define PIN(x) asm volatile("" : "+v"(x))
#define MFMA16(a, b, c) __builtin_amdgcn_mfma_f32_16x16x32_bf16(a, b, c, 0, 0, 0)
#define MFMA32(a, b, c) __builtin_amdgcn_mfma_f32_32x32x16_bf16(a, b, c, 0, 0, 0)
#define MFMA16_ACC(acc, a, b) asm volatile("v_mfma_f32_16x16x32_bf16 %0, %1, %2, %0" : "+v"(acc) : "v"(a), "v"(b))
#define MFMA_DRAIN() asm volatile("s_nop 15" ::: "memory")
#define RLX_AGENT __ATOMIC_RELAXED, __HIP_MEMORY_SCOPE_AGENT

#define STAMP(i) do {} while (0)
#define TS(v) do {} while (0)
#define PUT(i, v) do {} while (0)

__device__ __forceinline__ void glds16(const void* sbase, unsigned voff, unsigned ldsdst) {
    asm volatile("s_mov_b32 m0, %2\n\ts_nop 0\n\tglobal_load_lds_dwordx4 %0, %1" ::"v"(voff), "s"(sbase), "s"(ldsdst) : "memory");
}
__device__ __forceinline__ void glds16c(const void* sbase, unsigned voff, unsigned ldsdst) {
    asm volatile("s_mov_b32 m0, %2\n\ts_nop 0\n\tglobal_load_lds_dwordx4 %0, %1 sc1" ::"v"(voff), "s"(sbase), "s"(ldsdst) : "memory");
}
__device__ __forceinline__ bf16x8 lds_b128(lds_ptr p) { return *(const LAS bf16x8*)p; }
__device__ __forceinline__ s16x4 lds_tr(lds_ptr p) { return __builtin_amdgcn_ds_read_tr16_b64_v4i16((LAS s16x4*)p); }
__device__ __forceinline__ bf16x8 cat8(s16x4 a, s16x4 b) { return __builtin_shufflevector(a, b, 0, 1, 2, 3, 4, 5, 6, 7); }
__device__ __forceinline__ unsigned cvtpk(float lo, float hi) {
    f32x2 v = {lo, hi};
    return __builtin_bit_cast(unsigned, __builtin_convertvector(v, bf16x2_t));
}
__device__ __forceinline__ float u2f(unsigned u) { return __builtin_bit_cast(float, u); }
__device__ __forceinline__ unsigned f2u(float f) { return __builtin_bit_cast(unsigned, f); }
__device__ __forceinline__ float bf_lo(unsigned u) { return u2f(u << 16); }
__device__ __forceinline__ float bf_hi(unsigned u) { return u2f(u & 0xffff0000u); }
__device__ __forceinline__ void swap16(unsigned& a, unsigned& b) {
    auto r = __builtin_amdgcn_permlane16_swap(a, b, false, false);
    a = r[0]; b = r[1];
}
__device__ __forceinline__ void swap32(unsigned& a, unsigned& b) {
    auto r = __builtin_amdgcn_permlane32_swap(a, b, false, false);
    a = r[0]; b = r[1];
}
__device__ __forceinline__ int opaque_tid() { int t = threadIdx.x; asm volatile("" : "+v"(t)); return t; }
typedef unsigned long long u64;
struct Flags { u64* p; u64 tag; };
__device__ __forceinline__ void signal(const Flags& f, int word, int lane) {
    if (lane == 0) __hip_atomic_store((GAS u64*)(f.p + word * 16), f.tag, RLX_AGENT);
}
__device__ __forceinline__ u64 peek(const Flags& f, int word) {
    u64 v;
    asm volatile("global_load_dwordx2 %0, %1, off sc1\n\ts_waitcnt vmcnt(0)" : "=&v"(v) : "v"(f.p + word * 16) : "memory");
    return v;
}
__device__ __forceinline__ void wait_all(const Flags& f, int word, u64 v) {
#pragma unroll 1
    for (unsigned spin = 0; spin < (1u << 20) && !__all(v == f.tag); ++spin) { __builtin_amdgcn_s_sleep(1); v = peek(f, word); }
}
__device__ __forceinline__ void arrive(const Flags& f, int word, int lane, lds_ptr cnt, unsigned last) {
    if (lane == 0 && __hip_atomic_fetch_add((LAS unsigned*)cnt, 1u, __ATOMIC_RELAXED, __HIP_MEMORY_SCOPE_WORKGROUP) == last)
        __hip_atomic_store((GAS u64*)(f.p + word * 16), f.tag, RLX_AGENT);
}
#define PEEK_AHEAD(v, ptr, sel) asm volatile("s_cmp_lg_u32 %2, 0\n\ts_cbranch_scc1 1f\n\tglobal_load_dwordx2 %0, %1, off sc1\n1:" : "+v"(v) : "v"(ptr), "s"(sel) : "memory", "scc")
struct CodePf { const char* base; unsigned len; int first; };
#define CODE_PF(d) do { unsigned o_ = (unsigned)(cp.first + (opaque_tid() & 15)) * 128u; o_ = (o_ < cp.len) ? o_ : 0u; \
    asm volatile("global_load_dword %0, %1, %2" : "+v"(d) : "v"(o_), "s"(cp.base) : "memory"); } while (0)
#define PEEK_DONE(v) asm volatile("s_waitcnt vmcnt(0)" : "+v"(v) :: "memory")

template <int CTRL> __device__ __forceinline__ float dpp_add(float v) { return v + u2f(__builtin_amdgcn_update_dpp(0u, f2u(v), CTRL, 0xf, 0xf, true)); }
__device__ __forceinline__ float red4(float v) {
    unsigned a = f2u(v), b = a;
    swap16(a, b);
    v = u2f(a) + u2f(b);
    a = f2u(v); b = a;
    swap32(a, b);
    return u2f(a) + u2f(b);
}
__device__ __forceinline__ float wave_sum(float v) {
    v = dpp_add<0xB1>(v); v = dpp_add<0x4E>(v); v = dpp_add<0x141>(v); v = dpp_add<0x140>(v);
    return red4(v);
}
__device__ __forceinline__ f32x2 row_stats(u32x4& a, u32x4& b) {
    float v[16];
#pragma unroll
    for (int i = 0; i < 4; ++i) { v[2 * i] = bf_lo(a[i]); v[2 * i + 1] = bf_hi(a[i]); v[8 + 2 * i] = bf_lo(b[i]); v[8 + 2 * i + 1] = bf_hi(b[i]); }
    float s = 0.f;
#pragma unroll
    for (int i = 0; i < 16; ++i) s += v[i];
    const float mu = wave_sum(s) * (1.0f / DM);
    float q = 0.f;
#pragma unroll
    for (int i = 0; i < 16; ++i) { v[i] -= mu; q += v[i] * v[i]; }
#pragma unroll
    for (int i = 0; i < 4; ++i) { a[i] = cvtpk(v[2 * i], v[2 * i + 1]); b[i] = cvtpk(v[8 + 2 * i], v[8 + 2 * i + 1]); }
    return (f32x2){mu, 1.0f / __builtin_sqrtf(wave_sum(q) * (1.0f / DM) + LN_EPS)};
}

constexpr int SLOT = 32768, LDS_BYTES = 5 * SLOT;
constexpr int G1_A = 0, G1_W = 65536, G1_STAT = G1_W + 5 * 16384, G1_CS = G1_STAT + 2048, G1_CNT = G1_CS + 4 * 6 * 64;

struct G2 { const char* Ah; const char* Ao; const char* Wd; const char* Wo; };
__device__ __forceinline__ void g2_load_b(const G2& g, int j, unsigned dst, int vw, unsigned voffB) {
    const char* b = (j >= 28 ? g.Wo : g.Wd) + (size_t)(j * 64 + vw * 8) * (DM * 2);
    glds16(b, voffB, dst + 16384 + vw * 1024); glds16(b + 128, voffB, dst + 16384 + 8192 + vw * 1024);
}
__device__ __forceinline__ void g2_load_a(const G2& g, int j, unsigned dst, int vw, unsigned voffA0, unsigned voffA1) {
    const char* a = (j >= 28 ? g.Ao : g.Ah) + (size_t)j * (2 * GRP) + vw * 1024;
    glds16c(a, voffA0, dst + vw * 2048); glds16c(a, voffA1, dst + vw * 2048 + 1024);
}
__device__ __forceinline__ void g2_voff(int lane, unsigned& voffA0, unsigned& voffA1, unsigned& voffB0, unsigned& voffB1) {
    const int r8 = lane >> 3, u8 = lane & 7, ua = u8 ^ (r8 >> 1), ub = ua ^ 4;
    voffA0 = (unsigned)(ua >> 2) * GRP + r8 * 64 + (ua & 3) * 16;
    voffA1 = (unsigned)(ub >> 2) * GRP + (r8 + 8) * 64 + (ub & 3) * 16;
    voffB0 = (unsigned)(r8 * (DM * 2) + (u8 ^ (r8 & 2)) * 16);
    voffB1 = (unsigned)(r8 * (DM * 2) + (u8 ^ (4 | (r8 & 2))) * 16);
}

struct AttnOut { f32x16 o0, o1; f32x4 l0, l1; float m0, m1; };
struct HPend { u32x4 v[8]; unsigned off, step; const void* base; };
struct AttnLane {
    unsigned voffK, voffV; lds_ptr kb0, kb1, vb; const char* qp; bf16x8 ones;
    __device__ __forceinline__ void init(lds_ptr lds, const char* Qg, int wid, int lane) {
        const int r32 = lane & 31, hi = lane >> 5, kh = wid >> 2, qg = wid & 3, ksw = (r32 >> 2) & 3;
        const int uk = (lane & 3) ^ ((lane >> 4) & 3), uv = lane & 3;
        voffK = (unsigned)((uk >> 1) * 65536 + wid * 1024 + (lane >> 2) * 32 + (uk & 1) * 16);
        voffV = (unsigned)((uv >> 1) * 65536 + wid * 1024 + (lane >> 2) * 32 + (uv & 1) * 16);
        kb0 = lds + kh * 8192 + r32 * 64 + ((hi ^ ksw) * 16); kb1 = lds + kh * 8192 + r32 * 64 + (((2 | hi) ^ ksw) * 16);
        vb = lds + 16384 + kh * 8192 + (4 * hi + ((lane >> 2) & 3)) * 64 + ((lane >> 4) & 1) * 32 + (lane & 3) * 8;
        qp = Qg + (qg * 64 + r32) * 32 + hi * 16;
        const short v = (((lane >> 4) & 1) == ((lane >> 2) & 1)) ? (short)0x3f80 : (short)0;
        ones = (bf16x8){v, v, v, v, v, v, v, v};
    }
};
#define EX(v) __builtin_amdgcn_exp2f(v)
#define AT_LOAD_K(c, slot) do { const unsigned d_ = lds0 + wid * 2048 + (slot) * SLOT; const char* k_ = Kg + (c) * 8192; glds16c(k_, A.voffK, d_); glds16c(k_ + 512, A.voffK, d_ + 1024); } while (0)
#define AT_LOAD_V(c, slot) do { const unsigned d_ = lds0 + wid * 2048 + (slot) * SLOT + 16384; const char* v_ = Vg + (c) * 8192; glds16c(v_, A.voffV, d_); glds16c(v_ + 512, A.voffV, d_ + 1024); } while (0)
#define AT_PACK(S, i) __builtin_bit_cast(bf16x8, (u32x4){cvtpk(S[i], S[i + 1]), cvtpk(S[i + 2], S[i + 3]), cvtpk(S[i + 4], S[i + 5]), cvtpk(S[i + 6], S[i + 7])})
#define AT_Q_LOAD() asm volatile("global_load_dwordx4 %0, %4, off sc1\n\tglobal_load_dwordx4 %1, %4, off offset:1024 sc1\n\tglobal_load_dwordx4 %2, %5, off sc1\n\tglobal_load_dwordx4 %3, %5, off offset:1024 sc1" \
        : "=&v"(q00), "=&v"(q10), "=&v"(q01), "=&v"(q11) : "v"(A.qp), "v"(qp2) : "memory")
#define AT_Q_WAIT(n) asm volatile("s_waitcnt vmcnt(" #n ")" : "+v"(q00), "+v"(q01), "+v"(q10), "+v"(q11) :: "memory")
__device__ __forceinline__ float rowmax16(const f32x16& s) {
    float a = s[0];
#pragma unroll
    for (int r = 1; r < 16; ++r) a = __builtin_fmaxf(a, s[r]);
    unsigned x = f2u(a), y = x; swap32(x, y);
    return __builtin_fmaxf(u2f(x), u2f(y));
}

__device__ __forceinline__ void attn_safe(lds_ptr lds, unsigned lds0, const char* Qg, const char* Kg, const char* Vg, int wid, AttnOut& R) {
    AttnLane A; A.init(lds, Qg, wid, opaque_tid() & 63);
    bf16x8 q00, q01, q10, q11; const char* qp2 = A.qp + 65536;
    AT_Q_LOAD(); AT_Q_WAIT(0);
    f32x16 o0 = (f32x16){}, o1 = (f32x16){};
    f32x4 l0 = (f32x4){0.f, 0.f, 0.f, 0.f}, l1 = l0;
    float m0 = -__builtin_inff(), m1 = m0;
#pragma unroll 1
    for (int c = 0; c < 8; ++c) {
        AT_LOAD_K(c, 3); AT_LOAD_V(c, 3);
        WAIT_V(0); BAR(); CFENCE();
#pragma unroll 1
        for (int t = 0; t < 4; ++t) {
            const int to = 3 * SLOT + t * 2048;
            const bf16x8 ka = lds_b128(A.kb0 + to), kb = lds_b128(A.kb1 + to);
            const bf16x8 va = cat8(lds_tr(A.vb + to), lds_tr(A.vb + to + 512)), vc = cat8(lds_tr(A.vb + to + 1024), lds_tr(A.vb + to + 1536));
            f32x16 s0 = MFMA32(kb, q01, MFMA32(ka, q00, (f32x16){})), s1 = MFMA32(kb, q11, MFMA32(ka, q10, (f32x16){}));
            const float n0 = __builtin_fmaxf(m0, rowmax16(s0)), n1 = __builtin_fmaxf(m1, rowmax16(s1));
            const float f0 = EX(m0 - n0), f1 = EX(m1 - n1);
            m0 = n0; m1 = n1; o0 *= f0; o1 *= f1; l0 *= f0; l1 *= f1;
#pragma unroll
            for (int r = 0; r < 16; ++r) { s0[r] = EX(s0[r] - n0); s1[r] = EX(s1[r] - n1); }
            const bf16x8 p00 = AT_PACK(s0, 0), p01 = AT_PACK(s0, 8), p10 = AT_PACK(s1, 0), p11 = AT_PACK(s1, 8);
            o0 = MFMA32(vc, p01, MFMA32(va, p00, o0)); l0 = MFMA16(A.ones, p01, MFMA16(A.ones, p00, l0));
            o1 = MFMA32(vc, p11, MFMA32(va, p10, o1)); l1 = MFMA16(A.ones, p11, MFMA16(A.ones, p10, l1));
        }
        WAIT_L0(); BAR(); CFENCE();
    }
    R.o0 = o0; R.o1 = o1; R.l0 = l0; R.l1 = l1; R.m0 = m0; R.m1 = m1;
}

__device__ __forceinline__ void attn_fast(lds_ptr lds, unsigned lds0, const char* Qg, const char* Kg, const char* Vg, const G2& g2, const Flags& flags, int qkv0, int qkvword, int depword,
                                          int hsig, int c0, bool bump, const CodePf& cp, const HPend& hp, const char* wpd, const char* wpo, int prow, int wid, AttnOut& R
                                          ) {
    const int lane = opaque_tid() & 63;
    AttnLane A; A.init(lds, Qg, wid, lane);
    const lds_ptr kb0 = A.kb0, kb1 = A.kb1, vb = A.vb;
    const bf16x8 ones = A.ones;
    const __amdgpu_buffer_rsrc_t hr = __builtin_amdgcn_make_buffer_rsrc((void*)hp.base, 0, 8192, 0x00020000);
    f32x16 o0 = (f32x16){}, o1 = (f32x16){}, negm0 = (f32x16){}, negm1 = (f32x16){}, sA0, sA1, sB0, sB1;
    f32x4 l0 = (f32x4){0.f, 0.f, 0.f, 0.f}, l1 = l0;
    bf16x8 kfa, kfb, va, vc = (bf16x8){}, pf = (bf16x8){};
    PIN(o0); PIN(o1); PIN(l0); PIN(l1); PIN(vc); PIN(pf); PIN(A.voffK); PIN(A.voffV); PIN(A.qp);
    unsigned gA0, gA1, gB0, gB1; g2_voff(lane, gA0, gA1, gB0, gB1);
    unsigned gB = (wid & 1) ? gB1 : gB0;
    unsigned pfo = (unsigned)(((lane >> 3) & 1) * (DM * 2) + (lane & 7) * 128);
    const u64* hptr = flags.p + (F_H + depword) * 16;
    const u64* qptr = flags.p + qkvword * 16;
    const char* qp2 = A.qp + 65536;
    PIN(gA0); PIN(gA1); PIN(gB); PIN(pfo); PIN(hptr); PIN(qptr); PIN(qp2);
    if (wid == 0) {
        wait_all(flags, qkv0, peek(flags, qkv0));
        STAMP(13);
    }
    CFENCE(); BAR(); CFENCE();
    STAMP(4);
    bf16x8 q00, q01, q10, q11;
    AT_LOAD_K(c0, 0); AT_Q_LOAD(); AT_LOAD_V(c0, 0);
    u64 hv = 0;
    float m0, m1;
    AT_Q_WAIT(2);
    PEEK_AHEAD(hv, qptr, wid);
    BAR(); SBAR();
    STAMP(15);
    {
        kfa = lds_b128(kb0); kfb = lds_b128(kb1);
        sA0 = MFMA32(kfa, q00, negm0); sA1 = MFMA32(kfa, q10, negm1); sA0 = MFMA32(kfb, q01, sA0); sA1 = MFMA32(kfb, q11, sA1);
        kfa = lds_b128(kb0 + 2048); kfb = lds_b128(kb1 + 2048);
        m0 = rowmax16(sA0); m1 = rowmax16(sA1);
#pragma unroll
        for (int r = 0; r < 16; ++r) { sA0[r] -= m0; sA1[r] -= m1; negm0[r] = -m0; negm1[r] = -m1; }
        PIN(negm0); PIN(negm1);
    }
    u32x4 hA = hp.v[0], hB = hp.v[1], hC = hp.v[2], hD = hp.v[3];
    unsigned hoff = hp.off, pfd = 0;
#define AT_HST(v) do { __builtin_amdgcn_raw_buffer_store_b128(v, hr, hoff, 0, 16); hoff += hp.step; CFENCE(); } while (0)
    PEEK_DONE(hv);
    if (wid == 0) {
        wait_all(flags, qkvword, hv);
        if (bump) signal(flags, F_EPOCH, lane);
        STAMP(14);
    }
    CFENCE(); BAR(); SBAR();
    STAMP(5);
    AT_LOAD_K((c0 + 1) & 7, 1); AT_LOAD_V((c0 + 1) & 7, 1);
#define HS 1
    hoff += 8192;
    STAMP(19);
#define AT_GROUP(QK, RD, S, i, O, L, VF) do { \
        QK; SBAR(); RD; SBAR(); \
        S[i] = EX(S[i]); S[i + 1] = EX(S[i + 1]); S[i + 2] = EX(S[i + 2]); PIN(S); SBAR(); \
        O = MFMA32(VF, pf, O); SBAR(); \
        S[i + 3] = EX(S[i + 3]); S[i + 4] = EX(S[i + 4]); S[i + 5] = EX(S[i + 5]); PIN(S); SBAR(); \
        L = MFMA16(ones, pf, L); SBAR(); \
        S[i + 6] = EX(S[i + 6]); S[i + 7] = EX(S[i + 7]); \
        pf = AT_PACK(S, i); PIN(pf); SBAR(); } while (0)
#define AT_STEP(C0, C1, N0, N1, KO, VO) do { \
        AT_GROUP(N0 = MFMA32(kfa, q00, negm0), va = cat8(lds_tr(vb + (VO)), lds_tr(vb + (VO) + 512)), C0, 0, o1, l1, vc); \
        AT_GROUP(N1 = MFMA32(kfa, q10, negm1), { kfa = lds_b128(kb0 + (KO)); vc = cat8(lds_tr(vb + (VO) + 1024), lds_tr(vb + (VO) + 1536)); }, C1, 0, o0, l0, va); \
        AT_GROUP(N0 = MFMA32(kfb, q01, N0), (void)0, C0, 8, o1, l1, va); \
        AT_GROUP(N1 = MFMA32(kfb, q11, N1), kfb = lds_b128(kb1 + (KO)), C1, 8, o0, l0, vc); } while (0)
#pragma unroll 1
    for (int c = 0; c < 5; ++c) {
        const int so = (c & 3) * SLOT, sn = ((c + 1) & 3) * SLOT;
        if (c == HS) hoff = hp.off;
        AT_HST(hA);
        AT_STEP(sA0, sA1, sB0, sB1, so + 4096, so);
        AT_HST(hB);
        AT_STEP(sB0, sB1, sA0, sA1, so + 6144, so + 2048);
        if (c == 0) WAIT_V(2); else WAIT_V(5);
        WAIT_L0(); BAR(); SBAR();
        STAMP(20 + c);
        if (c == 3) CODE_PF(pfd);
        if (c == HS + 3 && wid == 0) signal(flags, hsig, lane);
        AT_LOAD_K((c0 + c + 2) & 7, (c + 2) & 3); AT_LOAD_V((c0 + c + 2) & 7, (c + 2) & 3); AT_HST(hC);
        {
            const int pi = (wid + 8 * c < 36) ? wid + 8 * c : 35, lr = prow + 2 * pi;
            const char* pp = (lr < 1792 ? wpd : wpo) + (size_t)lr * (DM * 2);
            asm volatile("global_load_dword %0, %1, %2" : "+v"(pfd) : "v"(pfo), "s"(pp) : "memory");
        }
        AT_STEP(sA0, sA1, sB0, sB1, sn, so + 4096);
        AT_HST(hD);
        AT_STEP(sB0, sB1, sA0, sA1, sn + 2048, so + 6144);
        if (c >= HS) { hA = hp.v[4]; hB = hp.v[5]; hC = hp.v[6]; hD = hp.v[7]; }
    }
#pragma unroll 1
    for (int c = 5; c < 8; ++c) {
        const int so = (c & 3) * SLOT, sn = (c == 7) ? so : ((c + 1) & 3) * SLOT;
        const int sel = (c ^ 6) | wid;
        if (sel == 0) { PEEK_DONE(hv); wait_all(flags, F_H + depword, hv); }
        if (c == 5) g2_load_b(g2, 0, lds0 + 4 * SLOT, wid, gB);
        if (c == 6) g2_load_b(g2, 1, lds0, wid, gB);
        if (c == 7) g2_load_a(g2, 1, lds0, wid, gA0, gA1);
        AT_STEP(sA0, sA1, sB0, sB1, so + 4096, so);

        AT_STEP(sB0, sB1, sA0, sA1, so + 6144, so + 2048);
        WAIT_V(0); WAIT_L0(); BAR(); SBAR();
        STAMP(20 + c);
        if (c == 5) { AT_LOAD_K((c0 + 7) & 7, 3); AT_LOAD_V((c0 + 7) & 7, 3); }
        if (c == 6) { g2_load_a(g2, 0, lds0 + 4 * SLOT, wid, gA0, gA1); CODE_PF(pfd); }
        if (c == 7) g2_load_a(g2, 2, lds0 + SLOT, wid, gA0, gA1);
        AT_STEP(sA0, sA1, sB0, sB1, sn, so + 4096);
        if (c == 6) g2_load_b(g2, 2, lds0 + SLOT, wid, gB);
        PEEK_AHEAD(hv, hptr, (c ^ 5) | wid);
        AT_STEP(sB0, sB1, sA0, sA1, sn + 2048, so + 6144);
    }
    STAMP(28);
    o1 = MFMA32(vc, pf, o1); l1 = MFMA16(ones, pf, l1);
    asm volatile("" :: "v"(pfd));
    R.o0 = o0; R.o1 = o1; R.l0 = l0; R.l1 = l1; R.m0 = m0; R.m1 = m1;
}

__global__ __launch_bounds__(512, 2) void k_main(const bf16_t* __restrict__ x, const bf16_t* __restrict__ wqkv, const bf16_t* __restrict__ wo, const bf16_t* __restrict__ wg,
                                                 const bf16_t* __restrict__ wu, const bf16_t* __restrict__ wd, bf16_t* __restrict__ out, unsigned char* ws) {
    __shared__ __attribute__((aligned(16))) unsigned char smem[LDS_BYTES];
    const lds_ptr lds = (lds_ptr)smem;
    const unsigned lds0 = (unsigned)(uintptr_t)lds;
    const int wid = __builtin_amdgcn_readfirstlane(threadIdx.x >> 6);
    const int bid = blockIdx.x, xcd = bid & 7, idx = bid >> 3;
    Flags flags; flags.p = (u64*)(ws + WS_FLAGS);
    const __amdgpu_buffer_rsrc_t wsr = __builtin_amdgcn_make_buffer_rsrc((void*)ws, 0, WS_END, 0x00020000);
#define ST16(v, off) __builtin_amdgcn_raw_buffer_store_b128(v, wsr, off, 0, 16)
    STAMP(0);
    unsigned cpd = 0;
    CodePf cp;
    asm volatile("s_getpc_b64 %0\n.Lk_begin:\n\ts_mov_b32 %1, .Lk_end-.Lk_begin" : "=s"(cp.base), "=s"(cp.len));
    cp.first = idx * 16;
    CODE_PF(cpd);
    HPend hp;
    {
        const int lane = threadIdx.x & 63, wr = wid >> 2, wc = wid & 3, js = idx & 3, rb = idx >> 2;
        const bool typeY = (js == 3);
        const int f0 = 448 * xcd + 128 * js;
        const char* P0 = (const char*)(wg + f0);
        const char* P2 = (const char*)(wu + f0);
        const char* P1 = typeY ? (const char*)(wqkv + 128 * xcd + 64) : (const char*)(wg + f0 + 64);
        const char* P3 = typeY ? (const char*)(wqkv + 1024 + 128 * xcd + 64) : (const char*)(wu + f0 + 64);
        const char* P4 = typeY ? (const char*)(wqkv + 2048 + 128 * xcd + 64) : (const char*)(wqkv + js * 1024 + 128 * xcd);
        const unsigned stepF = 32u * FF * 2u, stepQ = 32u * NQKV * 2u, step13 = typeY ? stepQ : stepF;
        const char* XA = (const char*)x + (size_t)rb * 256 * DM * 2;
        const char* XC = (const char*)ws + WS_XC + (size_t)rb * 256 * DM * 2;
        u64 ep; u32x4 xa0, xa1;
        const int srow = rb * 256 + (js * 8 + xcd) * 8 + wid;
        asm volatile("global_load_dwordx4 %1, %5, %6\n\tglobal_load_dwordx4 %2, %5, %6 offset:1024\n\tglobal_load_dwordx2 %0, %3, %4 sc1"
                     : "=&v"(ep), "=&v"(xa0), "=&v"(xa1)
                     : "v"(0u), "s"(flags.p), "v"((unsigned)(lane * 16)), "s"((const char*)x + (size_t)srow * DM * 2) : "memory");
        const int r8 = lane >> 3, u8 = lane & 7;
        const int gw = (r8 & 2) | ((wc & 1) << 2);
        const unsigned voffF = (unsigned)(((wc * 8 + r8) * FF + (u8 ^ gw) * 8) * 2);
        const unsigned voffQ = (unsigned)(((wc * 8 + r8) * NQKV + (u8 ^ gw) * 8) * 2);
        const unsigned voff13 = typeY ? voffQ : voffF;
        const unsigned voffA0 = (unsigned)(((32 * wid + r8) * DM + (u8 ^ (r8 >> 1)) * 8) * 2);
        const unsigned voffA1 = (unsigned)(((32 * wid + 8 + r8) * DM + (u8 ^ (4 | (r8 >> 1))) * 8) * 2);
        const unsigned dstA = lds0 + G1_A + wid * 4096, dstW = lds0 + G1_W + wc * 1024;
#define G1_LOAD_A(t) do { const char* b_ = XA + (t) * 128; const unsigned d_ = dstA + ((t) & 1) * 32768; \
        glds16(b_, voffA0, d_); glds16(b_, voffA1, d_ + 1024); glds16(b_ + 32768, voffA0, d_ + 2048); glds16(b_ + 32768, voffA1, d_ + 3072); } while (0)
#define G1_LOAD_W(hs) do { const unsigned d_ = dstW + ((hs) & 3) * 4096; \
        glds16(P0 + (size_t)(hs) * stepF, voffF, d_); glds16(P1 + (size_t)(hs) * step13, voff13, d_ + 16384); glds16(P2 + (size_t)(hs) * stepF, voffF, d_ + 32768); \
        glds16(P3 + (size_t)(hs) * step13, voff13, d_ + 49152); glds16(P4 + (size_t)(hs) * stepQ, voffQ, d_ + 65536); } while (0)
        STAMP(29);
        if (wr == 1) G1_LOAD_W(0);
        G1_LOAD_A(0);
        STAMP(30);

        int fr = lane & 15, fq = lane >> 4;
        const lds_ptr aB0 = lds + G1_A + (wr * 128 + fr) * 128 + ((fq ^ (fr >> 1)) * 16);
        const lds_ptr aB1 = lds + G1_A + (wr * 128 + fr) * 128 + (((4 | fq) ^ (fr >> 1)) * 16);
        const int tq = (lane >> 2) & 3, tp = lane & 3, tg = (tq & 2) | ((fq & 1) << 2);
        auto wbase = [&](int panel, int bp) { return lds + G1_W + panel * 16384 + (8 * fq + tq) * 128 + (((2 * bp + (tp >> 1)) ^ tg) * 16) + (tp & 1) * 8; };
        const int pX = wc >> 1, bX = 2 * (wc & 1);
        const lds_ptr wB0 = typeY ? wbase(0, wc) : wbase(pX, bX);
        const lds_ptr wB1 = typeY ? wbase(1, wc) : wbase(pX, bX + 1);
        const lds_ptr wB2 = typeY ? wbase(2, wc) : wbase(2 + pX, bX);
        const lds_ptr wB3 = typeY ? wbase(3, wc) : wbase(2 + pX, bX + 1);
        const lds_ptr wB4 = wbase(4, wc);
        if (threadIdx.x == 0) *(LAS unsigned*)(lds + G1_CNT) = 0u;
        f32x4 acc[8][5];
#pragma unroll
        for (int m = 0; m < 8; ++m)
#pragma unroll
            for (int b = 0; b < 5; ++b) { acc[m][b] = (f32x4){0.f, 0.f, 0.f, 0.f}; PIN(acc[m][b]); }
        asm volatile("s_cmp_lg_u32 %3, 0\n\ts_cbranch_scc1 1f\n\ts_waitcnt vmcnt(4)\n1:\n\ts_waitcnt vmcnt(9)" : "+v"(ep), "+v"(xa0), "+v"(xa1) : "s"(wr), "v"(cpd) : "memory", "scc");
        STAMP(31);
        {
            const unsigned elo = __builtin_amdgcn_readfirstlane((unsigned)ep), ehi = __builtin_amdgcn_readfirstlane((unsigned)(ep >> 32));
            flags.tag = (((u64)ehi << 32) | elo) * 0x9E3779B97F4A7C15ull + 0x632BE59BD9B4E019ull;
        }
        const unsigned tag32 = (unsigned)flags.tag;
        {
            const f32x2 st = row_stats(xa0, xa1);
            G1_LOAD_W(1 + wr);
            ST16(xa0, (lane >= 32) ? WS_XC + (unsigned)srow * 2048u + lane * 16 : WS_END);
            ST16(xa1, WS_XC + (unsigned)srow * 2048u + 1024 + lane * 16);
            ST16(((u32x4){f2u(st.x), tag32, f2u(st.y), tag32}), (lane == 0) ? WS_STAT + (unsigned)srow * 16u : WS_END);
            CFENCE();
        }
        STAMP(32);
        bf16x8 af[8], wf[5];
        float cs0 = 0.f, cs1 = 0.f, cs2 = 0.f;
#define CS1(c, w) "v_dot2c_f32_bf16 %" #c ", %25, %" #w "\n\t"
#define G1_CSUM(t, lim) do { const u32x4 w0_ = __builtin_bit_cast(u32x4, wf[0]), w1_ = __builtin_bit_cast(u32x4, wf[1]), w2_ = __builtin_bit_cast(u32x4, wf[2]), \
            w3_ = __builtin_bit_cast(u32x4, wf[3]), w4_ = __builtin_bit_cast(u32x4, wf[4]); \
        asm volatile("s_cmp_gt_u32 %24, %26\n\ts_cbranch_scc1 2f\n\ts_cmp_lg_u32 %23, 0\n\ts_cbranch_scc1 1f\n\t" \
            CS1(0, 3) CS1(1, 7) CS1(2, 19) CS1(0, 4) CS1(1, 8) CS1(2, 20) CS1(0, 5) CS1(1, 9) CS1(0, 6) CS1(1, 10) "s_nop 1\n\ts_branch 2f\n1:\n\t" \
            CS1(0, 11) CS1(1, 15) CS1(2, 21) CS1(0, 12) CS1(1, 16) CS1(2, 22) CS1(0, 13) CS1(1, 17) CS1(0, 14) CS1(1, 18) "s_nop 2\n2:" \
            : "+v"(cs0), "+v"(cs1), "+v"(cs2) \
            : "v"(w0_[0]), "v"(w0_[1]), "v"(w0_[2]), "v"(w0_[3]), "v"(w1_[0]), "v"(w1_[1]), "v"(w1_[2]), "v"(w1_[3]), "v"(w2_[0]), "v"(w2_[1]), "v"(w2_[2]), "v"(w2_[3]), \
              "v"(w3_[0]), "v"(w3_[1]), "v"(w3_[2]), "v"(w3_[3]), "v"(w4_[0]), "v"(w4_[1]), "v"(w4_[2]), "v"(w4_[3]), "s"(wr), "s"(t), "s"(0x3f803f80u), "i"(lim) : "scc"); } while (0)
#define G1_RW(i, B, slot) wf[i] = cat8(lds_tr(B + (slot) * 4096), lds_tr(B + (slot) * 4096 + 512))
#define G1_RA(m, AB, abuf) af[m] = lds_b128(AB + (abuf) * 32768 + (m) * 2048)
#define G1_SEG(AB, abuf, slot, L0, L1, L2, L3, L4, CLOSE) do { \
        L0; G1_RW(0, wB0, slot); G1_RW(1, wB1, slot); L1; G1_RW(2, wB2, slot); G1_RW(3, wB3, slot); L2; G1_RW(4, wB4, slot); G1_RA(0, AB, abuf); G1_RA(1, AB, abuf); \
        L3; G1_RA(2, AB, abuf); G1_RA(3, AB, abuf); G1_RA(4, AB, abuf); L4; G1_RA(5, AB, abuf); G1_RA(6, AB, abuf); G1_RA(7, AB, abuf); \
        SBAR(); G1_CSUM(t, 3); WAIT_L0(); SBAR(); if (CLOSE) { BAR(); SBAR(); } } while (0)
#define G1_SEG_A(abuf, slot, tt) do { const char* b_ = ((tt) < 4 ? XA : XC) + (tt) * 128; const unsigned d_ = dstA + (1 - (abuf)) * 32768; \
        G1_SEG(aB0, abuf, slot, glds16c(b_, voffA0, d_), glds16c(b_, voffA1, d_ + 1024), glds16c(b_ + 32768, voffA0, d_ + 2048), glds16c(b_ + 32768, voffA1, d_ + 3072), (void)0, true); } while (0)
#define G1_SEG_W(abuf, slot, hs, dslot, CLOSE) do { const unsigned d_ = dstW + (dslot) * 4096; \
        G1_SEG(aB1, abuf, slot, glds16(P0 + (size_t)(hs) * stepF, voffF, d_), glds16(P1 + (size_t)(hs) * step13, voff13, d_ + 16384), glds16(P2 + (size_t)(hs) * stepF, voffF, d_ + 32768), \
               glds16(P3 + (size_t)(hs) * step13, voff13, d_ + 49152), glds16(P4 + (size_t)(hs) * stepQ, voffQ, d_ + 65536), CLOSE); } while (0)
#define G1_MMA() do { __builtin_amdgcn_s_setprio(1); \
        _Pragma("unroll") for (int b = 0; b < 5; ++b) _Pragma("unroll") for (int m = 0; m < 8; ++m) MFMA16_ACC(acc[m][b], wf[b], af[m]); \
        __builtin_amdgcn_s_setprio(0); SBAR(); } while (0)

        STAMP(33);
        if (wr == 1) { WAIT_V(12); BAR(); }
        WAIT_V(8); WAIT_L0();
        STAMP(34);
        BAR();
        SBAR();
        STAMP(1);
        u64 sg = 0;
        const u64* sgp = flags.p + (F_XC + rb * 32 + (lane & 31)) * 16;
#pragma unroll 1
        for (int t = 0; t < 16; t += 2) {
            const int h1 = (2 * t + 3 + wr < 31) ? 2 * t + 3 + wr : 31, h2 = (2 * t + 5 + wr < 31) ? 2 * t + 5 + wr : 31, t2 = (t < 14) ? t + 2 : 15;
            const int sel = (t == 12) ? 0 : ((t ^ 2) | wid);
            STAMP(35 + (t >> 1));
            PEEK_AHEAD(sg, sgp, sel);
            if (t == 14) CODE_PF(cpd);
            G1_SEG_A(0, 0, t + 1); G1_MMA(); asm volatile("s_waitcnt vmcnt(4)" : "+v"(sg), "+v"(cpd) :: "memory"); BAR(); SBAR();
            if (t == 0 && wid == 4) signal(flags, F_XC + bid, lane);
            if (sel == 0) {
                const u64 want = (t == 12) ? ((u64)tag32 << 32) : flags.tag, mask = (t == 12) ? 0xffffffff00000000ull : ~0ull;
#pragma unroll 1
                for (unsigned spin = 0; spin < (1u << 20) && !__all((sg & mask) == want); ++spin)
                    asm volatile("s_sleep 1\n\tglobal_load_dwordx2 %0, %1, off sc1\n\ts_waitcnt vmcnt(0)" : "+v"(sg) : "v"(sgp) : "memory");
                const int tl = opaque_tid() & 63;
                if (t == 12) *(LAS unsigned*)(lds + G1_STAT + (wid * 32 + (tl & 31)) * 8 + (tl >> 5) * 4) = (unsigned)sg;
            }
            G1_SEG_W(0, 1, h1, (3 + wr) & 3, true); G1_MMA(); WAIT_V(5); BAR(); SBAR();
            G1_SEG_A(1, 2, t2); G1_MMA(); WAIT_V(4); BAR(); SBAR();
            G1_SEG_W(1, 3, h2, (1 + wr) & 3, false);
            if (t == 2) {
                const int tl = opaque_tid() & 63;
                const lds_ptr cp = lds + G1_CS + wc * 384 + (tl & 15) * 4;
                *(LAS float*)(cp + wr * 128) = red4(cs0); *(LAS float*)(cp + wr * 128 + 64) = red4(cs1); *(LAS float*)(cp + 256 + wr * 64) = red4(cs2);
                sgp = (const u64*)(ws + WS_STAT) + (rb * 256 + wid * 32 + (tl & 31)) * 2 + (tl >> 5);
                WAIT_L0();
            }
            SBAR(); BAR(); SBAR(); G1_MMA(); WAIT_V(5);
            if (wr == 0 || t < 14) BAR();
            SBAR();
        }
        MFMA_DRAIN(); SBAR();
        STAMP(2);

        const int tz = opaque_tid(), ln = tz & 63;
        fr = tz & 15; fq = (tz >> 4) & 3;
        const int row0 = rb * 256 + wr * 128 + fr;
        f32x4 csv[5]; f32x2 st[8];
#pragma unroll
        for (int b = 0; b < 5; ++b) csv[b] = *(const LAS f32x4*)(lds + G1_CS + wc * 384 + b * 64 + fq * 16);
        csv[4] += *(const LAS f32x4*)(lds + G1_CS + wc * 384 + 320 + fq * 16);
#pragma unroll
        for (int m = 0; m < 8; ++m) st[m] = *(const LAS f32x2*)(lds + G1_STAT + (wr * 128 + m * 16 + fr) * 8);
        auto lnv = [&](int m, int b, float sc) { return (acc[m][b] - st[m].x * csv[b]) * (st[m].y * sc); };
        auto glu = [&](int m, int bg, int bu) {
            const f32x4 g = lnv(m, bg, -LOG2E), u = lnv(m, bu, 1.0f);
            f32x4 h;
#pragma unroll
            for (int j = 0; j < 4; ++j) h[j] = u[j] * __builtin_amdgcn_rcpf(1.0f + __builtin_amdgcn_exp2f(g[j]));
            return h;
        };
        auto pack2 = [&](const f32x4& v0, const f32x4& v1) {
            unsigned p0 = cvtpk(v0[0], v0[1]), p1 = cvtpk(v0[2], v0[3]), q0 = cvtpk(v1[0], v1[1]), q1 = cvtpk(v1[2], v1[3]);
            swap16(p0, q0); swap16(p1, q1);
            return (u32x4){p0, p1, q0, q1};
        };
        const int hd = 4 * xcd + (typeY ? 2 : 0) + (wc >> 1);
        const unsigned qoff = WS_QKV + (unsigned)(hd * 2 + (wc & 1)) * 65536u + (unsigned)(row0 + (fq & 1) * 16) * 32u + (fq >> 1) * 16;
        constexpr unsigned KIND = NH * 2 * 65536u;
        if (!typeY) {
            const float sc4 = (js == 0) ? QSCALE : 1.0f;
#pragma unroll
            for (int m = 0; m < 8; m += 2) ST16(pack2(lnv(m, 4, sc4), lnv(m + 1, 4, sc4)), qoff + js * KIND + m * 512);
            CFENCE();
            hp.base = ws + WS_ACT + (unsigned)(14 * xcd + 4 * js + wc) * GRP + (unsigned)(rb * 256 + wr * 128) * 64u;
            hp.off = fr * 64 + (fq & 1) * 32 + (fq >> 1) * 16; hp.step = 1024;
#pragma unroll
            for (int m = 0; m < 8; ++m) {
                hp.v[m] = pack2(glu(m, 0, 2), glu(m, 1, 3));
                if (m == 5) { PIN(hp.v[5]); WAIT_V(0); arrive(flags, F_QKV + bid, ln, lds + G1_CNT, 7u); }
            }
        } else {
#pragma unroll
            for (int m = 0; m < 8; m += 2) {
                ST16(pack2(lnv(m, 3, 1.0f), lnv(m + 1, 3, 1.0f)), qoff + KIND + m * 512);
                ST16(pack2(lnv(m, 4, 1.0f), lnv(m + 1, 4, 1.0f)), qoff + 2 * KIND + m * 512);
                ST16(pack2(lnv(m, 1, QSCALE), lnv(m + 1, 1, QSCALE)), qoff + m * 512);
            }
            CFENCE();
            hp.base = ws + WS_ACT + (unsigned)(14 * xcd + 12 + (wc >> 1)) * GRP + (unsigned)(rb * 256 + wr * 128) * 64u;
            hp.off = (fr + (fq & 1) * 16) * 64 + (wc & 1) * 32 + (fq >> 1) * 16; hp.step = 2048;
#pragma unroll
            for (int m = 0; m < 8; m += 2) { hp.v[m >> 1] = pack2(glu(m, 0, 2), glu(m + 1, 0, 2)); hp.v[4 + (m >> 1)] = hp.v[m >> 1]; }
            PIN(hp.v[3]); WAIT_V(0); arrive(flags, F_QKV + bid, ln, lds + G1_CNT, 7u);
        }
#pragma unroll
        for (int i = 0; i < 8; ++i) PIN(hp.v[i]);
        STAMP(3);
    }

    const int z = xcd & 1, rbk = ((xcd >> 1) & 1) * 8 + (idx & 7), cbk = (xcd >> 2) * 4 + (idx >> 3);
    G2 g2;
    g2.Ah = (const char*)ws + WS_ACT + (size_t)z * 56 * GRP + rbk * 128 * 64;
    g2.Ao = (const char*)ws + WS_ACT + (size_t)(112 + z * 16 - 56) * GRP + rbk * 128 * 64;
    g2.Wd = (const char*)(wd + (size_t)z * 1792 * DM + cbk * 128);
    g2.Wo = (const char*)(wo + (size_t)z * 512 * DM + cbk * 128) - (size_t)1792 * DM * 2;
    {
        const int hl = idx & 3, qb = idx >> 2, h = 4 * xcd + hl;
        const int l0_ = opaque_tid() & 63;
        const int qkv0 = F_QKV + xcd + 8 * (qb * 4 + ((hl >= 2) ? 3 : l0_ % 3));
        const int qkvword = F_QKV + xcd + 8 * ((l0_ & 7) * 4 + ((hl >= 2) ? 3 : 1 + ((l0_ >> 3) & 1)));
        const int depword = (rbk >> 1) * 32 + ((l0_ >> 2) & 3) * 8 + 4 * z + (l0_ & 3);
        const char* Qg = (const char*)ws + WS_QKV + (size_t)h * 131072 + qb * 256 * 32;
        const char* Kg = (const char*)ws + WS_QKV + (size_t)(NH + h) * 131072;
        const char* Vg = (const char*)ws + WS_QKV + (size_t)(2 * NH + h) * 131072;
        const char* wpd = g2.Wd - (cbk & 3) * 256;
        const char* wpo = g2.Wo - (cbk & 3) * 256;
        AttnOut R;
#define DG
        attn_fast(lds, lds0, Qg, Kg, Vg, g2, flags, qkv0, qkvword, depword, F_H + bid, qb, bid == 0, cp, hp, wpd, wpo, idx * 72, wid, R DG);
        STAMP(6);
        const int tz = opaque_tid(), ln = tz & 63, w4 = wid & 3, fr = ln & 15, fq = ln >> 4;
        const lds_ptr po = lds + 2 * SLOT + w4 * 8192 + ln * 16, pl = lds + 3 * SLOT + 4096 + w4 * 1024 + ln * 16;
        volatile LAS unsigned* const badw = (volatile LAS unsigned*)(lds + 3 * SLOT + 8192 + 4096);
#define AT_PUBLISH() do { if (wid < 4) { \
            _Pragma("unroll") for (int i = 0; i < 4; ++i) { *(LAS f32x4*)(po + i * 1024) = (f32x4){R.o0[4 * i], R.o0[4 * i + 1], R.o0[4 * i + 2], R.o0[4 * i + 3]}; \
                *(LAS f32x4*)(po + 4096 + i * 1024) = (f32x4){R.o1[4 * i], R.o1[4 * i + 1], R.o1[4 * i + 2], R.o1[4 * i + 3]}; } \
            *(LAS f32x4*)pl = (f32x4){R.l0[0], R.m0, R.l1[0], R.m1}; } } while (0)
        {
            AT_PUBLISH();
            const f32x16 z16 = R.o0 * 0.0f + R.o1 * 0.0f;
            float zs = 0.f;
#pragma unroll
            for (int r = 0; r < 16; ++r) zs += z16[r];
            const bool ok = (zs == 0.f) && (R.l0[0] > 0.f) && (R.l0[0] < 0x1p96f) && (R.l1[0] > 0.f) && (R.l1[0] < 0x1p96f);
            const bool wave_bad = !__all(ok);
            if (ln == 0) badw[wid] = wave_bad ? 1u : 0u;
            STAMP(16);
            WAIT_V(0); STAMP(17); WAIT_L0(); BAR(); CFENCE();
            const unsigned anybad = badw[ln & 7];
            if (__any(anybad != 0u)) {
                WAIT_L0(); BAR(); CFENCE();
                attn_safe(lds, lds0, Qg, Kg, Vg, wid, R);
                AT_PUBLISH();
                WAIT_L0(); BAR(); CFENCE();
            }
        }
        STAMP(7);

        const int tile = rbk * 8 + cbk;
        if (wid >= 4) {
            unsigned vA0, vA1, vB0, vB1; g2_voff(ln, vA0, vA1, vB0, vB1);
#define G2_LOAD(j, sl) do { const unsigned d_ = lds0 + (sl) * SLOT; g2_load_b(g2, j, d_, 2 * w4, vB0); g2_load_a(g2, j, d_, 2 * w4, vA0, vA1); \
                g2_load_b(g2, j, d_, 2 * w4 + 1, vB1); g2_load_a(g2, j, d_, 2 * w4 + 1, vA0, vA1); } while (0)
            const f32x4 lm = *(const LAS f32x4*)pl;
            f32x4 t[8];
#pragma unroll
            for (int i = 0; i < 8; ++i) t[i] = *(const LAS f32x4*)(po + i * 1024);
            WAIT_L0(); BAR(); CFENCE();
            G2_LOAD(3, 2);
            u32x4 ow[4];
            const unsigned ooff = WS_ACT + (unsigned)(112 + h) * GRP + (unsigned)(qb * 256 + w4 * 64 + (ln & 31)) * 64u + (ln >> 5) * 16;
#pragma unroll
            for (int q = 0; q < 2; ++q) {
                BAR();
                if (q == 0) G2_LOAD(4, 3); else G2_LOAD(5, 4);
                const f32x16 ok_ = q ? R.o1 : R.o0;
                const float lk = q ? R.l1[0] : R.l0[0], mk = q ? R.m1 : R.m0, lo = lm[2 * q], mo = lm[2 * q + 1];
                const float mm = __builtin_fmaxf(mk, mo), fk = __builtin_amdgcn_exp2f(mk - mm), fo = __builtin_amdgcn_exp2f(mo - mm);
                const float rl = 1.0f / (lk * fk + lo * fo), ck = fk * rl, co = fo * rl;
                unsigned p[8];
#pragma unroll
                for (int i = 0; i < 4; ++i) {
                    const f32x4 u = t[4 * q + i];
                    p[2 * i] = cvtpk(ok_[4 * i] * ck + u[0] * co, ok_[4 * i + 1] * ck + u[1] * co);
                    p[2 * i + 1] = cvtpk(ok_[4 * i + 2] * ck + u[2] * co, ok_[4 * i + 3] * ck + u[3] * co);
                }
                swap32(p[0], p[2]); swap32(p[1], p[3]); swap32(p[4], p[6]); swap32(p[5], p[7]);
                ow[2 * q] = (u32x4){p[0], p[1], p[2], p[3]}; ow[2 * q + 1] = (u32x4){p[4], p[5], p[6], p[7]};
                PIN(ow[2 * q]); PIN(ow[2 * q + 1]);
            }
#pragma unroll
            for (int i = 0; i < 4; ++i) ST16(ow[i], ooff + (i >> 1) * 2048 + (i & 1) * 32);
            CFENCE();
            unsigned t0 = 0, t1 = 0, t2 = 0, t3 = 0, a0 = 0, a1 = 0, a2 = 0;
            WAIT_V(20); BAR(); G2_LOAD(6, 0);
            WAIT_V(20); BAR(); G2_LOAD(7, 1);
            WAIT_V(20);
            int sl = 2; cpd = 0;
#pragma unroll 1
            for (int j = 5; j < 33; ++j) {
                TS(t1);
                BAR();
                TS(t2);
                if (j == 29) CODE_PF(cpd);
                if (j == 6 && wid == 4) signal(flags, F_O + bid, ln);
                G2_LOAD(j + 3, sl);
                sl = (sl == 4) ? 0 : sl + 1;
                TS(t3);
                WAIT_V(16);
                TS(t0);
                a0 += t0 - t3; a1 += t2 - t1; a2 += t3 - t2;
            }
            PUT(50, a0); PUT(51, a1); PUT(52, a2);
            BAR();
            if (tz == 256) *(LAS unsigned*)lds = 0u;
            WAIT_V(8); WAIT_L0(); BAR(); WAIT_V(0); BAR();
                asm volatile("" :: "v"(cpd));
            if (wid == 4) wait_all(flags, F_X + tile * 2 + z, peek(flags, F_X + tile * 2 + z));
            CFENCE(); BAR();
            asm volatile(".Lk_end:");
        } else {
            const int wm = w4 >> 1, wn = w4 & 1;
            const unsigned aO0 = (wm * 64 + fr) * 128 + ((fq ^ (fr >> 1)) * 16), aO1 = aO0 ^ 64;
            const int tq = (ln >> 2) & 3, tp = ln & 3, tg = (tq & 2) | ((fq & 1) << 2);
            unsigned wO[4];
#pragma unroll
            for (int b = 0; b < 4; ++b) wO[b] = 16384 + wn * 8192 + (8 * fq + tq) * 128 + (((2 * b + (tp >> 1)) ^ tg) * 16) + (tp & 1) * 8;
            f32x4 acc[4][4];
#pragma unroll
            for (int m = 0; m < 4; ++m)
#pragma unroll
                for (int b = 0; b < 4; ++b) acc[m][b] = (f32x4){0.f, 0.f, 0.f, 0.f};
            bf16x8 xa[4], ya[4] = {}; s16x4 xl[4], xh[4], yl[4] = {}, yh[4] = {};
            lds_ptr pa0, pa1, pw0, pw1, pw2, pw3;
#define G2_BASES(so) do { pa0 = lds + aO0 + (so); pa1 = lds + aO1 + (so); pw0 = lds + wO[0] + (so); pw1 = lds + wO[1] + (so); pw2 = lds + wO[2] + (so); pw3 = lds + wO[3] + (so); } while (0)
#define G2_M(C, m, b) do { MFMA16_ACC(acc[m][b], cat8(C##l[b], C##h[b]), C##a[m]); SBAR(); } while (0)
#define RDQ(x) (x)
#define G2_HALF(C, N, AB, WO, RD_, TAIL) do { const bool RD = RDQ(RD_); \
            G2_M(C, 0, 0); if (RD) { N##l[0] = lds_tr(pw0 + (WO)); N##h[0] = lds_tr(pw0 + (WO) + 512); } SBAR(); \
            G2_M(C, 1, 0); if (RD) N##a[0] = lds_b128(AB); SBAR(); \
            G2_M(C, 2, 0); if (RD) N##a[1] = lds_b128((AB) + 2048); SBAR(); \
            G2_M(C, 3, 0); if (RD) N##a[2] = lds_b128((AB) + 4096); SBAR(); \
            G2_M(C, 0, 1); if (RD) N##a[3] = lds_b128((AB) + 6144); SBAR(); \
            G2_M(C, 1, 1); if (RD) { N##l[1] = lds_tr(pw1 + (WO)); N##h[1] = lds_tr(pw1 + (WO) + 512); } SBAR(); \
            G2_M(C, 2, 1); if (RD) { N##l[2] = lds_tr(pw2 + (WO)); N##h[2] = lds_tr(pw2 + (WO) + 512); } SBAR(); \
            G2_M(C, 3, 1); if (RD) { N##l[3] = lds_tr(pw3 + (WO)); N##h[3] = lds_tr(pw3 + (WO) + 512); } SBAR(); \
            G2_M(C, 0, 2); TAIL(0); SBAR(); G2_M(C, 1, 2); TAIL(1); SBAR(); G2_M(C, 2, 2); TAIL(2); SBAR(); G2_M(C, 3, 2); TAIL(3); SBAR(); \
            G2_M(C, 0, 3); G2_M(C, 1, 3); G2_M(C, 2, 3); G2_M(C, 3, 3); } while (0)
            int so = 4 * SLOT;
            G2_BASES(so);
            xl[0] = lds_tr(pw0); xh[0] = lds_tr(pw0 + 512); SBAR();
#pragma unroll
            for (int m = 0; m < 4; ++m) { xa[m] = lds_b128(pa0 + m * 2048); SBAR(); }
            xl[1] = lds_tr(pw1); xh[1] = lds_tr(pw1 + 512); xl[2] = lds_tr(pw2); xh[2] = lds_tr(pw2 + 512); xl[3] = lds_tr(pw3); xh[3] = lds_tr(pw3 + 512);
            const u64* const optr = flags.p + (F_O + depword) * 16;
            u64 ov = 0; unsigned t0 = 0, t1 = 0, a0 = 0;
            STAMP(8);
#define G2_NEXT(i) do { if ((i) == 0) so = (so == 4 * SLOT) ? 0 : so + SLOT; \
                if ((i) == 1) { pa0 = lds + aO0 + so; pa1 = lds + aO1 + so; PIN(pa0); PIN(pa1); } \
                if ((i) == 2) { pw0 = lds + wO[0] + so; pw1 = lds + wO[1] + so; PIN(pw0); PIN(pw1); } \
                if ((i) == 3) { pw2 = lds + wO[2] + so; pw3 = lds + wO[3] + so; PIN(pw2); PIN(pw3); } } while (0)
#define G2_NONE(i) do {} while (0)
            WAIT_L0(); BAR(); SBAR();
#pragma unroll 1
            for (int ph = 0; ph < 3; ++ph) {
                const int n = (ph == 0) ? 11 : (ph == 1) ? 1 : 5;
#pragma unroll 1
                for (int i = 0; i < n; ++i) {
                    WAIT_L0(); SBAR(); G2_HALF(x, y, pa1, 4096, true, G2_NEXT);
                    WAIT_L0(); BAR(); SBAR(); G2_HALF(y, x, pa0, 0, true, G2_NONE);
                    WAIT_L0(); SBAR(); G2_HALF(x, y, pa1, 4096, true, G2_NEXT);
                    WAIT_L0(); TS(t0); BAR(); TS(t1); SBAR(); a0 += t1 - t0;
                    G2_HALF(y, x, pa0, 0, true, G2_NONE);
                }
                PEEK_AHEAD(ov, optr, wid | ph);
                if ((wid | (ph ^ 1)) == 0) { PEEK_DONE(ov); wait_all(flags, F_O + depword, ov); }
            }
            const int row = rbk * 128 + wm * 64 + z * 32 + fr, col = cbk * 128 + wn * 64 + (fq & 1) * 16 + (fq >> 1) * 8;
            u32x4 xr[4];
#pragma unroll
            for (int i = 0; i < 4; ++i) xr[i] = *(const u32x4*)(x + (size_t)(row + (i >> 1) * 16) * DM + col + (i & 1) * 32);
            WAIT_L0(); SBAR(); G2_HALF(x, y, pa1, 4096, true, G2_NEXT);
            WAIT_L0(); BAR(); SBAR(); G2_HALF(y, x, pa0, 0, true, G2_NONE);
            WAIT_L0(); SBAR(); G2_HALF(x, y, pa1, 4096, true, G2_NONE);
            WAIT_L0(); SBAR(); G2_HALF(y, x, pa0, 0, false, G2_NONE);
            MFMA_DRAIN(); SBAR();
            STAMP(9); PUT(53, a0);
            f32x4 vk[8];
            const unsigned xo = WS_XCH + (unsigned)(tile * 2 + 1 - z) * 32768u + w4 * 8192 + ln * 16;
            if (z == 0) {
#pragma unroll
                for (int i = 0; i < 8; ++i) { ST16(__builtin_bit_cast(u32x4, acc[2 + (i >> 2)][i & 3]), xo + i * 1024); vk[i] = acc[i >> 2][i & 3]; }
            } else {
#pragma unroll
                for (int i = 0; i < 8; ++i) { ST16(__builtin_bit_cast(u32x4, acc[i >> 2][i & 3]), xo + i * 1024); vk[i] = acc[2 + (i >> 2)][i & 3]; }
            }
            WAIT_V(0);
            arrive(flags, F_X + tile * 2 + 1 - z, ln, lds, 3u);
            STAMP(10);
            CFENCE(); BAR(); CFENCE();
            STAMP(11);
            const unsigned xi = WS_XCH + (unsigned)(tile * 2 + z) * 32768u + w4 * 8192 + ln * 16;
#pragma unroll
            for (int i = 0; i < 8; ++i) vk[i] += __builtin_bit_cast(f32x4, __builtin_amdgcn_raw_buffer_load_b128(wsr, xi + i * 1024, 0, 16));
#pragma unroll
            for (int i = 0; i < 4; ++i) {
                const u32x4 a = __builtin_bit_cast(u32x4, vk[2 * i]), b = __builtin_bit_cast(u32x4, vk[2 * i + 1]);
                unsigned a0_ = a[0], a1 = a[1], a2 = a[2], a3 = a[3], b0 = b[0], b1 = b[1], b2 = b[2], b3 = b[3];
                swap16(a0_, b0); swap16(a1, b1); swap16(a2, b2); swap16(a3, b3);
                const u32x4 xx = xr[i];
                u32x4 w;
                w[0] = cvtpk(u2f(a0_) + bf_lo(xx[0]), u2f(a1) + bf_hi(xx[0]));
                w[1] = cvtpk(u2f(a2) + bf_lo(xx[1]), u2f(a3) + bf_hi(xx[1]));
                w[2] = cvtpk(u2f(b0) + bf_lo(xx[2]), u2f(b1) + bf_hi(xx[2]));
                w[3] = cvtpk(u2f(b2) + bf_lo(xx[3]), u2f(b3) + bf_hi(xx[3]));
                __builtin_amdgcn_raw_buffer_store_b128(w, __builtin_amdgcn_make_buffer_rsrc((void*)out, 0, SEQ * DM * 2, 0x00020000), (unsigned)((row + (i >> 1) * 16) * DM + col + (i & 1) * 32) * 2u, 0, 16);
            }
        }
    }
}

extern "C" void kernel_launch(void* const* d_in, const int* in_sizes, int n_in, void* d_out, int out_size, void* d_ws, size_t ws_size, hipStream_t stream) {
    const bf16_t* x = (const bf16_t*)d_in[0];
    k_main<<<256, 512, 0, stream>>>(x, (const bf16_t*)d_in[1], (const bf16_t*)d_in[2], (const bf16_t*)d_in[4], (const bf16_t*)d_in[5], (const bf16_t*)d_in[6],
                                    (bf16_t*)d_out, (unsigned char*)d_ws);
}
```

```cpp
#include <hip/hip_runtime.h>
#include <stdint.h>

typedef unsigned short bf16_t;
typedef short bf16x8 __attribute__((ext_vector_type(8)));
typedef short s16x4 __attribute__((ext_vector_type(4)));
typedef float f32x2 __attribute__((ext_vector_type(2)));
typedef float f32x4 __attribute__((ext_vector_type(4)));
typedef float f32x16 __attribute__((ext_vector_type(16)));
typedef unsigned u32x2 __attribute__((ext_vector_type(2)));
typedef unsigned u32x4 __attribute__((ext_vector_type(4)));
typedef __bf16 bf16x2_t __attribute__((ext_vector_type(2)));
#define LAS __attribute__((address_space(3)))
#define GAS __attribute__((address_space(1)))
typedef LAS unsigned char* lds_ptr;

constexpr int SEQ = 2048, DM = 1024, FF = 3584, NQKV = 3072, NH = 32;
constexpr float LN_EPS = 1e-5f;
constexpr float LOG2E = 1.4426950408889634f;
constexpr float QSCALE = 0.17677669529663687f * LOG2E;

constexpr unsigned WS_FLAGS = 0;
constexpr unsigned WS_STAT = 192 * 1024;
constexpr unsigned WS_QKV = 256 * 1024;
constexpr unsigned WS_ACT = WS_QKV + 3u * SEQ * DM * 2;
constexpr unsigned WS_XCH = WS_ACT + 144u * SEQ * 64;
constexpr unsigned WS_XC = WS_XCH + 256u * 32768;
constexpr unsigned WS_END = WS_XC + SEQ * DM * 2;
constexpr int F_EPOCH = 0, F_QKV = 1, F_H = 257, F_O = 513, F_X = 769, F_XC = 1025;
constexpr unsigned GRP = SEQ * 64;

#define SBAR() __builtin_amdgcn_sched_barrier(0)
#define BAR() __builtin_amdgcn_s_barrier()
#define WAIT_V(n) asm volatile("s_waitcnt vmcnt(" #n ")" ::: "memory")
#define WAIT_L0() do { __builtin_amdgcn_s_waitcnt(0xC07F); asm volatile("" ::: "memory"); } while (0)
#define CFENCE() asm volatile("" ::: "memory")
#define PIN(x) asm volatile("" : "+v"(x))
#define MFMA16(a, b, c) __builtin_amdgcn_mfma_f32_16x16x32_bf16(a, b, c, 0, 0, 0)
#define MFMA32(a, b, c) __builtin_amdgcn_mfma_f32_32x32x16_bf16(a, b, c, 0, 0, 0)
#define MFMA16_ACC(acc, a, b) asm volatile("v_mfma_f32_16x16x32_bf16 %0, %1, %2, %0" : "+v"(acc) : "v"(a), "v"(b))
#define MFMA_DRAIN() asm volatile("s_nop 15" ::: "memory")
#define RLX_AGENT __ATOMIC_RELAXED, __HIP_MEMORY_SCOPE_AGENT

#define STAMP(i) do {} while (0)
#define TS(v) do {} while (0)
#define PUT(i, v) do {} while (0)

__device__ __forceinline__ void glds16(const void* sbase, unsigned voff, unsigned ldsdst) {
    asm volatile("s_mov_b32 m0, %2\n\ts_nop 0\n\tglobal_load_lds_dwordx4 %0, %1" ::"v"(voff), "s"(sbase), "s"(ldsdst) : "memory");
}
__device__ __forceinline__ void glds16c(const void* sbase, unsigned voff, unsigned ldsdst) {
    asm volatile("s_mov_b32 m0, %2\n\ts_nop 0\n\tglobal_load_lds_dwordx4 %0, %1 sc1" ::"v"(voff), "s"(sbase), "s"(ldsdst) : "memory");
}
__device__ __forceinline__ bf16x8 lds_b128(lds_ptr p) { return *(const LAS bf16x8*)p; }
__device__ __forceinline__ s16x4 lds_tr(lds_ptr p) { return __builtin_amdgcn_ds_read_tr16_b64_v4i16((LAS s16x4*)p); }
__device__ __forceinline__ bf16x8 cat8(s16x4 a, s16x4 b) { return __builtin_shufflevector(a, b, 0, 1, 2, 3, 4, 5, 6, 7); }
__device__ __forceinline__ unsigned cvtpk(float lo, float hi) {
    f32x2 v = {lo, hi};
    return __builtin_bit_cast(unsigned, __builtin_convertvector(v, bf16x2_t));
}
__device__ __forceinline__ float u2f(unsigned u) { return __builtin_bit_cast(float, u); }
__device__ __forceinline__ unsigned f2u(float f) { return __builtin_bit_cast(unsigned, f); }
__device__ __forceinline__ float bf_lo(unsigned u) { return u2f(u << 16); }
__device__ __forceinline__ float bf_hi(unsigned u) { return u2f(u & 0xffff0000u); }
__device__ __forceinline__ void swap16(unsigned& a, unsigned& b) {
    auto r = __builtin_amdgcn_permlane16_swap(a, b, false, false);
    a = r[0]; b = r[1];
}
__device__ __forceinline__ void swap32(unsigned& a, unsigned& b) {
    auto r = __builtin_amdgcn_permlane32_swap(a, b, false, false);
    a = r[0]; b = r[1];
}
__device__ __forceinline__ int opaque_tid() { int t = threadIdx.x; asm volatile("" : "+v"(t)); return t; }
typedef unsigned long long u64;
struct Flags { u64* p; u64 tag; };
__device__ __forceinline__ void signal(const Flags& f, int word, int lane) {
    if (lane == 0) __hip_atomic_store((GAS u64*)(f.p + word * 16), f.tag, RLX_AGENT);
}
__device__ __forceinline__ u64 peek(const Flags& f, int word) {
    u64 v;
    asm volatile("global_load_dwordx2 %0, %1, off sc1\n\ts_waitcnt vmcnt(0)" : "=&v"(v) : "v"(f.p + word * 16) : "memory");
    return v;
}
__device__ __forceinline__ void wait_all(const Flags& f, int word, u64 v) {
#pragma unroll 1
    for (unsigned spin = 0; spin < (1u << 20) && !__all(v == f.tag); ++spin) { __builtin_amdgcn_s_sleep(1); v = peek(f, word); }
}
__device__ __forceinline__ void arrive(const Flags& f, int word, int lane, lds_ptr cnt, unsigned last) {
    if (lane == 0 && __hip_atomic_fetch_add((LAS unsigned*)cnt, 1u, __ATOMIC_RELAXED, __HIP_MEMORY_SCOPE_WORKGROUP) == last)
        __hip_atomic_store((GAS u64*)(f.p + word * 16), f.tag, RLX_AGENT);
}
#define PEEK_AHEAD(v, ptr, sel) asm volatile("s_cmp_lg_u32 %2, 0\n\ts_cbranch_scc1 1f\n\tglobal_load_dwordx2 %0, %1, off sc1\n1:" : "+v"(v) : "v"(ptr), "s"(sel) : "memory", "scc")
struct CodePf { const char* base; unsigned len; int first; };
#define CODE_PF(d) do { unsigned o_ = (unsigned)(cp.first + (opaque_tid() & 15)) * 128u; o_ = (o_ < cp.len) ? o_ : 0u; \
    asm volatile("global_load_dword %0, %1, %2" : "+v"(d) : "v"(o_), "s"(cp.base) : "memory"); } while (0)
#define PEEK_DONE(v) asm volatile("s_waitcnt vmcnt(0)" : "+v"(v) :: "memory")

template <int CTRL> __device__ __forceinline__ float dpp_add(float v) { return v + u2f(__builtin_amdgcn_update_dpp(0u, f2u(v), CTRL, 0xf, 0xf, true)); }
__device__ __forceinline__ float red4(float v) {
    unsigned a = f2u(v), b = a;
    swap16(a, b);
    v = u2f(a) + u2f(b);
    a = f2u(v); b = a;
    swap32(a, b);
    return u2f(a) + u2f(b);
}
__device__ __forceinline__ float wave_sum(float v) {
    v = dpp_add<0xB1>(v); v = dpp_add<0x4E>(v); v = dpp_add<0x141>(v); v = dpp_add<0x140>(v);
    return red4(v);
}
__device__ __forceinline__ f32x2 row_stats(u32x4& a, u32x4& b) {
    float v[16];
#pragma unroll
    for (int i = 0; i < 4; ++i) { v[2 * i] = bf_lo(a[i]); v[2 * i + 1] = bf_hi(a[i]); v[8 + 2 * i] = bf_lo(b[i]); v[8 + 2 * i + 1] = bf_hi(b[i]); }
    float s = 0.f;
#pragma unroll
    for (int i = 0; i < 16; ++i) s += v[i];
    const float mu = wave_sum(s) * (1.0f / DM);
    float q = 0.f;
#pragma unroll
    for (int i = 0; i < 16; ++i) { v[i] -= mu; q += v[i] * v[i]; }
#pragma unroll
    for (int i = 0; i < 4; ++i) { a[i] = cvtpk(v[2 * i], v[2 * i + 1]); b[i] = cvtpk(v[8 + 2 * i], v[8 + 2 * i + 1]); }
    return (f32x2){mu, 1.0f / __builtin_sqrtf(wave_sum(q) * (1.0f / DM) + LN_EPS)};
}

constexpr int SLOT = 32768, LDS_BYTES = 5 * SLOT;
constexpr int G1_A = 0, G1_W = 65536, G1_STAT = G1_W + 5 * 16384, G1_CS = G1_STAT + 2048, G1_CNT = G1_CS + 4 * 6 * 64;

#define NH0 30
static_assert(NH0 % 2 == 0 && NH0 >= 28 && NH0 <= 32, "");
struct G2 { const char* Ah; const char* Ao; const char* Wd; const char* Wo; int nh; };
__device__ __forceinline__ void g2_load_b(const G2& g, int j, unsigned dst, int vw, unsigned voffB) {
    const char* b = (j >= g.nh ? g.Wo : g.Wd) + (size_t)(j * 64 + vw * 8) * (DM * 2);
    glds16(b, voffB, dst + 16384 + vw * 1024); glds16(b + 128, voffB, dst + 16384 + 8192 + vw * 1024);
}
__device__ __forceinline__ void g2_load_a(const G2& g, int j, unsigned dst, int vw, unsigned voffA0, unsigned voffA1) {
    const char* a = (j >= g.nh ? g.Ao : g.Ah) + (size_t)j * (2 * GRP) + vw * 1024;
    glds16c(a, voffA0, dst + vw * 2048); glds16c(a, voffA1, dst + vw * 2048 + 1024);
}
__device__ __forceinline__ void g2_voff(int lane, unsigned& voffA0, unsigned& voffA1, unsigned& voffB0, unsigned& voffB1) {
    const int r8 = lane >> 3, u8 = lane & 7, ua = u8 ^ (r8 >> 1), ub = ua ^ 4;
    voffA0 = (unsigned)(ua >> 2) * GRP + r8 * 64 + (ua & 3) * 16;
    voffA1 = (unsigned)(ub >> 2) * GRP + (r8 + 8) * 64 + (ub & 3) * 16;
    voffB0 = (unsigned)(r8 * (DM * 2) + (u8 ^ (r8 & 2)) * 16);
    voffB1 = (unsigned)(r8 * (DM * 2) + (u8 ^ (4 | (r8 & 2))) * 16);
}

struct AttnOut { f32x16 o0, o1; f32x4 l0, l1; float m0, m1; };
struct HPend { u32x4 v[8]; unsigned off, step; const void* base; };
struct AttnLane {
    unsigned voffK, voffV; lds_ptr kb0, kb1, vb; const char* qp; bf16x8 ones;
    __device__ __forceinline__ void init(lds_ptr lds, const char* Qg, int wid, int lane) {
        const int r32 = lane & 31, hi = lane >> 5, kh = wid >> 2, qg = wid & 3, ksw = (r32 >> 2) & 3;
        const int uk = (lane & 3) ^ ((lane >> 4) & 3), uv = lane & 3;
        voffK = (unsigned)((uk >> 1) * 65536 + wid * 1024 + (lane >> 2) * 32 + (uk & 1) * 16);
        voffV = (unsigned)((uv >> 1) * 65536 + wid * 1024 + (lane >> 2) * 32 + (uv & 1) * 16);
        kb0 = lds + kh * 8192 + r32 * 64 + ((hi ^ ksw) * 16); kb1 = lds + kh * 8192 + r32 * 64 + (((2 | hi) ^ ksw) * 16);
        vb = lds + 16384 + kh * 8192 + (4 * hi + ((lane >> 2) & 3)) * 64 + ((lane >> 4) & 1) * 32 + (lane & 3) * 8;
        qp = Qg + (qg * 64 + r32) * 32 + hi * 16;
        const short v = (((lane >> 4) & 1) == ((lane >> 2) & 1)) ? (short)0x3f80 : (short)0;
        ones = (bf16x8){v, v, v, v, v, v, v, v};
    }
};
#define EX(v) __builtin_amdgcn_exp2f(v)
#define AT_LOAD_K(c, slot) do { const unsigned d_ = lds0 + wid * 2048 + (slot) * SLOT; const char* k_ = Kg + (c) * 8192; glds16c(k_, A.voffK, d_); glds16c(k_ + 512, A.voffK, d_ + 1024); } while (0)
#define AT_LOAD_V(c, slot) do { const unsigned d_ = lds0 + wid * 2048 + (slot) * SLOT + 16384; const char* v_ = Vg + (c) * 8192; glds16c(v_, A.voffV, d_); glds16c(v_ + 512, A.voffV, d_ + 1024); } while (0)
#define AT_PACK(S, i) __builtin_bit_cast(bf16x8, (u32x4){cvtpk(S[i], S[i + 1]), cvtpk(S[i + 2], S[i + 3]), cvtpk(S[i + 4], S[i + 5]), cvtpk(S[i + 6], S[i + 7])})
#define AT_Q_LOAD() asm volatile("global_load_dwordx4 %0, %4, off sc1\n\tglobal_load_dwordx4 %1, %4, off offset:1024 sc1\n\tglobal_load_dwordx4 %2, %5, off sc1\n\tglobal_load_dwordx4 %3, %5, off offset:1024 sc1" \
        : "=&v"(q00), "=&v"(q10), "=&v"(q01), "=&v"(q11) : "v"(A.qp), "v"(qp2) : "memory")
#define AT_Q_WAIT(n) asm volatile("s_waitcnt vmcnt(" #n ")" : "+v"(q00), "+v"(q01), "+v"(q10), "+v"(q11) :: "memory")
__device__ __forceinline__ float rowmax16(const f32x16& s) {
    float a = s[0];
#pragma unroll
    for (int r = 1; r < 16; ++r) a = __builtin_fmaxf(a, s[r]);
    unsigned x = f2u(a), y = x; swap32(x, y);
    return __builtin_fmaxf(u2f(x), u2f(y));
}

__device__ __forceinline__ void attn_safe(lds_ptr lds, unsigned lds0, const char* Qg, const char* Kg, const char* Vg, int wid, AttnOut& R) {
    AttnLane A; A.init(lds, Qg, wid, opaque_tid() & 63);
    bf16x8 q00, q01, q10, q11; const char* qp2 = A.qp + 65536;
    AT_Q_LOAD(); AT_Q_WAIT(0);
    f32x16 o0 = (f32x16){}, o1 = (f32x16){};
    f32x4 l0 = (f32x4){0.f, 0.f, 0.f, 0.f}, l1 = l0;
    float m0 = -__builtin_inff(), m1 = m0;
#pragma unroll 1
    for (int c = 0; c < 8; ++c) {
        AT_LOAD_K(c, 3); AT_LOAD_V(c, 3);
        WAIT_V(0); BAR(); CFENCE();
#pragma unroll 1
        for (int t = 0; t < 4; ++t) {
            const int to = 3 * SLOT + t * 2048;
            const bf16x8 ka = lds_b128(A.kb0 + to), kb = lds_b128(A.kb1 + to);
            const bf16x8 va = cat8(lds_tr(A.vb + to), lds_tr(A.vb + to + 512)), vc = cat8(lds_tr(A.vb + to + 1024), lds_tr(A.vb + to + 1536));
            f32x16 s0 = MFMA32(kb, q01, MFMA32(ka, q00, (f32x16){})), s1 = MFMA32(kb, q11, MFMA32(ka, q10, (f32x16){}));
            const float n0 = __builtin_fmaxf(m0, rowmax16(s0)), n1 = __builtin_fmaxf(m1, rowmax16(s1));
            const float f0 = EX(m0 - n0), f1 = EX(m1 - n1);
            m0 = n0; m1 = n1; o0 *= f0; o1 *= f1; l0 *= f0; l1 *= f1;
#pragma unroll
            for (int r = 0; r < 16; ++r) { s0[r] = EX(s0[r] - n0); s1[r] = EX(s1[r] - n1); }
            const bf16x8 p00 = AT_PACK(s0, 0), p01 = AT_PACK(s0, 8), p10 = AT_PACK(s1, 0), p11 = AT_PACK(s1, 8);
            o0 = MFMA32(vc, p01, MFMA32(va, p00, o0)); l0 = MFMA16(A.ones, p01, MFMA16(A.ones, p00, l0));
            o1 = MFMA32(vc, p11, MFMA32(va, p10, o1)); l1 = MFMA16(A.ones, p11, MFMA16(A.ones, p10, l1));
        }
        WAIT_L0(); BAR(); CFENCE();
    }
    R.o0 = o0; R.o1 = o1; R.l0 = l0; R.l1 = l1; R.m0 = m0; R.m1 = m1;
}

__device__ __forceinline__ void attn_fast(lds_ptr lds, unsigned lds0, const char* Qg, const char* Kg, const char* Vg, const G2& g2, const Flags& flags, int qkv0, int qkvword, int depword,
                                          int hsig, int c0, bool bump, const CodePf& cp, const HPend& hp, const char* wpd, const char* wpo, int prow, int wid, AttnOut& R
                                          ) {
    const int lane = opaque_tid() & 63;
    AttnLane A; A.init(lds, Qg, wid, lane);
    const lds_ptr kb0 = A.kb0, kb1 = A.kb1, vb = A.vb;
    const bf16x8 ones = A.ones;
    const __amdgpu_buffer_rsrc_t hr = __builtin_amdgcn_make_buffer_rsrc((void*)hp.base, 0, 8192, 0x00020000);
    f32x16 o0 = (f32x16){}, o1 = (f32x16){}, negm0 = (f32x16){}, negm1 = (f32x16){}, sA0, sA1, sB0, sB1;
    f32x4 l0 = (f32x4){0.f, 0.f, 0.f, 0.f}, l1 = l0;
    bf16x8 kfa, kfb, va, vc = (bf16x8){}, pf = (bf16x8){};
    PIN(o0); PIN(o1); PIN(l0); PIN(l1); PIN(vc); PIN(pf); PIN(A.voffK); PIN(A.voffV);
    unsigned gA0, gA1, gB0, gB1; g2_voff(lane, gA0, gA1, gB0, gB1);
    unsigned gB = (wid & 1) ? gB1 : gB0;
    unsigned pfo = (unsigned)(((lane >> 3) & 1) * (DM * 2) + (lane & 7) * 128);
    const u64* hptr = flags.p + (F_H + depword) * 16;
    const u64* qptr = flags.p + qkvword * 16;
    PIN(gA0); PIN(gA1); PIN(gB); PIN(pfo); PIN(hptr); PIN(qptr);
    if (wid == 0) {
        wait_all(flags, qkv0, peek(flags, qkv0));
        STAMP(13);
    }
    CFENCE(); BAR(); CFENCE();
    STAMP(4);
    AT_LOAD_K(c0, 0);
    glds16c(Qg, A.voffK, lds0 + wid * 2048 + 2 * SLOT); glds16c(Qg + 512, A.voffK, lds0 + wid * 2048 + 2 * SLOT + 1024);
    AT_LOAD_V(c0, 0);
    u64 hv = 0;
    float m0, m1;
    WAIT_V(2);
    PEEK_AHEAD(hv, qptr, wid);
    BAR(); SBAR();
    STAMP(15);
    const int qo = 2 * SLOT + (wid & 3) * 4096 - (wid >> 2) * 8192;
    const bf16x8 q00 = lds_b128(kb0 + qo), q10 = lds_b128(kb0 + qo + 2048), q01 = lds_b128(kb1 + qo), q11 = lds_b128(kb1 + qo + 2048);
    {
        kfa = lds_b128(kb0); kfb = lds_b128(kb1);
        sA0 = MFMA32(kfa, q00, negm0); sA1 = MFMA32(kfa, q10, negm1); sA0 = MFMA32(kfb, q01, sA0); sA1 = MFMA32(kfb, q11, sA1);
        kfa = lds_b128(kb0 + 2048); kfb = lds_b128(kb1 + 2048);
        m0 = rowmax16(sA0); m1 = rowmax16(sA1);
#pragma unroll
        for (int r = 0; r < 16; ++r) { sA0[r] -= m0; sA1[r] -= m1; negm0[r] = -m0; negm1[r] = -m1; }
        PIN(negm0); PIN(negm1);
    }
    unsigned pfd = 0;
    PEEK_DONE(hv);
    if (wid == 0) {
        wait_all(flags, qkvword, hv);
        if (bump) signal(flags, F_EPOCH, lane);
        STAMP(14);
    }
    CFENCE(); BAR(); SBAR();
    STAMP(5);
    AT_LOAD_K((c0 + 1) & 7, 1); AT_LOAD_V((c0 + 1) & 7, 1);
    STAMP(19);
#define AT_GROUP(QK, RD, S, i, O, L, VF) do { \
        QK; SBAR(); RD; SBAR(); \
        S[i] = EX(S[i]); S[i + 1] = EX(S[i + 1]); S[i + 2] = EX(S[i + 2]); PIN(S); SBAR(); \
        O = MFMA32(VF, pf, O); SBAR(); \
        S[i + 3] = EX(S[i + 3]); S[i + 4] = EX(S[i + 4]); S[i + 5] = EX(S[i + 5]); PIN(S); SBAR(); \
        L = MFMA16(ones, pf, L); SBAR(); \
        S[i + 6] = EX(S[i + 6]); S[i + 7] = EX(S[i + 7]); \
        pf = AT_PACK(S, i); PIN(pf); SBAR(); } while (0)
#define AT_STEP(C0, C1, N0, N1, KO, VO, H0, H1, H2, H3) do { \
        AT_GROUP(N0 = MFMA32(kfa, q00, negm0), { va = cat8(lds_tr(vb + (VO)), lds_tr(vb + (VO) + 512)); H0; }, C0, 0, o1, l1, vc); \
        AT_GROUP(N1 = MFMA32(kfa, q10, negm1), { kfa = lds_b128(kb0 + (KO)); vc = cat8(lds_tr(vb + (VO) + 1024), lds_tr(vb + (VO) + 1536)); H1; }, C1, 0, o0, l0, va); \
        AT_GROUP(N0 = MFMA32(kfb, q01, N0), H2, C0, 8, o1, l1, va); \
        AT_GROUP(N1 = MFMA32(kfb, q11, N1), { kfb = lds_b128(kb1 + (KO)); H3; }, C1, 8, o0, l0, vc); } while (0)
#define PRV 1
#define YP(p) asm volatile("s_cmp_lt_u32 %0, 4\n\ts_cbranch_scc1 1f\n\ts_setprio " #p "\n1:" :: "s"(wid) : "scc")
    if (PRV) __builtin_amdgcn_s_setprio(1);
    auto ylow = [&]() { if (PRV) YP(0); };
#define S0(c, ...) AT_STEP(sA0, sA1, sB0, sB1, ((c) & 3) * SLOT + 4096, ((c) & 3) * SLOT, __VA_ARGS__)
#define S1(c, ...) if (PRV == 1) YP(0); AT_STEP(sB0, sB1, sA0, sA1, ((c) & 3) * SLOT + 6144, ((c) & 3) * SLOT + 2048, __VA_ARGS__)
#define S2(c, ...) if (PRV == 1 || (PRV == 2 && (c) == 7)) YP(2); AT_STEP(sA0, sA1, sB0, sB1, (((c) == 7 ? 7 : (c) + 1) & 3) * SLOT, ((c) & 3) * SLOT + 4096, __VA_ARGS__)
#define S3(c, ...) AT_STEP(sB0, sB1, sA0, sA1, (((c) == 7 ? 7 : (c) + 1) & 3) * SLOT + 2048, ((c) & 3) * SLOT + 6144, __VA_ARGS__)
#define SY(c, n) do { WAIT_V(n); WAIT_L0(); BAR(); SBAR(); STAMP(20 + (c)); } while (0)
#define NO (void)0
#define KP(c, i) glds16c(Kg + ((c0 + (c)) & 7) * 8192 + (i) * 512, A.voffK, lds0 + wid * 2048 + ((c) & 3) * SLOT + (i) * 1024)
#define VP(c, i) glds16c(Vg + ((c0 + (c)) & 7) * 8192 + (i) * 512, A.voffV, lds0 + wid * 2048 + ((c) & 3) * SLOT + 16384 + (i) * 1024)
#define CH(c) KP(c, 0), KP(c, 1), VP(c, 0), VP(c, 1)
#define HS(i) do { __builtin_amdgcn_raw_buffer_store_b128(hp.v[i], hr, hp.off + (i) * hp.step, 0, 16); CFENCE(); } while (0)
#define WP(i) wpf(i)
#define GB(j, slot, i) glds16(g2.Wd + (size_t)((j) * 64 + wid * 8) * (DM * 2) + (i) * 128, gB, lds0 + (slot) * SLOT + 16384 + (i) * 8192 + wid * 1024)
#define GA(j, slot, i) glds16c(g2.Ah + (size_t)(j) * (2 * GRP) + wid * 1024, (i) ? gA1 : gA0, lds0 + (slot) * SLOT + wid * 2048 + (i) * 1024)
    auto wpf = [&](int i) {
        const int pi = (wid + 8 * i < g2.nh + 8) ? wid + 8 * i : 0, lr = prow + 2 * pi;
        asm volatile("global_load_dword %0, %1, %2" : "+v"(pfd) : "v"(pfo), "s"((lr < g2.nh * 64 ? wpd : wpo) + (size_t)lr * (DM * 2)) : "memory");
    };
    auto cpf = [&]() { CODE_PF(pfd); };
    auto hpeek = [&]() { PEEK_AHEAD(hv, hptr, wid); };
    S0(0, NO, NO, NO, NO);         S1(0, NO, NO, NO, NO);    SY(0, 0);  S2(0, CH(2)); S3(0, CH(3));
    S0(1, WP(0), NO, HS(0), NO);   S1(1, NO, NO, HS(1), NO); SY(1, 7);  S2(1, CH(4)); S3(1, WP(1), NO, HS(2), NO);
    S0(2, NO, NO, HS(3), NO);      S1(2, NO, NO, HS(4), NO); SY(2, 11); S2(2, CH(5)); S3(2, WP(2), NO, HS(5), NO);
    S0(3, NO, NO, HS(6), NO);      S1(3, NO, NO, HS(7), NO); SY(3, 12); S2(3, CH(6)); S3(3, WP(3), NO, NO, NO);
    S0(4, NO, NO, NO, NO);         S1(4, NO, NO, NO, NO);    SY(4, 5);
    if (wid == 0) signal(flags, hsig, lane);
                                                                        S2(4, CH(7)); S3(4, WP(4), NO, NO, NO);
    S0(5, GB(0, 4, 0), GB(0, 4, 1), NO, NO); S1(5, NO, NO, NO, NO); SY(5, 2); S2(5, GB(1, 0, 0), GB(1, 0, 1), NO, NO); S3(5, NO, NO, NO, NO);
    S0(6, hpeek(), NO, NO, NO);
    if (wid == 0) { PEEK_DONE(hv); wait_all(flags, F_H + depword, hv); }
                                   S1(6, NO, NO, NO, NO);    SY(6, 0);  S2(6, GA(0, 4, 0), GA(0, 4, 1), GB(2, 1, 0), GB(2, 1, 1)); S3(6, GA(1, 0, 0), GA(1, 0, 1), cpf(), NO);
    S0(7, GA(2, 1, 0), GA(2, 1, 1), NO, NO); S1(7, NO, NO, NO, NO); SY(7, 0); S2(7, NO, NO, NO, NO); S3(7, NO, NO, ylow(), NO);
    __builtin_amdgcn_s_setprio(0);
#undef S0
#undef S1
#undef S2
#undef S3
#undef NO
    STAMP(28);
    o1 = MFMA32(vc, pf, o1); l1 = MFMA16(ones, pf, l1);
    asm volatile("" :: "v"(pfd));
    R.o0 = o0; R.o1 = o1; R.l0 = l0; R.l1 = l1; R.m0 = m0; R.m1 = m1;
}

__global__ __launch_bounds__(512, 2) void k_main(const bf16_t* __restrict__ x, const bf16_t* __restrict__ wqkv, const bf16_t* __restrict__ wo, const bf16_t* __restrict__ wg,
                                                 const bf16_t* __restrict__ wu, const bf16_t* __restrict__ wd, bf16_t* __restrict__ out, unsigned char* ws) {
    __shared__ __attribute__((aligned(16))) unsigned char smem[LDS_BYTES];
    const lds_ptr lds = (lds_ptr)smem;
    const unsigned lds0 = (unsigned)(uintptr_t)lds;
    const int wid = __builtin_amdgcn_readfirstlane(threadIdx.x >> 6);
    const int bid = blockIdx.x, xcd = bid & 7, idx = bid >> 3;
    Flags flags; flags.p = (u64*)(ws + WS_FLAGS);
    const __amdgpu_buffer_rsrc_t wsr = __builtin_amdgcn_make_buffer_rsrc((void*)ws, 0, WS_END, 0x00020000);
#define ST16(v, off) __builtin_amdgcn_raw_buffer_store_b128(v, wsr, off, 0, 16)
    STAMP(0);
    unsigned cpd = 0;
    CodePf cp;
    asm volatile("s_getpc_b64 %0\n.Lk_begin:\n\ts_mov_b32 %1, .Lk_end-.Lk_begin" : "=s"(cp.base), "=s"(cp.len));
    cp.first = idx * 16;
    HPend hp;
    {
        const int lane = threadIdx.x & 63, wr = wid >> 2, wc = wid & 3, js = idx & 3, rb = idx >> 2;
        const bool typeY = (js == 3);
        const int f0 = 448 * xcd + 128 * js;
        const char* P0 = (const char*)(wg + f0);
        const char* P2 = (const char*)(wu + f0);
        const char* P1 = typeY ? (const char*)(wqkv + 128 * xcd + 64) : (const char*)(wg + f0 + 64);
        const char* P3 = typeY ? (const char*)(wqkv + 1024 + 128 * xcd + 64) : (const char*)(wu + f0 + 64);
        const char* P4 = typeY ? (const char*)(wqkv + 2048 + 128 * xcd + 64) : (const char*)(wqkv + js * 1024 + 128 * xcd);
        const unsigned stepF = 32u * FF * 2u, stepQ = 32u * NQKV * 2u, step13 = typeY ? stepQ : stepF;
        const char* XA = (const char*)x + (size_t)rb * 256 * DM * 2;
        const char* XC = (const char*)ws + WS_XC + (size_t)rb * 256 * DM * 2;
        const int r8 = lane >> 3, u8 = lane & 7;
        const int gw = (r8 & 2) | ((wc & 1) << 2);
        const unsigned voffF = (unsigned)(((wc * 8 + r8) * FF + (u8 ^ gw) * 8) * 2);
        const unsigned voffQ = (unsigned)(((wc * 8 + r8) * NQKV + (u8 ^ gw) * 8) * 2);
        const unsigned voff13 = typeY ? voffQ : voffF;
        const unsigned voffA0 = (unsigned)(((32 * wid + r8) * DM + (u8 ^ (r8 >> 1)) * 8) * 2);
        const unsigned voffA1 = (unsigned)(((32 * wid + 8 + r8) * DM + (u8 ^ (4 | (r8 >> 1))) * 8) * 2);
        const unsigned dstA = lds0 + G1_A + wid * 4096, dstW = lds0 + G1_W + wc * 1024;
#define G1_LOAD_A(t) do { const char* b_ = XA + (t) * 128; const unsigned d_ = dstA + ((t) & 1) * 32768; \
        glds16(b_, voffA0, d_); glds16(b_, voffA1, d_ + 1024); glds16(b_ + 32768, voffA0, d_ + 2048); glds16(b_ + 32768, voffA1, d_ + 3072); } while (0)
#define G1_LOAD_W(hs) do { const unsigned d_ = dstW + ((hs) & 3) * 4096; \
        glds16(P0 + (size_t)(hs) * stepF, voffF, d_); glds16(P1 + (size_t)(hs) * step13, voff13, d_ + 16384); glds16(P2 + (size_t)(hs) * stepF, voffF, d_ + 32768); \
        glds16(P3 + (size_t)(hs) * step13, voff13, d_ + 49152); glds16(P4 + (size_t)(hs) * stepQ, voffQ, d_ + 65536); } while (0)
        STAMP(29);
        if (wr == 1) G1_LOAD_W(0);
        G1_LOAD_A(0);
        u32x4 xa0, xa1;
        const int srow = rb * 256 + (js * 8 + xcd) * 8 + wid;
        asm volatile("global_load_dwordx4 %0, %2, %3\n\tglobal_load_dwordx4 %1, %2, %3 offset:1024"
                     : "=&v"(xa0), "=&v"(xa1) : "v"((unsigned)(lane * 16)), "s"((const char*)x + (size_t)srow * DM * 2) : "memory");
        CODE_PF(cpd);
        STAMP(30);

        int fr = lane & 15, fq = lane >> 4;
        const lds_ptr aB0 = lds + G1_A + (wr * 128 + fr) * 128 + ((fq ^ (fr >> 1)) * 16);
        const lds_ptr aB1 = lds + G1_A + (wr * 128 + fr) * 128 + (((4 | fq) ^ (fr >> 1)) * 16);
        const int tq = (lane >> 2) & 3, tp = lane & 3, tg = (tq & 2) | ((fq & 1) << 2);
        auto wbase = [&](int panel, int bp) { return lds + G1_W + panel * 16384 + (8 * fq + tq) * 128 + (((2 * bp + (tp >> 1)) ^ tg) * 16) + (tp & 1) * 8; };
        const int pX = wc >> 1, bX = 2 * (wc & 1);
        const lds_ptr wB0 = typeY ? wbase(0, wc) : wbase(pX, bX);
        const lds_ptr wB1 = typeY ? wbase(1, wc) : wbase(pX, bX + 1);
        const lds_ptr wB2 = typeY ? wbase(2, wc) : wbase(2 + pX, bX);
        const lds_ptr wB3 = typeY ? wbase(3, wc) : wbase(2 + pX, bX + 1);
        const lds_ptr wB4 = wbase(4, wc);
        if (threadIdx.x == 0) *(LAS unsigned*)(lds + G1_CNT) = 0u;
        f32x4 acc[8][5];
#pragma unroll
        for (int m = 0; m < 8; ++m)
#pragma unroll
            for (int b = 0; b < 5; ++b) { acc[m][b] = (f32x4){0.f, 0.f, 0.f, 0.f}; PIN(acc[m][b]); }
        if (wr == 1) { WAIT_V(7); BAR(); }
        asm volatile("s_waitcnt vmcnt(1)" : "+v"(xa0), "+v"(xa1) :: "memory");
        STAMP(31);
        u64 sg;
        {
            const f32x2 st = row_stats(xa0, xa1);
            G1_LOAD_W(1 + wr);
            ST16(xa0, (lane >= 32) ? WS_XC + (unsigned)srow * 2048u + lane * 16 : WS_END);
            ST16(xa1, WS_XC + (unsigned)srow * 2048u + 1024 + lane * 16);
            __builtin_amdgcn_raw_buffer_store_b64(__builtin_bit_cast(u32x2, st), wsr, (lane == 0) ? WS_STAT + (unsigned)srow * 8u : WS_END, 0, 16);
            asm volatile("global_load_dwordx2 %0, %1, %2 sc1" : "=&v"(sg) : "v"(0u), "s"(flags.p) : "memory");
        }
        STAMP(32);
        bf16x8 af[8], wf[5];
        float cs0 = 0.f, cs1 = 0.f, cs2 = 0.f;
#define CS1(c, w) "v_dot2c_f32_bf16 %" #c ", %25, %" #w "\n\t"
#define G1_CSUM(t, lim) do { const u32x4 w0_ = __builtin_bit_cast(u32x4, wf[0]), w1_ = __builtin_bit_cast(u32x4, wf[1]), w2_ = __builtin_bit_cast(u32x4, wf[2]), \
            w3_ = __builtin_bit_cast(u32x4, wf[3]), w4_ = __builtin_bit_cast(u32x4, wf[4]); \
        asm volatile("s_cmp_gt_u32 %24, %26\n\ts_cbranch_scc1 2f\n\ts_cmp_lg_u32 %23, 0\n\ts_cbranch_scc1 1f\n\t" \
            CS1(0, 3) CS1(1, 7) CS1(2, 19) CS1(0, 4) CS1(1, 8) CS1(2, 20) CS1(0, 5) CS1(1, 9) CS1(0, 6) CS1(1, 10) "s_nop 1\n\ts_branch 2f\n1:\n\t" \
            CS1(0, 11) CS1(1, 15) CS1(2, 21) CS1(0, 12) CS1(1, 16) CS1(2, 22) CS1(0, 13) CS1(1, 17) CS1(0, 14) CS1(1, 18) "s_nop 2\n2:" \
            : "+v"(cs0), "+v"(cs1), "+v"(cs2) \
            : "v"(w0_[0]), "v"(w0_[1]), "v"(w0_[2]), "v"(w0_[3]), "v"(w1_[0]), "v"(w1_[1]), "v"(w1_[2]), "v"(w1_[3]), "v"(w2_[0]), "v"(w2_[1]), "v"(w2_[2]), "v"(w2_[3]), \
              "v"(w3_[0]), "v"(w3_[1]), "v"(w3_[2]), "v"(w3_[3]), "v"(w4_[0]), "v"(w4_[1]), "v"(w4_[2]), "v"(w4_[3]), "s"(wr), "s"(t), "s"(0x3f803f80u), "i"(lim) : "scc"); } while (0)
#define G1_RW(i, B, slot) wf[i] = cat8(lds_tr(B + (slot) * 4096), lds_tr(B + (slot) * 4096 + 512))
#define G1_RA(m, AB, abuf) af[m] = lds_b128(AB + (abuf) * 32768 + (m) * 2048)
#define G1_SEG(AB, abuf, slot, L0, L1, L2, L3, L4, CLOSE) do { \
        L0; G1_RW(0, wB0, slot); G1_RW(1, wB1, slot); L1; G1_RW(2, wB2, slot); G1_RW(3, wB3, slot); L2; G1_RW(4, wB4, slot); G1_RA(0, AB, abuf); G1_RA(1, AB, abuf); \
        L3; G1_RA(2, AB, abuf); G1_RA(3, AB, abuf); G1_RA(4, AB, abuf); L4; G1_RA(5, AB, abuf); G1_RA(6, AB, abuf); G1_RA(7, AB, abuf); \
        SBAR(); G1_CSUM(t, 3); WAIT_L0(); SBAR(); if (CLOSE) { BAR(); SBAR(); } } while (0)
#define G1_SEG_A(abuf, slot, tt) do { const char* b_ = ((tt) < 4 ? XA : XC) + (tt) * 128; const unsigned d_ = dstA + (1 - (abuf)) * 32768; \
        G1_SEG(aB0, abuf, slot, glds16c(b_, voffA0, d_), glds16c(b_, voffA1, d_ + 1024), glds16c(b_ + 32768, voffA0, d_ + 2048), glds16c(b_ + 32768, voffA1, d_ + 3072), (void)0, true); } while (0)
#define G1_SEG_W(abuf, slot, hs, dslot, CLOSE) do { const unsigned d_ = dstW + (dslot) * 4096; \
        G1_SEG(aB1, abuf, slot, glds16(P0 + (size_t)(hs) * stepF, voffF, d_), glds16(P1 + (size_t)(hs) * step13, voff13, d_ + 16384), glds16(P2 + (size_t)(hs) * stepF, voffF, d_ + 32768), \
               glds16(P3 + (size_t)(hs) * step13, voff13, d_ + 49152), glds16(P4 + (size_t)(hs) * stepQ, voffQ, d_ + 65536), CLOSE); } while (0)
#define G1_MMA() do { __builtin_amdgcn_s_setprio(1); \
        _Pragma("unroll") for (int b = 0; b < 5; ++b) _Pragma("unroll") for (int i = 0; i < 8; ++i) { const int m = (b & 1) ? 7 - i : i; MFMA16_ACC(acc[m][b], wf[b], af[m]); } \
        __builtin_amdgcn_s_setprio(0); SBAR(); } while (0)

        STAMP(33);
        WAIT_L0();
        STAMP(34);
        BAR();
        SBAR();
        STAMP(1);
        flags.tag = 0;
        const u64* sgp = flags.p + (F_XC + rb * 32 + (lane & 31)) * 16;
#pragma unroll 1
        for (int t = 0; t < 16; t += 2) {
            const int h1 = (2 * t + 3 + wr < 31) ? 2 * t + 3 + wr : 31, h2 = (2 * t + 5 + wr < 31) ? 2 * t + 5 + wr : 31, t2 = (t < 14) ? t + 2 : 15;
            const int sel = (t == 12) ? 0 : ((t ^ 2) | wid);
            STAMP(35 + (t >> 1));
            PEEK_AHEAD(sg, sgp, sel);
            if (t == 14) CODE_PF(cpd);
            G1_SEG_A(0, 0, t + 1); G1_MMA(); asm volatile("s_waitcnt vmcnt(4)" : "+v"(sg), "+v"(cpd) :: "memory"); BAR(); SBAR();
            if (t == 0) {
                const unsigned elo = __builtin_amdgcn_readfirstlane((unsigned)sg), ehi = __builtin_amdgcn_readfirstlane((unsigned)(sg >> 32));
                flags.tag = (((u64)ehi << 32) | elo) * 0x9E3779B97F4A7C15ull + 0x632BE59BD9B4E019ull;
                if (wid == 4) signal(flags, F_XC + bid, lane);
            }
            if (sel == 0) {
                if (t == 12) *(LAS u64*)(lds + G1_STAT + (wid * 32 + (opaque_tid() & 31)) * 8) = sg;
                else wait_all(flags, F_XC + rb * 32 + (opaque_tid() & 31), sg);
            }
            G1_SEG_W(0, 1, h1, (3 + wr) & 3, true); G1_MMA(); WAIT_V(5); BAR(); SBAR();
            G1_SEG_A(1, 2, t2); G1_MMA(); WAIT_V(4); BAR(); SBAR();
            G1_SEG_W(1, 3, h2, (1 + wr) & 3, false);
            if (t == 2) {
                const int tl = opaque_tid() & 63;
                const lds_ptr cp = lds + G1_CS + wc * 384 + (tl & 15) * 4;
                *(LAS float*)(cp + wr * 128) = red4(cs0); *(LAS float*)(cp + wr * 128 + 64) = red4(cs1); *(LAS float*)(cp + 256 + wr * 64) = red4(cs2);
                sgp = (const u64*)(ws + WS_STAT) + rb * 256 + wid * 32 + (tl & 31);
                WAIT_L0();
            }
            SBAR(); BAR(); SBAR(); G1_MMA(); WAIT_V(5);
            if (wr == 0 || t < 14) BAR();
            SBAR();
        }
        MFMA_DRAIN(); SBAR();
        STAMP(2);

        const int tz = opaque_tid(), ln = tz & 63;
        fr = tz & 15; fq = (tz >> 4) & 3;
        const int row0 = rb * 256 + wr * 128 + fr;
        f32x4 csv[5]; f32x2 st[8];
#pragma unroll
        for (int b = 0; b < 5; ++b) csv[b] = *(const LAS f32x4*)(lds + G1_CS + wc * 384 + b * 64 + fq * 16);
        csv[4] += *(const LAS f32x4*)(lds + G1_CS + wc * 384 + 320 + fq * 16);
#pragma unroll
        for (int m = 0; m < 8; ++m) st[m] = *(const LAS f32x2*)(lds + G1_STAT + (wr * 128 + m * 16 + fr) * 8);
        auto lnv = [&](int m, int b, float sc) { return (acc[m][b] - st[m].x * csv[b]) * (st[m].y * sc); };
        auto glu = [&](int m, int bg, int bu) {
            const f32x4 g = lnv(m, bg, -LOG2E), u = lnv(m, bu, 1.0f);
            f32x4 h;
#pragma unroll
            for (int j = 0; j < 4; ++j) h[j] = u[j] * __builtin_amdgcn_rcpf(1.0f + __builtin_amdgcn_exp2f(g[j]));
            return h;
        };
        auto pack2 = [&](const f32x4& v0, const f32x4& v1) {
            unsigned p0 = cvtpk(v0[0], v0[1]), p1 = cvtpk(v0[2], v0[3]), q0 = cvtpk(v1[0], v1[1]), q1 = cvtpk(v1[2], v1[3]);
            swap16(p0, q0); swap16(p1, q1);
            return (u32x4){p0, p1, q0, q1};
        };
        const int hd = 4 * xcd + (typeY ? 2 : 0) + (wc >> 1);
        const unsigned qoff = WS_QKV + (unsigned)(hd * 2 + (wc & 1)) * 65536u + (unsigned)(row0 + (fq & 1) * 16) * 32u + (fq >> 1) * 16;
        constexpr unsigned KIND = NH * 2 * 65536u;
        if (!typeY) {
            const float sc4 = (js == 0) ? QSCALE : 1.0f;
#pragma unroll
            for (int m = 0; m < 8; m += 2) ST16(pack2(lnv(m, 4, sc4), lnv(m + 1, 4, sc4)), qoff + js * KIND + m * 512);
            CFENCE();
            hp.base = ws + WS_ACT + (unsigned)(14 * xcd + 4 * js + wc) * GRP + (unsigned)(rb * 256 + wr * 128) * 64u;
            hp.off = fr * 64 + (fq & 1) * 32 + (fq >> 1) * 16; hp.step = 1024;
#pragma unroll
            for (int m = 0; m < 8; ++m) {
                hp.v[m] = pack2(glu(m, 0, 2), glu(m, 1, 3));
                if (m == 5) { PIN(hp.v[5]); WAIT_V(0); arrive(flags, F_QKV + bid, ln, lds + G1_CNT, 7u); }
            }
        } else {
#pragma unroll
            for (int m = 0; m < 8; m += 2) {
                ST16(pack2(lnv(m, 3, 1.0f), lnv(m + 1, 3, 1.0f)), qoff + KIND + m * 512);
                ST16(pack2(lnv(m, 4, 1.0f), lnv(m + 1, 4, 1.0f)), qoff + 2 * KIND + m * 512);
                ST16(pack2(lnv(m, 1, QSCALE), lnv(m + 1, 1, QSCALE)), qoff + m * 512);
            }
            CFENCE();
            hp.base = ws + WS_ACT + (unsigned)(14 * xcd + 12 + (wc >> 1)) * GRP + (unsigned)(rb * 256 + wr * 128) * 64u;
            hp.off = (fr + (fq & 1) * 16) * 64 + (wc & 1) * 32 + (fq >> 1) * 16; hp.step = 2048;
#pragma unroll
            for (int m = 0; m < 8; m += 2) { hp.v[m >> 1] = pack2(glu(m, 0, 2), glu(m + 1, 0, 2)); hp.v[4 + (m >> 1)] = hp.v[m >> 1]; }
            PIN(hp.v[3]); WAIT_V(0); arrive(flags, F_QKV + bid, ln, lds + G1_CNT, 7u);
        }
#pragma unroll
        for (int i = 0; i < 8; ++i) PIN(hp.v[i]);
        STAMP(3);
    }

    const int z = xcd & 1, rbk = ((xcd >> 1) & 1) * 8 + (idx & 7), cbk = (xcd >> 2) * 4 + (idx >> 3);
    G2 g2;
    g2.nh = z ? 56 - NH0 : NH0;
    g2.Ah = (const char*)ws + WS_ACT + (size_t)(z * 2 * NH0) * GRP + rbk * 128 * 64;
    g2.Ao = (const char*)ws + WS_ACT + (size_t)(112 + z * 16 - 2 * g2.nh) * GRP + rbk * 128 * 64;
    g2.Wd = (const char*)(wd + (size_t)z * NH0 * 64 * DM + cbk * 128);
    g2.Wo = (const char*)(wo + (size_t)z * 512 * DM + cbk * 128) - (size_t)g2.nh * 64 * DM * 2;
    {
        const int hl = idx & 3, qb = idx >> 2, h = 4 * xcd + hl;
        const int l0_ = opaque_tid() & 63;
        const int qkv0 = F_QKV + xcd + 8 * (qb * 4 + ((hl >= 2) ? 3 : l0_ % 3));
        const int qkvword = F_QKV + xcd + 8 * ((l0_ & 7) * 4 + ((hl >= 2) ? 3 : 1 + ((l0_ >> 3) & 1)));
        const int odep = (rbk >> 1) * 32 + ((l0_ >> 2) & 3) * 8 + 4 * z + (l0_ & 3);
        const int depword = (l0_ & 16) ? (odep & ~7) + 4 : odep;
        const char* Qg = (const char*)ws + WS_QKV + (size_t)h * 131072 + qb * 256 * 32;
        const char* Kg = (const char*)ws + WS_QKV + (size_t)(NH + h) * 131072;
        const char* Vg = (const char*)ws + WS_QKV + (size_t)(2 * NH + h) * 131072;
        const char* wpd = g2.Wd - (cbk & 3) * 256;
        const char* wpo = g2.Wo - (cbk & 3) * 256;
        AttnOut R;
#define DG
        attn_fast(lds, lds0, Qg, Kg, Vg, g2, flags, qkv0, qkvword, depword, F_H + bid, qb, bid == 0, cp, hp, wpd, wpo, idx * 2 * (g2.nh + 8), wid, R DG);
        STAMP(6);
        const int tz = opaque_tid(), ln = tz & 63, w4 = wid & 3, fr = ln & 15, fq = ln >> 4;
        const lds_ptr po = lds + 2 * SLOT + w4 * 8192 + ln * 16, pl = lds + 3 * SLOT + 4096 + w4 * 1024 + ln * 16;
        volatile LAS unsigned* const badw = (volatile LAS unsigned*)(lds + 3 * SLOT + 8192 + 4096);
#define AT_PUBLISH() do { if (wid < 4) { \
            _Pragma("unroll") for (int i = 0; i < 4; ++i) { *(LAS f32x4*)(po + i * 1024) = (f32x4){R.o0[4 * i], R.o0[4 * i + 1], R.o0[4 * i + 2], R.o0[4 * i + 3]}; \
                *(LAS f32x4*)(po + 4096 + i * 1024) = (f32x4){R.o1[4 * i], R.o1[4 * i + 1], R.o1[4 * i + 2], R.o1[4 * i + 3]}; } \
            *(LAS f32x4*)pl = (f32x4){R.l0[0], R.m0, R.l1[0], R.m1}; } } while (0)
        {
            AT_PUBLISH();
            const f32x16 z16 = R.o0 * 0.0f + R.o1 * 0.0f;
            float zs = 0.f;
#pragma unroll
            for (int r = 0; r < 16; ++r) zs += z16[r];
            const bool ok = (zs == 0.f) && (R.l0[0] > 0.f) && (R.l0[0] < 0x1p96f) && (R.l1[0] > 0.f) && (R.l1[0] < 0x1p96f);
            const bool wave_bad = !__all(ok);
            if (ln == 0) badw[wid] = wave_bad ? 1u : 0u;
            STAMP(16);
            WAIT_V(0); STAMP(17); WAIT_L0(); BAR(); CFENCE();
            const unsigned anybad = badw[ln & 7];
            if (__any(anybad != 0u)) {
                WAIT_L0(); BAR(); CFENCE();
                attn_safe(lds, lds0, Qg, Kg, Vg, wid, R);
                AT_PUBLISH();
                WAIT_L0(); BAR(); CFENCE();
            }
        }
        STAMP(7);

        const int tile = rbk * 8 + cbk, nt = g2.nh + 8;
        if (wid >= 4) {
            unsigned vA0, vA1, vB0, vB1; g2_voff(ln, vA0, vA1, vB0, vB1);
#define G2_LOAD(j, sl) do { const unsigned d_ = lds0 + (sl) * SLOT; g2_load_b(g2, j, d_, 2 * w4, vB0); g2_load_a(g2, j, d_, 2 * w4, vA0, vA1); \
                g2_load_b(g2, j, d_, 2 * w4 + 1, vB1); g2_load_a(g2, j, d_, 2 * w4 + 1, vA0, vA1); } while (0)
            const f32x4 lm = *(const LAS f32x4*)pl;
            f32x4 t[8];
#pragma unroll
            for (int i = 0; i < 8; ++i) t[i] = *(const LAS f32x4*)(po + i * 1024);
            WAIT_L0(); BAR(); CFENCE();
            G2_LOAD(3, 2);
            u32x4 ow[4];
            const unsigned ooff = WS_ACT + (unsigned)(112 + h) * GRP + (unsigned)(qb * 256 + w4 * 64 + (ln & 31)) * 64u + (ln >> 5) * 16;
#pragma unroll
            for (int q = 0; q < 2; ++q) {
                BAR();
                if (q == 0) G2_LOAD(4, 3); else G2_LOAD(5, 4);
                const f32x16 ok_ = q ? R.o1 : R.o0;
                const float lk = q ? R.l1[0] : R.l0[0], mk = q ? R.m1 : R.m0, lo = lm[2 * q], mo = lm[2 * q + 1];
                const float mm = __builtin_fmaxf(mk, mo), fk = __builtin_amdgcn_exp2f(mk - mm), fo = __builtin_amdgcn_exp2f(mo - mm);
                const float rl = 1.0f / (lk * fk + lo * fo), ck = fk * rl, co = fo * rl;
                unsigned p[8];
#pragma unroll
                for (int i = 0; i < 4; ++i) {
                    const f32x4 u = t[4 * q + i];
                    p[2 * i] = cvtpk(ok_[4 * i] * ck + u[0] * co, ok_[4 * i + 1] * ck + u[1] * co);
                    p[2 * i + 1] = cvtpk(ok_[4 * i + 2] * ck + u[2] * co, ok_[4 * i + 3] * ck + u[3] * co);
                }
                swap32(p[0], p[2]); swap32(p[1], p[3]); swap32(p[4], p[6]); swap32(p[5], p[7]);
                ow[2 * q] = (u32x4){p[0], p[1], p[2], p[3]}; ow[2 * q + 1] = (u32x4){p[4], p[5], p[6], p[7]};
                PIN(ow[2 * q]); PIN(ow[2 * q + 1]);
            }
#pragma unroll
            for (int i = 0; i < 4; ++i) ST16(ow[i], ooff + (i >> 1) * 2048 + (i & 1) * 32);
            CFENCE();
            WAIT_V(20); BAR(); G2_LOAD(6, 0);
            WAIT_V(20); BAR(); G2_LOAD(7, 1);
            WAIT_V(20);
            int sl = 2; cpd = 0;
#pragma unroll 1
            for (int j = 5; j < nt - 3; ++j) {
                BAR();
                if (j == 29) CODE_PF(cpd);
                if (j == 6 && wid == 4) signal(flags, F_O + bid, ln);
                G2_LOAD(j + 3, sl);
                sl = (sl == 4) ? 0 : sl + 1;
                WAIT_V(16);
            }
            BAR();
            if (tz == 256) *(LAS unsigned*)(lds + sl * SLOT) = 0u;
            WAIT_V(8); WAIT_L0(); BAR(); WAIT_V(0); BAR();
            asm volatile("" :: "v"(cpd));
            if (wid == 4) wait_all(flags, F_X + tile * 2 + z, peek(flags, F_X + tile * 2 + z));
            CFENCE(); BAR();
            asm volatile(".Lk_end:");
        } else {
            const int wm = w4 >> 1, wn = w4 & 1;
            const unsigned aO0 = (wm * 64 + fr) * 128 + ((fq ^ (fr >> 1)) * 16), aO1 = aO0 ^ 64;
            const int tq = (ln >> 2) & 3, tp = ln & 3, tg = (tq & 2) | ((fq & 1) << 2);
            unsigned wO[4];
#pragma unroll
            for (int b = 0; b < 4; ++b) wO[b] = 16384 + wn * 8192 + (8 * fq + tq) * 128 + (((2 * b + (tp >> 1)) ^ tg) * 16) + (tp & 1) * 8;
            f32x4 acc[4][4];
#pragma unroll
            for (int m = 0; m < 4; ++m)
#pragma unroll
                for (int b = 0; b < 4; ++b) acc[m][b] = (f32x4){0.f, 0.f, 0.f, 0.f};
            bf16x8 xa[4], ya[4] = {}; s16x4 xl[4], xh[4], yl[4] = {}, yh[4] = {};
            lds_ptr pa0, pa1, pw0, pw1, pw2, pw3;
#define G2_BASES(so) do { pa0 = lds + aO0 + (so); pa1 = lds + aO1 + (so); pw0 = lds + wO[0] + (so); pw1 = lds + wO[1] + (so); pw2 = lds + wO[2] + (so); pw3 = lds + wO[3] + (so); } while (0)
#define G2_M(C, m, b) do { MFMA16_ACC(acc[m][b], cat8(C##l[b], C##h[b]), C##a[m]); SBAR(); } while (0)
#define RDQ(x) (x)
#define G2_HALF(C, N, AB, WO, RD_, TAIL) do { const bool RD = RDQ(RD_); \
            G2_M(C, 0, 0); if (RD) { N##l[0] = lds_tr(pw0 + (WO)); N##h[0] = lds_tr(pw0 + (WO) + 512); } SBAR(); \
            G2_M(C, 1, 0); if (RD) N##a[0] = lds_b128(AB); SBAR(); \
            G2_M(C, 2, 0); if (RD) N##a[1] = lds_b128((AB) + 2048); SBAR(); \
            G2_M(C, 3, 0); if (RD) N##a[2] = lds_b128((AB) + 4096); SBAR(); \
            G2_M(C, 0, 1); if (RD) N##a[3] = lds_b128((AB) + 6144); SBAR(); \
            G2_M(C, 1, 1); if (RD) { N##l[1] = lds_tr(pw1 + (WO)); N##h[1] = lds_tr(pw1 + (WO) + 512); } SBAR(); \
            G2_M(C, 2, 1); if (RD) { N##l[2] = lds_tr(pw2 + (WO)); N##h[2] = lds_tr(pw2 + (WO) + 512); } SBAR(); \
            G2_M(C, 3, 1); if (RD) { N##l[3] = lds_tr(pw3 + (WO)); N##h[3] = lds_tr(pw3 + (WO) + 512); } SBAR(); \
            G2_M(C, 0, 2); TAIL(0); SBAR(); G2_M(C, 1, 2); TAIL(1); SBAR(); G2_M(C, 2, 2); TAIL(2); SBAR(); G2_M(C, 3, 2); TAIL(3); SBAR(); \
            G2_M(C, 0, 3); G2_M(C, 1, 3); G2_M(C, 2, 3); G2_M(C, 3, 3); } while (0)
            int so = 4 * SLOT;
            G2_BASES(so);
            xl[0] = lds_tr(pw0); xh[0] = lds_tr(pw0 + 512); SBAR();
#pragma unroll
            for (int m = 0; m < 4; ++m) { xa[m] = lds_b128(pa0 + m * 2048); SBAR(); }
            xl[1] = lds_tr(pw1); xh[1] = lds_tr(pw1 + 512); xl[2] = lds_tr(pw2); xh[2] = lds_tr(pw2 + 512); xl[3] = lds_tr(pw3); xh[3] = lds_tr(pw3 + 512);
            const u64* const optr = flags.p + (F_O + odep) * 16;
            u64 ov = 0;
            STAMP(8);
#define G2_NEXT(i) do { if ((i) == 0) so = (so == 4 * SLOT) ? 0 : so + SLOT; \
                if ((i) == 1) { pa0 = lds + aO0 + so; pa1 = lds + aO1 + so; PIN(pa0); PIN(pa1); } \
                if ((i) == 2) { pw0 = lds + wO[0] + so; pw1 = lds + wO[1] + so; PIN(pw0); PIN(pw1); } \
                if ((i) == 3) { pw2 = lds + wO[2] + so; pw3 = lds + wO[3] + so; PIN(pw2); PIN(pw3); } } while (0)
#define G2_NONE(i) do {} while (0)
            WAIT_L0(); BAR(); SBAR();
#pragma unroll 1
            for (int ph = 0; ph < 3; ++ph) {
                const int n = (ph == 0) ? g2.nh / 2 - 3 : (ph == 1) ? 1 : 5;
#pragma unroll 1
                for (int i = 0; i < n; ++i) {
                    WAIT_L0(); SBAR(); G2_HALF(x, y, pa1, 4096, true, G2_NEXT);
                    WAIT_L0(); BAR(); SBAR(); G2_HALF(y, x, pa0, 0, true, G2_NONE);
                    WAIT_L0(); SBAR(); G2_HALF(x, y, pa1, 4096, true, G2_NEXT);
                    WAIT_L0(); BAR(); SBAR(); G2_HALF(y, x, pa0, 0, true, G2_NONE);
                }
                PEEK_AHEAD(ov, optr, wid | ph);
                if ((wid | (ph ^ 1)) == 0) { PEEK_DONE(ov); wait_all(flags, F_O + odep, ov); }
            }
            const int row = rbk * 128 + wm * 64 + z * 32 + fr, col = cbk * 128 + wn * 64 + (fq & 1) * 16 + (fq >> 1) * 8;
            u32x4 xr[4];
#pragma unroll
            for (int i = 0; i < 4; ++i) xr[i] = *(const u32x4*)(x + (size_t)(row + (i >> 1) * 16) * DM + col + (i & 1) * 32);
            WAIT_L0(); SBAR(); G2_HALF(x, y, pa1, 4096, true, G2_NEXT);
            WAIT_L0(); BAR(); SBAR(); G2_HALF(y, x, pa0, 0, true, G2_NONE);
            WAIT_L0(); SBAR(); G2_HALF(x, y, pa1, 4096, true, G2_NONE);
            WAIT_L0(); SBAR(); G2_HALF(y, x, pa0, 0, false, G2_NONE);
            MFMA_DRAIN(); SBAR();
            STAMP(9);
            f32x4 vk[8];
            const unsigned xo = WS_XCH + (unsigned)(tile * 2 + 1 - z) * 32768u + w4 * 8192 + ln * 16;
            if (z == 0) {
#pragma unroll
                for (int i = 0; i < 8; ++i) { ST16(__builtin_bit_cast(u32x4, acc[2 + (i >> 2)][i & 3]), xo + i * 1024); vk[i] = acc[i >> 2][i & 3]; }
            } else {
#pragma unroll
                for (int i = 0; i < 8; ++i) { ST16(__builtin_bit_cast(u32x4, acc[i >> 2][i & 3]), xo + i * 1024); vk[i] = acc[2 + (i >> 2)][i & 3]; }
            }
            WAIT_V(0);
            arrive(flags, F_X + tile * 2 + 1 - z, ln, lds + ((so == 4 * SLOT) ? 0 : so + SLOT), 3u);
            STAMP(10);
            CFENCE(); BAR(); CFENCE();
            STAMP(11);
            const unsigned xi = WS_XCH + (unsigned)(tile * 2 + z) * 32768u + w4 * 8192 + ln * 16;
#pragma unroll
            for (int i = 0; i < 8; ++i) vk[i] += __builtin_bit_cast(f32x4, __builtin_amdgcn_raw_buffer_load_b128(wsr, xi + i * 1024, 0, 16));
#pragma unroll
            for (int i = 0; i < 4; ++i) {
                const u32x4 a = __builtin_bit_cast(u32x4, vk[2 * i]), b = __builtin_bit_cast(u32x4, vk[2 * i + 1]);
                unsigned a0_ = a[0], a1 = a[1], a2 = a[2], a3 = a[3], b0 = b[0], b1 = b[1], b2 = b[2], b3 = b[3];
                swap16(a0_, b0); swap16(a1, b1); swap16(a2, b2); swap16(a3, b3);
                const u32x4 xx = xr[i];
                u32x4 w;
                w[0] = cvtpk(u2f(a0_) + bf_lo(xx[0]), u2f(a1) + bf_hi(xx[0]));
                w[1] = cvtpk(u2f(a2) + bf_lo(xx[1]), u2f(a3) + bf_hi(xx[1]));
                w[2] = cvtpk(u2f(b0) + bf_lo(xx[2]), u2f(b1) + bf_hi(xx[2]));
                w[3] = cvtpk(u2f(b2) + bf_lo(xx[3]), u2f(b3) + bf_hi(xx[3]));
                __builtin_amdgcn_raw_buffer_store_b128(w, __builtin_amdgcn_make_buffer_rsrc((void*)out, 0, SEQ * DM * 2, 0x00020000), (unsigned)((row + (i >> 1) * 16) * DM + col + (i & 1) * 32) * 2u, 0, 16);
            }
        }
    }
}

extern "C" void kernel_launch(void* const* d_in, const int* in_sizes, int n_in, void* d_out, int out_size, void* d_ws, size_t ws_size, hipStream_t stream) {
    const bf16_t* x = (const bf16_t*)d_in[0];
    k_main<<<256, 512, 0, stream>>>(x, (const bf16_t*)d_in[1], (const bf16_t*)d_in[2], (const bf16_t*)d_in[4], (const bf16_t*)d_in[5], (const bf16_t*)d_in[6],
                                    (bf16_t*)d_out, (unsigned char*)d_ws);
}
```

```cpp
#include <hip/hip_runtime.h>
#include <stdint.h>

typedef unsigned short bf16_t;
typedef short bf16x8 __attribute__((ext_vector_type(8)));
typedef short s16x4 __attribute__((ext_vector_type(4)));
typedef float f32x2 __attribute__((ext_vector_type(2)));
typedef float f32x4 __attribute__((ext_vector_type(4)));
typedef float f32x16 __attribute__((ext_vector_type(16)));
typedef unsigned u32x2 __attribute__((ext_vector_type(2)));
typedef unsigned u32x4 __attribute__((ext_vector_type(4)));
typedef __bf16 bf16x2_t __attribute__((ext_vector_type(2)));
#define LAS __attribute__((address_space(3)))
#define GAS __attribute__((address_space(1)))
typedef LAS unsigned char* lds_ptr;

constexpr int SEQ = 2048, DM = 1024, FF = 3584, NQKV = 3072, NH = 32;
constexpr float LN_EPS = 1e-5f;
constexpr float LOG2E = 1.4426950408889634f;
constexpr float QSCALE = 0.17677669529663687f * LOG2E;

constexpr unsigned WS_FLAGS = 0;
constexpr unsigned WS_STAT = 192 * 1024;
constexpr unsigned WS_QKV = 256 * 1024;
constexpr unsigned WS_ACT = WS_QKV + 3u * SEQ * DM * 2;
constexpr unsigned WS_XCH = WS_ACT + 144u * SEQ * 64;
constexpr unsigned WS_XC = WS_XCH + 256u * 32768;
constexpr unsigned WS_END = WS_XC + SEQ * DM * 2;
constexpr int F_EPOCH = 0, F_QKV = 1, F_H = 257, F_O = 513, F_X = 769, F_XC = 1025;
constexpr unsigned GRP = SEQ * 64;

#define SBAR() __builtin_amdgcn_sched_barrier(0)
#define BAR() __builtin_amdgcn_s_barrier()
#define WAIT_V(n) asm volatile("s_waitcnt vmcnt(" #n ")" ::: "memory")
#define WAIT_L0() do { __builtin_amdgcn_s_waitcnt(0xC07F); asm volatile("" ::: "memory"); } while (0)
#define CFENCE() asm volatile("" ::: "memory")
#define PIN(x) asm volatile("" : "+v"(x))
#define MFMA16(a, b, c) __builtin_amdgcn_mfma_f32_16x16x32_bf16(a, b, c, 0, 0, 0)
#define MFMA32(a, b, c) __builtin_amdgcn_mfma_f32_32x32x16_bf16(a, b, c, 0, 0, 0)
#define MFMA16_ACC(acc, a, b) asm volatile("v_mfma_f32_16x16x32_bf16 %0, %1, %2, %0" : "+v"(acc) : "v"(a), "v"(b))
#define MFMA32_ACC(acc, a, b) asm volatile("v_mfma_f32_32x32x16_bf16 %0, %1, %2, %0" : "+v"(acc) : "v"(a), "v"(b))
#define MFMA32_SET(d, a, b, c) asm volatile("v_mfma_f32_32x32x16_bf16 %0, %1, %2, %3" : "=&v"(d) : "v"(a), "v"(b), "v"(c))
#define MFMA_DRAIN() asm volatile("s_nop 15" ::: "memory")
#define RLX_AGENT __ATOMIC_RELAXED, __HIP_MEMORY_SCOPE_AGENT

#define STAMP(i) do {} while (0)
#define TS(v) do {} while (0)
#define PUT(i, v) do {} while (0)

__device__ __forceinline__ void glds16(const void* sbase, unsigned voff, unsigned ldsdst) {
    asm volatile("s_mov_b32 m0, %2\n\ts_nop 0\n\tglobal_load_lds_dwordx4 %0, %1" ::"v"(voff), "s"(sbase), "s"(ldsdst) : "memory");
}
__device__ __forceinline__ void glds16c(const void* sbase, unsigned voff, unsigned ldsdst) {
    asm volatile("s_mov_b32 m0, %2\n\ts_nop 0\n\tglobal_load_lds_dwordx4 %0, %1 sc1" ::"v"(voff), "s"(sbase), "s"(ldsdst) : "memory");
}
__device__ __forceinline__ bf16x8 lds_b128(lds_ptr p) { return *(const LAS bf16x8*)p; }
__device__ __forceinline__ s16x4 lds_tr(lds_ptr p) { return __builtin_amdgcn_ds_read_tr16_b64_v4i16((LAS s16x4*)p); }
__device__ __forceinline__ bf16x8 cat8(s16x4 a, s16x4 b) { return __builtin_shufflevector(a, b, 0, 1, 2, 3, 4, 5, 6, 7); }
__device__ __forceinline__ unsigned cvtpk(float lo, float hi) {
    f32x2 v = {lo, hi};
    return __builtin_bit_cast(unsigned, __builtin_convertvector(v, bf16x2_t));
}
__device__ __forceinline__ float u2f(unsigned u) { return __builtin_bit_cast(float, u); }
__device__ __forceinline__ unsigned f2u(float f) { return __builtin_bit_cast(unsigned, f); }
__device__ __forceinline__ float bf_lo(unsigned u) { return u2f(u << 16); }
__device__ __forceinline__ float bf_hi(unsigned u) { return u2f(u & 0xffff0000u); }
__device__ __forceinline__ void swap16(unsigned& a, unsigned& b) {
    auto r = __builtin_amdgcn_permlane16_swap(a, b, false, false);
    a = r[0]; b = r[1];
}
__device__ __forceinline__ void swap32(unsigned& a, unsigned& b) {
    auto r = __builtin_amdgcn_permlane32_swap(a, b, false, false);
    a = r[0]; b = r[1];
}
__device__ __forceinline__ int opaque_tid() { int t = threadIdx.x; asm volatile("" : "+v"(t)); return t; }
typedef unsigned long long u64;
struct Flags { u64* p; u64 tag; };
__device__ __forceinline__ void signal(const Flags& f, int word, int lane) {
    if (lane == 0) __hip_atomic_store((GAS u64*)(f.p + word * 16), f.tag, RLX_AGENT);
}
__device__ __forceinline__ u64 peek(const Flags& f, int word) {
    u64 v;
    asm volatile("global_load_dwordx2 %0, %1, off sc1\n\ts_waitcnt vmcnt(0)" : "=&v"(v) : "v"(f.p + word * 16) : "memory");
    return v;
}
__device__ __forceinline__ void wait_all(const Flags& f, int word, u64 v) {
#pragma unroll 1
    for (unsigned spin = 0; spin < (1u << 20) && !__all(v == f.tag); ++spin) { __builtin_amdgcn_s_sleep(1); v = peek(f, word); }
}
__device__ __forceinline__ void arrive(const Flags& f, int word, int lane, lds_ptr cnt, unsigned last) {
    if (lane == 0 && __hip_atomic_fetch_add((LAS unsigned*)cnt, 1u, __ATOMIC_RELAXED, __HIP_MEMORY_SCOPE_WORKGROUP) == last)
        __hip_atomic_store((GAS u64*)(f.p + word * 16), f.tag, RLX_AGENT);
}
#define PEEK_AHEAD(v, ptr, sel) asm volatile("s_cmp_lg_u32 %2, 0\n\ts_cbranch_scc1 1f\n\tglobal_load_dwordx2 %0, %1, off sc1\n1:" : "+v"(v) : "v"(ptr), "s"(sel) : "memory", "scc")
struct CodePf { const char* base; unsigned len; int first; };
#define CODE_PF(d) do { unsigned o_ = (unsigned)(cp.first + (opaque_tid() & 15)) * 128u; o_ = (o_ < cp.len) ? o_ : 0u; \
    asm volatile("global_load_dword %0, %1, %2" : "+v"(d) : "v"(o_), "s"(cp.base) : "memory"); } while (0)
#define PEEK_DONE(v) asm volatile("s_waitcnt vmcnt(0)" : "+v"(v) :: "memory")

template <int CTRL> __device__ __forceinline__ float dpp_add(float v) { return v + u2f(__builtin_amdgcn_update_dpp(0u, f2u(v), CTRL, 0xf, 0xf, true)); }
__device__ __forceinline__ float red4(float v) {
    unsigned a = f2u(v), b = a;
    swap16(a, b);
    v = u2f(a) + u2f(b);
    a = f2u(v); b = a;
    swap32(a, b);
    return u2f(a) + u2f(b);
}
__device__ __forceinline__ float wave_sum(float v) {
    v = dpp_add<0xB1>(v); v = dpp_add<0x4E>(v); v = dpp_add<0x141>(v); v = dpp_add<0x140>(v);
    return red4(v);
}
__device__ __forceinline__ f32x2 row_stats(u32x4& a, u32x4& b) {
    float v[16];
#pragma unroll
    for (int i = 0; i < 4; ++i) { v[2 * i] = bf_lo(a[i]); v[2 * i + 1] = bf_hi(a[i]); v[8 + 2 * i] = bf_lo(b[i]); v[8 + 2 * i + 1] = bf_hi(b[i]); }
    float s = 0.f;
#pragma unroll
    for (int i = 0; i < 16; ++i) s += v[i];
    const float mu = wave_sum(s) * (1.0f / DM);
    float q = 0.f;
#pragma unroll
    for (int i = 0; i < 16; ++i) { v[i] -= mu; q += v[i] * v[i]; }
#pragma unroll
    for (int i = 0; i < 4; ++i) { a[i] = cvtpk(v[2 * i], v[2 * i + 1]); b[i] = cvtpk(v[8 + 2 * i], v[8 + 2 * i + 1]); }
    return (f32x2){mu, 1.0f / __builtin_sqrtf(wave_sum(q) * (1.0f / DM) + LN_EPS)};
}

constexpr int SLOT = 32768, LDS_BYTES = 5 * SLOT;
constexpr int G1_A = 0, G1_W = 65536, G1_STAT = G1_W + 5 * 16384, G1_CS = G1_STAT + 2048, G1_CNT = G1_CS + 4 * 6 * 64;

#define NH0 30
static_assert(NH0 % 2 == 0 && NH0 >= 28 && NH0 <= 32, "");
struct G2 { const char* Ah; const char* Ao; const char* Wd; const char* Wo; int nh; unsigned rowb; u64 split; };
__device__ __forceinline__ void g2_load_b(const G2& g, int j, unsigned dst, int vw, unsigned voffB) {
    const char* b = (j >= g.nh ? g.Wo : g.Wd) + (size_t)(j * 64 + vw * 8) * (DM * 2);
    glds16(b, voffB, dst + 16384 + vw * 1024); glds16(b + 128, voffB, dst + 16384 + 8192 + vw * 1024);
}
struct G2Off { unsigned A0, A1, N0, N1, B0, B1; };
__device__ __forceinline__ void g2_load_a(const G2& g, int j, unsigned dst, int vw, const G2Off& o) {
    const unsigned sp = (unsigned)(g.split >> j) & 1u;
    const char* a = (j >= g.nh ? g.Ao : g.Ah) + (size_t)j * (2 * GRP) + ((g.rowb + vw * 1024) >> sp);
    glds16c(a, sp ? o.N0 : o.A0, dst + vw * 2048); glds16c(a, sp ? o.N1 : o.A1, dst + vw * 2048 + 1024);
}
__device__ __forceinline__ void g2_voff(int lane, G2Off& o) {
    const int r8 = lane >> 3, u8 = lane & 7, ua = u8 ^ (r8 >> 1), ub = ua ^ 4;
    o.A0 = (unsigned)(ua >> 2) * GRP + r8 * 64 + (ua & 3) * 16;
    o.A1 = (unsigned)(ub >> 2) * GRP + (r8 + 8) * 64 + (ub & 3) * 16;
    o.N0 = (unsigned)(ua >> 1) * (GRP / 2) + r8 * 32 + (ua & 1) * 16;
    o.N1 = (unsigned)(ub >> 1) * (GRP / 2) + (r8 + 8) * 32 + (ub & 1) * 16;
    o.B0 = (unsigned)(r8 * (DM * 2) + (u8 ^ (r8 & 2)) * 16);
    o.B1 = (unsigned)(r8 * (DM * 2) + (u8 ^ (4 | (r8 & 2))) * 16);
}
constexpr u64 split_mask(int first, int n) { u64 m = 0; for (int j = 0; j < n; ++j) if ((first + j) % 7 == 6) m |= 1ull << j; return m; }
static_assert(((split_mask(0, NH0) | split_mask(NH0, 56 - NH0)) & 7) == 0, "phase B's tail prefetches K-tiles 0..2 as plain groups");

struct AttnOut { f32x16 o0, o1; f32x4 l0, l1; float m0, m1; };
struct HPend { u32x4 v[8]; unsigned off, size; const void* base; };
struct AttnLane {
    unsigned voffK, voffV; lds_ptr kb0, kb1, vb; const char* qp; bf16x8 ones;
    __device__ __forceinline__ void init(lds_ptr lds, const char* Qg, int wid, int lane) {
        const int r32 = lane & 31, hi = lane >> 5, kh = wid >> 2, qg = wid & 3, ksw = (r32 >> 2) & 3;
        const int uk = (lane & 3) ^ ((lane >> 4) & 3), uv = lane & 3;
        voffK = (unsigned)((uk >> 1) * 65536 + wid * 1024 + (lane >> 2) * 32 + (uk & 1) * 16);
        voffV = (unsigned)((uv >> 1) * 65536 + wid * 1024 + (lane >> 2) * 32 + (uv & 1) * 16);
        kb0 = lds + kh * 8192 + r32 * 64 + ((hi ^ ksw) * 16); kb1 = lds + kh * 8192 + r32 * 64 + (((2 | hi) ^ ksw) * 16);
        vb = lds + 16384 + kh * 8192 + (4 * hi + ((lane >> 2) & 3)) * 64 + ((lane >> 4) & 1) * 32 + (lane & 3) * 8;
        qp = Qg + (qg * 64 + r32) * 32 + hi * 16;
        const short v = (((lane >> 4) & 1) == ((lane >> 2) & 1)) ? (short)0x3f80 : (short)0;
        ones = (bf16x8){v, v, v, v, v, v, v, v};
    }
};
#define EX(v) __builtin_amdgcn_exp2f(v)
#define AT_LOAD_K(c, slot) do { const unsigned d_ = lds0 + wid * 2048 + (slot) * SLOT; const char* k_ = Kg + (c) * 8192; glds16c(k_, A.voffK, d_); glds16c(k_ + 512, A.voffK, d_ + 1024); } while (0)
#define AT_LOAD_V(c, slot) do { const unsigned d_ = lds0 + wid * 2048 + (slot) * SLOT + 16384; const char* v_ = Vg + (c) * 8192; glds16c(v_, A.voffV, d_); glds16c(v_ + 512, A.voffV, d_ + 1024); } while (0)
#define AT_PACK(S, i) __builtin_bit_cast(bf16x8, (u32x4){cvtpk(S[i], S[i + 1]), cvtpk(S[i + 2], S[i + 3]), cvtpk(S[i + 4], S[i + 5]), cvtpk(S[i + 6], S[i + 7])})
#define AT_Q_LOAD() asm volatile("global_load_dwordx4 %0, %4, off sc1\n\tglobal_load_dwordx4 %1, %4, off offset:1024 sc1\n\tglobal_load_dwordx4 %2, %5, off sc1\n\tglobal_load_dwordx4 %3, %5, off offset:1024 sc1" \
        : "=&v"(q00), "=&v"(q10), "=&v"(q01), "=&v"(q11) : "v"(A.qp), "v"(qp2) : "memory")
#define AT_Q_WAIT(n) asm volatile("s_waitcnt vmcnt(" #n ")" : "+v"(q00), "+v"(q01), "+v"(q10), "+v"(q11) :: "memory")
__device__ __forceinline__ float rowmax16(const f32x16& s) {
    float a = s[0];
#pragma unroll
    for (int r = 1; r < 16; ++r) a = __builtin_fmaxf(a, s[r]);
    unsigned x = f2u(a), y = x; swap32(x, y);
    return __builtin_fmaxf(u2f(x), u2f(y));
}

__device__ __forceinline__ void attn_safe(lds_ptr lds, unsigned lds0, const char* Qg, const char* Kg, const char* Vg, int wid, AttnOut& R) {
    AttnLane A; A.init(lds, Qg, wid, opaque_tid() & 63);
    bf16x8 q00, q01, q10, q11; const char* qp2 = A.qp + 65536;
    AT_Q_LOAD(); AT_Q_WAIT(0);
    f32x16 o0 = (f32x16){}, o1 = (f32x16){};
    f32x4 l0 = (f32x4){0.f, 0.f, 0.f, 0.f}, l1 = l0;
    float m0 = -__builtin_inff(), m1 = m0;
#pragma unroll 1
    for (int c = 0; c < 8; ++c) {
        AT_LOAD_K(c, 3); AT_LOAD_V(c, 3);
        WAIT_V(0); BAR(); CFENCE();
#pragma unroll 1
        for (int t = 0; t < 4; ++t) {
            const int to = 3 * SLOT + t * 2048;
            const bf16x8 ka = lds_b128(A.kb0 + to), kb = lds_b128(A.kb1 + to);
            const bf16x8 va = cat8(lds_tr(A.vb + to), lds_tr(A.vb + to + 512)), vc = cat8(lds_tr(A.vb + to + 1024), lds_tr(A.vb + to + 1536));
            f32x16 s0 = MFMA32(kb, q01, MFMA32(ka, q00, (f32x16){})), s1 = MFMA32(kb, q11, MFMA32(ka, q10, (f32x16){}));
            const float n0 = __builtin_fmaxf(m0, rowmax16(s0)), n1 = __builtin_fmaxf(m1, rowmax16(s1));
            const float f0 = EX(m0 - n0), f1 = EX(m1 - n1);
            m0 = n0; m1 = n1; o0 *= f0; o1 *= f1; l0 *= f0; l1 *= f1;
#pragma unroll
            for (int r = 0; r < 16; ++r) { s0[r] = EX(s0[r] - n0); s1[r] = EX(s1[r] - n1); }
            const bf16x8 p00 = AT_PACK(s0, 0), p01 = AT_PACK(s0, 8), p10 = AT_PACK(s1, 0), p11 = AT_PACK(s1, 8);
            o0 = MFMA32(vc, p01, MFMA32(va, p00, o0)); l0 = MFMA16(A.ones, p01, MFMA16(A.ones, p00, l0));
            o1 = MFMA32(vc, p11, MFMA32(va, p10, o1)); l1 = MFMA16(A.ones, p11, MFMA16(A.ones, p10, l1));
        }
        WAIT_L0(); BAR(); CFENCE();
    }
    R.o0 = o0; R.o1 = o1; R.l0 = l0; R.l1 = l1; R.m0 = m0; R.m1 = m1;
}

__device__ __forceinline__ void attn_fast(lds_ptr lds, unsigned lds0, const char* Qg, const char* Kg, const char* Vg, const G2& g2, const Flags& flags, int qkv0, int qkvword, int depword,
                                          int hsig, int c0, bool bump, const CodePf& cp, const HPend& hp, const char* wpd, const char* wpo, int prow, int wid, AttnOut& R
                                          ) {
    const int lane = opaque_tid() & 63;
    AttnLane A; A.init(lds, Qg, wid, lane);
    lds_ptr kb0 = A.kb0, kb1 = A.kb1, vb = A.vb;
    PIN(kb0); PIN(kb1); PIN(vb);
    lds_ptr kh0 = kb0 + 2 * SLOT, kh1 = kb1 + 2 * SLOT, vh = vb + 2 * SLOT;
    PIN(kh0); PIN(kh1); PIN(vh);
    const bf16x8 ones = A.ones;
    const __amdgpu_buffer_rsrc_t hr = __builtin_amdgcn_make_buffer_rsrc((void*)hp.base, 0, hp.size, 0x00020000);
    f32x16 o0 = (f32x16){}, o1 = (f32x16){}, negm0 = (f32x16){}, negm1 = (f32x16){}, sA0, sA1, sB0, sB1;
    f32x4 l0 = (f32x4){0.f, 0.f, 0.f, 0.f}, l1 = l0;
    bf16x8 kfa, kfb, va, vc = (bf16x8){}, pf = (bf16x8){};
    PIN(o0); PIN(o1); PIN(l0); PIN(l1); PIN(vc); PIN(pf); PIN(A.voffK); PIN(A.voffV);
    G2Off go; g2_voff(lane, go);
    unsigned gA0 = go.A0, gA1 = go.A1, gB = (wid & 1) ? go.B1 : go.B0;
    unsigned pfo = (unsigned)(((lane >> 3) & 1) * (DM * 2) + (lane & 7) * 128);
    const u64* hptr = flags.p + (F_H + depword) * 16;
    const u64* qptr = flags.p + qkvword * 16;
    PIN(gA0); PIN(gA1); PIN(gB); PIN(pfo); PIN(hptr); PIN(qptr);
    if (wid == 0) {
        wait_all(flags, qkv0, peek(flags, qkv0));
        STAMP(13);
    }
    CFENCE(); BAR(); CFENCE();
    STAMP(4);
    AT_LOAD_K(c0, 0);
    glds16c(Qg, A.voffK, lds0 + wid * 2048 + 2 * SLOT); glds16c(Qg + 512, A.voffK, lds0 + wid * 2048 + 2 * SLOT + 1024);
    AT_LOAD_V(c0, 0);
    u64 hv = 0;
    float m0, m1;
    WAIT_V(2);
    PEEK_AHEAD(hv, qptr, wid);
    BAR(); SBAR();
    STAMP(15);
    const int qo = 2 * SLOT + (wid & 3) * 4096 - (wid >> 2) * 8192;
    const bf16x8 q00 = lds_b128(kb0 + qo), q10 = lds_b128(kb0 + qo + 2048), q01 = lds_b128(kb1 + qo), q11 = lds_b128(kb1 + qo + 2048);
    {
        kfa = lds_b128(kb0); kfb = lds_b128(kb1);
        sA0 = MFMA32(kfa, q00, negm0); sA1 = MFMA32(kfa, q10, negm1); sA0 = MFMA32(kfb, q01, sA0); sA1 = MFMA32(kfb, q11, sA1);
        kfa = lds_b128(kb0 + 2048); kfb = lds_b128(kb1 + 2048);
        m0 = rowmax16(sA0); m1 = rowmax16(sA1);
#pragma unroll
        for (int r = 0; r < 16; ++r) { sA0[r] -= m0; sA1[r] -= m1; negm0[r] = -m0; negm1[r] = -m1; }
        PIN(negm0); PIN(negm1);
    }
    unsigned pfd = 0;
    PEEK_DONE(hv);
    if (wid == 0) {
        wait_all(flags, qkvword, hv);
        if (bump) signal(flags, F_EPOCH, lane);
        STAMP(14);
    }
    CFENCE(); BAR(); SBAR();
    STAMP(5);
    AT_LOAD_K((c0 + 1) & 7, 1); AT_LOAD_V((c0 + 1) & 7, 1);
    STAMP(19);
#define AT_GROUP(QK, RD, S, i, O, L, VF) do { \
        QK; SBAR(); RD; SBAR(); \
        float e0, e1, e2, e3, e4, e5, e6, e7; \
        asm volatile("v_exp_f32 %0, %13\n\tv_exp_f32 %1, %14\n\tv_exp_f32 %2, %15\n\tv_mfma_f32_32x32x16_bf16 %8, %10, %11, %8\n\t" \
                     "v_exp_f32 %3, %16\n\tv_exp_f32 %4, %17\n\tv_exp_f32 %5, %18\n\tv_mfma_f32_16x16x32_bf16 %9, %12, %11, %9\n\tv_exp_f32 %6, %19\n\tv_exp_f32 %7, %20" \
            : "=&v"(e0), "=&v"(e1), "=&v"(e2), "=&v"(e3), "=&v"(e4), "=&v"(e5), "=&v"(e6), "=&v"(e7), "+v"(O), "+v"(L) \
            : "v"(VF), "v"(pf), "v"(ones), "v"(S[i]), "v"(S[i + 1]), "v"(S[i + 2]), "v"(S[i + 3]), "v"(S[i + 4]), "v"(S[i + 5]), "v"(S[i + 6]), "v"(S[i + 7])); \
        pf = __builtin_bit_cast(bf16x8, (u32x4){cvtpk(e0, e1), cvtpk(e2, e3), cvtpk(e4, e5), cvtpk(e6, e7)}); SBAR(); } while (0)
#define LO(lo, hi, off) (((off) >= 2 * SLOT ? hi : lo) + ((off) & (2 * SLOT - 1)))
#define AT_STEP(C0, C1, N0, N1, QK, KR, KO, VO, H0, H1, H2, H3) do { \
        AT_GROUP(if (QK) MFMA32_SET(N0, kfa, q00, negm0), { va = cat8(lds_tr(LO(vb, vh, VO)), lds_tr(LO(vb, vh, VO) + 512)); H0; }, C0, 0, o1, l1, vc); \
        AT_GROUP(if (QK) MFMA32_SET(N1, kfa, q10, negm1), { if (KR) kfa = lds_b128(LO(kb0, kh0, KO)); vc = cat8(lds_tr(LO(vb, vh, VO) + 1024), lds_tr(LO(vb, vh, VO) + 1536)); H1; }, C1, 0, o0, l0, va); \
        AT_GROUP(if (QK) MFMA32_ACC(N0, kfb, q01), H2, C0, 8, o1, l1, va); \
        AT_GROUP(if (QK) MFMA32_ACC(N1, kfb, q11), { if (KR) kfb = lds_b128(LO(kb1, kh1, KO)); H3; }, C1, 8, o0, l0, vc); } while (0)
#define PRV 1
#define YP(p) asm volatile("s_cmp_lt_u32 %0, 4\n\ts_cbranch_scc1 1f\n\ts_setprio " #p "\n1:" :: "s"(wid) : "scc")
    if (PRV) __builtin_amdgcn_s_setprio(1);
    auto ylow = [&]() { if (PRV) YP(0); };
#define S0(c, ...) AT_STEP(sA0, sA1, sB0, sB1, true, true, ((c) & 3) * SLOT + 4096, ((c) & 3) * SLOT, __VA_ARGS__)
#define S1(c, ...) if (PRV == 1) YP(0); AT_STEP(sB0, sB1, sA0, sA1, true, true, ((c) & 3) * SLOT + 6144, ((c) & 3) * SLOT + 2048, __VA_ARGS__)
#define S2(c, ...) if (PRV == 1 || (PRV == 2 && (c) == 7)) YP(2); AT_STEP(sA0, sA1, sB0, sB1, true, (c) < 7, (((c) + 1) & 3) * SLOT, ((c) & 3) * SLOT + 4096, __VA_ARGS__)
#define S3(c, ...) AT_STEP(sB0, sB1, sA0, sA1, (c) < 7, (c) < 7, (((c) + 1) & 3) * SLOT + 2048, ((c) & 3) * SLOT + 6144, __VA_ARGS__)
#define SY(c, n) do { WAIT_V(n); WAIT_L0(); BAR(); SBAR(); STAMP(20 + (c)); } while (0)
#define NO (void)0
#define KP(c, i) glds16c(Kg + ((c0 + (c)) & 7) * 8192 + (i) * 512, A.voffK, lds0 + wid * 2048 + ((c) & 3) * SLOT + (i) * 1024)
#define VP(c, i) glds16c(Vg + ((c0 + (c)) & 7) * 8192 + (i) * 512, A.voffV, lds0 + wid * 2048 + ((c) & 3) * SLOT + 16384 + (i) * 1024)
#define CH(c) KP(c, 0), KP(c, 1), VP(c, 0), VP(c, 1)
#define HS(i) do { __builtin_amdgcn_raw_buffer_store_b128(hp.v[i], hr, hp.off + (i) * 1024, 0, 16); CFENCE(); } while (0)
#define WP(i) wpf(i)
#define GB(j, slot, i) glds16(g2.Wd + (size_t)((j) * 64 + wid * 8) * (DM * 2) + (i) * 128, gB, lds0 + (slot) * SLOT + 16384 + (i) * 8192 + wid * 1024)
#define GA(j, slot, i) glds16c(g2.Ah + (size_t)(j) * (2 * GRP) + g2.rowb + wid * 1024, (i) ? gA1 : gA0, lds0 + (slot) * SLOT + wid * 2048 + (i) * 1024)
    auto wpf = [&](int i) {
        const int pi = (wid + 8 * i < g2.nh + 8) ? wid + 8 * i : 0, lr = prow + 2 * pi;
        asm volatile("global_load_dword %0, %1, %2" : "+v"(pfd) : "v"(pfo), "s"((lr < g2.nh * 64 ? wpd : wpo) + (size_t)lr * (DM * 2)) : "memory");
    };
    auto cpf = [&]() { CODE_PF(pfd); };
    auto hpeek = [&]() { PEEK_AHEAD(hv, hptr, wid); };
    S0(0, NO, NO, NO, NO);         S1(0, NO, NO, NO, NO);    SY(0, 0);  S2(0, CH(2)); S3(0, CH(3));
    S0(1, WP(0), NO, HS(0), NO);   S1(1, NO, NO, HS(1), NO); SY(1, 7);  S2(1, CH(4)); S3(1, WP(1), NO, HS(2), NO);
    S0(2, NO, NO, HS(3), NO);      S1(2, NO, NO, HS(4), NO); SY(2, 11); S2(2, CH(5)); S3(2, WP(2), NO, HS(5), NO);
    S0(3, NO, NO, HS(6), NO);      S1(3, NO, NO, HS(7), NO); SY(3, 12); S2(3, CH(6)); S3(3, WP(3), NO, NO, NO);
    S0(4, NO, NO, NO, NO);         S1(4, NO, NO, NO, NO);    SY(4, 5);
    if (wid == 0) signal(flags, hsig, lane);
                                                                        S2(4, CH(7)); S3(4, WP(4), NO, NO, NO);
    S0(5, GB(0, 4, 0), GB(0, 4, 1), NO, NO); S1(5, NO, NO, NO, NO); SY(5, 2); S2(5, GB(1, 0, 0), GB(1, 0, 1), NO, NO); S3(5, NO, NO, NO, NO);
    S0(6, hpeek(), NO, NO, NO);
    if (wid == 0) { PEEK_DONE(hv); wait_all(flags, F_H + depword, hv); }
                                   S1(6, NO, NO, NO, NO);    SY(6, 0);  S2(6, GA(0, 4, 0), GA(0, 4, 1), GB(2, 1, 0), GB(2, 1, 1)); S3(6, GA(1, 0, 0), GA(1, 0, 1), cpf(), NO);
    S0(7, GA(2, 1, 0), GA(2, 1, 1), NO, NO); S1(7, NO, NO, NO, NO); SY(7, 0); S2(7, NO, NO, NO, NO); S3(7, NO, NO, ylow(), NO);
    __builtin_amdgcn_s_setprio(0);
#undef S0
#undef S1
#undef S2
#undef S3
#undef NO
    STAMP(28);
    asm volatile("s_nop 1" ::: "memory");
    MFMA32_ACC(o1, vc, pf); MFMA16_ACC(l1, ones, pf);
    MFMA_DRAIN(); SBAR();
    asm volatile("" :: "v"(pfd));
    R.o0 = o0; R.o1 = o1; R.l0 = l0; R.l1 = l1; R.m0 = m0; R.m1 = m1;
}

__global__ __launch_bounds__(512, 2) void k_main(const bf16_t* __restrict__ x, const bf16_t* __restrict__ wqkv, const bf16_t* __restrict__ wo, const bf16_t* __restrict__ wg,
                                                 const bf16_t* __restrict__ wu, const bf16_t* __restrict__ wd, bf16_t* __restrict__ out, unsigned char* ws) {
    __shared__ __attribute__((aligned(16))) unsigned char smem[LDS_BYTES];
    const lds_ptr lds = (lds_ptr)smem;
    const unsigned lds0 = (unsigned)(uintptr_t)lds;
    const int wid = __builtin_amdgcn_readfirstlane(threadIdx.x >> 6);
    const int bid = blockIdx.x, xcd = bid & 7, idx = bid >> 3;
    Flags flags; flags.p = (u64*)(ws + WS_FLAGS);
    const __amdgpu_buffer_rsrc_t wsr = __builtin_amdgcn_make_buffer_rsrc((void*)ws, 0, WS_END, 0x00020000);
#define ST16(v, off) __builtin_amdgcn_raw_buffer_store_b128(v, wsr, off, 0, 16)
    STAMP(0);
    unsigned cpd = 0;
    CodePf cp;
    asm volatile("s_getpc_b64 %0\n.Lk_begin:\n\ts_mov_b32 %1, .Lk_end-.Lk_begin" : "=s"(cp.base), "=s"(cp.len));
    cp.first = idx * 16;
    HPend hp;
    {
        const int lane = threadIdx.x & 63, wr = wid >> 2, wc = wid & 3, js = idx & 3, rb = idx >> 2;
        const bool typeY = (js == 3);
        const int f0 = 448 * xcd + 128 * js;
        const char* P0 = (const char*)(wg + f0);
        const char* P2 = (const char*)(wu + f0);
        const char* P1 = typeY ? (const char*)(wqkv + 128 * xcd + 64) : (const char*)(wg + f0 + 64);
        const char* P3 = typeY ? (const char*)(wqkv + 1024 + 128 * xcd + 64) : (const char*)(wu + f0 + 64);
        const char* P4 = typeY ? (const char*)(wqkv + 2048 + 128 * xcd + 64) : (const char*)(wqkv + js * 1024 + 128 * xcd);
        const unsigned stepF = 32u * FF * 2u, stepQ = 32u * NQKV * 2u, step13 = typeY ? stepQ : stepF;
        const char* XA = (const char*)x + (size_t)rb * 256 * DM * 2;
        const char* XC = (const char*)ws + WS_XC + (size_t)rb * 256 * DM * 2;
        const int r8 = lane >> 3, u8 = lane & 7;
        const int gw = (r8 & 2) | ((wc & 1) << 2);
        const unsigned voffF = (unsigned)(((wc * 8 + r8) * FF + (u8 ^ gw) * 8) * 2);
        const unsigned voffQ = (unsigned)(((wc * 8 + r8) * NQKV + (u8 ^ gw) * 8) * 2);
        const unsigned voff13 = typeY ? voffQ : voffF;
        const unsigned voffA0 = (unsigned)(((32 * wid + r8) * DM + (u8 ^ (r8 >> 1)) * 8) * 2);
        const unsigned voffA1 = (unsigned)(((32 * wid + 8 + r8) * DM + (u8 ^ (4 | (r8 >> 1))) * 8) * 2);
        const unsigned dstA = lds0 + G1_A + wid * 4096, dstW = lds0 + G1_W + wc * 1024;
#define G1_LOAD_A(t) do { const char* b_ = XA + (t) * 128; const unsigned d_ = dstA + ((t) & 1) * 32768; \
        glds16(b_, voffA0, d_); glds16(b_, voffA1, d_ + 1024); glds16(b_ + 32768, voffA0, d_ + 2048); glds16(b_ + 32768, voffA1, d_ + 3072); } while (0)
#define G1_LOAD_W(hs) do { const unsigned d_ = dstW + ((hs) & 3) * 4096; \
        glds16(P0 + (size_t)(hs) * stepF, voffF, d_); glds16(P1 + (size_t)(hs) * step13, voff13, d_ + 16384); glds16(P2 + (size_t)(hs) * stepF, voffF, d_ + 32768); \
        glds16(P3 + (size_t)(hs) * step13, voff13, d_ + 49152); glds16(P4 + (size_t)(hs) * stepQ, voffQ, d_ + 65536); } while (0)
        STAMP(29);
        if (wr == 1) G1_LOAD_W(0);
        G1_LOAD_A(0);
        u32x4 xa0, xa1;
        const int srow = rb * 256 + (js * 8 + xcd) * 8 + wid;
        asm volatile("global_load_dwordx4 %0, %2, %3\n\tglobal_load_dwordx4 %1, %2, %3 offset:1024"
                     : "=&v"(xa0), "=&v"(xa1) : "v"((unsigned)(lane * 16)), "s"((const char*)x + (size_t)srow * DM * 2) : "memory");
        CODE_PF(cpd);
        STAMP(30);

        int fr = lane & 15, fq = lane >> 4;
        const lds_ptr aB0 = lds + G1_A + (wr * 128 + fr) * 128 + ((fq ^ (fr >> 1)) * 16);
        const lds_ptr aB1 = lds + G1_A + (wr * 128 + fr) * 128 + (((4 | fq) ^ (fr >> 1)) * 16);
        const int tq = (lane >> 2) & 3, tp = lane & 3, tg = (tq & 2) | ((fq & 1) << 2);
        auto wbase = [&](int panel, int bp) { return lds + G1_W + panel * 16384 + (8 * fq + tq) * 128 + (((2 * bp + (tp >> 1)) ^ tg) * 16) + (tp & 1) * 8; };
        const int pX = wc >> 1, bX = 2 * (wc & 1);
        const lds_ptr wB0 = typeY ? wbase(0, wc) : wbase(pX, bX);
        const lds_ptr wB1 = typeY ? wbase(1, wc) : wbase(pX, bX + 1);
        const lds_ptr wB2 = typeY ? wbase(2, wc) : wbase(2 + pX, bX);
        const lds_ptr wB3 = typeY ? wbase(3, wc) : wbase(2 + pX, bX + 1);
        const lds_ptr wB4 = wbase(4, wc);
        if (threadIdx.x == 0) *(LAS unsigned*)(lds + G1_CNT) = 0u;
        f32x4 acc[8][5];
#pragma unroll
        for (int m = 0; m < 8; ++m)
#pragma unroll
            for (int b = 0; b < 5; ++b) { acc[m][b] = (f32x4){0.f, 0.f, 0.f, 0.f}; PIN(acc[m][b]); }
        if (wr == 1) { WAIT_V(7); BAR(); }
        asm volatile("s_waitcnt vmcnt(1)" : "+v"(xa0), "+v"(xa1) :: "memory");
        STAMP(31);
        u64 sg;
        {
            const f32x2 st = row_stats(xa0, xa1);
            G1_LOAD_W(1 + wr);
            ST16(xa0, (lane >= 32) ? WS_XC + (unsigned)srow * 2048u + lane * 16 : WS_END);
            ST16(xa1, WS_XC + (unsigned)srow * 2048u + 1024 + lane * 16);
            __builtin_amdgcn_raw_buffer_store_b64(__builtin_bit_cast(u32x2, st), wsr, (lane == 0) ? WS_STAT + (unsigned)srow * 8u : WS_END, 0, 16);
            asm volatile("global_load_dwordx2 %0, %1, %2 sc1" : "=&v"(sg) : "v"(0u), "s"(flags.p) : "memory");
        }
        STAMP(32);
        bf16x8 af[8], wf[5];
        float cs0 = 0.f, cs1 = 0.f, cs2 = 0.f;
#define CS1(c, w) "v_dot2c_f32_bf16 %" #c ", %25, %" #w "\n\t"
#define G1_CSUM(t, lim) do { const u32x4 w0_ = __builtin_bit_cast(u32x4, wf[0]), w1_ = __builtin_bit_cast(u32x4, wf[1]), w2_ = __builtin_bit_cast(u32x4, wf[2]), \
            w3_ = __builtin_bit_cast(u32x4, wf[3]), w4_ = __builtin_bit_cast(u32x4, wf[4]); \
        asm volatile("s_cmp_gt_u32 %24, %26\n\ts_cbranch_scc1 2f\n\ts_cmp_lg_u32 %23, 0\n\ts_cbranch_scc1 1f\n\t" \
            CS1(0, 3) CS1(1, 7) CS1(2, 19) CS1(0, 4) CS1(1, 8) CS1(2, 20) CS1(0, 5) CS1(1, 9) CS1(0, 6) CS1(1, 10) "s_nop 1\n\ts_branch 2f\n1:\n\t" \
            CS1(0, 11) CS1(1, 15) CS1(2, 21) CS1(0, 12) CS1(1, 16) CS1(2, 22) CS1(0, 13) CS1(1, 17) CS1(0, 14) CS1(1, 18) "s_nop 2\n2:" \
            : "+v"(cs0), "+v"(cs1), "+v"(cs2) \
            : "v"(w0_[0]), "v"(w0_[1]), "v"(w0_[2]), "v"(w0_[3]), "v"(w1_[0]), "v"(w1_[1]), "v"(w1_[2]), "v"(w1_[3]), "v"(w2_[0]), "v"(w2_[1]), "v"(w2_[2]), "v"(w2_[3]), \
              "v"(w3_[0]), "v"(w3_[1]), "v"(w3_[2]), "v"(w3_[3]), "v"(w4_[0]), "v"(w4_[1]), "v"(w4_[2]), "v"(w4_[3]), "s"(wr), "s"(t), "s"(0x3f803f80u), "i"(lim) : "scc"); } while (0)
#define G1_RW(i, B, slot) wf[i] = cat8(lds_tr(B + (slot) * 4096), lds_tr(B + (slot) * 4096 + 512))
#define G1_RA(m, AB, abuf) af[m] = lds_b128(AB + (abuf) * 32768 + (m) * 2048)
#define G1_SEG(AB, abuf, slot, L0, L1, L2, L3, L4, CLOSE) do { \
        L0; G1_RW(0, wB0, slot); G1_RW(1, wB1, slot); L1; G1_RW(2, wB2, slot); G1_RW(3, wB3, slot); L2; G1_RW(4, wB4, slot); G1_RA(0, AB, abuf); G1_RA(1, AB, abuf); \
        L3; G1_RA(2, AB, abuf); G1_RA(3, AB, abuf); G1_RA(4, AB, abuf); L4; G1_RA(5, AB, abuf); G1_RA(6, AB, abuf); G1_RA(7, AB, abuf); \
        SBAR(); G1_CSUM(t, 3); WAIT_L0(); SBAR(); if (CLOSE) { BAR(); SBAR(); } } while (0)
#define G1_SEG_A(abuf, slot, tt) do { const char* b_ = ((tt) < 4 ? XA : XC) + (tt) * 128; const unsigned d_ = dstA + (1 - (abuf)) * 32768; \
        G1_SEG(aB0, abuf, slot, glds16c(b_, voffA0, d_), glds16c(b_, voffA1, d_ + 1024), glds16c(b_ + 32768, voffA0, d_ + 2048), glds16c(b_ + 32768, voffA1, d_ + 3072), (void)0, true); } while (0)
#define G1_SEG_W(abuf, slot, hs, dslot, CLOSE) do { const unsigned d_ = dstW + (dslot) * 4096; \
        G1_SEG(aB1, abuf, slot, glds16(P0 + (size_t)(hs) * stepF, voffF, d_), glds16(P1 + (size_t)(hs) * step13, voff13, d_ + 16384), glds16(P2 + (size_t)(hs) * stepF, voffF, d_ + 32768), \
               glds16(P3 + (size_t)(hs) * step13, voff13, d_ + 49152), glds16(P4 + (size_t)(hs) * stepQ, voffQ, d_ + 65536), CLOSE); } while (0)
#define G1_MMA() do { __builtin_amdgcn_s_setprio(1); \
        _Pragma("unroll") for (int b = 0; b < 5; ++b) _Pragma("unroll") for (int i = 0; i < 8; ++i) { const int m = (b & 1) ? 7 - i : i; MFMA16_ACC(acc[m][b], wf[b], af[m]); } \
        __builtin_amdgcn_s_setprio(0); SBAR(); } while (0)

        STAMP(33);
        WAIT_L0();
        STAMP(34);
        BAR();
        SBAR();
        STAMP(1);
        flags.tag = 0;
        const u64* sgp = flags.p + (F_XC + rb * 32 + (lane & 31)) * 16;
#pragma unroll 1
        for (int t = 0; t < 16; t += 2) {
            const int h1 = (2 * t + 3 + wr < 31) ? 2 * t + 3 + wr : 31, h2 = (2 * t + 5 + wr < 31) ? 2 * t + 5 + wr : 31, t2 = (t < 14) ? t + 2 : 15;
            const int sel = (t == 12) ? 0 : ((t ^ 2) | wid);
            STAMP(35 + (t >> 1));
            PEEK_AHEAD(sg, sgp, sel);
            if (t == 14) CODE_PF(cpd);
            G1_SEG_A(0, 0, t + 1); G1_MMA(); asm volatile("s_waitcnt vmcnt(4)" : "+v"(sg), "+v"(cpd) :: "memory"); BAR(); SBAR();
            if (t == 0) {
                const unsigned elo = __builtin_amdgcn_readfirstlane((unsigned)sg), ehi = __builtin_amdgcn_readfirstlane((unsigned)(sg >> 32));
                flags.tag = (((u64)ehi << 32) | elo) * 0x9E3779B97F4A7C15ull + 0x632BE59BD9B4E019ull;
                if (wid == 4) signal(flags, F_XC + bid, lane);
            }
            if (sel == 0) {
                if (t == 12) *(LAS u64*)(lds + G1_STAT + (wid * 32 + (opaque_tid() & 31)) * 8) = sg;
                else wait_all(flags, F_XC + rb * 32 + (opaque_tid() & 31), sg);
            }
            G1_SEG_W(0, 1, h1, (3 + wr) & 3, true); G1_MMA(); WAIT_V(5); BAR(); SBAR();
            G1_SEG_A(1, 2, t2); G1_MMA(); WAIT_V(4); BAR(); SBAR();
            G1_SEG_W(1, 3, h2, (1 + wr) & 3, false);
            if (t == 2) {
                const int tl = opaque_tid() & 63;
                const lds_ptr cp = lds + G1_CS + wc * 384 + (tl & 15) * 4;
                *(LAS float*)(cp + wr * 128) = red4(cs0); *(LAS float*)(cp + wr * 128 + 64) = red4(cs1); *(LAS float*)(cp + 256 + wr * 64) = red4(cs2);
                sgp = (const u64*)(ws + WS_STAT) + rb * 256 + wid * 32 + (tl & 31);
                WAIT_L0();
            }
            SBAR(); BAR(); SBAR(); G1_MMA(); WAIT_V(5);
            if (wr == 0 || t < 14) BAR();
            SBAR();
        }
        MFMA_DRAIN(); SBAR();
        STAMP(2);

        const int tz = opaque_tid(), ln = tz & 63;
        fr = tz & 15; fq = (tz >> 4) & 3;
        const int row0 = rb * 256 + wr * 128 + fr;
        f32x4 csv[5]; f32x2 st[8];
#pragma unroll
        for (int b = 0; b < 5; ++b) csv[b] = *(const LAS f32x4*)(lds + G1_CS + wc * 384 + b * 64 + fq * 16);
        csv[4] += *(const LAS f32x4*)(lds + G1_CS + wc * 384 + 320 + fq * 16);
#pragma unroll
        for (int m = 0; m < 8; ++m) st[m] = *(const LAS f32x2*)(lds + G1_STAT + (wr * 128 + m * 16 + fr) * 8);
        auto lnv = [&](int m, int b, float sc) { return (acc[m][b] - st[m].x * csv[b]) * (st[m].y * sc); };
        auto glu = [&](int m, int bg, int bu) {
            const f32x4 g = lnv(m, bg, -LOG2E), u = lnv(m, bu, 1.0f);
            f32x4 h;
#pragma unroll
            for (int j = 0; j < 4; ++j) h[j] = u[j] * __builtin_amdgcn_rcpf(1.0f + __builtin_amdgcn_exp2f(g[j]));
            return h;
        };
        auto pack2 = [&](const f32x4& v0, const f32x4& v1) {
            unsigned p0 = cvtpk(v0[0], v0[1]), p1 = cvtpk(v0[2], v0[3]), q0 = cvtpk(v1[0], v1[1]), q1 = cvtpk(v1[2], v1[3]);
            swap16(p0, q0); swap16(p1, q1);
            return (u32x4){p0, p1, q0, q1};
        };
        const int hd = 4 * xcd + (typeY ? 2 : 0) + (wc >> 1);
        const unsigned qoff = WS_QKV + (unsigned)(hd * 2 + (wc & 1)) * 65536u + (unsigned)(row0 + (fq & 1) * 16) * 32u + (fq >> 1) * 16;
        constexpr unsigned KIND = NH * 2 * 65536u;
        if (!typeY) {
            const float sc4 = (js == 0) ? QSCALE : 1.0f;
#pragma unroll
            for (int m = 0; m < 8; m += 2) ST16(pack2(lnv(m, 4, sc4), lnv(m + 1, 4, sc4)), qoff + js * KIND + m * 512);
            CFENCE();
            hp.base = ws + WS_ACT + (unsigned)(14 * xcd + 4 * js + wc) * GRP + (unsigned)(rb * 256 + wr * 128) * 64u;
            hp.off = fr * 64 + (fq & 1) * 32 + (fq >> 1) * 16; hp.size = 8192;
#pragma unroll
            for (int m = 0; m < 8; ++m) {
                hp.v[m] = pack2(glu(m, 0, 2), glu(m, 1, 3));
                if (m == 5) { PIN(hp.v[5]); WAIT_V(0); arrive(flags, F_QKV + bid, ln, lds + G1_CNT, 7u); }
            }
        } else {
#pragma unroll
            for (int m = 0; m < 8; m += 2) {
                ST16(pack2(lnv(m, 3, 1.0f), lnv(m + 1, 3, 1.0f)), qoff + KIND + m * 512);
                ST16(pack2(lnv(m, 4, 1.0f), lnv(m + 1, 4, 1.0f)), qoff + 2 * KIND + m * 512);
                ST16(pack2(lnv(m, 1, QSCALE), lnv(m + 1, 1, QSCALE)), qoff + m * 512);
            }
            CFENCE();
            hp.base = ws + WS_ACT + (unsigned)(14 * xcd + 12 + (wc >> 1)) * GRP + (wc & 1) * (GRP / 2) + (unsigned)(rb * 256 + wr * 128) * 32u;
            hp.off = (fr + (fq & 1) * 16) * 32 + (fq >> 1) * 16; hp.size = 4096;
#pragma unroll
            for (int m = 0; m < 8; m += 2) { hp.v[m >> 1] = pack2(glu(m, 0, 2), glu(m + 1, 0, 2)); hp.v[4 + (m >> 1)] = hp.v[m >> 1]; }
            PIN(hp.v[3]); WAIT_V(0); arrive(flags, F_QKV + bid, ln, lds + G1_CNT, 7u);
        }
#pragma unroll
        for (int i = 0; i < 8; ++i) PIN(hp.v[i]);
        STAMP(3);
    }

    const int z = xcd & 1, rbk = ((xcd >> 1) & 1) * 8 + (idx & 7), cbk = (xcd >> 2) * 4 + (idx >> 3);
    G2 g2;
    g2.nh = z ? 56 - NH0 : NH0;
    g2.rowb = rbk * 128 * 64;
    g2.split = z ? split_mask(NH0, 56 - NH0) : split_mask(0, NH0);
    g2.Ah = (const char*)ws + WS_ACT + (size_t)(z * 2 * NH0) * GRP;
    g2.Ao = (const char*)ws + WS_ACT + (size_t)(112 + z * 16 - 2 * g2.nh) * GRP;
    g2.Wd = (const char*)(wd + (size_t)z * NH0 * 64 * DM + cbk * 128);
    g2.Wo = (const char*)(wo + (size_t)z * 512 * DM + cbk * 128) - (size_t)g2.nh * 64 * DM * 2;
    {
        const int hl = idx & 3, qb = idx >> 2, h = 4 * xcd + hl;
        const int l0_ = opaque_tid() & 63;
        const int qkv0 = F_QKV + xcd + 8 * (qb * 4 + ((hl >= 2) ? 3 : l0_ % 3));
        const int qkvword = F_QKV + xcd + 8 * ((l0_ & 7) * 4 + ((hl >= 2) ? 3 : 1 + ((l0_ >> 3) & 1)));
        const int odep = (rbk >> 1) * 32 + ((l0_ >> 2) & 3) * 8 + 4 * z + (l0_ & 3);
        const int depword = (l0_ & 16) ? (odep & ~7) + 4 : odep;
        const char* Qg = (const char*)ws + WS_QKV + (size_t)h * 131072 + qb * 256 * 32;
        const char* Kg = (const char*)ws + WS_QKV + (size_t)(NH + h) * 131072;
        const char* Vg = (const char*)ws + WS_QKV + (size_t)(2 * NH + h) * 131072;
        const char* wpd = g2.Wd - (cbk & 3) * 256;
        const char* wpo = g2.Wo - (cbk & 3) * 256;
        AttnOut R;
#define DG
        attn_fast(lds, lds0, Qg, Kg, Vg, g2, flags, qkv0, qkvword, depword, F_H + bid, qb, bid == 0, cp, hp, wpd, wpo, idx * 2 * (g2.nh + 8), wid, R DG);
        STAMP(6);
        const int tz = opaque_tid(), ln = tz & 63, w4 = wid & 3, fr = ln & 15, fq = ln >> 4;
        lds_ptr po0 = lds + 2 * SLOT + w4 * 4096 + ln * 16, po1 = lds + 2 * SLOT + 16384 + w4 * 2048 + ln * 16;
        PIN(po0); PIN(po1);
        auto po = [&](int i) { return i < 4 ? po0 + i * 1024 : po1 + (i >> 1 & 1) * 8192 + (i & 1) * 1024; };
        const lds_ptr pl = po0 + SLOT;
        volatile LAS unsigned* const badw = (volatile LAS unsigned*)(lds + 3 * SLOT + 1024);
        const int wm = w4 >> 1, wn = w4 & 1;
        const unsigned aO0 = (wm * 64 + fr) * 128 + ((fq ^ (fr >> 1)) * 16), aO1 = aO0 ^ 64;
        const int tq = (ln >> 2) & 3, tp = ln & 3, tg = (tq & 2) | ((fq & 1) << 2);
        unsigned wO[4];
#pragma unroll
        for (int b = 0; b < 4; ++b) wO[b] = 16384 + wn * 8192 + (8 * fq + tq) * 128 + (((2 * b + (tp >> 1)) ^ tg) * 16) + (tp & 1) * 8;
        f32x4 acc[4][4];
        bf16x8 xa[4], ya[4]; s16x4 xl[4], xh[4], yl[4], yh[4];
        lds_ptr pa0, pa1, pw0, pw1, pw2, pw3;
        int so = 4 * SLOT;
        auto handover = [&]() {
            if (wid < 4) {
#pragma unroll
                for (int i = 0; i < 4; ++i) {
                    *(LAS f32x4*)po(i) = (f32x4){R.o0[4 * i], R.o0[4 * i + 1], R.o0[4 * i + 2], R.o0[4 * i + 3]};
                    *(LAS f32x4*)po(4 + i) = (f32x4){R.o1[4 * i], R.o1[4 * i + 1], R.o1[4 * i + 2], R.o1[4 * i + 3]};
                }
                *(LAS f32x4*)pl = (f32x4){R.l0[0], R.m0, R.l1[0], R.m1};
                xl[0] = lds_tr(pw0); xh[0] = lds_tr(pw0 + 512);
#pragma unroll
                for (int m = 0; m < 4; ++m) xa[m] = lds_b128(pa0 + m * 2048);
                xl[1] = lds_tr(pw1); xh[1] = lds_tr(pw1 + 512); xl[2] = lds_tr(pw2); xh[2] = lds_tr(pw2 + 512); xl[3] = lds_tr(pw3); xh[3] = lds_tr(pw3 + 512);
#pragma unroll
                for (int m = 0; m < 4; ++m)
#pragma unroll
                    for (int b = 0; b < 4; ++b) { acc[m][b] = (f32x4){0.f, 0.f, 0.f, 0.f}; PIN(acc[m][b]); }
            }
        };
        G2Off go; g2_voff(ln, go);
        PIN(go.A0); PIN(go.A1); PIN(go.N0); PIN(go.N1); PIN(go.B0); PIN(go.B1);
        pa0 = lds + aO0 + so; pa1 = lds + aO1 + so; pw0 = lds + wO[0] + so; pw1 = lds + wO[1] + so; pw2 = lds + wO[2] + so; pw3 = lds + wO[3] + so;
        PIN(pa0); PIN(pa1); PIN(pw0); PIN(pw1); PIN(pw2); PIN(pw3);
        {
            handover();
            const f32x16 z16 = R.o0 * 0.0f + R.o1 * 0.0f;
            const f32x4 z4 = (z16.s0123 + z16.s4567) + (z16.s89ab + z16.scdef);
            const f32x2 z2 = z4.lo + z4.hi;
            const float zs = z2.x + z2.y;
            const bool ok = (zs == 0.f) && (R.l0[0] > 0.f) && (R.l0[0] < 0x1p96f) && (R.l1[0] > 0.f) && (R.l1[0] < 0x1p96f);
            const bool wave_bad = !__all(ok);
            if (ln == 0) badw[wid] = wave_bad ? 1u : 0u;
            STAMP(16);
            WAIT_L0(); BAR(); CFENCE();
            const unsigned anybad = badw[ln & 7];
            if (__builtin_expect(__any(anybad != 0u), 0)) {
                WAIT_L0(); BAR(); CFENCE();
                attn_safe(lds, lds0, Qg, Kg, Vg, wid, R);
                handover();
                WAIT_L0(); BAR(); CFENCE();
            }
        }
        STAMP(7);

        const int tile = rbk * 8 + cbk, nt = g2.nh + 8;
        if (__builtin_expect(wid >= 4, 0)) {
#define G2_LOAD0(j, sl) do { const unsigned d_ = lds0 + (sl) * SLOT; g2_load_b(g2, j, d_, 2 * w4, go.B0); g2_load_a(g2, j, d_, 2 * w4, go); } while (0)
#define G2_LOAD1(j, sl) do { const unsigned d_ = lds0 + (sl) * SLOT; g2_load_b(g2, j, d_, 2 * w4 + 1, go.B1); g2_load_a(g2, j, d_, 2 * w4 + 1, go); } while (0)
#define G2_LOAD(j, sl) do { G2_LOAD0(j, sl); G2_LOAD1(j, sl); } while (0)
            const f32x4 lm = *(const LAS f32x4*)pl;
            f32x4 t[8];
#pragma unroll
            for (int i = 0; i < 8; ++i) t[i] = *(const LAS f32x4*)po(i);
            WAIT_L0();
            G2_LOAD(3, 2);
            BAR();
            G2_LOAD(4, 3);
            float ck[2], co[2]; unsigned p[16];
            auto coef = [&](int q) {
                const float lk = q ? R.l1[0] : R.l0[0], mk = q ? R.m1 : R.m0, lo = lm[2 * q], mo = lm[2 * q + 1];
                const float mm = __builtin_fmaxf(mk, mo), fk = __builtin_amdgcn_exp2f(mk - mm), fo = __builtin_amdgcn_exp2f(mo - mm);
                const float rl = 1.0f / (lk * fk + lo * fo);
                ck[q] = fk * rl; co[q] = fo * rl; PIN(ck[q]); PIN(co[q]); SBAR();
            };
            auto half = [&](int q, int hh) {
                const f32x16 ok_ = q ? R.o1 : R.o0;
                unsigned* pp = p + 8 * q + 4 * hh;
#pragma unroll
                for (int i = 0; i < 2; ++i) {
                    const int g = 2 * hh + i; const f32x4 u = t[4 * q + g];
                    pp[2 * i] = cvtpk(ok_[4 * g] * ck[q] + u[0] * co[q], ok_[4 * g + 1] * ck[q] + u[1] * co[q]);
                    pp[2 * i + 1] = cvtpk(ok_[4 * g + 2] * ck[q] + u[2] * co[q], ok_[4 * g + 3] * ck[q] + u[3] * co[q]);
                }
                swap32(pp[0], pp[2]); swap32(pp[1], pp[3]);
                PIN(pp[0]); PIN(pp[1]); PIN(pp[2]); PIN(pp[3]); SBAR();
            };
            const unsigned ooff = WS_ACT + (unsigned)(112 + h) * GRP + (unsigned)(qb * 256 + w4 * 64 + fr) * 64u + (fq & 1) * 32 + (fq >> 1) * 16;
            auto ost = [&](int q) {
                unsigned* pp = p + 8 * q;
                swap16(pp[0], pp[4]); swap16(pp[1], pp[5]); swap16(pp[2], pp[6]); swap16(pp[3], pp[7]);
                ST16(((u32x4){pp[0], pp[1], pp[2], pp[3]}), ooff + q * 2048); ST16(((u32x4){pp[4], pp[5], pp[6], pp[7]}), ooff + q * 2048 + 1024); CFENCE();
            };
            SBAR(); coef(0); coef(1);
            WAIT_V(8);  BAR(); G2_LOAD(5, 4); G2_LOAD0(6, 0);
            WAIT_V(12); BAR(); G2_LOAD1(6, 0); G2_LOAD(7, 1);
            WAIT_V(16); BAR(); G2_LOAD0(8, 2);  SBAR(); half(0, 0); G2_LOAD1(8, 2);
            WAIT_V(16); BAR(); G2_LOAD0(9, 3);  SBAR(); half(0, 1); G2_LOAD1(9, 3);  ost(0);
            WAIT_V(18); BAR(); G2_LOAD0(10, 4); SBAR(); half(1, 0); G2_LOAD1(10, 4);
            WAIT_V(18); BAR(); G2_LOAD0(11, 0); SBAR(); half(1, 1); G2_LOAD1(11, 0); ost(1);
            WAIT_V(20); BAR(); G2_LOAD(12, 1);
            WAIT_V(18); BAR(); G2_LOAD(13, 2);
            WAIT_V(18);
            int sl = 3; cpd = 0;
#pragma unroll 1
            for (int j = 11; j < nt - 3; ++j) {
                BAR();
                if (j == 29) CODE_PF(cpd);
                if (j == 12 && wid == 4) signal(flags, F_O + bid, ln);
                G2_LOAD(j + 3, sl);
                sl = (sl == 4) ? 0 : sl + 1;
                WAIT_V(16);
            }
            BAR();
            if (tz == 256) *(LAS unsigned*)(lds + sl * SLOT) = 0u;
            WAIT_V(8); WAIT_L0(); BAR(); WAIT_V(0); BAR();
            asm volatile("" :: "v"(cpd));
            if (wid == 4) wait_all(flags, F_X + tile * 2 + z, peek(flags, F_X + tile * 2 + z));
            CFENCE(); BAR();
            asm volatile(".Lk_end:");
        } else {
#define G2_M(C, m, b) do { MFMA16_ACC(acc[m][b], cat8(C##l[b], C##h[b]), C##a[m]); SBAR(); } while (0)
#define G2_HALF(C, N, AB, WO, RD, TAIL) do { \
            G2_M(C, 0, 0); if (RD) { N##l[0] = lds_tr(pw0 + (WO)); N##h[0] = lds_tr(pw0 + (WO) + 512); } SBAR(); \
            G2_M(C, 1, 0); if (RD) N##a[0] = lds_b128(AB); SBAR(); \
            G2_M(C, 2, 0); if (RD) N##a[1] = lds_b128((AB) + 2048); SBAR(); \
            G2_M(C, 3, 0); if (RD) N##a[2] = lds_b128((AB) + 4096); SBAR(); \
            G2_M(C, 0, 1); if (RD) N##a[3] = lds_b128((AB) + 6144); SBAR(); \
            G2_M(C, 1, 1); if (RD) { N##l[1] = lds_tr(pw1 + (WO)); N##h[1] = lds_tr(pw1 + (WO) + 512); } SBAR(); \
            G2_M(C, 2, 1); if (RD) { N##l[2] = lds_tr(pw2 + (WO)); N##h[2] = lds_tr(pw2 + (WO) + 512); } SBAR(); \
            G2_M(C, 3, 1); if (RD) { N##l[3] = lds_tr(pw3 + (WO)); N##h[3] = lds_tr(pw3 + (WO) + 512); } SBAR(); \
            G2_M(C, 0, 2); TAIL(0); SBAR(); G2_M(C, 1, 2); TAIL(1); SBAR(); G2_M(C, 2, 2); TAIL(2); SBAR(); G2_M(C, 3, 2); TAIL(3); SBAR(); \
            G2_M(C, 0, 3); G2_M(C, 1, 3); G2_M(C, 2, 3); G2_M(C, 3, 3); } while (0)
            const u64* const optr = flags.p + (F_O + odep) * 16;
            u64 ov = 0;
            STAMP(8); STAMP(43);
#define G2_NEXT(i) do { if ((i) == 0) so = (so == 4 * SLOT) ? 0 : so + SLOT; \
                if ((i) == 1) { pa0 = lds + aO0 + so; pa1 = lds + aO1 + so; PIN(pa0); PIN(pa1); } \
                if ((i) == 2) { pw0 = lds + wO[0] + so; pw1 = lds + wO[1] + so; PIN(pw0); PIN(pw1); } \
                if ((i) == 3) { pw2 = lds + wO[2] + so; pw3 = lds + wO[3] + so; PIN(pw2); PIN(pw3); } } while (0)
#define G2_NONE(i) do {} while (0)
            WAIT_L0(); SBAR(); G2_HALF(x, y, pa1, 4096, true, G2_NEXT);
            WAIT_L0(); SBAR(); G2_HALF(y, x, pa0, 0, true, G2_NONE);
            BAR();
            WAIT_L0(); SBAR(); G2_HALF(x, y, pa1, 4096, true, G2_NEXT);
            WAIT_L0(); SBAR(); G2_HALF(y, x, pa0, 0, true, G2_NONE);
#pragma unroll 1
            for (int ph = 0; ph < 3; ++ph) {
                const int n = (ph == 0) ? g2.nh / 2 - 4 : (ph == 1) ? 1 : 5;
#pragma unroll 1
                for (int i = 0; i < n; ++i) {
                    WAIT_L0(); SBAR(); G2_HALF(x, y, pa1, 4096, true, G2_NEXT);
                    WAIT_L0(); BAR(); SBAR(); G2_HALF(y, x, pa0, 0, true, G2_NONE);
                    WAIT_L0(); SBAR(); G2_HALF(x, y, pa1, 4096, true, G2_NEXT);
                    WAIT_L0(); BAR(); SBAR(); G2_HALF(y, x, pa0, 0, true, G2_NONE);
                }
                PEEK_AHEAD(ov, optr, wid | ph);
                if ((wid | (ph ^ 1)) == 0) { PEEK_DONE(ov); wait_all(flags, F_O + odep, ov); }
            }
            const int row = rbk * 128 + wm * 64 + z * 32 + fr, col = cbk * 128 + wn * 64 + (fq & 1) * 16 + (fq >> 1) * 8;
            u32x4 xr[4];
#pragma unroll
            for (int i = 0; i < 4; ++i) xr[i] = *(const u32x4*)(x + (size_t)(row + (i >> 1) * 16) * DM + col + (i & 1) * 32);
            WAIT_L0(); SBAR(); G2_HALF(x, y, pa1, 4096, true, G2_NEXT);
            WAIT_L0(); BAR(); SBAR(); G2_HALF(y, x, pa0, 0, true, G2_NONE);
            WAIT_L0(); SBAR(); G2_HALF(x, y, pa1, 4096, true, G2_NONE);
            WAIT_L0(); SBAR(); G2_HALF(y, x, pa0, 0, false, G2_NONE);
            MFMA_DRAIN(); SBAR();
            STAMP(9);
            f32x4 vk[8];
            const unsigned xo = WS_XCH + (unsigned)(tile * 2 + 1 - z) * 32768u + w4 * 8192 + ln * 16;
            if (z == 0) {
#pragma unroll
                for (int i = 0; i < 8; ++i) { ST16(__builtin_bit_cast(u32x4, acc[2 + (i >> 2)][i & 3]), xo + i * 1024); vk[i] = acc[i >> 2][i & 3]; }
            } else {
#pragma unroll
                for (int i = 0; i < 8; ++i) { ST16(__builtin_bit_cast(u32x4, acc[i >> 2][i & 3]), xo + i * 1024); vk[i] = acc[2 + (i >> 2)][i & 3]; }
            }
            WAIT_V(0);
            arrive(flags, F_X + tile * 2 + 1 - z, ln, lds + ((so == 4 * SLOT) ? 0 : so + SLOT), 3u);
            STAMP(10);
            CFENCE(); BAR(); CFENCE();
            STAMP(11);
            const unsigned xi = WS_XCH + (unsigned)(tile * 2 + z) * 32768u + w4 * 8192 + ln * 16;
#pragma unroll
            for (int i = 0; i < 8; ++i) vk[i] += __builtin_bit_cast(f32x4, __builtin_amdgcn_raw_buffer_load_b128(wsr, xi + i * 1024, 0, 16));
            const __amdgpu_buffer_rsrc_t outr = __builtin_amdgcn_make_buffer_rsrc((void*)out, 0, SEQ * DM * 2, 0x00020000);
            u32x4 w[4];
#pragma unroll
            for (int i = 0; i < 4; ++i) {
                const u32x4 a = __builtin_bit_cast(u32x4, vk[2 * i]), b = __builtin_bit_cast(u32x4, vk[2 * i + 1]);
                unsigned a0 = a[0], a1 = a[1], a2 = a[2], a3 = a[3], b0 = b[0], b1 = b[1], b2 = b[2], b3 = b[3];
                swap16(a0, b0); swap16(a1, b1); swap16(a2, b2); swap16(a3, b3);
                const u32x4 xx = xr[i];
                w[i][0] = cvtpk(u2f(a0) + bf_lo(xx[0]), u2f(a1) + bf_hi(xx[0]));
                w[i][1] = cvtpk(u2f(a2) + bf_lo(xx[1]), u2f(a3) + bf_hi(xx[1]));
                w[i][2] = cvtpk(u2f(b0) + bf_lo(xx[2]), u2f(b1) + bf_hi(xx[2]));
                w[i][3] = cvtpk(u2f(b2) + bf_lo(xx[3]), u2f(b3) + bf_hi(xx[3]));
            }
            const unsigned oo = (unsigned)((row - (fr & 8)) * DM + col + (fr & 8) * 4) * 2u;
#pragma unroll
            for (int m = 0; m < 2; ++m) {
                u32x4 lo, hi;
#pragma unroll
                for (int k = 0; k < 4; ++k) {
                    lo[k] = __builtin_amdgcn_update_dpp(w[2 * m][k], w[2 * m + 1][k], 0x128, 0xf, 0xc, false);
                    hi[k] = __builtin_amdgcn_update_dpp(w[2 * m + 1][k], w[2 * m][k], 0x128, 0xf, 0x3, false);
                }
                __builtin_amdgcn_raw_buffer_store_b128(lo, outr, oo + m * 16 * DM * 2, 0, 16);
                __builtin_amdgcn_raw_buffer_store_b128(hi, outr, oo + (m * 16 + 8) * DM * 2, 0, 16);
            }
        }
    }
}

extern "C" void kernel_launch(void* const* d_in, const int* in_sizes, int n_in, void* d_out, int out_size, void* d_ws, size_t ws_size, hipStream_t stream) {
    const bf16_t* x = (const bf16_t*)d_in[0];
    k_main<<<256, 512, 0, stream>>>(x, (const bf16_t*)d_in[1], (const bf16_t*)d_in[2], (const bf16_t*)d_in[4], (const bf16_t*)d_in[5], (const bf16_t*)d_in[6],
                                    (bf16_t*)d_out, (unsigned char*)d_ws);
}
```

```cpp
#include <hip/hip_runtime.h>
#include <stdint.h>

typedef unsigned short bf16_t;
typedef short bf16x8 __attribute__((ext_vector_type(8)));
typedef short s16x4 __attribute__((ext_vector_type(4)));
typedef float f32x2 __attribute__((ext_vector_type(2)));
typedef float f32x4 __attribute__((ext_vector_type(4)));
typedef float f32x16 __attribute__((ext_vector_type(16)));
typedef unsigned u32x2 __attribute__((ext_vector_type(2)));
typedef unsigned u32x4 __attribute__((ext_vector_type(4)));
typedef __bf16 bf16x2_t __attribute__((ext_vector_type(2)));
#define LAS __attribute__((address_space(3)))
#define GAS __attribute__((address_space(1)))
typedef LAS unsigned char* lds_ptr;

constexpr int SEQ = 2048, DM = 1024, FF = 3584, NQKV = 3072, NH = 32;
constexpr float LN_EPS = 1e-5f;
constexpr float LOG2E = 1.4426950408889634f;
constexpr float QSCALE = 0.17677669529663687f * LOG2E;

constexpr unsigned WS_FLAGS = 0;
constexpr unsigned WS_STAT = 192 * 1024;
constexpr unsigned WS_QKV = 256 * 1024;
constexpr unsigned WS_ACT = WS_QKV + 3u * SEQ * DM * 2;
constexpr unsigned WS_XCH = WS_ACT + 144u * SEQ * 64;
constexpr unsigned WS_XC = WS_XCH + 256u * 32768;
constexpr unsigned WS_END = WS_XC + SEQ * DM * 2;
constexpr int F_EPOCH = 0, F_QKV = 1, F_H = 257, F_O = 513, F_X = 769, F_XC = 1025;
constexpr unsigned GRP = SEQ * 64;

#define SBAR() __builtin_amdgcn_sched_barrier(0)
#define BAR() __builtin_amdgcn_s_barrier()
#define WAIT_V(n) asm volatile("s_waitcnt vmcnt(" #n ")" ::: "memory")
#define WAIT_L0() do { __builtin_amdgcn_s_waitcnt(0xC07F); asm volatile("" ::: "memory"); } while (0)
#define CFENCE() asm volatile("" ::: "memory")
#define PIN(x) asm volatile("" : "+v"(x))
#define MFMA16(a, b, c) __builtin_amdgcn_mfma_f32_16x16x32_bf16(a, b, c, 0, 0, 0)
#define MFMA32(a, b, c) __builtin_amdgcn_mfma_f32_32x32x16_bf16(a, b, c, 0, 0, 0)
#define MFMA16_ACC(acc, a, b) asm volatile("v_mfma_f32_16x16x32_bf16 %0, %1, %2, %0" : "+v"(acc) : "v"(a), "v"(b))
#define MFMA32_ACC(acc, a, b) asm volatile("v_mfma_f32_32x32x16_bf16 %0, %1, %2, %0" : "+v"(acc) : "v"(a), "v"(b))
#define MFMA32_SET(d, a, b, c) asm volatile("v_mfma_f32_32x32x16_bf16 %0, %1, %2, %3" : "=&v"(d) : "v"(a), "v"(b), "v"(c))
#define MFMA_DRAIN() asm volatile("s_nop 15" ::: "memory")
#define RLX_AGENT __ATOMIC_RELAXED, __HIP_MEMORY_SCOPE_AGENT

#define STAMP(i) do {} while (0)
#define TS(v) do {} while (0)
#define PUT(i, v) do {} while (0)

__device__ __forceinline__ void glds16(const void* sbase, unsigned voff, unsigned ldsdst) {
    asm volatile("s_mov_b32 m0, %2\n\ts_nop 0\n\tglobal_load_lds_dwordx4 %0, %1" ::"v"(voff), "s"(sbase), "s"(ldsdst) : "memory");
}
__device__ __forceinline__ void glds16c(const void* sbase, unsigned voff, unsigned ldsdst) {
    asm volatile("s_mov_b32 m0, %2\n\ts_nop 0\n\tglobal_load_lds_dwordx4 %0, %1 sc1" ::"v"(voff), "s"(sbase), "s"(ldsdst) : "memory");
}
__device__ __forceinline__ bf16x8 lds_b128(lds_ptr p) { return *(const LAS bf16x8*)p; }
__device__ __forceinline__ s16x4 lds_tr(lds_ptr p) { return __builtin_amdgcn_ds_read_tr16_b64_v4i16((LAS s16x4*)p); }
__device__ __forceinline__ bf16x8 cat8(s16x4 a, s16x4 b) { return __builtin_shufflevector(a, b, 0, 1, 2, 3, 4, 5, 6, 7); }
__device__ __forceinline__ unsigned cvtpk(float lo, float hi) {
    f32x2 v = {lo, hi};
    return __builtin_bit_cast(unsigned, __builtin_convertvector(v, bf16x2_t));
}
__device__ __forceinline__ float u2f(unsigned u) { return __builtin_bit_cast(float, u); }
__device__ __forceinline__ unsigned f2u(float f) { return __builtin_bit_cast(unsigned, f); }
__device__ __forceinline__ float bf_lo(unsigned u) { return u2f(u << 16); }
__device__ __forceinline__ float bf_hi(unsigned u) { return u2f(u & 0xffff0000u); }
__device__ __forceinline__ void swap16(unsigned& a, unsigned& b) {
    auto r = __builtin_amdgcn_permlane16_swap(a, b, false, false);
    a = r[0]; b = r[1];
}
__device__ __forceinline__ void swap32(unsigned& a, unsigned& b) {
    auto r = __builtin_amdgcn_permlane32_swap(a, b, false, false);
    a = r[0]; b = r[1];
}
__device__ __forceinline__ int opaque_tid() { int t = threadIdx.x; asm volatile("" : "+v"(t)); return t; }
typedef unsigned long long u64;
struct Flags { u64* p; u64 tag; };
__device__ __forceinline__ void signal(const Flags& f, int word, int lane) {
    if (lane == 0) __hip_atomic_store((GAS u64*)(f.p + word * 16), f.tag, RLX_AGENT);
}
__device__ __forceinline__ u64 peek(const Flags& f, int word) {
    u64 v;
    asm volatile("global_load_dwordx2 %0, %1, off sc1\n\ts_waitcnt vmcnt(0)" : "=&v"(v) : "v"(f.p + word * 16) : "memory");
    return v;
}
__device__ __forceinline__ void wait_all(const Flags& f, int word, u64 v) {
#pragma unroll 1
    for (unsigned spin = 0; spin < (1u << 20) && !__all(v == f.tag); ++spin) { __builtin_amdgcn_s_sleep(1); v = peek(f, word); }
}
__device__ __forceinline__ void arrive(const Flags& f, int word, int lane, lds_ptr cnt, unsigned last) {
    if (lane == 0 && __hip_atomic_fetch_add((LAS unsigned*)cnt, 1u, __ATOMIC_RELAXED, __HIP_MEMORY_SCOPE_WORKGROUP) == last)
        __hip_atomic_store((GAS u64*)(f.p + word * 16), f.tag, RLX_AGENT);
}
#define PEEK_AHEAD(v, ptr, sel) asm volatile("s_cmp_lg_u32 %2, 0\n\ts_cbranch_scc1 1f\n\tglobal_load_dwordx2 %0, %1, off sc1\n1:" : "+v"(v) : "v"(ptr), "s"(sel) : "memory", "scc")
struct CodePf { const char* base; unsigned len; int first; };
#define CODE_PF(d) do { unsigned o_ = (unsigned)(cp.first + (opaque_tid() & 15)) * 128u; o_ = (o_ < cp.len) ? o_ : 0u; \
    asm volatile("global_load_dword %0, %1, %2" : "+v"(d) : "v"(o_), "s"(cp.base) : "memory"); } while (0)
#define PEEK_DONE(v) asm volatile("s_waitcnt vmcnt(0)" : "+v"(v) :: "memory")

template <int CTRL> __device__ __forceinline__ float dpp_add(float v) { return v + u2f(__builtin_amdgcn_update_dpp(0u, f2u(v), CTRL, 0xf, 0xf, true)); }
__device__ __forceinline__ float red4(float v) {
    unsigned a = f2u(v), b = a;
    swap16(a, b);
    v = u2f(a) + u2f(b);
    a = f2u(v); b = a;
    swap32(a, b);
    return u2f(a) + u2f(b);
}
__device__ __forceinline__ float wave_sum(float v) {
    v = dpp_add<0xB1>(v); v = dpp_add<0x4E>(v); v = dpp_add<0x141>(v); v = dpp_add<0x140>(v);
    return red4(v);
}
__device__ __forceinline__ f32x2 row_stats(u32x4& a, u32x4& b) {
    float v[16];
#pragma unroll
    for (int i = 0; i < 4; ++i) { v[2 * i] = bf_lo(a[i]); v[2 * i + 1] = bf_hi(a[i]); v[8 + 2 * i] = bf_lo(b[i]); v[8 + 2 * i + 1] = bf_hi(b[i]); }
    float s = 0.f;
#pragma unroll
    for (int i = 0; i < 16; ++i) s += v[i];
    const float mu = wave_sum(s) * (1.0f / DM);
    float q = 0.f;
#pragma unroll
    for (int i = 0; i < 16; ++i) { v[i] -= mu; q += v[i] * v[i]; }
#pragma unroll
    for (int i = 0; i < 4; ++i) { a[i] = cvtpk(v[2 * i], v[2 * i + 1]); b[i] = cvtpk(v[8 + 2 * i], v[8 + 2 * i + 1]); }
    return (f32x2){mu, 1.0f / __builtin_sqrtf(wave_sum(q) * (1.0f / DM) + LN_EPS)};
}

constexpr int SLOT = 32768, LDS_BYTES = 5 * SLOT;
constexpr int G1_A = 0, G1_W = 65536, G1_STAT = G1_W + 5 * 16384, G1_CS = G1_STAT + 2048, G1_CNT = G1_CS + 4 * 6 * 64;

#define NH0 30
static_assert(NH0 % 2 == 0 && NH0 >= 28 && NH0 <= 32, "");
struct G2 { const char* Ah; const char* Ao; const char* Wd; const char* Wo; int nh; unsigned rowb; u64 split; };
__device__ __forceinline__ void g2_load_b(const G2& g, int j, unsigned dst, int vw, unsigned voffB) {
    const char* b = (j >= g.nh ? g.Wo : g.Wd) + (size_t)(j * 64 + vw * 8) * (DM * 2);
    glds16(b, voffB, dst + 16384 + vw * 1024); glds16(b + 128, voffB, dst + 16384 + 8192 + vw * 1024);
}
struct G2Off { unsigned A0, A1, N0, N1, B0, B1; };
__device__ __forceinline__ void g2_load_a(const G2& g, int j, unsigned dst, int vw, const G2Off& o) {
    const unsigned sp = (unsigned)(g.split >> j) & 1u;
    const char* a = (j >= g.nh ? g.Ao : g.Ah) + (size_t)j * (2 * GRP) + ((g.rowb + vw * 1024) >> sp);
    glds16c(a, sp ? o.N0 : o.A0, dst + vw * 2048); glds16c(a, sp ? o.N1 : o.A1, dst + vw * 2048 + 1024);
}
__device__ __forceinline__ void g2_voff(int lane, G2Off& o) {
    const int r8 = lane >> 3, u8 = lane & 7, ua = u8 ^ (r8 >> 1), ub = ua ^ 4;
    o.A0 = (unsigned)(ua >> 2) * GRP + r8 * 64 + (ua & 3) * 16;
    o.A1 = (unsigned)(ub >> 2) * GRP + (r8 + 8) * 64 + (ub & 3) * 16;
    o.N0 = (unsigned)(ua >> 1) * (GRP / 2) + r8 * 32 + (ua & 1) * 16;
    o.N1 = (unsigned)(ub >> 1) * (GRP / 2) + (r8 + 8) * 32 + (ub & 1) * 16;
    o.B0 = (unsigned)(r8 * (DM * 2) + (u8 ^ (r8 & 2)) * 16);
    o.B1 = (unsigned)(r8 * (DM * 2) + (u8 ^ (4 | (r8 & 2))) * 16);
}
constexpr u64 split_mask(int first, int n) { u64 m = 0; for (int j = 0; j < n; ++j) if ((first + j) % 7 == 6) m |= 1ull << j; return m; }
static_assert(((split_mask(0, NH0) | split_mask(NH0, 56 - NH0)) & 7) == 0, "phase B's tail prefetches K-tiles 0..2 as plain groups");

struct AttnOut { f32x16 o0, o1; f32x4 l0, l1; float m0, m1; };
struct HPend { u32x4 v[8]; unsigned off, size; const void* base; };
struct AttnLane {
    unsigned voffK, voffV; lds_ptr kb0, kb1, vb; const char* qp; bf16x8 ones;
    __device__ __forceinline__ void init(lds_ptr lds, const char* Qg, int wid, int lane) {
        const int r32 = lane & 31, hi = lane >> 5, kh = wid >> 2, qg = wid & 3, ksw = (r32 >> 2) & 3;
        const int uk = (lane & 3) ^ ((lane >> 4) & 3), uv = lane & 3;
        voffK = (unsigned)((uk >> 1) * 65536 + wid * 1024 + (lane >> 2) * 32 + (uk & 1) * 16);
        voffV = (unsigned)((uv >> 1) * 65536 + wid * 1024 + (lane >> 2) * 32 + (uv & 1) * 16);
        kb0 = lds + kh * 8192 + r32 * 64 + ((hi ^ ksw) * 16); kb1 = lds + kh * 8192 + r32 * 64 + (((2 | hi) ^ ksw) * 16);
        vb = lds + 16384 + kh * 8192 + (4 * hi + ((lane >> 2) & 3)) * 64 + ((lane >> 4) & 1) * 32 + (lane & 3) * 8;
        qp = Qg + (qg * 64 + r32) * 32 + hi * 16;
        const short v = (((lane >> 4) & 1) == ((lane >> 2) & 1)) ? (short)0x3f80 : (short)0;
        ones = (bf16x8){v, v, v, v, v, v, v, v};
    }
};
#define EX(v) __builtin_amdgcn_exp2f(v)
#define AT_LOAD_K(c, slot) do { const unsigned d_ = lds0 + wid * 2048 + (slot) * SLOT; const char* k_ = Kg + (c) * 8192; glds16c(k_, A.voffK, d_); glds16c(k_ + 512, A.voffK, d_ + 1024); } while (0)
#define AT_LOAD_V(c, slot) do { const unsigned d_ = lds0 + wid * 2048 + (slot) * SLOT + 16384; const char* v_ = Vg + (c) * 8192; glds16c(v_, A.voffV, d_); glds16c(v_ + 512, A.voffV, d_ + 1024); } while (0)
#define AT_PACK(S, i) __builtin_bit_cast(bf16x8, (u32x4){cvtpk(S[i], S[i + 1]), cvtpk(S[i + 2], S[i + 3]), cvtpk(S[i + 4], S[i + 5]), cvtpk(S[i + 6], S[i + 7])})
#define AT_Q_LOAD() asm volatile("global_load_dwordx4 %0, %4, off sc1\n\tglobal_load_dwordx4 %1, %4, off offset:1024 sc1\n\tglobal_load_dwordx4 %2, %5, off sc1\n\tglobal_load_dwordx4 %3, %5, off offset:1024 sc1" \
        : "=&v"(q00), "=&v"(q10), "=&v"(q01), "=&v"(q11) : "v"(A.qp), "v"(qp2) : "memory")
#define AT_Q_WAIT(n) asm volatile("s_waitcnt vmcnt(" #n ")" : "+v"(q00), "+v"(q01), "+v"(q10), "+v"(q11) :: "memory")
__device__ __forceinline__ float rowmax16(const f32x16& s) {
    float a = s[0];
#pragma unroll
    for (int r = 1; r < 16; ++r) a = __builtin_fmaxf(a, s[r]);
    unsigned x = f2u(a), y = x; swap32(x, y);
    return __builtin_fmaxf(u2f(x), u2f(y));
}

__device__ __forceinline__ void attn_safe(lds_ptr lds, unsigned lds0, const char* Qg, const char* Kg, const char* Vg, int wid, AttnOut& R) {
    AttnLane A; A.init(lds, Qg, wid, opaque_tid() & 63);
    bf16x8 q00, q01, q10, q11; const char* qp2 = A.qp + 65536;
    AT_Q_LOAD(); AT_Q_WAIT(0);
    f32x16 o0 = (f32x16){}, o1 = (f32x16){};
    f32x4 l0 = (f32x4){0.f, 0.f, 0.f, 0.f}, l1 = l0;
    float m0 = -__builtin_inff(), m1 = m0;
#pragma unroll 1
    for (int c = 0; c < 8; ++c) {
        AT_LOAD_K(c, 3); AT_LOAD_V(c, 3);
        WAIT_V(0); BAR(); CFENCE();
#pragma unroll 1
        for (int t = 0; t < 4; ++t) {
            const int to = 3 * SLOT + t * 2048;
            const bf16x8 ka = lds_b128(A.kb0 + to), kb = lds_b128(A.kb1 + to);
            const bf16x8 va = cat8(lds_tr(A.vb + to), lds_tr(A.vb + to + 512)), vc = cat8(lds_tr(A.vb + to + 1024), lds_tr(A.vb + to + 1536));
            f32x16 s0 = MFMA32(kb, q01, MFMA32(ka, q00, (f32x16){})), s1 = MFMA32(kb, q11, MFMA32(ka, q10, (f32x16){}));
            const float n0 = __builtin_fmaxf(m0, rowmax16(s0)), n1 = __builtin_fmaxf(m1, rowmax16(s1));
            const float f0 = EX(m0 - n0), f1 = EX(m1 - n1);
            m0 = n0; m1 = n1; o0 *= f0; o1 *= f1; l0 *= f0; l1 *= f1;
#pragma unroll
            for (int r = 0; r < 16; ++r) { s0[r] = EX(s0[r] - n0); s1[r] = EX(s1[r] - n1); }
            const bf16x8 p00 = AT_PACK(s0, 0), p01 = AT_PACK(s0, 8), p10 = AT_PACK(s1, 0), p11 = AT_PACK(s1, 8);
            o0 = MFMA32(vc, p01, MFMA32(va, p00, o0)); l0 = MFMA16(A.ones, p01, MFMA16(A.ones, p00, l0));
            o1 = MFMA32(vc, p11, MFMA32(va, p10, o1)); l1 = MFMA16(A.ones, p11, MFMA16(A.ones, p10, l1));
        }
        WAIT_L0(); BAR(); CFENCE();
    }
    R.o0 = o0; R.o1 = o1; R.l0 = l0; R.l1 = l1; R.m0 = m0; R.m1 = m1;
}

__device__ __forceinline__ void attn_fast(lds_ptr lds, unsigned lds0, const char* Qg, const char* Kg, const char* Vg, const G2& g2, const Flags& flags, int qkv0, int qkvword, int depword,
                                          int hsig, int c0, bool bump, const CodePf& cp, const HPend& hp, const char* wpd, const char* wpo, int prow, int wid, AttnOut& R
                                          ) {
    const int lane = opaque_tid() & 63;
    AttnLane A; A.init(lds, Qg, wid, lane);
    lds_ptr kb0 = A.kb0, kb1 = A.kb1, vb = A.vb;
    PIN(kb0); PIN(kb1); PIN(vb);
    lds_ptr kh0 = kb0 + 2 * SLOT, kh1 = kb1 + 2 * SLOT, vh = vb + 2 * SLOT;
    PIN(kh0); PIN(kh1); PIN(vh);
    const bf16x8 ones = A.ones;
    const __amdgpu_buffer_rsrc_t hr = __builtin_amdgcn_make_buffer_rsrc((void*)hp.base, 0, hp.size, 0x00020000);
    f32x16 o0 = (f32x16){}, o1 = (f32x16){}, negm0 = (f32x16){}, negm1 = (f32x16){}, sA0, sA1, sB0, sB1;
    f32x4 l0 = (f32x4){0.f, 0.f, 0.f, 0.f}, l1 = l0;
    bf16x8 kfa, kfb, va, vc = (bf16x8){}, pf = (bf16x8){};
    PIN(o0); PIN(o1); PIN(l0); PIN(l1); PIN(vc); PIN(pf); PIN(A.voffK); PIN(A.voffV);
    G2Off go; g2_voff(lane, go);
    unsigned gA0 = go.A0, gA1 = go.A1, gB = (wid & 1) ? go.B1 : go.B0;
    unsigned pfo = (unsigned)(((lane >> 3) & 1) * (DM * 2) + (lane & 7) * 128);
    const u64* hptr = flags.p + (F_H + depword) * 16;
    const u64* qptr = flags.p + qkvword * 16;
    PIN(gA0); PIN(gA1); PIN(gB); PIN(pfo); PIN(hptr); PIN(qptr);
    if (wid == 0) {
        wait_all(flags, qkv0, peek(flags, qkv0));
        STAMP(13);
    }
    CFENCE(); BAR(); CFENCE();
    STAMP(4);
    AT_LOAD_K(c0, 0);
    glds16c(Qg, A.voffK, lds0 + wid * 2048 + 2 * SLOT); glds16c(Qg + 512, A.voffK, lds0 + wid * 2048 + 2 * SLOT + 1024);
    AT_LOAD_V(c0, 0);
    u64 hv = 0;
    float m0, m1;
    WAIT_V(2);
    PEEK_AHEAD(hv, qptr, wid);
    BAR(); SBAR();
    STAMP(15);
    const int qo = 2 * SLOT + (wid & 3) * 4096 - (wid >> 2) * 8192;
    const bf16x8 q00 = lds_b128(kb0 + qo), q10 = lds_b128(kb0 + qo + 2048), q01 = lds_b128(kb1 + qo), q11 = lds_b128(kb1 + qo + 2048);
    {
        kfa = lds_b128(kb0); kfb = lds_b128(kb1);
        sA0 = MFMA32(kfa, q00, negm0); sA1 = MFMA32(kfa, q10, negm1); sA0 = MFMA32(kfb, q01, sA0); sA1 = MFMA32(kfb, q11, sA1);
        kfa = lds_b128(kb0 + 2048); kfb = lds_b128(kb1 + 2048);
        m0 = rowmax16(sA0); m1 = rowmax16(sA1);
#pragma unroll
        for (int r = 0; r < 16; ++r) { sA0[r] -= m0; sA1[r] -= m1; negm0[r] = -m0; negm1[r] = -m1; }
        PIN(negm0); PIN(negm1);
    }
    unsigned pfd = 0;
    PEEK_DONE(hv);
    if (wid == 0) {
        wait_all(flags, qkvword, hv);
        if (bump) signal(flags, F_EPOCH, lane);
        STAMP(14);
    }
    CFENCE(); BAR(); SBAR();
    STAMP(5);
    AT_LOAD_K((c0 + 1) & 7, 1); AT_LOAD_V((c0 + 1) & 7, 1);
    STAMP(19);
#define AT_GROUP(QK, RD, S, i, O, L, VF) do { \
        QK; SBAR(); RD; SBAR(); \
        float e0, e1, e2, e3, e4, e5, e6, e7; \
        asm volatile("v_exp_f32 %0, %13\n\tv_exp_f32 %1, %14\n\tv_exp_f32 %2, %15\n\tv_mfma_f32_32x32x16_bf16 %8, %10, %11, %8\n\t" \
                     "v_exp_f32 %3, %16\n\tv_exp_f32 %4, %17\n\tv_exp_f32 %5, %18\n\tv_mfma_f32_16x16x32_bf16 %9, %12, %11, %9\n\tv_exp_f32 %6, %19\n\tv_exp_f32 %7, %20" \
            : "=&v"(e0), "=&v"(e1), "=&v"(e2), "=&v"(e3), "=&v"(e4), "=&v"(e5), "=&v"(e6), "=&v"(e7), "+v"(O), "+v"(L) \
            : "v"(VF), "v"(pf), "v"(ones), "v"(S[i]), "v"(S[i + 1]), "v"(S[i + 2]), "v"(S[i + 3]), "v"(S[i + 4]), "v"(S[i + 5]), "v"(S[i + 6]), "v"(S[i + 7])); \
        pf = __builtin_bit_cast(bf16x8, (u32x4){cvtpk(e0, e1), cvtpk(e2, e3), cvtpk(e4, e5), cvtpk(e6, e7)}); SBAR(); } while (0)
#define LO(lo, hi, off) (((off) >= 2 * SLOT ? hi : lo) + ((off) & (2 * SLOT - 1)))
#define AT_STEP(C0, C1, N0, N1, QK, KR, KO, VO, H0, H1, H2, H3) do { \
        AT_GROUP(if (QK) MFMA32_SET(N0, kfa, q00, negm0), { va = cat8(lds_tr(LO(vb, vh, VO)), lds_tr(LO(vb, vh, VO) + 512)); H0; }, C0, 0, o1, l1, vc); \
        AT_GROUP(if (QK) MFMA32_SET(N1, kfa, q10, negm1), { if (KR) kfa = lds_b128(LO(kb0, kh0, KO)); vc = cat8(lds_tr(LO(vb, vh, VO) + 1024), lds_tr(LO(vb, vh, VO) + 1536)); H1; }, C1, 0, o0, l0, va); \
        AT_GROUP(if (QK) MFMA32_ACC(N0, kfb, q01), H2, C0, 8, o1, l1, va); \
        AT_GROUP(if (QK) MFMA32_ACC(N1, kfb, q11), { if (KR) kfb = lds_b128(LO(kb1, kh1, KO)); H3; }, C1, 8, o0, l0, vc); } while (0)
#define PRV 1
#define YP(p) asm volatile("s_cmp_lt_u32 %0, 4\n\ts_cbranch_scc1 1f\n\ts_setprio " #p "\n1:" :: "s"(wid) : "scc")
    if (PRV) __builtin_amdgcn_s_setprio(1);
    auto ylow = [&]() { if (PRV) YP(0); };
#define S0(c, ...) AT_STEP(sA0, sA1, sB0, sB1, true, true, ((c) & 3) * SLOT + 4096, ((c) & 3) * SLOT, __VA_ARGS__)
#define S1(c, ...) if (PRV == 1) YP(0); AT_STEP(sB0, sB1, sA0, sA1, true, true, ((c) & 3) * SLOT + 6144, ((c) & 3) * SLOT + 2048, __VA_ARGS__)
#define S2(c, ...) if (PRV == 1 || (PRV == 2 && (c) == 7)) YP(2); AT_STEP(sA0, sA1, sB0, sB1, true, (c) < 7, (((c) + 1) & 3) * SLOT, ((c) & 3) * SLOT + 4096, __VA_ARGS__)
#define S3(c, ...) AT_STEP(sB0, sB1, sA0, sA1, (c) < 7, (c) < 7, (((c) + 1) & 3) * SLOT + 2048, ((c) & 3) * SLOT + 6144, __VA_ARGS__)
#define SY(c, n) do { WAIT_V(n); WAIT_L0(); BAR(); SBAR(); STAMP(20 + (c)); } while (0)
#define NO (void)0
#define KP(c, i) glds16c(Kg + ((c0 + (c)) & 7) * 8192 + (i) * 512, A.voffK, lds0 + wid * 2048 + ((c) & 3) * SLOT + (i) * 1024)
#define VP(c, i) glds16c(Vg + ((c0 + (c)) & 7) * 8192 + (i) * 512, A.voffV, lds0 + wid * 2048 + ((c) & 3) * SLOT + 16384 + (i) * 1024)
#define CH(c) KP(c, 0), KP(c, 1), VP(c, 0), VP(c, 1)
#define HS(i) do { __builtin_amdgcn_raw_buffer_store_b128(hp.v[i], hr, hp.off + (i) * 1024, 0, 16); CFENCE(); } while (0)
#define WP(i) wpf(i)
#define GB(j, slot, i) glds16(g2.Wd + (size_t)((j) * 64 + wid * 8) * (DM * 2) + (i) * 128, gB, lds0 + (slot) * SLOT + 16384 + (i) * 8192 + wid * 1024)
#define GA(j, slot, i) glds16c(g2.Ah + (size_t)(j) * (2 * GRP) + g2.rowb + wid * 1024, (i) ? gA1 : gA0, lds0 + (slot) * SLOT + wid * 2048 + (i) * 1024)
    auto wpf = [&](int i) {
        const int pi = (wid + 8 * i < g2.nh + 8) ? wid + 8 * i : 0, lr = prow + 2 * pi;
        asm volatile("global_load_dword %0, %1, %2" : "+v"(pfd) : "v"(pfo), "s"((lr < g2.nh * 64 ? wpd : wpo) + (size_t)lr * (DM * 2)) : "memory");
    };
    auto cpf = [&]() { CODE_PF(pfd); };
    auto hpeek = [&]() { PEEK_AHEAD(hv, hptr, wid); };
    S0(0, NO, NO, NO, NO);         S1(0, NO, NO, NO, NO);    SY(0, 0);  S2(0, CH(2)); S3(0, CH(3));
    S0(1, WP(0), NO, HS(0), NO);   S1(1, NO, NO, HS(1), NO); SY(1, 7);  S2(1, CH(4)); S3(1, WP(1), NO, HS(2), NO);
    S0(2, NO, NO, HS(3), NO);      S1(2, NO, NO, HS(4), NO); SY(2, 11); S2(2, CH(5)); S3(2, WP(2), NO, HS(5), NO);
    S0(3, NO, NO, HS(6), NO);      S1(3, NO, NO, HS(7), NO); SY(3, 12); S2(3, CH(6)); S3(3, WP(3), NO, NO, NO);
    S0(4, NO, NO, NO, NO);         S1(4, NO, NO, NO, NO);    SY(4, 5);
    if (wid == 0) signal(flags, hsig, lane);
                                                                        S2(4, CH(7)); S3(4, WP(4), NO, NO, NO);
    S0(5, GB(0, 4, 0), GB(0, 4, 1), NO, NO); S1(5, NO, NO, NO, NO); SY(5, 2); S2(5, GB(1, 0, 0), GB(1, 0, 1), NO, NO); S3(5, NO, NO, NO, NO);
    S0(6, hpeek(), NO, NO, NO);
    if (wid == 0) { PEEK_DONE(hv); wait_all(flags, F_H + depword, hv); }
                                   S1(6, NO, NO, NO, NO);    SY(6, 0);  S2(6, GA(0, 4, 0), GA(0, 4, 1), GB(2, 1, 0), GB(2, 1, 1)); S3(6, GA(1, 0, 0), GA(1, 0, 1), cpf(), NO);
    S0(7, GA(2, 1, 0), GA(2, 1, 1), NO, NO); S1(7, NO, NO, NO, NO); SY(7, 0); S2(7, NO, NO, NO, NO); S3(7, NO, NO, ylow(), NO);
    __builtin_amdgcn_s_setprio(0);
#undef S0
#undef S1
#undef S2
#undef S3
#undef NO
    STAMP(28);
    asm volatile("s_nop 1" ::: "memory");
    MFMA32_ACC(o1, vc, pf); MFMA16_ACC(l1, ones, pf);
    MFMA_DRAIN(); SBAR();
    asm volatile("" :: "v"(pfd));
    R.o0 = o0; R.o1 = o1; R.l0 = l0; R.l1 = l1; R.m0 = m0; R.m1 = m1;
}

__global__ __launch_bounds__(512, 2) void k_main(const bf16_t* __restrict__ x, const bf16_t* __restrict__ wqkv, const bf16_t* __restrict__ wo, const bf16_t* __restrict__ wg,
                                                 const bf16_t* __restrict__ wu, const bf16_t* __restrict__ wd, bf16_t* __restrict__ out, unsigned char* ws) {
    __shared__ __attribute__((aligned(16))) unsigned char smem[LDS_BYTES];
    const lds_ptr lds = (lds_ptr)smem;
    const unsigned lds0 = (unsigned)(uintptr_t)lds;
    const int wid = __builtin_amdgcn_readfirstlane(threadIdx.x >> 6);
    const int bid = blockIdx.x, xcd = bid & 7, idx = bid >> 3;
    Flags flags; flags.p = (u64*)(ws + WS_FLAGS);
    const __amdgpu_buffer_rsrc_t wsr = __builtin_amdgcn_make_buffer_rsrc((void*)ws, 0, WS_END, 0x00020000);
#define ST16(v, off) __builtin_amdgcn_raw_buffer_store_b128(v, wsr, off, 0, 16)
    STAMP(0);
    unsigned cpd = 0;
    CodePf cp;
    asm volatile("s_getpc_b64 %0\n.Lk_begin:\n\ts_mov_b32 %1, .Lk_end-.Lk_begin" : "=s"(cp.base), "=s"(cp.len));
    cp.first = idx * 16;
    HPend hp;
    {
        const int lane = threadIdx.x & 63, wr = wid >> 2, wc = wid & 3, js = idx & 3, rb = idx >> 2;
        const bool typeY = (js == 3);
        const int f0 = 448 * xcd + 128 * js;
        const char* P0 = (const char*)(wg + f0);
        const char* P2 = (const char*)(wu + f0);
        const char* P1 = typeY ? (const char*)(wqkv + 128 * xcd + 64) : (const char*)(wg + f0 + 64);
        const char* P3 = typeY ? (const char*)(wqkv + 1024 + 128 * xcd + 64) : (const char*)(wu + f0 + 64);
        const char* P4 = typeY ? (const char*)(wqkv + 2048 + 128 * xcd + 64) : (const char*)(wqkv + js * 1024 + 128 * xcd);
        const unsigned stepF = 32u * FF * 2u, stepQ = 32u * NQKV * 2u, step13 = typeY ? stepQ : stepF;
        const char* XA = (const char*)x + (size_t)rb * 256 * DM * 2;
        const char* XC = (const char*)ws + WS_XC + (size_t)rb * 256 * DM * 2;
        const int r8 = lane >> 3, u8 = lane & 7;
        const int gw = (r8 & 2) | ((wc & 1) << 2);
        const unsigned voffF = (unsigned)(((wc * 8 + r8) * FF + (u8 ^ gw) * 8) * 2);
        const unsigned voffQ = (unsigned)(((wc * 8 + r8) * NQKV + (u8 ^ gw) * 8) * 2);
        const unsigned voff13 = typeY ? voffQ : voffF;
        const unsigned voffA0 = (unsigned)(((32 * wid + r8) * DM + (u8 ^ (r8 >> 1)) * 8) * 2);
        const unsigned voffA1 = (unsigned)(((32 * wid + 8 + r8) * DM + (u8 ^ (4 | (r8 >> 1))) * 8) * 2);
        const unsigned dstA = lds0 + G1_A + wid * 4096, dstW = lds0 + G1_W + wc * 1024;
#define G1_LOAD_A(t) do { const char* b_ = XA + (t) * 128; const unsigned d_ = dstA + ((t) & 1) * 32768; \
        glds16(b_, voffA0, d_); glds16(b_, voffA1, d_ + 1024); glds16(b_ + 32768, voffA0, d_ + 2048); glds16(b_ + 32768, voffA1, d_ + 3072); } while (0)
#define G1_LOAD_W(hs) do { const unsigned d_ = dstW + ((hs) & 3) * 4096; \
        glds16(P0 + (size_t)(hs) * stepF, voffF, d_); glds16(P1 + (size_t)(hs) * step13, voff13, d_ + 16384); glds16(P2 + (size_t)(hs) * stepF, voffF, d_ + 32768); \
        glds16(P3 + (size_t)(hs) * step13, voff13, d_ + 49152); glds16(P4 + (size_t)(hs) * stepQ, voffQ, d_ + 65536); } while (0)
        STAMP(29);
        if (wr == 1) G1_LOAD_W(0);
        G1_LOAD_A(0);
        u32x4 xa0, xa1;
        const int srow = rb * 256 + (js * 8 + xcd) * 8 + wid;
        asm volatile("global_load_dwordx4 %0, %2, %3\n\tglobal_load_dwordx4 %1, %2, %3 offset:1024"
                     : "=&v"(xa0), "=&v"(xa1) : "v"((unsigned)(lane * 16)), "s"((const char*)x + (size_t)srow * DM * 2) : "memory");
        u64 sg;
        {
            const int l7 = lane & 7;
            const unsigned eo = l7 == 0 ? 0u : l7 < 6 ? (unsigned)(1 + (l7 - 1) * 256 + bid) * 128u : WS_STAT + (unsigned)srow * 8u;
            asm volatile("global_load_dwordx2 %0, %1, %2 sc1" : "=&v"(sg) : "v"(eo), "s"(flags.p) : "memory");
        }
        CODE_PF(cpd);
        STAMP(30);

        int fr = lane & 15, fq = lane >> 4;
        const lds_ptr aB0 = lds + G1_A + (wr * 128 + fr) * 128 + ((fq ^ (fr >> 1)) * 16);
        const lds_ptr aB1 = lds + G1_A + (wr * 128 + fr) * 128 + (((4 | fq) ^ (fr >> 1)) * 16);
        const int tq = (lane >> 2) & 3, tp = lane & 3, tg = (tq & 2) | ((fq & 1) << 2);
        auto wbase = [&](int panel, int bp) { return lds + G1_W + panel * 16384 + (8 * fq + tq) * 128 + (((2 * bp + (tp >> 1)) ^ tg) * 16) + (tp & 1) * 8; };
        const int pX = wc >> 1, bX = 2 * (wc & 1);
        const lds_ptr wB0 = typeY ? wbase(0, wc) : wbase(pX, bX);
        const lds_ptr wB1 = typeY ? wbase(1, wc) : wbase(pX, bX + 1);
        const lds_ptr wB2 = typeY ? wbase(2, wc) : wbase(2 + pX, bX);
        const lds_ptr wB3 = typeY ? wbase(3, wc) : wbase(2 + pX, bX + 1);
        const lds_ptr wB4 = wbase(4, wc);
        if (threadIdx.x == 0) *(LAS unsigned*)(lds + G1_CNT) = 0u;
        f32x4 acc[8][5];
#pragma unroll
        for (int m = 0; m < 8; ++m)
#pragma unroll
            for (int b = 0; b < 5; ++b) { acc[m][b] = (f32x4){0.f, 0.f, 0.f, 0.f}; PIN(acc[m][b]); }
        if (wr == 1) { WAIT_V(8); BAR(); }
        asm volatile("s_waitcnt vmcnt(2)" : "+v"(xa0), "+v"(xa1) :: "memory");
        STAMP(31);
        {
            const f32x2 st = row_stats(xa0, xa1);
            G1_LOAD_W(1 + wr);
            ST16(xa0, (lane >= 32) ? WS_XC + (unsigned)srow * 2048u + lane * 16 : WS_END);
            ST16(xa1, WS_XC + (unsigned)srow * 2048u + 1024 + lane * 16);
            __builtin_amdgcn_raw_buffer_store_b64(__builtin_bit_cast(u32x2, st), wsr, (lane == 0) ? WS_STAT + (unsigned)srow * 8u : WS_END, 0, 16);
        }
        STAMP(32);
        bf16x8 af[8], wf[5];
        float cs0 = 0.f, cs1 = 0.f, cs2 = 0.f;
#define CS1(c, w) "v_dot2c_f32_bf16 %" #c ", %25, %" #w "\n\t"
#define G1_CSUM(t, lim) do { const u32x4 w0_ = __builtin_bit_cast(u32x4, wf[0]), w1_ = __builtin_bit_cast(u32x4, wf[1]), w2_ = __builtin_bit_cast(u32x4, wf[2]), \
            w3_ = __builtin_bit_cast(u32x4, wf[3]), w4_ = __builtin_bit_cast(u32x4, wf[4]); \
        asm volatile("s_cmp_gt_u32 %24, %26\n\ts_cbranch_scc1 2f\n\ts_cmp_lg_u32 %23, 0\n\ts_cbranch_scc1 1f\n\t" \
            CS1(0, 3) CS1(1, 7) CS1(2, 19) CS1(0, 4) CS1(1, 8) CS1(2, 20) CS1(0, 5) CS1(1, 9) CS1(0, 6) CS1(1, 10) "s_nop 1\n\ts_branch 2f\n1:\n\t" \
            CS1(0, 11) CS1(1, 15) CS1(2, 21) CS1(0, 12) CS1(1, 16) CS1(2, 22) CS1(0, 13) CS1(1, 17) CS1(0, 14) CS1(1, 18) "s_nop 2\n2:" \
            : "+v"(cs0), "+v"(cs1), "+v"(cs2) \
            : "v"(w0_[0]), "v"(w0_[1]), "v"(w0_[2]), "v"(w0_[3]), "v"(w1_[0]), "v"(w1_[1]), "v"(w1_[2]), "v"(w1_[3]), "v"(w2_[0]), "v"(w2_[1]), "v"(w2_[2]), "v"(w2_[3]), \
              "v"(w3_[0]), "v"(w3_[1]), "v"(w3_[2]), "v"(w3_[3]), "v"(w4_[0]), "v"(w4_[1]), "v"(w4_[2]), "v"(w4_[3]), "s"(wr), "s"(t), "s"(0x3f803f80u), "i"(lim) : "scc"); } while (0)
#define G1_RW(i, B, slot) wf[i] = cat8(lds_tr(B + (slot) * 4096), lds_tr(B + (slot) * 4096 + 512))
#define G1_RA(m, AB, abuf) af[m] = lds_b128(AB + (abuf) * 32768 + (m) * 2048)
#define G1_SEG(AB, abuf, slot, L0, L1, L2, L3, L4, CLOSE) do { \
        L0; G1_RW(0, wB0, slot); G1_RW(1, wB1, slot); L1; G1_RW(2, wB2, slot); G1_RW(3, wB3, slot); L2; G1_RW(4, wB4, slot); G1_RA(0, AB, abuf); G1_RA(1, AB, abuf); \
        L3; G1_RA(2, AB, abuf); G1_RA(3, AB, abuf); G1_RA(4, AB, abuf); L4; G1_RA(5, AB, abuf); G1_RA(6, AB, abuf); G1_RA(7, AB, abuf); \
        SBAR(); G1_CSUM(t, 3); WAIT_L0(); SBAR(); if (CLOSE) { BAR(); SBAR(); } } while (0)
#define G1_SEG_A(abuf, slot, tt) do { const char* b_ = ((tt) < 4 ? XA : XC) + (tt) * 128; const unsigned d_ = dstA + (1 - (abuf)) * 32768; \
        G1_SEG(aB0, abuf, slot, glds16c(b_, voffA0, d_), glds16c(b_, voffA1, d_ + 1024), glds16c(b_ + 32768, voffA0, d_ + 2048), glds16c(b_ + 32768, voffA1, d_ + 3072), (void)0, true); } while (0)
#define G1_SEG_W(abuf, slot, hs, dslot, CLOSE) do { const unsigned d_ = dstW + (dslot) * 4096; \
        G1_SEG(aB1, abuf, slot, glds16(P0 + (size_t)(hs) * stepF, voffF, d_), glds16(P1 + (size_t)(hs) * step13, voff13, d_ + 16384), glds16(P2 + (size_t)(hs) * stepF, voffF, d_ + 32768), \
               glds16(P3 + (size_t)(hs) * step13, voff13, d_ + 49152), glds16(P4 + (size_t)(hs) * stepQ, voffQ, d_ + 65536), CLOSE); } while (0)
#define G1_MMA() do { __builtin_amdgcn_s_setprio(1); \
        _Pragma("unroll") for (int b = 0; b < 5; ++b) _Pragma("unroll") for (int i = 0; i < 8; ++i) { const int m = (b & 1) ? 7 - i : i; MFMA16_ACC(acc[m][b], wf[b], af[m]); } \
        __builtin_amdgcn_s_setprio(0); SBAR(); } while (0)

        STAMP(33);
        WAIT_L0();
        STAMP(34);
        BAR();
        SBAR();
        STAMP(1);
        flags.tag = 0;
        const u64* sgp = flags.p + (F_XC + rb * 32 + (lane & 31)) * 16;
#pragma unroll 1
        for (int t = 0; t < 16; t += 2) {
            const int h1 = (2 * t + 3 + wr < 31) ? 2 * t + 3 + wr : 31, h2 = (2 * t + 5 + wr < 31) ? 2 * t + 5 + wr : 31, t2 = (t < 14) ? t + 2 : 15;
            const int sel = (t == 12) ? 0 : ((t ^ 2) | wid);
            STAMP(35 + (t >> 1));
            PEEK_AHEAD(sg, sgp, sel);
            if (t == 14) CODE_PF(cpd);
            G1_SEG_A(0, 0, t + 1); G1_MMA(); asm volatile("s_waitcnt vmcnt(4)" : "+v"(sg), "+v"(cpd) :: "memory"); BAR(); SBAR();
            if (t == 0) {
                const unsigned elo = __builtin_amdgcn_readfirstlane((unsigned)sg), ehi = __builtin_amdgcn_readfirstlane((unsigned)(sg >> 32));
                flags.tag = (((u64)ehi << 32) | elo) * 0x9E3779B97F4A7C15ull + 0x632BE59BD9B4E019ull;
                if (wid == 4) signal(flags, F_XC + bid, lane);
            }
            if (sel == 0) {
                if (t == 12) *(LAS u64*)(lds + G1_STAT + (wid * 32 + (opaque_tid() & 31)) * 8) = sg;
                else wait_all(flags, F_XC + rb * 32 + (opaque_tid() & 31), sg);
            }
            G1_SEG_W(0, 1, h1, (3 + wr) & 3, true); G1_MMA(); WAIT_V(5); BAR(); SBAR();
            G1_SEG_A(1, 2, t2); G1_MMA(); WAIT_V(4); BAR(); SBAR();
            G1_SEG_W(1, 3, h2, (1 + wr) & 3, false);
            if (t == 2) {
                const int tl = opaque_tid() & 63;
                const lds_ptr cp = lds + G1_CS + wc * 384 + (tl & 15) * 4;
                *(LAS float*)(cp + wr * 128) = red4(cs0); *(LAS float*)(cp + wr * 128 + 64) = red4(cs1); *(LAS float*)(cp + 256 + wr * 64) = red4(cs2);
                sgp = (const u64*)(ws + WS_STAT) + rb * 256 + wid * 32 + (tl & 31);
                WAIT_L0();
            }
            SBAR(); BAR(); SBAR(); G1_MMA(); WAIT_V(5);
            if (wr == 0 || t < 14) BAR();
            SBAR();
        }
        MFMA_DRAIN(); SBAR();
        STAMP(2);

        const int tz = opaque_tid(), ln = tz & 63;
        fr = tz & 15; fq = (tz >> 4) & 3;
        const int row0 = rb * 256 + wr * 128 + fr;
        f32x4 csv[5]; f32x2 st[8];
#pragma unroll
        for (int b = 0; b < 5; ++b) csv[b] = *(const LAS f32x4*)(lds + G1_CS + wc * 384 + b * 64 + fq * 16);
        csv[4] += *(const LAS f32x4*)(lds + G1_CS + wc * 384 + 320 + fq * 16);
#pragma unroll
        for (int m = 0; m < 8; ++m) st[m] = *(const LAS f32x2*)(lds + G1_STAT + (wr * 128 + m * 16 + fr) * 8);
        auto lnv = [&](int m, int b, float sc) { return (acc[m][b] - st[m].x * csv[b]) * (st[m].y * sc); };
        auto glu = [&](int m, int bg, int bu) {
            const f32x4 g = lnv(m, bg, -LOG2E), u = lnv(m, bu, 1.0f);
            f32x4 h;
#pragma unroll
            for (int j = 0; j < 4; ++j) h[j] = u[j] * __builtin_amdgcn_rcpf(1.0f + __builtin_amdgcn_exp2f(g[j]));
            return h;
        };
        auto pack2 = [&](const f32x4& v0, const f32x4& v1) {
            unsigned p0 = cvtpk(v0[0], v0[1]), p1 = cvtpk(v0[2], v0[3]), q0 = cvtpk(v1[0], v1[1]), q1 = cvtpk(v1[2], v1[3]);
            swap16(p0, q0); swap16(p1, q1);
            return (u32x4){p0, p1, q0, q1};
        };
        const int hd = 4 * xcd + (typeY ? 2 : 0) + (wc >> 1);
        const unsigned qoff = WS_QKV + (unsigned)(hd * 2 + (wc & 1)) * 65536u + (unsigned)(row0 + (fq & 1) * 16) * 32u + (fq >> 1) * 16;
        constexpr unsigned KIND = NH * 2 * 65536u;
        if (!typeY) {
            const float sc4 = (js == 0) ? QSCALE : 1.0f;
#pragma unroll
            for (int m = 0; m < 8; m += 2) ST16(pack2(lnv(m, 4, sc4), lnv(m + 1, 4, sc4)), qoff + js * KIND + m * 512);
            CFENCE();
            hp.base = ws + WS_ACT + (unsigned)(14 * xcd + 4 * js + wc) * GRP + (unsigned)(rb * 256 + wr * 128) * 64u;
            hp.off = fr * 64 + (fq & 1) * 32 + (fq >> 1) * 16; hp.size = 8192;
#pragma unroll
            for (int m = 0; m < 8; ++m) {
                hp.v[m] = pack2(glu(m, 0, 2), glu(m, 1, 3));
                if (m == 5) { PIN(hp.v[5]); WAIT_V(0); arrive(flags, F_QKV + bid, ln, lds + G1_CNT, 7u); }
            }
        } else {
#pragma unroll
            for (int m = 0; m < 8; m += 2) {
                ST16(pack2(lnv(m, 3, 1.0f), lnv(m + 1, 3, 1.0f)), qoff + KIND + m * 512);
                ST16(pack2(lnv(m, 4, 1.0f), lnv(m + 1, 4, 1.0f)), qoff + 2 * KIND + m * 512);
                ST16(pack2(lnv(m, 1, QSCALE), lnv(m + 1, 1, QSCALE)), qoff + m * 512);
            }
            CFENCE();
            hp.base = ws + WS_ACT + (unsigned)(14 * xcd + 12 + (wc >> 1)) * GRP + (wc & 1) * (GRP / 2) + (unsigned)(rb * 256 + wr * 128) * 32u;
            hp.off = (fr + (fq & 1) * 16) * 32 + (fq >> 1) * 16; hp.size = 4096;
#pragma unroll
            for (int m = 0; m < 8; m += 2) { hp.v[m >> 1] = pack2(glu(m, 0, 2), glu(m + 1, 0, 2)); hp.v[4 + (m >> 1)] = hp.v[m >> 1]; }
            PIN(hp.v[3]); WAIT_V(0); arrive(flags, F_QKV + bid, ln, lds + G1_CNT, 7u);
        }
#pragma unroll
        for (int i = 0; i < 8; ++i) PIN(hp.v[i]);
        STAMP(3);
    }

    const int z = xcd & 1, rbk = ((xcd >> 1) & 1) * 8 + (idx & 7), cbk = (xcd >> 2) * 4 + (idx >> 3);
    G2 g2;
    g2.nh = z ? 56 - NH0 : NH0;
    g2.rowb = rbk * 128 * 64;
    g2.split = z ? split_mask(NH0, 56 - NH0) : split_mask(0, NH0);
    g2.Ah = (const char*)ws + WS_ACT + (size_t)(z * 2 * NH0) * GRP;
    g2.Ao = (const char*)ws + WS_ACT + (size_t)(112 + z * 16 - 2 * g2.nh) * GRP;
    g2.Wd = (const char*)(wd + (size_t)z * NH0 * 64 * DM + cbk * 128);
    g2.Wo = (const char*)(wo + (size_t)z * 512 * DM + cbk * 128) - (size_t)g2.nh * 64 * DM * 2;
    {
        const int hl = idx & 3, qb = idx >> 2, h = 4 * xcd + hl;
        const int l0_ = opaque_tid() & 63;
        const int qkv0 = F_QKV + xcd + 8 * (qb * 4 + ((hl >= 2) ? 3 : l0_ % 3));
        const int qkvword = F_QKV + xcd + 8 * ((l0_ & 7) * 4 + ((hl >= 2) ? 3 : 1 + ((l0_ >> 3) & 1)));
        const int odep = (rbk >> 1) * 32 + ((l0_ >> 2) & 3) * 8 + 4 * z + (l0_ & 3);
        const int depword = (l0_ & 16) ? (odep & ~7) + 4 : odep;
        const char* Qg = (const char*)ws + WS_QKV + (size_t)h * 131072 + qb * 256 * 32;
        const char* Kg = (const char*)ws + WS_QKV + (size_t)(NH + h) * 131072;
        const char* Vg = (const char*)ws + WS_QKV + (size_t)(2 * NH + h) * 131072;
        const char* wpd = g2.Wd - (cbk & 3) * 256;
        const char* wpo = g2.Wo - (cbk & 3) * 256;
        AttnOut R;
#define DG
        attn_fast(lds, lds0, Qg, Kg, Vg, g2, flags, qkv0, qkvword, depword, F_H + bid, qb, bid == 0, cp, hp, wpd, wpo, idx * 2 * (g2.nh + 8), wid, R DG);
        STAMP(6);
        const int tz = opaque_tid(), ln = tz & 63, w4 = wid & 3, fr = ln & 15, fq = ln >> 4;
        lds_ptr po0 = lds + 2 * SLOT + w4 * 4096 + ln * 16, po1 = lds + 2 * SLOT + 16384 + w4 * 2048 + ln * 16;
        PIN(po0); PIN(po1);
        auto po = [&](int i) { return i < 4 ? po0 + i * 1024 : po1 + (i >> 1 & 1) * 8192 + (i & 1) * 1024; };
        const lds_ptr pl = po0 + SLOT;
        volatile LAS unsigned* const badw = (volatile LAS unsigned*)(lds + 3 * SLOT + 1024);
        const int wm = w4 >> 1, wn = w4 & 1;
        const unsigned aO0 = (wm * 64 + fr) * 128 + ((fq ^ (fr >> 1)) * 16), aO1 = aO0 ^ 64;
        const int tq = (ln >> 2) & 3, tp = ln & 3, tg = (tq & 2) | ((fq & 1) << 2);
        unsigned wO[4];
#pragma unroll
        for (int b = 0; b < 4; ++b) wO[b] = 16384 + wn * 8192 + (8 * fq + tq) * 128 + (((2 * b + (tp >> 1)) ^ tg) * 16) + (tp & 1) * 8;
        f32x4 acc[4][4];
        bf16x8 xa[4], ya[4]; s16x4 xl[4], xh[4], yl[4], yh[4];
        lds_ptr pa0, pa1, pw0, pw1, pw2, pw3;
        int so = 4 * SLOT;
        auto handover = [&]() {
            if (wid < 4) {
#pragma unroll
                for (int i = 0; i < 4; ++i) {
                    *(LAS f32x4*)po(i) = (f32x4){R.o0[4 * i], R.o0[4 * i + 1], R.o0[4 * i + 2], R.o0[4 * i + 3]};
                    *(LAS f32x4*)po(4 + i) = (f32x4){R.o1[4 * i], R.o1[4 * i + 1], R.o1[4 * i + 2], R.o1[4 * i + 3]};
                }
                *(LAS f32x4*)pl = (f32x4){R.l0[0], R.m0, R.l1[0], R.m1};
                xl[0] = lds_tr(pw0); xh[0] = lds_tr(pw0 + 512);
#pragma unroll
                for (int m = 0; m < 4; ++m) xa[m] = lds_b128(pa0 + m * 2048);
                xl[1] = lds_tr(pw1); xh[1] = lds_tr(pw1 + 512); xl[2] = lds_tr(pw2); xh[2] = lds_tr(pw2 + 512); xl[3] = lds_tr(pw3); xh[3] = lds_tr(pw3 + 512);
#pragma unroll
                for (int m = 0; m < 4; ++m)
#pragma unroll
                    for (int b = 0; b < 4; ++b) { acc[m][b] = (f32x4){0.f, 0.f, 0.f, 0.f}; PIN(acc[m][b]); }
            }
        };
        G2Off go; g2_voff(ln, go);
        PIN(go.A0); PIN(go.A1); PIN(go.N0); PIN(go.N1); PIN(go.B0); PIN(go.B1);
        pa0 = lds + aO0 + so; pa1 = lds + aO1 + so; pw0 = lds + wO[0] + so; pw1 = lds + wO[1] + so; pw2 = lds + wO[2] + so; pw3 = lds + wO[3] + so;
        PIN(pa0); PIN(pa1); PIN(pw0); PIN(pw1); PIN(pw2); PIN(pw3);
        {
            handover();
            const f32x16 z16 = R.o0 * 0.0f + R.o1 * 0.0f;
            const f32x4 z4 = (z16.s0123 + z16.s4567) + (z16.s89ab + z16.scdef);
            const f32x2 z2 = z4.lo + z4.hi;
            const float zs = z2.x + z2.y;
            const bool ok = (zs == 0.f) && (R.l0[0] > 0.f) && (R.l0[0] < 0x1p96f) && (R.l1[0] > 0.f) && (R.l1[0] < 0x1p96f);
            const bool wave_bad = !__all(ok);
            if (ln == 0) badw[wid] = wave_bad ? 1u : 0u;
            STAMP(16);
            WAIT_L0(); BAR(); CFENCE();
            const unsigned anybad = badw[ln & 7];
            if (__builtin_expect(__any(anybad != 0u), 0)) {
                WAIT_L0(); BAR(); CFENCE();
                attn_safe(lds, lds0, Qg, Kg, Vg, wid, R);
                handover();
                WAIT_L0(); BAR(); CFENCE();
            }
        }
        STAMP(7);

        const int tile = rbk * 8 + cbk, nt = g2.nh + 8;
        if (__builtin_expect(wid >= 4, 0)) {
#define G2_LOAD0(j, sl) do { const unsigned d_ = lds0 + (sl) * SLOT; g2_load_b(g2, j, d_, 2 * w4, go.B0); g2_load_a(g2, j, d_, 2 * w4, go); } while (0)
#define G2_LOAD1(j, sl) do { const unsigned d_ = lds0 + (sl) * SLOT; g2_load_b(g2, j, d_, 2 * w4 + 1, go.B1); g2_load_a(g2, j, d_, 2 * w4 + 1, go); } while (0)
#define G2_LOAD(j, sl) do { G2_LOAD0(j, sl); G2_LOAD1(j, sl); } while (0)
            const f32x4 lm = *(const LAS f32x4*)pl;
            f32x4 t[8];
#pragma unroll
            for (int i = 0; i < 8; ++i) t[i] = *(const LAS f32x4*)po(i);
            WAIT_L0();
            G2_LOAD(3, 2);
            BAR();
            G2_LOAD(4, 3);
            float ck[2], co[2]; unsigned p[16];
            auto coef = [&](int q) {
                const float lk = q ? R.l1[0] : R.l0[0], mk = q ? R.m1 : R.m0, lo = lm[2 * q], mo = lm[2 * q + 1];
                const float mm = __builtin_fmaxf(mk, mo), fk = __builtin_amdgcn_exp2f(mk - mm), fo = __builtin_amdgcn_exp2f(mo - mm);
                const float rl = 1.0f / (lk * fk + lo * fo);
                ck[q] = fk * rl; co[q] = fo * rl; PIN(ck[q]); PIN(co[q]); SBAR();
            };
            auto half = [&](int q, int hh) {
                const f32x16 ok_ = q ? R.o1 : R.o0;
                unsigned* pp = p + 8 * q + 4 * hh;
#pragma unroll
                for (int i = 0; i < 2; ++i) {
                    const int g = 2 * hh + i; const f32x4 u = t[4 * q + g];
                    pp[2 * i] = cvtpk(ok_[4 * g] * ck[q] + u[0] * co[q], ok_[4 * g + 1] * ck[q] + u[1] * co[q]);
                    pp[2 * i + 1] = cvtpk(ok_[4 * g + 2] * ck[q] + u[2] * co[q], ok_[4 * g + 3] * ck[q] + u[3] * co[q]);
                }
                swap32(pp[0], pp[2]); swap32(pp[1], pp[3]);
                PIN(pp[0]); PIN(pp[1]); PIN(pp[2]); PIN(pp[3]); SBAR();
            };
            const unsigned ooff = WS_ACT + (unsigned)(112 + h) * GRP + (unsigned)(qb * 256 + w4 * 64 + fr) * 64u + (fq & 1) * 32 + (fq >> 1) * 16;
            auto ost = [&](int q) {
                unsigned* pp = p + 8 * q;
                swap16(pp[0], pp[4]); swap16(pp[1], pp[5]); swap16(pp[2], pp[6]); swap16(pp[3], pp[7]);
                ST16(((u32x4){pp[0], pp[1], pp[2], pp[3]}), ooff + q * 2048); ST16(((u32x4){pp[4], pp[5], pp[6], pp[7]}), ooff + q * 2048 + 1024); CFENCE();
            };
            SBAR(); coef(0); coef(1);
            WAIT_V(8);  BAR(); G2_LOAD(5, 4); G2_LOAD0(6, 0);
            WAIT_V(12); BAR(); G2_LOAD1(6, 0); G2_LOAD(7, 1);
            WAIT_V(16); BAR(); G2_LOAD0(8, 2);  SBAR(); half(0, 0); G2_LOAD1(8, 2);
            WAIT_V(16); BAR(); G2_LOAD0(9, 3);  SBAR(); half(0, 1); G2_LOAD1(9, 3);  ost(0);
            WAIT_V(18); BAR(); G2_LOAD0(10, 4); SBAR(); half(1, 0); G2_LOAD1(10, 4);
            WAIT_V(18); BAR(); G2_LOAD0(11, 0); SBAR(); half(1, 1); G2_LOAD1(11, 0); ost(1);
            WAIT_V(20); BAR(); G2_LOAD(12, 1);
            WAIT_V(18); BAR(); G2_LOAD(13, 2);
            WAIT_V(18);
            int sl = 3; cpd = 0;
#pragma unroll 1
            for (int j = 11; j < nt - 3; ++j) {
                BAR();
                if (j == 29) CODE_PF(cpd);
                if (j == 12 && wid == 4) signal(flags, F_O + bid, ln);
                G2_LOAD(j + 3, sl);
                sl = (sl == 4) ? 0 : sl + 1;
                WAIT_V(16);
            }
            BAR();
            if (tz == 256) *(LAS unsigned*)(lds + sl * SLOT) = 0u;
            WAIT_V(8); WAIT_L0(); BAR(); WAIT_V(0); BAR();
            asm volatile("" :: "v"(cpd));
            if (wid == 4) wait_all(flags, F_X + tile * 2 + z, peek(flags, F_X + tile * 2 + z));
            CFENCE(); BAR();
            asm volatile(".Lk_end:");
        } else {
#define G2_M(C, m, b) do { MFMA16_ACC(acc[m][b], cat8(C##l[b], C##h[b]), C##a[m]); SBAR(); } while (0)
#define G2_HALF(C, N, AB, WO, RD, TAIL) do { \
            G2_M(C, 0, 0); if (RD) { N##l[0] = lds_tr(pw0 + (WO)); N##h[0] = lds_tr(pw0 + (WO) + 512); } SBAR(); \
            G2_M(C, 1, 0); if (RD) N##a[0] = lds_b128(AB); SBAR(); \
            G2_M(C, 2, 0); if (RD) N##a[1] = lds_b128((AB) + 2048); SBAR(); \
            G2_M(C, 3, 0); if (RD) N##a[2] = lds_b128((AB) + 4096); SBAR(); \
            G2_M(C, 0, 1); if (RD) N##a[3] = lds_b128((AB) + 6144); SBAR(); \
            G2_M(C, 1, 1); if (RD) { N##l[1] = lds_tr(pw1 + (WO)); N##h[1] = lds_tr(pw1 + (WO) + 512); } SBAR(); \
            G2_M(C, 2, 1); if (RD) { N##l[2] = lds_tr(pw2 + (WO)); N##h[2] = lds_tr(pw2 + (WO) + 512); } SBAR(); \
            G2_M(C, 3, 1); if (RD) { N##l[3] = lds_tr(pw3 + (WO)); N##h[3] = lds_tr(pw3 + (WO) + 512); } SBAR(); \
            G2_M(C, 0, 2); TAIL(0); SBAR(); G2_M(C, 1, 2); TAIL(1); SBAR(); G2_M(C, 2, 2); TAIL(2); SBAR(); G2_M(C, 3, 2); TAIL(3); SBAR(); \
            G2_M(C, 0, 3); G2_M(C, 1, 3); G2_M(C, 2, 3); G2_M(C, 3, 3); } while (0)
            const u64* const optr = flags.p + (F_O + odep) * 16;
            u64 ov = 0;
            STAMP(8); STAMP(43);
#define G2_NEXT(i) do { if ((i) == 0) so = (so == 4 * SLOT) ? 0 : so + SLOT; \
                if ((i) == 1) { pa0 = lds + aO0 + so; pa1 = lds + aO1 + so; PIN(pa0); PIN(pa1); } \
                if ((i) == 2) { pw0 = lds + wO[0] + so; pw1 = lds + wO[1] + so; PIN(pw0); PIN(pw1); } \
                if ((i) == 3) { pw2 = lds + wO[2] + so; pw3 = lds + wO[3] + so; PIN(pw2); PIN(pw3); } } while (0)
#define G2_NONE(i) do {} while (0)
            WAIT_L0(); SBAR(); G2_HALF(x, y, pa1, 4096, true, G2_NEXT);
            WAIT_L0(); SBAR(); G2_HALF(y, x, pa0, 0, true, G2_NONE);
            BAR();
            WAIT_L0(); SBAR(); G2_HALF(x, y, pa1, 4096, true, G2_NEXT);
            WAIT_L0(); SBAR(); G2_HALF(y, x, pa0, 0, true, G2_NONE);
#pragma unroll 1
            for (int ph = 0; ph < 3; ++ph) {
                const int n = (ph == 0) ? g2.nh / 2 - 4 : (ph == 1) ? 1 : 5;
#pragma unroll 1
                for (int i = 0; i < n; ++i) {
                    WAIT_L0(); SBAR(); G2_HALF(x, y, pa1, 4096, true, G2_NEXT);
                    WAIT_L0(); BAR(); SBAR(); G2_HALF(y, x, pa0, 0, true, G2_NONE);
                    WAIT_L0(); SBAR(); G2_HALF(x, y, pa1, 4096, true, G2_NEXT);
                    WAIT_L0(); BAR(); SBAR(); G2_HALF(y, x, pa0, 0, true, G2_NONE);
                }
                PEEK_AHEAD(ov, optr, wid | ph);
                if ((wid | (ph ^ 1)) == 0) { PEEK_DONE(ov); wait_all(flags, F_O + odep, ov); }
            }
            const int row = rbk * 128 + wm * 64 + z * 32 + fr, col = cbk * 128 + wn * 64 + (fq & 1) * 16 + (fq >> 1) * 8;
            u32x4 xr[4];
#pragma unroll
            for (int i = 0; i < 4; ++i) xr[i] = *(const u32x4*)(x + (size_t)(row + (i >> 1) * 16) * DM + col + (i & 1) * 32);
            WAIT_L0(); SBAR(); G2_HALF(x, y, pa1, 4096, true, G2_NEXT);
            WAIT_L0(); BAR(); SBAR(); G2_HALF(y, x, pa0, 0, true, G2_NONE);
            WAIT_L0(); SBAR(); G2_HALF(x, y, pa1, 4096, true, G2_NONE);
            WAIT_L0(); SBAR(); G2_HALF(y, x, pa0, 0, false, G2_NONE);
            MFMA_DRAIN(); SBAR();
            STAMP(9);
            f32x4 vk[8];
            const unsigned xo = WS_XCH + (unsigned)(tile * 2 + 1 - z) * 32768u + w4 * 8192 + ln * 16;
            if (z == 0) {
#pragma unroll
                for (int i = 0; i < 8; ++i) { ST16(__builtin_bit_cast(u32x4, acc[2 + (i >> 2)][i & 3]), xo + i * 1024); vk[i] = acc[i >> 2][i & 3]; }
            } else {
#pragma unroll
                for (int i = 0; i < 8; ++i) { ST16(__builtin_bit_cast(u32x4, acc[i >> 2][i & 3]), xo + i * 1024); vk[i] = acc[2 + (i >> 2)][i & 3]; }
            }
            WAIT_V(0);
            arrive(flags, F_X + tile * 2 + 1 - z, ln, lds + ((so == 4 * SLOT) ? 0 : so + SLOT), 3u);
            STAMP(10);
            CFENCE(); BAR(); CFENCE();
            STAMP(11);
            const unsigned xi = WS_XCH + (unsigned)(tile * 2 + z) * 32768u + w4 * 8192 + ln * 16;
#pragma unroll
            for (int i = 0; i < 8; ++i) vk[i] += __builtin_bit_cast(f32x4, __builtin_amdgcn_raw_buffer_load_b128(wsr, xi + i * 1024, 0, 16));
            const __amdgpu_buffer_rsrc_t outr = __builtin_amdgcn_make_buffer_rsrc((void*)out, 0, SEQ * DM * 2, 0x00020000);
            u32x4 w[4];
#pragma unroll
            for (int i = 0; i < 4; ++i) {
                const u32x4 a = __builtin_bit_cast(u32x4, vk[2 * i]), b = __builtin_bit_cast(u32x4, vk[2 * i + 1]);
                unsigned a0 = a[0], a1 = a[1], a2 = a[2], a3 = a[3], b0 = b[0], b1 = b[1], b2 = b[2], b3 = b[3];
                swap16(a0, b0); swap16(a1, b1); swap16(a2, b2); swap16(a3, b3);
                const u32x4 xx = xr[i];
                w[i][0] = cvtpk(u2f(a0) + bf_lo(xx[0]), u2f(a1) + bf_hi(xx[0]));
                w[i][1] = cvtpk(u2f(a2) + bf_lo(xx[1]), u2f(a3) + bf_hi(xx[1]));
                w[i][2] = cvtpk(u2f(b0) + bf_lo(xx[2]), u2f(b1) + bf_hi(xx[2]));
                w[i][3] = cvtpk(u2f(b2) + bf_lo(xx[3]), u2f(b3) + bf_hi(xx[3]));
            }
            const unsigned oo = (unsigned)((row - (fr & 8)) * DM + col + (fr & 8) * 4) * 2u;
#pragma unroll
            for (int m = 0; m < 2; ++m) {
                u32x4 lo, hi;
#pragma unroll
                for (int k = 0; k < 4; ++k) {
                    lo[k] = __builtin_amdgcn_update_dpp(w[2 * m][k], w[2 * m + 1][k], 0x128, 0xf, 0xc, false);
                    hi[k] = __builtin_amdgcn_update_dpp(w[2 * m + 1][k], w[2 * m][k], 0x128, 0xf, 0x3, false);
                }
                __builtin_amdgcn_raw_buffer_store_b128(lo, outr, oo + m * 16 * DM * 2, 0, 16);
                __builtin_amdgcn_raw_buffer_store_b128(hi, outr, oo + (m * 16 + 8) * DM * 2, 0, 16);
            }
        }
    }
}

extern "C" void kernel_launch(void* const* d_in, const int* in_sizes, int n_in, void* d_out, int out_size, void* d_ws, size_t ws_size, hipStream_t stream) {
    const bf16_t* x = (const bf16_t*)d_in[0];
    k_main<<<256, 512, 0, stream>>>(x, (const bf16_t*)d_in[1], (const bf16_t*)d_in[2], (const bf16_t*)d_in[4], (const bf16_t*)d_in[5], (const bf16_t*)d_in[6],
                                    (bf16_t*)d_out, (unsigned char*)d_ws);
}
```

```cpp
#include <hip/hip_runtime.h>
#include <stdint.h>

typedef unsigned short bf16_t;
typedef short bf16x8 __attribute__((ext_vector_type(8)));
typedef short s16x4 __attribute__((ext_vector_type(4)));
typedef float f32x2 __attribute__((ext_vector_type(2)));
typedef float f32x4 __attribute__((ext_vector_type(4)));
typedef float f32x16 __attribute__((ext_vector_type(16)));
typedef unsigned u32x2 __attribute__((ext_vector_type(2)));
typedef unsigned u32x4 __attribute__((ext_vector_type(4)));
typedef __bf16 bf16x2_t __attribute__((ext_vector_type(2)));
#define LAS __attribute__((address_space(3)))
#define GAS __attribute__((address_space(1)))
typedef LAS unsigned char* lds_ptr;

constexpr int SEQ = 2048, DM = 1024, FF = 3584, NQKV = 3072, NH = 32;
constexpr float LN_EPS = 1e-5f;
constexpr float LOG2E = 1.4426950408889634f;
constexpr float QSCALE = 0.17677669529663687f * LOG2E;

constexpr unsigned WS_FLAGS = 0;
constexpr unsigned WS_STAT = 192 * 1024;
constexpr unsigned WS_QKV = 256 * 1024;
constexpr unsigned WS_ACT = WS_QKV + 3u * SEQ * DM * 2;
constexpr unsigned WS_XCH = WS_ACT + 144u * SEQ * 64;
constexpr unsigned WS_XC = WS_XCH + 256u * 32768;
constexpr unsigned WS_END = WS_XC + SEQ * DM * 2;
constexpr int F_EPOCH = 0, F_QKV = 1, F_H = 257, F_O = 513, F_X = 769, F_XC = 1025;
constexpr unsigned GRP = SEQ * 64;

#define SBAR() __builtin_amdgcn_sched_barrier(0)
#define BAR() __builtin_amdgcn_s_barrier()
#define WAIT_V(n) asm volatile("s_waitcnt vmcnt(" #n ")" ::: "memory")
#define WAIT_L0() do { __builtin_amdgcn_s_waitcnt(0xC07F); asm volatile("" ::: "memory"); } while (0)
#define CFENCE() asm volatile("" ::: "memory")
#define PIN(x) asm volatile("" : "+v"(x))
#define MFMA16(a, b, c) __builtin_amdgcn_mfma_f32_16x16x32_bf16(a, b, c, 0, 0, 0)
#define MFMA32(a, b, c) __builtin_amdgcn_mfma_f32_32x32x16_bf16(a, b, c, 0, 0, 0)
#define MFMA16_ACC(acc, a, b) asm volatile("v_mfma_f32_16x16x32_bf16 %0, %1, %2, %0" : "+v"(acc) : "v"(a), "v"(b))
#define MFMA32_ACC(acc, a, b) asm volatile("v_mfma_f32_32x32x16_bf16 %0, %1, %2, %0" : "+v"(acc) : "v"(a), "v"(b))
#define MFMA32_SET(d, a, b, c) asm volatile("v_mfma_f32_32x32x16_bf16 %0, %1, %2, %3" : "=&v"(d) : "v"(a), "v"(b), "v"(c))
#define MFMA_DRAIN() asm volatile("s_nop 15" ::: "memory")
#define RLX_AGENT __ATOMIC_RELAXED, __HIP_MEMORY_SCOPE_AGENT

#define STAMP(i) do {} while (0)
#define TS(v) do {} while (0)
#define PUT(i, v) do {} while (0)

__device__ __forceinline__ void glds16(const void* sbase, unsigned voff, unsigned ldsdst) {
    asm volatile("s_mov_b32 m0, %2\n\ts_nop 0\n\tglobal_load_lds_dwordx4 %0, %1" ::"v"(voff), "s"(sbase), "s"(ldsdst) : "memory");
}
__device__ __forceinline__ void glds16c(const void* sbase, unsigned voff, unsigned ldsdst) {
    asm volatile("s_mov_b32 m0, %2\n\ts_nop 0\n\tglobal_load_lds_dwordx4 %0, %1 sc1" ::"v"(voff), "s"(sbase), "s"(ldsdst) : "memory");
}
__device__ __forceinline__ bf16x8 lds_b128(lds_ptr p) { return *(const LAS bf16x8*)p; }
__device__ __forceinline__ s16x4 lds_tr(lds_ptr p) { return __builtin_amdgcn_ds_read_tr16_b64_v4i16((LAS s16x4*)p); }
__device__ __forceinline__ bf16x8 cat8(s16x4 a, s16x4 b) { return __builtin_shufflevector(a, b, 0, 1, 2, 3, 4, 5, 6, 7); }
__device__ __forceinline__ unsigned cvtpk(float lo, float hi) {
    f32x2 v = {lo, hi};
    return __builtin_bit_cast(unsigned, __builtin_convertvector(v, bf16x2_t));
}
__device__ __forceinline__ float u2f(unsigned u) { return __builtin_bit_cast(float, u); }
__device__ __forceinline__ unsigned f2u(float f) { return __builtin_bit_cast(unsigned, f); }
__device__ __forceinline__ float bf_lo(unsigned u) { return u2f(u << 16); }
__device__ __forceinline__ float bf_hi(unsigned u) { return u2f(u & 0xffff0000u); }
__device__ __forceinline__ void swap16(unsigned& a, unsigned& b) {
    auto r = __builtin_amdgcn_permlane16_swap(a, b, false, false);
    a = r[0]; b = r[1];
}
__device__ __forceinline__ void swap32(unsigned& a, unsigned& b) {
    auto r = __builtin_amdgcn_permlane32_swap(a, b, false, false);
    a = r[0]; b = r[1];
}
__device__ __forceinline__ int opaque_tid() { int t = threadIdx.x; asm volatile("" : "+v"(t)); return t; }
typedef unsigned long long u64;
struct Flags { u64* p; u64 tag; };
__device__ __forceinline__ void signal(const Flags& f, int word, int lane) {
    if (lane == 0) __hip_atomic_store((GAS u64*)(f.p + word * 16), f.tag, RLX_AGENT);
}
__device__ __forceinline__ u64 peek(const Flags& f, int word) {
    u64 v;
    asm volatile("global_load_dwordx2 %0, %1, off sc1\n\ts_waitcnt vmcnt(0)" : "=&v"(v) : "v"(f.p + word * 16) : "memory");
    return v;
}
__device__ __forceinline__ void wait_all(const Flags& f, int word, u64 v) {
#pragma unroll 1
    for (unsigned spin = 0; spin < (1u << 20) && !__all(v == f.tag); ++spin) { __builtin_amdgcn_s_sleep(1); v = peek(f, word); }
}
__device__ __forceinline__ void arrive(const Flags& f, int word, int lane, lds_ptr cnt, unsigned last) {
    if (lane == 0 && __hip_atomic_fetch_add((LAS unsigned*)cnt, 1u, __ATOMIC_RELAXED, __HIP_MEMORY_SCOPE_WORKGROUP) == last)
        __hip_atomic_store((GAS u64*)(f.p + word * 16), f.tag, RLX_AGENT);
}
#define PEEK_AHEAD(v, ptr, sel) asm volatile("s_cmp_lg_u32 %2, 0\n\ts_cbranch_scc1 1f\n\tglobal_load_dwordx2 %0, %1, off sc1\n1:" : "+v"(v) : "v"(ptr), "s"(sel) : "memory", "scc")
struct CodePf { const char* base; unsigned len; int first; };
#define CODE_PF(d) do { unsigned o_ = (unsigned)(cp.first + (opaque_tid() & 15)) * 128u; o_ = (o_ < cp.len) ? o_ : 0u; \
    asm volatile("global_load_dword %0, %1, %2" : "+v"(d) : "v"(o_), "s"(cp.base) : "memory"); } while (0)
#define PEEK_DONE(v) asm volatile("s_waitcnt vmcnt(0)" : "+v"(v) :: "memory")

template <int CTRL> __device__ __forceinline__ float dpp_add(float v) { return v + u2f(__builtin_amdgcn_update_dpp(0u, f2u(v), CTRL, 0xf, 0xf, true)); }
__device__ __forceinline__ float red4(float v) {
    unsigned a = f2u(v), b = a;
    swap16(a, b);
    v = u2f(a) + u2f(b);
    a = f2u(v); b = a;
    swap32(a, b);
    return u2f(a) + u2f(b);
}
__device__ __forceinline__ float wave_sum(float v) {
    v = dpp_add<0xB1>(v); v = dpp_add<0x4E>(v); v = dpp_add<0x141>(v); v = dpp_add<0x140>(v);
    return red4(v);
}
__device__ __forceinline__ f32x2 row_stats(u32x4& a, u32x4& b) {
    float v[16];
#pragma unroll
    for (int i = 0; i < 4; ++i) { v[2 * i] = bf_lo(a[i]); v[2 * i + 1] = bf_hi(a[i]); v[8 + 2 * i] = bf_lo(b[i]); v[8 + 2 * i + 1] = bf_hi(b[i]); }
    float s = 0.f;
#pragma unroll
    for (int i = 0; i < 16; ++i) s += v[i];
    const float mu = wave_sum(s) * (1.0f / DM);
    float q = 0.f;
#pragma unroll
    for (int i = 0; i < 16; ++i) { v[i] -= mu; q += v[i] * v[i]; }
#pragma unroll
    for (int i = 0; i < 4; ++i) { a[i] = cvtpk(v[2 * i], v[2 * i + 1]); b[i] = cvtpk(v[8 + 2 * i], v[8 + 2 * i + 1]); }
    return (f32x2){mu, 1.0f / __builtin_sqrtf(wave_sum(q) * (1.0f / DM) + LN_EPS)};
}

constexpr int SLOT = 32768, LDS_BYTES = 5 * SLOT;
constexpr int G1_A = 0, G1_W = 65536, G1_STAT = G1_W + 5 * 16384, G1_CS = G1_STAT + 2048, G1_CNT = G1_CS + 4 * 6 * 64;

#define NH0 30
static_assert(NH0 >= 28 && NH0 <= 32, "");
struct G2 { const char* Ah; const char* Ao; const char* Wd; const char* Wo; int nh; unsigned rowb; u64 split; };
__device__ __forceinline__ void g2_load_b(const G2& g, int j, unsigned dst, int vw, unsigned voffB) {
    const char* b = (j >= g.nh ? g.Wo : g.Wd) + (size_t)(j * 64 + vw * 8) * (DM * 2);
    glds16(b, voffB, dst + 16384 + vw * 1024); glds16(b + 128, voffB, dst + 16384 + 8192 + vw * 1024);
}
struct G2Off { unsigned A0, A1, N0, N1, B0, B1; };
__device__ __forceinline__ void g2_load_a(const G2& g, int j, unsigned dst, int vw, const G2Off& o) {
    const unsigned sp = (unsigned)(g.split >> j) & 1u;
    const char* a = (j >= g.nh ? g.Ao : g.Ah) + (size_t)j * (2 * GRP) + ((g.rowb + vw * 1024) >> sp);
    glds16c(a, sp ? o.N0 : o.A0, dst + vw * 2048); glds16c(a, sp ? o.N1 : o.A1, dst + vw * 2048 + 1024);
}
__device__ __forceinline__ void g2_voff(int lane, G2Off& o) {
    const int r8 = lane >> 3, u8 = lane & 7, ua = u8 ^ (r8 >> 1), ub = ua ^ 4;
    o.A0 = (unsigned)(ua >> 2) * GRP + r8 * 64 + (ua & 3) * 16;
    o.A1 = (unsigned)(ub >> 2) * GRP + (r8 + 8) * 64 + (ub & 3) * 16;
    o.N0 = (unsigned)(ua >> 1) * (GRP / 2) + r8 * 32 + (ua & 1) * 16;
    o.N1 = (unsigned)(ub >> 1) * (GRP / 2) + (r8 + 8) * 32 + (ub & 1) * 16;
    o.B0 = (unsigned)(r8 * (DM * 2) + (u8 ^ (r8 & 2)) * 16);
    o.B1 = (unsigned)(r8 * (DM * 2) + (u8 ^ (4 | (r8 & 2))) * 16);
}
constexpr u64 split_mask(int first, int n) { u64 m = 0; for (int j = 0; j < n; ++j) if ((first + j) % 7 == 6) m |= 1ull << j; return m; }
static_assert(((split_mask(0, NH0) | split_mask(NH0, 56 - NH0)) & 7) == 0, "phase B's tail prefetches K-tiles 0..2 as plain groups");

struct AttnOut { f32x16 o0, o1; f32x4 l0, l1; float m0, m1; };
struct HPend { u32x4 v[8]; unsigned off, size; const void* base; };
struct AttnLane {
    unsigned voffK, voffV; lds_ptr kb0, kb1, vb; const char* qp; bf16x8 ones;
    __device__ __forceinline__ void init(lds_ptr lds, const char* Qg, int wid, int lane) {
        const int r32 = lane & 31, hi = lane >> 5, kh = wid >> 2, qg = wid & 3, ksw = (r32 >> 2) & 3;
        const int uk = (lane & 3) ^ ((lane >> 4) & 3), uv = lane & 3;
        voffK = (unsigned)((uk >> 1) * 65536 + wid * 1024 + (lane >> 2) * 32 + (uk & 1) * 16);
        voffV = (unsigned)((uv >> 1) * 65536 + wid * 1024 + (lane >> 2) * 32 + (uv & 1) * 16);
        kb0 = lds + kh * 8192 + r32 * 64 + ((hi ^ ksw) * 16); kb1 = lds + kh * 8192 + r32 * 64 + (((2 | hi) ^ ksw) * 16);
        vb = lds + 16384 + kh * 8192 + (4 * hi + ((lane >> 2) & 3)) * 64 + ((lane >> 4) & 1) * 32 + (lane & 3) * 8;
        qp = Qg + (qg * 64 + r32) * 32 + hi * 16;
        const short v = (((lane >> 4) & 1) == ((lane >> 2) & 1)) ? (short)0x3f80 : (short)0;
        ones = (bf16x8){v, v, v, v, v, v, v, v};
    }
};
#define EX(v) __builtin_amdgcn_exp2f(v)
#define AT_LOAD_K(c, slot) do { const unsigned d_ = lds0 + wid * 2048 + (slot) * SLOT; const char* k_ = Kg + (c) * 8192; glds16c(k_, A.voffK, d_); glds16c(k_ + 512, A.voffK, d_ + 1024); } while (0)
#define AT_LOAD_V(c, slot) do { const unsigned d_ = lds0 + wid * 2048 + (slot) * SLOT + 16384; const char* v_ = Vg + (c) * 8192; glds16c(v_, A.voffV, d_); glds16c(v_ + 512, A.voffV, d_ + 1024); } while (0)
#define AT_PACK(S, i) __builtin_bit_cast(bf16x8, (u32x4){cvtpk(S[i], S[i + 1]), cvtpk(S[i + 2], S[i + 3]), cvtpk(S[i + 4], S[i + 5]), cvtpk(S[i + 6], S[i + 7])})
#define AT_Q_LOAD() asm volatile("global_load_dwordx4 %0, %4, off sc1\n\tglobal_load_dwordx4 %1, %4, off offset:1024 sc1\n\tglobal_load_dwordx4 %2, %5, off sc1\n\tglobal_load_dwordx4 %3, %5, off offset:1024 sc1" \
        : "=&v"(q00), "=&v"(q10), "=&v"(q01), "=&v"(q11) : "v"(A.qp), "v"(qp2) : "memory")
#define AT_Q_WAIT(n) asm volatile("s_waitcnt vmcnt(" #n ")" : "+v"(q00), "+v"(q01), "+v"(q10), "+v"(q11) :: "memory")
__device__ __forceinline__ float rowmax16(const f32x16& s) {
    float a = s[0];
#pragma unroll
    for (int r = 1; r < 16; ++r) a = __builtin_fmaxf(a, s[r]);
    unsigned x = f2u(a), y = x; swap32(x, y);
    return __builtin_fmaxf(u2f(x), u2f(y));
}

__device__ __forceinline__ void attn_safe(lds_ptr lds, unsigned lds0, const char* Qg, const char* Kg, const char* Vg, int wid, AttnOut& R) {
    AttnLane A; A.init(lds, Qg, wid, opaque_tid() & 63);
    bf16x8 q00, q01, q10, q11; const char* qp2 = A.qp + 65536;
    AT_Q_LOAD(); AT_Q_WAIT(0);
    f32x16 o0 = (f32x16){}, o1 = (f32x16){};
    f32x4 l0 = (f32x4){0.f, 0.f, 0.f, 0.f}, l1 = l0;
    float m0 = -__builtin_inff(), m1 = m0;
#pragma unroll 1
    for (int c = 0; c < 8; ++c) {
        AT_LOAD_K(c, 3); AT_LOAD_V(c, 3);
        WAIT_V(0); BAR(); CFENCE();
#pragma unroll 1
        for (int t = 0; t < 4; ++t) {
            const int to = 3 * SLOT + t * 2048;
            const bf16x8 ka = lds_b128(A.kb0 + to), kb = lds_b128(A.kb1 + to);
            const bf16x8 va = cat8(lds_tr(A.vb + to), lds_tr(A.vb + to + 512)), vc = cat8(lds_tr(A.vb + to + 1024), lds_tr(A.vb + to + 1536));
            f32x16 s0 = MFMA32(kb, q01, MFMA32(ka, q00, (f32x16){})), s1 = MFMA32(kb, q11, MFMA32(ka, q10, (f32x16){}));
            const float n0 = __builtin_fmaxf(m0, rowmax16(s0)), n1 = __builtin_fmaxf(m1, rowmax16(s1));
            const float f0 = EX(m0 - n0), f1 = EX(m1 - n1);
            m0 = n0; m1 = n1; o0 *= f0; o1 *= f1; l0 *= f0; l1 *= f1;
#pragma unroll
            for (int r = 0; r < 16; ++r) { s0[r] = EX(s0[r] - n0); s1[r] = EX(s1[r] - n1); }
            const bf16x8 p00 = AT_PACK(s0, 0), p01 = AT_PACK(s0, 8), p10 = AT_PACK(s1, 0), p11 = AT_PACK(s1, 8);
            o0 = MFMA32(vc, p01, MFMA32(va, p00, o0)); l0 = MFMA16(A.ones, p01, MFMA16(A.ones, p00, l0));
            o1 = MFMA32(vc, p11, MFMA32(va, p10, o1)); l1 = MFMA16(A.ones, p11, MFMA16(A.ones, p10, l1));
        }
        WAIT_L0(); BAR(); CFENCE();
    }
    R.o0 = o0; R.o1 = o1; R.l0 = l0; R.l1 = l1; R.m0 = m0; R.m1 = m1;
}

__device__ __forceinline__ void attn_fast(lds_ptr lds, unsigned lds0, const char* Qg, const char* Kg, const char* Vg, const G2& g2, const Flags& flags, int qkv0, int qkvword, int depword,
                                          int hsig, int c0, bool bump, const CodePf& cp, const HPend& hp, const char* wpd, const char* wpo, int prow, int wid, AttnOut& R
                                          ) {
    const int lane = opaque_tid() & 63;
    AttnLane A; A.init(lds, Qg, wid, lane);
    lds_ptr kb0 = A.kb0, kb1 = A.kb1, vb = A.vb;
    PIN(kb0); PIN(kb1); PIN(vb);
    lds_ptr kh0 = kb0 + 2 * SLOT, kh1 = kb1 + 2 * SLOT, vh = vb + 2 * SLOT;
    PIN(kh0); PIN(kh1); PIN(vh);
    const bf16x8 ones = A.ones;
    const __amdgpu_buffer_rsrc_t hr = __builtin_amdgcn_make_buffer_rsrc((void*)hp.base, 0, hp.size, 0x00020000);
    f32x16 o0 = (f32x16){}, o1 = (f32x16){}, negm0 = (f32x16){}, negm1 = (f32x16){}, sA0, sA1, sB0, sB1;
    f32x4 l0 = (f32x4){0.f, 0.f, 0.f, 0.f}, l1 = l0;
    bf16x8 kfa, kfb, va, vc = (bf16x8){}, pf = (bf16x8){};
    PIN(o0); PIN(o1); PIN(l0); PIN(l1); PIN(vc); PIN(pf); PIN(A.voffK); PIN(A.voffV);
    G2Off go; g2_voff(lane, go);
    unsigned gA0 = go.A0, gA1 = go.A1, gB = (wid & 1) ? go.B1 : go.B0;
    unsigned pfo = (unsigned)(((lane >> 3) & 1) * (DM * 2) + (lane & 7) * 128);
    const u64* hptr = flags.p + (F_H + depword) * 16;
    const u64* qptr = flags.p + qkvword * 16;
    PIN(gA0); PIN(gA1); PIN(gB); PIN(pfo); PIN(hptr); PIN(qptr);
    if (wid == 0) {
        wait_all(flags, qkv0, peek(flags, qkv0));
        STAMP(13);
    }
    CFENCE(); BAR(); CFENCE();
    STAMP(4);
    AT_LOAD_K(c0, 0);
    glds16c(Qg, A.voffK, lds0 + wid * 2048 + 2 * SLOT); glds16c(Qg + 512, A.voffK, lds0 + wid * 2048 + 2 * SLOT + 1024);
    AT_LOAD_V(c0, 0);
    u64 hv = 0;
    float m0, m1;
    WAIT_V(2);
    PEEK_AHEAD(hv, qptr, wid);
    BAR(); SBAR();
    STAMP(15);
    const int qo = 2 * SLOT + (wid & 3) * 4096 - (wid >> 2) * 8192;
    const bf16x8 q00 = lds_b128(kb0 + qo), q10 = lds_b128(kb0 + qo + 2048), q01 = lds_b128(kb1 + qo), q11 = lds_b128(kb1 + qo + 2048);
    {
        kfa = lds_b128(kb0); kfb = lds_b128(kb1);
        sA0 = MFMA32(kfa, q00, negm0); sA1 = MFMA32(kfa, q10, negm1); sA0 = MFMA32(kfb, q01, sA0); sA1 = MFMA32(kfb, q11, sA1);
        kfa = lds_b128(kb0 + 2048); kfb = lds_b128(kb1 + 2048);
        m0 = rowmax16(sA0); m1 = rowmax16(sA1);
#pragma unroll
        for (int r = 0; r < 16; ++r) { sA0[r] -= m0; sA1[r] -= m1; negm0[r] = -m0; negm1[r] = -m1; }
        PIN(negm0); PIN(negm1);
    }
    unsigned pfd = 0;
    PEEK_DONE(hv);
    if (wid == 0) {
        wait_all(flags, qkvword, hv);
        if (bump) signal(flags, F_EPOCH, lane);
        STAMP(14);
    }
    CFENCE(); BAR(); SBAR();
    STAMP(5);
    AT_LOAD_K((c0 + 1) & 7, 1); AT_LOAD_V((c0 + 1) & 7, 1);
    STAMP(19);
#define AT_GROUP(QK, RD, S, i, O, L, VF) do { \
        QK; SBAR(); RD; SBAR(); \
        float e0, e1, e2, e3, e4, e5, e6, e7; \
        asm volatile("v_exp_f32 %0, %13\n\tv_exp_f32 %1, %14\n\tv_exp_f32 %2, %15\n\tv_mfma_f32_32x32x16_bf16 %8, %10, %11, %8\n\t" \
                     "v_exp_f32 %3, %16\n\tv_exp_f32 %4, %17\n\tv_exp_f32 %5, %18\n\tv_mfma_f32_16x16x32_bf16 %9, %12, %11, %9\n\tv_exp_f32 %6, %19\n\tv_exp_f32 %7, %20" \
            : "=&v"(e0), "=&v"(e1), "=&v"(e2), "=&v"(e3), "=&v"(e4), "=&v"(e5), "=&v"(e6), "=&v"(e7), "+v"(O), "+v"(L) \
            : "v"(VF), "v"(pf), "v"(ones), "v"(S[i]), "v"(S[i + 1]), "v"(S[i + 2]), "v"(S[i + 3]), "v"(S[i + 4]), "v"(S[i + 5]), "v"(S[i + 6]), "v"(S[i + 7])); \
        pf = __builtin_bit_cast(bf16x8, (u32x4){cvtpk(e0, e1), cvtpk(e2, e3), cvtpk(e4, e5), cvtpk(e6, e7)}); SBAR(); } while (0)
#define LO(lo, hi, off) (((off) >= 2 * SLOT ? hi : lo) + ((off) & (2 * SLOT - 1)))
#define AT_STEP(C0, C1, N0, N1, QK, KR, KO, VO, H0, H1, H2, H3) do { \
        AT_GROUP(if (QK) MFMA32_SET(N0, kfa, q00, negm0), { va = cat8(lds_tr(LO(vb, vh, VO)), lds_tr(LO(vb, vh, VO) + 512)); H0; }, C0, 0, o1, l1, vc); \
        AT_GROUP(if (QK) MFMA32_SET(N1, kfa, q10, negm1), { if (KR) kfa = lds_b128(LO(kb0, kh0, KO)); vc = cat8(lds_tr(LO(vb, vh, VO) + 1024), lds_tr(LO(vb, vh, VO) + 1536)); H1; }, C1, 0, o0, l0, va); \
        AT_GROUP(if (QK) MFMA32_ACC(N0, kfb, q01), H2, C0, 8, o1, l1, va); \
        AT_GROUP(if (QK) MFMA32_ACC(N1, kfb, q11), { if (KR) kfb = lds_b128(LO(kb1, kh1, KO)); H3; }, C1, 8, o0, l0, vc); } while (0)
#define PRV 1
#define YP(p) asm volatile("s_cmp_lt_u32 %0, 4\n\ts_cbranch_scc1 1f\n\ts_setprio " #p "\n1:" :: "s"(wid) : "scc")
    if (PRV) __builtin_amdgcn_s_setprio(1);
    auto ylow = [&]() { if (PRV) YP(0); };
#define S0(c, ...) AT_STEP(sA0, sA1, sB0, sB1, true, true, ((c) & 3) * SLOT + 4096, ((c) & 3) * SLOT, __VA_ARGS__)
#define S1(c, ...) if (PRV == 1) YP(0); AT_STEP(sB0, sB1, sA0, sA1, true, true, ((c) & 3) * SLOT + 6144, ((c) & 3) * SLOT + 2048, __VA_ARGS__)
#define S2(c, ...) if (PRV == 1 || (PRV == 2 && (c) == 7)) YP(2); AT_STEP(sA0, sA1, sB0, sB1, true, (c) < 7, (((c) + 1) & 3) * SLOT, ((c) & 3) * SLOT + 4096, __VA_ARGS__)
#define S3(c, ...) AT_STEP(sB0, sB1, sA0, sA1, (c) < 7, (c) < 7, (((c) + 1) & 3) * SLOT + 2048, ((c) & 3) * SLOT + 6144, __VA_ARGS__)
#define SY(c, n) do { WAIT_V(n); WAIT_L0(); BAR(); SBAR(); STAMP(20 + (c)); } while (0)
#define NO (void)0
#define KP(c, i) glds16c(Kg + ((c0 + (c)) & 7) * 8192 + (i) * 512, A.voffK, lds0 + wid * 2048 + ((c) & 3) * SLOT + (i) * 1024)
#define VP(c, i) glds16c(Vg + ((c0 + (c)) & 7) * 8192 + (i) * 512, A.voffV, lds0 + wid * 2048 + ((c) & 3) * SLOT + 16384 + (i) * 1024)
#define CH(c) KP(c, 0), KP(c, 1), VP(c, 0), VP(c, 1)
#define HS(i) do { __builtin_amdgcn_raw_buffer_store_b128(hp.v[i], hr, hp.off + (i) * 1024, 0, 16); CFENCE(); } while (0)
#define WP(i) wpf(i)
#define GB(j, slot, i) glds16(g2.Wd + (size_t)((j) * 64 + wid * 8) * (DM * 2) + (i) * 128, gB, lds0 + (slot) * SLOT + 16384 + (i) * 8192 + wid * 1024)
#define GA(j, slot, i) glds16c(g2.Ah + (size_t)(j) * (2 * GRP) + g2.rowb + wid * 1024, (i) ? gA1 : gA0, lds0 + (slot) * SLOT + wid * 2048 + (i) * 1024)
    auto wpf = [&](int i) {
        const int pi = (wid + 8 * i < g2.nh + 8) ? wid + 8 * i : 0, lr = prow + 2 * pi;
        asm volatile("global_load_dword %0, %1, %2" : "+v"(pfd) : "v"(pfo), "s"((lr < g2.nh * 64 ? wpd : wpo) + (size_t)lr * (DM * 2)) : "memory");
    };
    auto cpf = [&]() { CODE_PF(pfd); };
    auto hpeek = [&]() { PEEK_AHEAD(hv, hptr, wid); };
    S0(0, NO, NO, NO, NO);         S1(0, NO, NO, NO, NO);    SY(0, 0);  S2(0, CH(2)); S3(0, CH(3));
    S0(1, WP(0), NO, HS(0), NO);   S1(1, NO, NO, HS(1), NO); SY(1, 7);  S2(1, CH(4)); S3(1, WP(1), NO, HS(2), NO);
    S0(2, NO, NO, HS(3), NO);      S1(2, NO, NO, HS(4), NO); SY(2, 11); S2(2, CH(5)); S3(2, WP(2), NO, HS(5), NO);
    S0(3, NO, NO, HS(6), NO);      S1(3, NO, NO, HS(7), NO); SY(3, 12); S2(3, CH(6)); S3(3, WP(3), NO, NO, NO);
    S0(4, NO, NO, NO, NO);         S1(4, NO, NO, NO, NO);    SY(4, 5);
    if (wid == 0) signal(flags, hsig, lane);
                                                                        S2(4, CH(7)); S3(4, WP(4), NO, NO, NO);
    S0(5, GB(0, 4, 0), GB(0, 4, 1), NO, NO); S1(5, NO, NO, NO, NO); SY(5, 2); S2(5, GB(1, 0, 0), GB(1, 0, 1), NO, NO); S3(5, NO, NO, NO, NO);
    S0(6, hpeek(), NO, NO, NO);
    if (wid == 0) { PEEK_DONE(hv); wait_all(flags, F_H + depword, hv); }
                                   S1(6, NO, NO, NO, NO);    SY(6, 0);  S2(6, GA(0, 4, 0), GA(0, 4, 1), GB(2, 1, 0), GB(2, 1, 1)); S3(6, GA(1, 0, 0), GA(1, 0, 1), cpf(), NO);
    S0(7, GA(2, 1, 0), GA(2, 1, 1), NO, NO); S1(7, NO, NO, NO, NO); SY(7, 0); S2(7, NO, NO, NO, NO); S3(7, NO, NO, ylow(), NO);
    __builtin_amdgcn_s_setprio(0);
#undef S0
#undef S1
#undef S2
#undef S3
#undef NO
    STAMP(28);
    asm volatile("s_nop 1" ::: "memory");
    MFMA32_ACC(o1, vc, pf); MFMA16_ACC(l1, ones, pf);
    MFMA_DRAIN(); SBAR();
    asm volatile("" :: "v"(pfd));
    R.o0 = o0; R.o1 = o1; R.l0 = l0; R.l1 = l1; R.m0 = m0; R.m1 = m1;
}

__global__ __launch_bounds__(512, 2) void k_main(const bf16_t* __restrict__ x, const bf16_t* __restrict__ wqkv, const bf16_t* __restrict__ wo, const bf16_t* __restrict__ wg,
                                                 const bf16_t* __restrict__ wu, const bf16_t* __restrict__ wd, bf16_t* __restrict__ out, unsigned char* ws) {
    __shared__ __attribute__((aligned(16))) unsigned char smem[LDS_BYTES];
    const lds_ptr lds = (lds_ptr)smem;
    const unsigned lds0 = (unsigned)(uintptr_t)lds;
    const int wid = __builtin_amdgcn_readfirstlane(threadIdx.x >> 6);
    const int bid = blockIdx.x, xcd = bid & 7, idx = bid >> 3;
    Flags flags; flags.p = (u64*)(ws + WS_FLAGS);
    const __amdgpu_buffer_rsrc_t wsr = __builtin_amdgcn_make_buffer_rsrc((void*)ws, 0, WS_END, 0x00020000);
#define ST16(v, off) __builtin_amdgcn_raw_buffer_store_b128(v, wsr, off, 0, 16)
    STAMP(0);
    unsigned cpd = 0;
    CodePf cp;
    asm volatile("s_getpc_b64 %0\n.Lk_begin:\n\ts_mov_b32 %1, .Lk_end-.Lk_begin" : "=s"(cp.base), "=s"(cp.len));
    cp.first = idx * 16;
    HPend hp;
    {
        const int lane = threadIdx.x & 63, wr = wid >> 2, wc = wid & 3, js = idx & 3, rb = idx >> 2;
        const bool typeY = (js == 3);
        const int f0 = 448 * xcd + 128 * js;
        const char* P0 = (const char*)(wg + f0);
        const char* P2 = (const char*)(wu + f0);
        const char* P1 = typeY ? (const char*)(wqkv + 128 * xcd + 64) : (const char*)(wg + f0 + 64);
        const char* P3 = typeY ? (const char*)(wqkv + 1024 + 128 * xcd + 64) : (const char*)(wu + f0 + 64);
        const char* P4 = typeY ? (const char*)(wqkv + 2048 + 128 * xcd + 64) : (const char*)(wqkv + js * 1024 + 128 * xcd);
        const unsigned stepF = 32u * FF * 2u, stepQ = 32u * NQKV * 2u, step13 = typeY ? stepQ : stepF;
        const char* XA = (const char*)x + (size_t)rb * 256 * DM * 2;
        const char* XC = (const char*)ws + WS_XC + (size_t)rb * 256 * DM * 2;
        const int r8 = lane >> 3, u8 = lane & 7;
        const int gw = (r8 & 2) | ((wc & 1) << 2);
        const unsigned voffF = (unsigned)(((wc * 8 + r8) * FF + (u8 ^ gw) * 8) * 2);
        const unsigned voffQ = (unsigned)(((wc * 8 + r8) * NQKV + (u8 ^ gw) * 8) * 2);
        const unsigned voff13 = typeY ? voffQ : voffF;
        const unsigned voffA0 = (unsigned)(((32 * wid + r8) * DM + (u8 ^ (r8 >> 1)) * 8) * 2);
        const unsigned voffA1 = (unsigned)(((32 * wid + 8 + r8) * DM + (u8 ^ (4 | (r8 >> 1))) * 8) * 2);
        const unsigned dstA = lds0 + G1_A + wid * 4096, dstW = lds0 + G1_W + wc * 1024;
#define G1_LOAD_A(t) do { const char* b_ = XA + (t) * 128; const unsigned d_ = dstA + ((t) & 1) * 32768; \
        glds16(b_, voffA0, d_); glds16(b_, voffA1, d_ + 1024); glds16(b_ + 32768, voffA0, d_ + 2048); glds16(b_ + 32768, voffA1, d_ + 3072); } while (0)
#define G1_LOAD_W(hs) do { const unsigned d_ = dstW + ((hs) & 3) * 4096; \
        glds16(P0 + (size_t)(hs) * stepF, voffF, d_); glds16(P1 + (size_t)(hs) * step13, voff13, d_ + 16384); glds16(P2 + (size_t)(hs) * stepF, voffF, d_ + 32768); \
        glds16(P3 + (size_t)(hs) * step13, voff13, d_ + 49152); glds16(P4 + (size_t)(hs) * stepQ, voffQ, d_ + 65536); } while (0)
        STAMP(29);
        if (wr == 1) G1_LOAD_W(0);
        G1_LOAD_A(0);
        u32x4 xa0, xa1;
        const int srow = rb * 256 + (js * 8 + xcd) * 8 + wid;
        asm volatile("global_load_dwordx4 %0, %2, %3\n\tglobal_load_dwordx4 %1, %2, %3 offset:1024"
                     : "=&v"(xa0), "=&v"(xa1) : "v"((unsigned)(lane * 16)), "s"((const char*)x + (size_t)srow * DM * 2) : "memory");
        CODE_PF(cpd);
        STAMP(30);

        int fr = lane & 15, fq = lane >> 4;
        const lds_ptr aB0 = lds + G1_A + (wr * 128 + fr) * 128 + ((fq ^ (fr >> 1)) * 16);
        const lds_ptr aB1 = lds + G1_A + (wr * 128 + fr) * 128 + (((4 | fq) ^ (fr >> 1)) * 16);
        const int tq = (lane >> 2) & 3, tp = lane & 3, tg = (tq & 2) | ((fq & 1) << 2);
        auto wbase = [&](int panel, int bp) { return lds + G1_W + panel * 16384 + (8 * fq + tq) * 128 + (((2 * bp + (tp >> 1)) ^ tg) * 16) + (tp & 1) * 8; };
        const int pX = wc >> 1, bX = 2 * (wc & 1);
        const lds_ptr wB0 = typeY ? wbase(0, wc) : wbase(pX, bX);
        const lds_ptr wB1 = typeY ? wbase(1, wc) : wbase(pX, bX + 1);
        const lds_ptr wB2 = typeY ? wbase(2, wc) : wbase(2 + pX, bX);
        const lds_ptr wB3 = typeY ? wbase(3, wc) : wbase(2 + pX, bX + 1);
        const lds_ptr wB4 = wbase(4, wc);
        if (threadIdx.x == 0) *(LAS unsigned*)(lds + G1_CNT) = 0u;
        f32x4 acc[8][5];
#pragma unroll
        for (int m = 0; m < 8; ++m)
#pragma unroll
            for (int b = 0; b < 5; ++b) { acc[m][b] = (f32x4){0.f, 0.f, 0.f, 0.f}; PIN(acc[m][b]); }
        if (wr == 1) { WAIT_V(7); BAR(); }
        asm volatile("s_waitcnt vmcnt(1)" : "+v"(xa0), "+v"(xa1) :: "memory");
        STAMP(31);
        u64 sg;
        {
            const f32x2 st = row_stats(xa0, xa1);
            G1_LOAD_W(1 + wr);
            ST16(xa0, (lane >= 32) ? WS_XC + (unsigned)srow * 2048u + lane * 16 : WS_END);
            ST16(xa1, WS_XC + (unsigned)srow * 2048u + 1024 + lane * 16);
            __builtin_amdgcn_raw_buffer_store_b64(__builtin_bit_cast(u32x2, st), wsr, (lane == 0) ? WS_STAT + (unsigned)srow * 8u : WS_END, 0, 16);
            asm volatile("global_load_dwordx2 %0, %1, %2 sc1" : "=&v"(sg) : "v"(0u), "s"(flags.p) : "memory");
        }
        STAMP(32);
        bf16x8 af[8], wf[5];
        float cs0 = 0.f, cs1 = 0.f, cs2 = 0.f;
#define CS1(c, w) "v_dot2c_f32_bf16 %" #c ", %25, %" #w "\n\t"
#define G1_CSUM(t, lim) do { const u32x4 w0_ = __builtin_bit_cast(u32x4, wf[0]), w1_ = __builtin_bit_cast(u32x4, wf[1]), w2_ = __builtin_bit_cast(u32x4, wf[2]), \
            w3_ = __builtin_bit_cast(u32x4, wf[3]), w4_ = __builtin_bit_cast(u32x4, wf[4]); \
        asm volatile("s_cmp_gt_u32 %24, %26\n\ts_cbranch_scc1 2f\n\ts_cmp_lg_u32 %23, 0\n\ts_cbranch_scc1 1f\n\t" \
            CS1(0, 3) CS1(1, 7) CS1(2, 19) CS1(0, 4) CS1(1, 8) CS1(2, 20) CS1(0, 5) CS1(1, 9) CS1(0, 6) CS1(1, 10) "s_nop 1\n\ts_branch 2f\n1:\n\t" \
            CS1(0, 11) CS1(1, 15) CS1(2, 21) CS1(0, 12) CS1(1, 16) CS1(2, 22) CS1(0, 13) CS1(1, 17) CS1(0, 14) CS1(1, 18) "s_nop 2\n2:" \
            : "+v"(cs0), "+v"(cs1), "+v"(cs2) \
            : "v"(w0_[0]), "v"(w0_[1]), "v"(w0_[2]), "v"(w0_[3]), "v"(w1_[0]), "v"(w1_[1]), "v"(w1_[2]), "v"(w1_[3]), "v"(w2_[0]), "v"(w2_[1]), "v"(w2_[2]), "v"(w2_[3]), \
              "v"(w3_[0]), "v"(w3_[1]), "v"(w3_[2]), "v"(w3_[3]), "v"(w4_[0]), "v"(w4_[1]), "v"(w4_[2]), "v"(w4_[3]), "s"(wr), "s"(t), "s"(0x3f803f80u), "i"(lim) : "scc"); } while (0)
#define G1_RW(i, B, slot) wf[i] = cat8(lds_tr(B + (slot) * 4096), lds_tr(B + (slot) * 4096 + 512))
#define G1_RA(m, AB, abuf) af[m] = lds_b128(AB + (abuf) * 32768 + (m) * 2048)
#define G1_SEG(AB, abuf, slot, L0, L1, L2, L3, L4, CLOSE) do { \
        L0; G1_RW(0, wB0, slot); G1_RW(1, wB1, slot); L1; G1_RW(2, wB2, slot); G1_RW(3, wB3, slot); L2; G1_RW(4, wB4, slot); G1_RA(0, AB, abuf); G1_RA(1, AB, abuf); \
        L3; G1_RA(2, AB, abuf); G1_RA(3, AB, abuf); G1_RA(4, AB, abuf); L4; G1_RA(5, AB, abuf); G1_RA(6, AB, abuf); G1_RA(7, AB, abuf); \
        SBAR(); G1_CSUM(t, 3); WAIT_L0(); SBAR(); if (CLOSE) { BAR(); SBAR(); } } while (0)
#define G1_SEG_A(abuf, slot, tt) do { const char* b_ = ((tt) < 4 ? XA : XC) + (tt) * 128; const unsigned d_ = dstA + (1 - (abuf)) * 32768; \
        G1_SEG(aB0, abuf, slot, glds16c(b_, voffA0, d_), glds16c(b_, voffA1, d_ + 1024), glds16c(b_ + 32768, voffA0, d_ + 2048), glds16c(b_ + 32768, voffA1, d_ + 3072), (void)0, true); } while (0)
#define G1_SEG_W(abuf, slot, hs, dslot, CLOSE) do { const unsigned d_ = dstW + (dslot) * 4096; \
        G1_SEG(aB1, abuf, slot, glds16(P0 + (size_t)(hs) * stepF, voffF, d_), glds16(P1 + (size_t)(hs) * step13, voff13, d_ + 16384), glds16(P2 + (size_t)(hs) * stepF, voffF, d_ + 32768), \
               glds16(P3 + (size_t)(hs) * step13, voff13, d_ + 49152), glds16(P4 + (size_t)(hs) * stepQ, voffQ, d_ + 65536), CLOSE); } while (0)
#define EB 2
#define FIRST_WAIT() asm volatile("s_waitcnt vmcnt(4)" : "+v"(sg), "+v"(cpd) :: "memory")
#define G1_MM(i0, i1) _Pragma("unroll") for (int i = (i0); i < (i1); ++i) { const int b = i >> 3, m = (b & 1) ? 7 - (i & 7) : (i & 7); MFMA16_ACC(acc[m][b], wf[b], af[m]); }
#define G1_MMA(CLOSE) do { __builtin_amdgcn_s_setprio(1); G1_MM(0, 40 - EB); SBAR(); __builtin_amdgcn_s_setprio(2); CLOSE; SBAR(); G1_MM(40 - EB, 40); __builtin_amdgcn_s_setprio(0); SBAR(); } while (0)

        STAMP(33);
        WAIT_L0();
        STAMP(34);
        BAR();
        SBAR();
        STAMP(1);
        flags.tag = 0;
        const u64* sgp = flags.p + (F_XC + rb * 32 + (lane & 31)) * 16;
#pragma unroll 1
        for (int t = 0; t < 16; t += 2) {
            const int h1 = (2 * t + 3 + wr < 31) ? 2 * t + 3 + wr : 31, h2 = (2 * t + 5 + wr < 31) ? 2 * t + 5 + wr : 31, t2 = (t < 14) ? t + 2 : 15;
            const int sel = (t == 12) ? 0 : ((t ^ 2) | wid);
            STAMP(35 + (t >> 1));
            PEEK_AHEAD(sg, sgp, sel);
            if (t == 14) CODE_PF(cpd);
            G1_SEG_A(0, 0, t + 1); G1_MMA({ FIRST_WAIT(); BAR(); });
            if (t == 0) {
                const unsigned elo = __builtin_amdgcn_readfirstlane((unsigned)sg), ehi = __builtin_amdgcn_readfirstlane((unsigned)(sg >> 32));
                flags.tag = (((u64)ehi << 32) | elo) * 0x9E3779B97F4A7C15ull + 0x632BE59BD9B4E019ull;
                if (wid == 4) signal(flags, F_XC + bid, lane);
            }
            if (sel == 0) {
                if (t == 12) *(LAS u64*)(lds + G1_STAT + (wid * 32 + (opaque_tid() & 31)) * 8) = sg;
                else wait_all(flags, F_XC + rb * 32 + (opaque_tid() & 31), sg);
            }
            G1_SEG_W(0, 1, h1, (3 + wr) & 3, true); G1_MMA({ WAIT_V(5); BAR(); });
            G1_SEG_A(1, 2, t2); G1_MMA({ WAIT_V(4); BAR(); });
            G1_SEG_W(1, 3, h2, (1 + wr) & 3, false);
            if (t == 2) {
                const int tl = opaque_tid() & 63;
                const lds_ptr cp = lds + G1_CS + wc * 384 + (tl & 15) * 4;
                *(LAS float*)(cp + wr * 128) = red4(cs0); *(LAS float*)(cp + wr * 128 + 64) = red4(cs1); *(LAS float*)(cp + 256 + wr * 64) = red4(cs2);
                sgp = (const u64*)(ws + WS_STAT) + rb * 256 + wid * 32 + (tl & 31);
                WAIT_L0();
            }
            SBAR(); BAR(); SBAR();
            G1_MMA(asm volatile("s_waitcnt vmcnt(5)\n\ts_cmp_eq_u32 %0, 15\n\ts_cbranch_scc1 1f\n\ts_barrier\n1:" :: "s"(t + wr) : "memory", "scc"));
        }
        MFMA_DRAIN(); SBAR();
        STAMP(2);

        const int tz = opaque_tid(), ln = tz & 63;
        fr = tz & 15; fq = (tz >> 4) & 3;
        const int row0 = rb * 256 + wr * 128 + fr;
        f32x4 csv[5]; f32x2 st[8];
#pragma unroll
        for (int b = 0; b < 5; ++b) csv[b] = *(const LAS f32x4*)(lds + G1_CS + wc * 384 + b * 64 + fq * 16);
        csv[4] += *(const LAS f32x4*)(lds + G1_CS + wc * 384 + 320 + fq * 16);
#pragma unroll
        for (int m = 0; m < 8; ++m) st[m] = *(const LAS f32x2*)(lds + G1_STAT + (wr * 128 + m * 16 + fr) * 8);
        auto lnv = [&](int m, int b, float sc) { return (acc[m][b] - st[m].x * csv[b]) * (st[m].y * sc); };
        auto glu = [&](int m, int bg, int bu) {
            const f32x4 g = lnv(m, bg, -LOG2E), u = lnv(m, bu, 1.0f);
            f32x4 h;
#pragma unroll
            for (int j = 0; j < 4; ++j) h[j] = u[j] * __builtin_amdgcn_rcpf(1.0f + __builtin_amdgcn_exp2f(g[j]));
            return h;
        };
        auto pack2 = [&](const f32x4& v0, const f32x4& v1) {
            unsigned p0 = cvtpk(v0[0], v0[1]), p1 = cvtpk(v0[2], v0[3]), q0 = cvtpk(v1[0], v1[1]), q1 = cvtpk(v1[2], v1[3]);
            swap16(p0, q0); swap16(p1, q1);
            return (u32x4){p0, p1, q0, q1};
        };
        const int hd = 4 * xcd + (typeY ? 2 : 0) + (wc >> 1);
        const unsigned qoff = WS_QKV + (unsigned)(hd * 2 + (wc & 1)) * 65536u + (unsigned)(row0 + (fq & 1) * 16) * 32u + (fq >> 1) * 16;
        constexpr unsigned KIND = NH * 2 * 65536u;
        if (!typeY) {
            const float sc4 = (js == 0) ? QSCALE : 1.0f;
#pragma unroll
            for (int m = 0; m < 8; m += 2) ST16(pack2(lnv(m, 4, sc4), lnv(m + 1, 4, sc4)), qoff + js * KIND + m * 512);
            CFENCE();
            hp.base = ws + WS_ACT + (unsigned)(14 * xcd + 4 * js + wc) * GRP + (unsigned)(rb * 256 + wr * 128) * 64u;
            hp.off = fr * 64 + (fq & 1) * 32 + (fq >> 1) * 16; hp.size = 8192;
#pragma unroll
            for (int m = 0; m < 8; ++m) {
                hp.v[m] = pack2(glu(m, 0, 2), glu(m, 1, 3));
                if (m == 5) { PIN(hp.v[5]); WAIT_V(0); arrive(flags, F_QKV + bid, ln, lds + G1_CNT, 7u); }
            }
        } else {
#pragma unroll
            for (int m = 0; m < 8; m += 2) {
                ST16(pack2(lnv(m, 3, 1.0f), lnv(m + 1, 3, 1.0f)), qoff + KIND + m * 512);
                ST16(pack2(lnv(m, 4, 1.0f), lnv(m + 1, 4, 1.0f)), qoff + 2 * KIND + m * 512);
                ST16(pack2(lnv(m, 1, QSCALE), lnv(m + 1, 1, QSCALE)), qoff + m * 512);
            }
            CFENCE();
            hp.base = ws + WS_ACT + (unsigned)(14 * xcd + 12 + (wc >> 1)) * GRP + (wc & 1) * (GRP / 2) + (unsigned)(rb * 256 + wr * 128) * 32u;
            hp.off = (fr + (fq & 1) * 16) * 32 + (fq >> 1) * 16; hp.size = 4096;
#pragma unroll
            for (int m = 0; m < 8; m += 2) { hp.v[m >> 1] = pack2(glu(m, 0, 2), glu(m + 1, 0, 2)); hp.v[4 + (m >> 1)] = hp.v[m >> 1]; }
            PIN(hp.v[3]); WAIT_V(0); arrive(flags, F_QKV + bid, ln, lds + G1_CNT, 7u);
        }
#pragma unroll
        for (int i = 0; i < 8; ++i) PIN(hp.v[i]);
        STAMP(3);
    }

    const int z = xcd & 1, rbk = ((xcd >> 1) & 1) * 8 + (idx & 7), cbk = (xcd >> 2) * 4 + (idx >> 3);
    G2 g2;
    g2.nh = z ? 56 - NH0 : NH0;
    g2.rowb = rbk * 128 * 64;
    g2.split = z ? split_mask(NH0, 56 - NH0) : split_mask(0, NH0);
    g2.Ah = (const char*)ws + WS_ACT + (size_t)(z * 2 * NH0) * GRP;
    g2.Ao = (const char*)ws + WS_ACT + (size_t)(112 + z * 16 - 2 * g2.nh) * GRP;
    g2.Wd = (const char*)(wd + (size_t)z * NH0 * 64 * DM + cbk * 128);
    g2.Wo = (const char*)(wo + (size_t)z * 512 * DM + cbk * 128) - (size_t)g2.nh * 64 * DM * 2;
    {
        const int hl = idx & 3, qb = idx >> 2, h = 4 * xcd + hl;
        const int l0_ = opaque_tid() & 63;
        const int qkv0 = F_QKV + xcd + 8 * (qb * 4 + ((hl >= 2) ? 3 : l0_ % 3));
        const int qkvword = F_QKV + xcd + 8 * ((l0_ & 7) * 4 + ((hl >= 2) ? 3 : 1 + ((l0_ >> 3) & 1)));
        const int odep = (rbk >> 1) * 32 + ((l0_ >> 2) & 3) * 8 + 4 * z + (l0_ & 3);
        const int depword = (l0_ & 16) ? (odep & ~7) + 4 : odep;
        const char* Qg = (const char*)ws + WS_QKV + (size_t)h * 131072 + qb * 256 * 32;
        const char* Kg = (const char*)ws + WS_QKV + (size_t)(NH + h) * 131072;
        const char* Vg = (const char*)ws + WS_QKV + (size_t)(2 * NH + h) * 131072;
        const char* wpd = g2.Wd - (cbk & 3) * 256;
        const char* wpo = g2.Wo - (cbk & 3) * 256;
        AttnOut R;
#define DG
        attn_fast(lds, lds0, Qg, Kg, Vg, g2, flags, qkv0, qkvword, depword, F_H + bid, qb, bid == 0, cp, hp, wpd, wpo, idx * 2 * (g2.nh + 8), wid, R DG);
        STAMP(6);
        const int tz = opaque_tid(), ln = tz & 63, w4 = wid & 3, fr = ln & 15, fq = ln >> 4;
        lds_ptr po0 = lds + 2 * SLOT + w4 * 4096 + ln * 16, po1 = lds + 2 * SLOT + 16384 + w4 * 2048 + ln * 16;
        PIN(po0); PIN(po1);
        auto po = [&](int i) { return i < 4 ? po0 + i * 1024 : po1 + (i >> 1 & 1) * 8192 + (i & 1) * 1024; };
        const lds_ptr pl = po0 + SLOT;
        volatile LAS unsigned* const badw = (volatile LAS unsigned*)(lds + 3 * SLOT + 1024);
        const int wm = w4 >> 1, wn = w4 & 1;
        const unsigned aO0 = (wm * 64 + fr) * 128 + ((fq ^ (fr >> 1)) * 16), aO1 = aO0 ^ 64;
        const int tq = (ln >> 2) & 3, tp = ln & 3, tg = (tq & 2) | ((fq & 1) << 2);
        unsigned wO[4];
#pragma unroll
        for (int b = 0; b < 4; ++b) wO[b] = 16384 + wn * 8192 + (8 * fq + tq) * 128 + (((2 * b + (tp >> 1)) ^ tg) * 16) + (tp & 1) * 8;
        f32x4 acc[4][4];
        bf16x8 xa[4], ya[4]; s16x4 xl[4], xh[4], yl[4], yh[4];
        lds_ptr pa0, pa1, pw0, pw1, pw2, pw3;
        int so = 4 * SLOT;
        auto handover = [&]() {
            if (wid < 4) {
#pragma unroll
                for (int i = 0; i < 4; ++i) {
                    *(LAS f32x4*)po(i) = (f32x4){R.o0[4 * i], R.o0[4 * i + 1], R.o0[4 * i + 2], R.o0[4 * i + 3]};
                    *(LAS f32x4*)po(4 + i) = (f32x4){R.o1[4 * i], R.o1[4 * i + 1], R.o1[4 * i + 2], R.o1[4 * i + 3]};
                }
                *(LAS f32x4*)pl = (f32x4){R.l0[0], R.m0, R.l1[0], R.m1};
                xl[0] = lds_tr(pw0); xh[0] = lds_tr(pw0 + 512);
#pragma unroll
                for (int m = 0; m < 4; ++m) xa[m] = lds_b128(pa0 + m * 2048);
                xl[1] = lds_tr(pw1); xh[1] = lds_tr(pw1 + 512); xl[2] = lds_tr(pw2); xh[2] = lds_tr(pw2 + 512); xl[3] = lds_tr(pw3); xh[3] = lds_tr(pw3 + 512);
#pragma unroll
                for (int m = 0; m < 4; ++m)
#pragma unroll
                    for (int b = 0; b < 4; ++b) { acc[m][b] = (f32x4){0.f, 0.f, 0.f, 0.f}; PIN(acc[m][b]); }
            }
        };
        G2Off go; g2_voff(ln, go);
        PIN(go.A0); PIN(go.A1); PIN(go.N0); PIN(go.N1); PIN(go.B0); PIN(go.B1);
        pa0 = lds + aO0 + so; pa1 = lds + aO1 + so; pw0 = lds + wO[0] + so; pw1 = lds + wO[1] + so; pw2 = lds + wO[2] + so; pw3 = lds + wO[3] + so;
        PIN(pa0); PIN(pa1); PIN(pw0); PIN(pw1); PIN(pw2); PIN(pw3);
        {
            handover();
            const f32x16 z16 = R.o0 * 0.0f + R.o1 * 0.0f;
            const f32x4 z4 = (z16.s0123 + z16.s4567) + (z16.s89ab + z16.scdef);
            const f32x2 z2 = z4.lo + z4.hi;
            const float zs = z2.x + z2.y;
            const bool ok = (zs == 0.f) && (R.l0[0] > 0.f) && (R.l0[0] < 0x1p96f) && (R.l1[0] > 0.f) && (R.l1[0] < 0x1p96f);
            const bool wave_bad = !__all(ok);
            if (ln == 0) badw[wid] = wave_bad ? 1u : 0u;
            STAMP(16);
            WAIT_L0(); BAR(); CFENCE();
            const unsigned anybad = badw[ln & 7];
            if (__builtin_expect(__any(anybad != 0u), 0)) {
                WAIT_L0(); BAR(); CFENCE();
                attn_safe(lds, lds0, Qg, Kg, Vg, wid, R);
                handover();
                WAIT_L0(); BAR(); CFENCE();
            }
        }
        STAMP(7);

        const int tile = rbk * 8 + cbk, nt = g2.nh + 8;
        if (__builtin_expect(wid >= 4, 0)) {
#define G2_LOAD0(j, sl) do { const unsigned d_ = lds0 + (sl) * SLOT; g2_load_b(g2, j, d_, 2 * w4, go.B0); g2_load_a(g2, j, d_, 2 * w4, go); } while (0)
#define G2_LOAD1(j, sl) do { const unsigned d_ = lds0 + (sl) * SLOT; g2_load_b(g2, j, d_, 2 * w4 + 1, go.B1); g2_load_a(g2, j, d_, 2 * w4 + 1, go); } while (0)
#define G2_LOAD(j, sl) do { G2_LOAD0(j, sl); G2_LOAD1(j, sl); } while (0)
            const f32x4 lm = *(const LAS f32x4*)pl;
            f32x4 t[8];
#pragma unroll
            for (int i = 0; i < 8; ++i) t[i] = *(const LAS f32x4*)po(i);
            WAIT_L0();
            G2_LOAD(3, 2);
            BAR();
            G2_LOAD(4, 3);
            float ck[2], co[2]; unsigned p[16];
            auto coef = [&](int q) {
                const float lk = q ? R.l1[0] : R.l0[0], mk = q ? R.m1 : R.m0, lo = lm[2 * q], mo = lm[2 * q + 1];
                const float mm = __builtin_fmaxf(mk, mo), fk = __builtin_amdgcn_exp2f(mk - mm), fo = __builtin_amdgcn_exp2f(mo - mm);
                const float rl = 1.0f / (lk * fk + lo * fo);
                ck[q] = fk * rl; co[q] = fo * rl; PIN(ck[q]); PIN(co[q]); SBAR();
            };
            auto half = [&](int q, int hh) {
                const f32x16 ok_ = q ? R.o1 : R.o0;
                unsigned* pp = p + 8 * q + 4 * hh;
#pragma unroll
                for (int i = 0; i < 2; ++i) {
                    const int g = 2 * hh + i; const f32x4 u = t[4 * q + g];
                    pp[2 * i] = cvtpk(ok_[4 * g] * ck[q] + u[0] * co[q], ok_[4 * g + 1] * ck[q] + u[1] * co[q]);
                    pp[2 * i + 1] = cvtpk(ok_[4 * g + 2] * ck[q] + u[2] * co[q], ok_[4 * g + 3] * ck[q] + u[3] * co[q]);
                }
                swap32(pp[0], pp[2]); swap32(pp[1], pp[3]);
                PIN(pp[0]); PIN(pp[1]); PIN(pp[2]); PIN(pp[3]); SBAR();
            };
            const unsigned ooff = WS_ACT + (unsigned)(112 + h) * GRP + (unsigned)(qb * 256 + w4 * 64 + fr) * 64u + (fq & 1) * 32 + (fq >> 1) * 16;
            auto ost = [&](int q) {
                unsigned* pp = p + 8 * q;
                swap16(pp[0], pp[4]); swap16(pp[1], pp[5]); swap16(pp[2], pp[6]); swap16(pp[3], pp[7]);
                ST16(((u32x4){pp[0], pp[1], pp[2], pp[3]}), ooff + q * 2048); ST16(((u32x4){pp[4], pp[5], pp[6], pp[7]}), ooff + q * 2048 + 1024); CFENCE();
            };
            SBAR(); coef(0); coef(1);
            WAIT_V(8);  BAR(); G2_LOAD(5, 4); G2_LOAD0(6, 0);
            WAIT_V(12); BAR(); G2_LOAD1(6, 0); G2_LOAD(7, 1);
            WAIT_V(16); BAR(); G2_LOAD0(8, 2);  SBAR(); half(0, 0); G2_LOAD1(8, 2);
            WAIT_V(16); BAR(); G2_LOAD0(9, 3);  SBAR(); half(0, 1); G2_LOAD1(9, 3);  ost(0);
            WAIT_V(18); BAR(); G2_LOAD0(10, 4); SBAR(); half(1, 0); G2_LOAD1(10, 4);
            WAIT_V(18); BAR(); G2_LOAD0(11, 0); SBAR(); half(1, 1); G2_LOAD1(11, 0); ost(1);
            WAIT_V(20); BAR(); G2_LOAD(12, 1);
            WAIT_V(18); BAR(); G2_LOAD(13, 2);
            WAIT_V(18);
            int sl = 3; cpd = 0;
#pragma unroll 1
            for (int j = 11; j < nt - 3; ++j) {
                BAR();
                if (j == 29) CODE_PF(cpd);
                if (j == 12 && wid == 4) signal(flags, F_O + bid, ln);
                G2_LOAD(j + 3, sl);
                sl = (sl == 4) ? 0 : sl + 1;
                WAIT_V(16);
            }
            BAR();
            if (tz == 256) *(LAS unsigned*)(lds + sl * SLOT) = 0u;
            WAIT_V(8); WAIT_L0(); BAR(); WAIT_V(0); BAR();
            asm volatile("" :: "v"(cpd));
            if (wid == 4) wait_all(flags, F_X + tile * 2 + z, peek(flags, F_X + tile * 2 + z));
            CFENCE(); BAR();
            asm volatile(".Lk_end:");
        } else {
#define G2_M(C, m, b) do { MFMA16_ACC(acc[m][b], cat8(C##l[b], C##h[b]), C##a[m]); SBAR(); } while (0)
#define G2_HALF(C, N, AB, WO, RD, TAIL) do { \
            G2_M(C, 0, 0); if (RD) { N##l[0] = lds_tr(pw0 + (WO)); N##h[0] = lds_tr(pw0 + (WO) + 512); } SBAR(); \
            G2_M(C, 1, 0); if (RD) N##a[0] = lds_b128(AB); SBAR(); \
            G2_M(C, 2, 0); if (RD) N##a[1] = lds_b128((AB) + 2048); SBAR(); \
            G2_M(C, 3, 0); if (RD) N##a[2] = lds_b128((AB) + 4096); SBAR(); \
            G2_M(C, 0, 1); if (RD) N##a[3] = lds_b128((AB) + 6144); SBAR(); \
            G2_M(C, 1, 1); if (RD) { N##l[1] = lds_tr(pw1 + (WO)); N##h[1] = lds_tr(pw1 + (WO) + 512); } SBAR(); \
            G2_M(C, 2, 1); if (RD) { N##l[2] = lds_tr(pw2 + (WO)); N##h[2] = lds_tr(pw2 + (WO) + 512); } SBAR(); \
            G2_M(C, 3, 1); if (RD) { N##l[3] = lds_tr(pw3 + (WO)); N##h[3] = lds_tr(pw3 + (WO) + 512); } SBAR(); \
            G2_M(C, 0, 2); TAIL(0); SBAR(); G2_M(C, 1, 2); TAIL(1); SBAR(); G2_M(C, 2, 2); TAIL(2); SBAR(); G2_M(C, 3, 2); TAIL(3); SBAR(); \
            G2_M(C, 0, 3); G2_M(C, 1, 3); G2_M(C, 2, 3); G2_M(C, 3, 3); } while (0)
            const u64* const optr = flags.p + (F_O + odep) * 16;
            u64 ov = 0;
            STAMP(8); STAMP(43);
#define G2_NEXT(i) do { if ((i) == 0) so = (so == 4 * SLOT) ? 0 : so + SLOT; \
                if ((i) == 1) { pa0 = lds + aO0 + so; pa1 = lds + aO1 + so; PIN(pa0); PIN(pa1); } \
                if ((i) == 2) { pw0 = lds + wO[0] + so; pw1 = lds + wO[1] + so; PIN(pw0); PIN(pw1); } \
                if ((i) == 3) { pw2 = lds + wO[2] + so; pw3 = lds + wO[3] + so; PIN(pw2); PIN(pw3); } } while (0)
#define G2_NONE(i) do {} while (0)
            WAIT_L0(); SBAR(); G2_HALF(x, y, pa1, 4096, true, G2_NEXT);
            WAIT_L0(); SBAR(); G2_HALF(y, x, pa0, 0, true, G2_NONE);
            BAR();
            WAIT_L0(); SBAR(); G2_HALF(x, y, pa1, 4096, true, G2_NEXT);
            WAIT_L0(); SBAR(); G2_HALF(y, x, pa0, 0, true, G2_NONE);
            if (NH0 & 1) {
                WAIT_L0(); SBAR(); G2_HALF(x, y, pa1, 4096, true, G2_NEXT);
                WAIT_L0(); BAR(); SBAR(); G2_HALF(y, x, pa0, 0, true, G2_NONE);
            }
#pragma unroll 1
            for (int ph = 0; ph < 3; ++ph) {
                const int n = (ph == 0) ? g2.nh / 2 - 4 : (ph == 1) ? 1 : 5;
#pragma unroll 1
                for (int i = 0; i < n; ++i) {
                    WAIT_L0(); SBAR(); G2_HALF(x, y, pa1, 4096, true, G2_NEXT);
                    WAIT_L0(); BAR(); SBAR(); G2_HALF(y, x, pa0, 0, true, G2_NONE);
                    WAIT_L0(); SBAR(); G2_HALF(x, y, pa1, 4096, true, G2_NEXT);
                    WAIT_L0(); BAR(); SBAR(); G2_HALF(y, x, pa0, 0, true, G2_NONE);
                }
                PEEK_AHEAD(ov, optr, wid | ph);
                if ((wid | (ph ^ 1)) == 0) { PEEK_DONE(ov); wait_all(flags, F_O + odep, ov); }
            }
            const int row = rbk * 128 + wm * 64 + z * 32 + fr, col = cbk * 128 + wn * 64 + (fq & 1) * 16 + (fq >> 1) * 8;
            u32x4 xr[4];
#pragma unroll
            for (int i = 0; i < 4; ++i) xr[i] = *(const u32x4*)(x + (size_t)(row + (i >> 1) * 16) * DM + col + (i & 1) * 32);
            WAIT_L0(); SBAR(); G2_HALF(x, y, pa1, 4096, true, G2_NEXT);
            WAIT_L0(); BAR(); SBAR(); G2_HALF(y, x, pa0, 0, true, G2_NONE);
            WAIT_L0(); SBAR(); G2_HALF(x, y, pa1, 4096, true, G2_NONE);
            WAIT_L0(); SBAR(); G2_HALF(y, x, pa0, 0, false, G2_NONE);
            MFMA_DRAIN(); SBAR();
            STAMP(9);
            f32x4 vk[8];
            const unsigned xo = WS_XCH + (unsigned)(tile * 2 + 1 - z) * 32768u + w4 * 8192 + ln * 16;
            if (z == 0) {
#pragma unroll
                for (int i = 0; i < 8; ++i) { ST16(__builtin_bit_cast(u32x4, acc[2 + (i >> 2)][i & 3]), xo + i * 1024); vk[i] = acc[i >> 2][i & 3]; }
            } else {
#pragma unroll
                for (int i = 0; i < 8; ++i) { ST16(__builtin_bit_cast(u32x4, acc[i >> 2][i & 3]), xo + i * 1024); vk[i] = acc[2 + (i >> 2)][i & 3]; }
            }
            WAIT_V(0);
            arrive(flags, F_X + tile * 2 + 1 - z, ln, lds + ((so == 4 * SLOT) ? 0 : so + SLOT), 3u);
            STAMP(10);
            CFENCE(); BAR(); CFENCE();
            STAMP(11);
            const unsigned xi = WS_XCH + (unsigned)(tile * 2 + z) * 32768u + w4 * 8192 + ln * 16;
#pragma unroll
            for (int i = 0; i < 8; ++i) vk[i] += __builtin_bit_cast(f32x4, __builtin_amdgcn_raw_buffer_load_b128(wsr, xi + i * 1024, 0, 16));
            const __amdgpu_buffer_rsrc_t outr = __builtin_amdgcn_make_buffer_rsrc((void*)out, 0, SEQ * DM * 2, 0x00020000);
            u32x4 w[4];
#pragma unroll
            for (int i = 0; i < 4; ++i) {
                const u32x4 a = __builtin_bit_cast(u32x4, vk[2 * i]), b = __builtin_bit_cast(u32x4, vk[2 * i + 1]);
                unsigned a0 = a[0], a1 = a[1], a2 = a[2], a3 = a[3], b0 = b[0], b1 = b[1], b2 = b[2], b3 = b[3];
                swap16(a0, b0); swap16(a1, b1); swap16(a2, b2); swap16(a3, b3);
                const u32x4 xx = xr[i];
                w[i][0] = cvtpk(u2f(a0) + bf_lo(xx[0]), u2f(a1) + bf_hi(xx[0]));
                w[i][1] = cvtpk(u2f(a2) + bf_lo(xx[1]), u2f(a3) + bf_hi(xx[1]));
                w[i][2] = cvtpk(u2f(b0) + bf_lo(xx[2]), u2f(b1) + bf_hi(xx[2]));
                w[i][3] = cvtpk(u2f(b2) + bf_lo(xx[3]), u2f(b3) + bf_hi(xx[3]));
            }
            const unsigned oo = (unsigned)((row - (fr & 8)) * DM + col + (fr & 8) * 4) * 2u;
#pragma unroll
            for (int m = 0; m < 2; ++m) {
                u32x4 lo, hi;
#pragma unroll
                for (int k = 0; k < 4; ++k) {
                    lo[k] = __builtin_amdgcn_update_dpp(w[2 * m][k], w[2 * m + 1][k], 0x128, 0xf, 0xc, false);
                    hi[k] = __builtin_amdgcn_update_dpp(w[2 * m + 1][k], w[2 * m][k], 0x128, 0xf, 0x3, false);
                }
                __builtin_amdgcn_raw_buffer_store_b128(lo, outr, oo + m * 16 * DM * 2, 0, 16);
                __builtin_amdgcn_raw_buffer_store_b128(hi, outr, oo + (m * 16 + 8) * DM * 2, 0, 16);
            }
        }
    }
}

extern "C" void kernel_launch(void* const* d_in, const int* in_sizes, int n_in, void* d_out, int out_size, void* d_ws, size_t ws_size, hipStream_t stream) {
    const bf16_t* x = (const bf16_t*)d_in[0];
    k_main<<<256, 512, 0, stream>>>(x, (const bf16_t*)d_in[1], (const bf16_t*)d_in[2], (const bf16_t*)d_in[4], (const bf16_t*)d_in[5], (const bf16_t*)d_in[6],
                                    (bf16_t*)d_out, (unsigned char*)d_ws);
}
```

```cpp
#include <hip/hip_runtime.h>
#include <stdint.h>

typedef unsigned short bf16_t;
typedef short bf16x8 __attribute__((ext_vector_type(8)));
typedef short s16x4 __attribute__((ext_vector_type(4)));
typedef float f32x2 __attribute__((ext_vector_type(2)));
typedef float f32x4 __attribute__((ext_vector_type(4)));
typedef float f32x16 __attribute__((ext_vector_type(16)));
typedef unsigned u32x2 __attribute__((ext_vector_type(2)));
typedef unsigned u32x4 __attribute__((ext_vector_type(4)));
typedef __bf16 bf16x2_t __attribute__((ext_vector_type(2)));
#define LAS __attribute__((address_space(3)))
#define GAS __attribute__((address_space(1)))
typedef LAS unsigned char* lds_ptr;

constexpr int SEQ = 2048, DM = 1024, FF = 3584, NQKV = 3072, NH = 32;
constexpr float LN_EPS = 1e-5f;
constexpr float LOG2E = 1.4426950408889634f;
constexpr float QSCALE = 0.17677669529663687f * LOG2E;

constexpr unsigned WS_FLAGS = 0;
constexpr unsigned WS_STAT = 192 * 1024;
constexpr unsigned WS_QKV = 256 * 1024;
constexpr unsigned WS_ACT = WS_QKV + 3u * SEQ * DM * 2;
constexpr unsigned WS_XCH = WS_ACT + 144u * SEQ * 64;
constexpr unsigned WS_XC = WS_XCH + 256u * 32768;
constexpr unsigned WS_END = WS_XC + SEQ * DM * 2;
constexpr int F_EPOCH = 0, F_QKV = 1, F_H = 257, F_O = 513, F_X = 769, F_XC = 1025;
constexpr unsigned GRP = SEQ * 64;

#define SBAR() __builtin_amdgcn_sched_barrier(0)
#define BAR() __builtin_amdgcn_s_barrier()
#define WAIT_V(n) asm volatile("s_waitcnt vmcnt(" #n ")" ::: "memory")
#define WAIT_L0() do { __builtin_amdgcn_s_waitcnt(0xC07F); asm volatile("" ::: "memory"); } while (0)
#define CFENCE() asm volatile("" ::: "memory")
#define PIN(x) asm volatile("" : "+v"(x))
#define MFMA16(a, b, c) __builtin_amdgcn_mfma_f32_16x16x32_bf16(a, b, c, 0, 0, 0)
#define MFMA32(a, b, c) __builtin_amdgcn_mfma_f32_32x32x16_bf16(a, b, c, 0, 0, 0)
#define MFMA16_ACC(acc, a, b) asm volatile("v_mfma_f32_16x16x32_bf16 %0, %1, %2, %0" : "+v"(acc) : "v"(a), "v"(b))
#define MFMA32_ACC(acc, a, b) asm volatile("v_mfma_f32_32x32x16_bf16 %0, %1, %2, %0" : "+v"(acc) : "v"(a), "v"(b))
#define MFMA32_SET(d, a, b, c) asm volatile("v_mfma_f32_32x32x16_bf16 %0, %1, %2, %3" : "=&v"(d) : "v"(a), "v"(b), "v"(c))
#define MFMA_DRAIN() asm volatile("s_nop 15" ::: "memory")
#define RLX_AGENT __ATOMIC_RELAXED, __HIP_MEMORY_SCOPE_AGENT

#define STAMP(i) do {} while (0)
#define TS(v) do {} while (0)
#define PUT(i, v) do {} while (0)

__device__ __forceinline__ void glds16(const void* sbase, unsigned voff, unsigned ldsdst) {
    asm volatile("s_mov_b32 m0, %2\n\ts_nop 0\n\tglobal_load_lds_dwordx4 %0, %1" ::"v"(voff), "s"(sbase), "s"(ldsdst) : "memory");
}
__device__ __forceinline__ void glds16c(const void* sbase, unsigned voff, unsigned ldsdst) {
    asm volatile("s_mov_b32 m0, %2\n\ts_nop 0\n\tglobal_load_lds_dwordx4 %0, %1 sc1" ::"v"(voff), "s"(sbase), "s"(ldsdst) : "memory");
}
__device__ __forceinline__ bf16x8 lds_b128(lds_ptr p) { return *(const LAS bf16x8*)p; }
__device__ __forceinline__ s16x4 lds_tr(lds_ptr p) { return __builtin_amdgcn_ds_read_tr16_b64_v4i16((LAS s16x4*)p); }
__device__ __forceinline__ bf16x8 cat8(s16x4 a, s16x4 b) { return __builtin_shufflevector(a, b, 0, 1, 2, 3, 4, 5, 6, 7); }
__device__ __forceinline__ unsigned cvtpk(float lo, float hi) {
    f32x2 v = {lo, hi};
    return __builtin_bit_cast(unsigned, __builtin_convertvector(v, bf16x2_t));
}
__device__ __forceinline__ float u2f(unsigned u) { return __builtin_bit_cast(float, u); }
__device__ __forceinline__ unsigned f2u(float f) { return __builtin_bit_cast(unsigned, f); }
__device__ __forceinline__ float bf_lo(unsigned u) { return u2f(u << 16); }
__device__ __forceinline__ float bf_hi(unsigned u) { return u2f(u & 0xffff0000u); }
__device__ __forceinline__ void swap16(unsigned& a, unsigned& b) {
    auto r = __builtin_amdgcn_permlane16_swap(a, b, false, false);
    a = r[0]; b = r[1];
}
__device__ __forceinline__ void swap32(unsigned& a, unsigned& b) {
    auto r = __builtin_amdgcn_permlane32_swap(a, b, false, false);
    a = r[0]; b = r[1];
}
__device__ __forceinline__ int opaque_tid() { int t = threadIdx.x; asm volatile("" : "+v"(t)); return t; }
typedef unsigned long long u64;
struct Flags { u64* p; u64 tag; };
__device__ __forceinline__ void signal(const Flags& f, int word, int lane) {
    if (lane == 0) __hip_atomic_store((GAS u64*)(f.p + word * 16), f.tag, RLX_AGENT);
}
__device__ __forceinline__ u64 peek(const Flags& f, int word) {
    u64 v;
    asm volatile("global_load_dwordx2 %0, %1, off sc1\n\ts_waitcnt vmcnt(0)" : "=&v"(v) : "v"(f.p + word * 16) : "memory");
    return v;
}
__device__ __forceinline__ void wait_all(const Flags& f, int word, u64 v) {
#pragma unroll 1
    for (unsigned spin = 0; spin < (1u << 20) && !__all(v == f.tag); ++spin) { __builtin_amdgcn_s_sleep(1); v = peek(f, word); }
}
__device__ __forceinline__ void arrive(const Flags& f, int word, int lane, lds_ptr cnt, unsigned last) {
    if (lane == 0 && __hip_atomic_fetch_add((LAS unsigned*)cnt, 1u, __ATOMIC_RELAXED, __HIP_MEMORY_SCOPE_WORKGROUP) == last)
        __hip_atomic_store((GAS u64*)(f.p + word * 16), f.tag, RLX_AGENT);
}
#define PEEK_AHEAD(v, ptr, sel) asm volatile("s_cmp_lg_u32 %2, 0\n\ts_cbranch_scc1 1f\n\tglobal_load_dwordx2 %0, %1, off sc1\n1:" : "+v"(v) : "v"(ptr), "s"(sel) : "memory", "scc")
struct CodePf { const char* base; unsigned len; int first; };
#define CODE_PF(d) do { unsigned o_ = (unsigned)(cp.first + (opaque_tid() & 15)) * 128u; o_ = (o_ < cp.len) ? o_ : 0u; \
    asm volatile("global_load_dword %0, %1, %2" : "+v"(d) : "v"(o_), "s"(cp.base) : "memory"); } while (0)
#define PEEK_DONE(v) asm volatile("s_waitcnt vmcnt(0)" : "+v"(v) :: "memory")

template <int CTRL> __device__ __forceinline__ float dpp_add(float v) { return v + u2f(__builtin_amdgcn_update_dpp(0u, f2u(v), CTRL, 0xf, 0xf, true)); }
__device__ __forceinline__ float red4(float v) {
    unsigned a = f2u(v), b = a;
    swap16(a, b);
    v = u2f(a) + u2f(b);
    a = f2u(v); b = a;
    swap32(a, b);
    return u2f(a) + u2f(b);
}
__device__ __forceinline__ float wave_sum(float v) {
    v = dpp_add<0xB1>(v); v = dpp_add<0x4E>(v); v = dpp_add<0x141>(v); v = dpp_add<0x140>(v);
    return red4(v);
}
__device__ __forceinline__ f32x2 row_stats(u32x4& a, u32x4& b) {
    float v[16];
#pragma unroll
    for (int i = 0; i < 4; ++i) { v[2 * i] = bf_lo(a[i]); v[2 * i + 1] = bf_hi(a[i]); v[8 + 2 * i] = bf_lo(b[i]); v[8 + 2 * i + 1] = bf_hi(b[i]); }
    float s = 0.f;
#pragma unroll
    for (int i = 0; i < 16; ++i) s += v[i];
    const float mu = wave_sum(s) * (1.0f / DM);
    float q = 0.f;
#pragma unroll
    for (int i = 0; i < 16; ++i) { v[i] -= mu; q += v[i] * v[i]; }
#pragma unroll
    for (int i = 0; i < 4; ++i) { a[i] = cvtpk(v[2 * i], v[2 * i + 1]); b[i] = cvtpk(v[8 + 2 * i], v[8 + 2 * i + 1]); }
    return (f32x2){mu, 1.0f / __builtin_sqrtf(wave_sum(q) * (1.0f / DM) + LN_EPS)};
}

constexpr int SLOT = 32768, LDS_BYTES = 5 * SLOT;
constexpr int G1_A = 0, G1_W = 65536, G1_STAT = G1_W + 5 * 16384, G1_CS = G1_STAT + 2048, G1_CNT = G1_CS + 4 * 6 * 64;

#define NH0 30
static_assert(NH0 >= 28 && NH0 <= 32, "");
struct G2 { const char* Ah; const char* Ao; const char* Wd; const char* Wo; int nh; unsigned rowb; u64 split; };
__device__ __forceinline__ void g2_load_b(const G2& g, int j, unsigned dst, int vw, unsigned voffB) {
    const char* b = (j >= g.nh ? g.Wo : g.Wd) + (size_t)(j * 64 + vw * 8) * (DM * 2);
    glds16(b, voffB, dst + 16384 + vw * 1024); glds16(b + 128, voffB, dst + 16384 + 8192 + vw * 1024);
}
struct G2Off { unsigned A0, A1, N0, N1, B0, B1; };
__device__ __forceinline__ void g2_load_a(const G2& g, int j, unsigned dst, int vw, const G2Off& o) {
    const unsigned sp = (unsigned)(g.split >> j) & 1u;
    const char* a = (j >= g.nh ? g.Ao : g.Ah) + (size_t)j * (2 * GRP) + ((g.rowb + vw * 1024) >> sp);
    glds16c(a, sp ? o.N0 : o.A0, dst + vw * 2048); glds16c(a, sp ? o.N1 : o.A1, dst + vw * 2048 + 1024);
}
__device__ __forceinline__ void g2_voff(int lane, G2Off& o) {
    const int r8 = lane >> 3, u8 = lane & 7, ua = u8 ^ (r8 >> 1), ub = ua ^ 4;
    o.A0 = (unsigned)(ua >> 2) * GRP + r8 * 64 + (ua & 3) * 16;
    o.A1 = (unsigned)(ub >> 2) * GRP + (r8 + 8) * 64 + (ub & 3) * 16;
    o.N0 = (unsigned)(ua >> 1) * (GRP / 2) + r8 * 32 + (ua & 1) * 16;
    o.N1 = (unsigned)(ub >> 1) * (GRP / 2) + (r8 + 8) * 32 + (ub & 1) * 16;
    o.B0 = (unsigned)(r8 * (DM * 2) + (u8 ^ (r8 & 2)) * 16);
    o.B1 = (unsigned)(r8 * (DM * 2) + (u8 ^ (4 | (r8 & 2))) * 16);
}
constexpr u64 split_mask(int first, int n) { u64 m = 0; for (int j = 0; j < n; ++j) if ((first + j) % 7 == 6) m |= 1ull << j; return m; }
static_assert(((split_mask(0, NH0) | split_mask(NH0, 56 - NH0)) & 7) == 0, "phase B's tail prefetches K-tiles 0..2 as plain groups");

struct AttnOut { f32x16 o0, o1; f32x4 l0, l1; float m0, m1; bf16x8 q00, q01, q10, q11; };
struct HPend { u32x4 v[8]; unsigned off, size; const void* base; };
struct AttnLane {
    unsigned voffK, voffV; lds_ptr kb0, kb1, vb; bf16x8 ones;
    __device__ __forceinline__ void init(lds_ptr lds, int wid, int lane) {
        const int r32 = lane & 31, hi = lane >> 5, kh = wid >> 2, ksw = (r32 >> 2) & 3;
        const int uk = (lane & 3) ^ ((lane >> 4) & 3), uv = lane & 3;
        voffK = (unsigned)((uk >> 1) * 65536 + wid * 1024 + (lane >> 2) * 32 + (uk & 1) * 16);
        voffV = (unsigned)((uv >> 1) * 65536 + wid * 1024 + (lane >> 2) * 32 + (uv & 1) * 16);
        kb0 = lds + kh * 8192 + r32 * 64 + ((hi ^ ksw) * 16); kb1 = lds + kh * 8192 + r32 * 64 + (((2 | hi) ^ ksw) * 16);
        vb = lds + 16384 + kh * 8192 + (4 * hi + ((lane >> 2) & 3)) * 64 + ((lane >> 4) & 1) * 32 + (lane & 3) * 8;
        const short v = (((lane >> 4) & 1) == ((lane >> 2) & 1)) ? (short)0x3f80 : (short)0;
        ones = (bf16x8){v, v, v, v, v, v, v, v};
    }
};
#define EX(v) __builtin_amdgcn_exp2f(v)
#define AT_LOAD_K(c, slot) do { const unsigned d_ = lds0 + wid * 2048 + (slot) * SLOT; const char* k_ = Kg + (c) * 8192; glds16c(k_, A.voffK, d_); glds16c(k_ + 512, A.voffK, d_ + 1024); } while (0)
#define AT_LOAD_V(c, slot) do { const unsigned d_ = lds0 + wid * 2048 + (slot) * SLOT + 16384; const char* v_ = Vg + (c) * 8192; glds16c(v_, A.voffV, d_); glds16c(v_ + 512, A.voffV, d_ + 1024); } while (0)
#define AT_PACK(S, i) __builtin_bit_cast(bf16x8, (u32x4){cvtpk(S[i], S[i + 1]), cvtpk(S[i + 2], S[i + 3]), cvtpk(S[i + 4], S[i + 5]), cvtpk(S[i + 6], S[i + 7])})
__device__ __forceinline__ float rowmax16(const f32x16& s) {
    float a = s[0];
#pragma unroll
    for (int r = 1; r < 16; ++r) a = __builtin_fmaxf(a, s[r]);
    unsigned x = f2u(a), y = x; swap32(x, y);
    return __builtin_fmaxf(u2f(x), u2f(y));
}

__device__ __forceinline__ void attn_safe(lds_ptr lds, unsigned lds0, const char* Kg, const char* Vg, int wid, AttnOut& R) {
    AttnLane A; A.init(lds, wid, opaque_tid() & 63);
    const bf16x8 q00 = R.q00, q01 = R.q01, q10 = R.q10, q11 = R.q11;
    f32x16 o0 = (f32x16){}, o1 = (f32x16){};
    f32x4 l0 = (f32x4){0.f, 0.f, 0.f, 0.f}, l1 = l0;
    float m0 = -__builtin_inff(), m1 = m0;
#pragma unroll 1
    for (int c = 0; c < 8; ++c) {
        AT_LOAD_K(c, 3); AT_LOAD_V(c, 3);
        WAIT_V(0); BAR(); CFENCE();
#pragma unroll 1
        for (int t = 0; t < 4; ++t) {
            const int to = 3 * SLOT + t * 2048;
            const bf16x8 ka = lds_b128(A.kb0 + to), kb = lds_b128(A.kb1 + to);
            const bf16x8 va = cat8(lds_tr(A.vb + to), lds_tr(A.vb + to + 512)), vc = cat8(lds_tr(A.vb + to + 1024), lds_tr(A.vb + to + 1536));
            f32x16 s0 = MFMA32(kb, q01, MFMA32(ka, q00, (f32x16){})), s1 = MFMA32(kb, q11, MFMA32(ka, q10, (f32x16){}));
            const float n0 = __builtin_fmaxf(m0, rowmax16(s0)), n1 = __builtin_fmaxf(m1, rowmax16(s1));
            const float f0 = EX(m0 - n0), f1 = EX(m1 - n1);
            m0 = n0; m1 = n1; o0 *= f0; o1 *= f1; l0 *= f0; l1 *= f1;
#pragma unroll
            for (int r = 0; r < 16; ++r) { s0[r] = EX(s0[r] - n0); s1[r] = EX(s1[r] - n1); }
            const bf16x8 p00 = AT_PACK(s0, 0), p01 = AT_PACK(s0, 8), p10 = AT_PACK(s1, 0), p11 = AT_PACK(s1, 8);
            o0 = MFMA32(vc, p01, MFMA32(va, p00, o0)); l0 = MFMA16(A.ones, p01, MFMA16(A.ones, p00, l0));
            o1 = MFMA32(vc, p11, MFMA32(va, p10, o1)); l1 = MFMA16(A.ones, p11, MFMA16(A.ones, p10, l1));
        }
        WAIT_L0(); BAR(); CFENCE();
    }
    R.o0 = o0; R.o1 = o1; R.l0 = l0; R.l1 = l1; R.m0 = m0; R.m1 = m1;
}

__device__ __forceinline__ void attn_fast(lds_ptr lds, unsigned lds0, const char* Kg, const char* Vg, const G2& g2, const Flags& flags, int qkvword, int depword,
                                          int hsig, int c0, bool bump, const CodePf& cp, const HPend& hp, const char* wpd, const char* wpo, int prow, int wid, AttnOut& R
                                          ) {
    const int lane = opaque_tid() & 63;
    AttnLane A; A.init(lds, wid, lane);
    lds_ptr kb0 = A.kb0, kb1 = A.kb1, vb = A.vb;
    PIN(kb0); PIN(kb1); PIN(vb);
    lds_ptr kh0 = kb0 + 2 * SLOT, kh1 = kb1 + 2 * SLOT, vh = vb + 2 * SLOT;
    PIN(kh0); PIN(kh1); PIN(vh);
    const bf16x8 ones = A.ones;
    const __amdgpu_buffer_rsrc_t hr = __builtin_amdgcn_make_buffer_rsrc((void*)hp.base, 0, hp.size, 0x00020000);
    f32x16 o0 = (f32x16){}, o1 = (f32x16){}, negm0 = (f32x16){}, negm1 = (f32x16){}, sA0, sA1, sB0, sB1;
    f32x4 l0 = (f32x4){0.f, 0.f, 0.f, 0.f}, l1 = l0;
    bf16x8 kfa, kfb, va, vc = (bf16x8){}, pf = (bf16x8){};
    PIN(o0); PIN(o1); PIN(l0); PIN(l1); PIN(vc); PIN(pf); PIN(A.voffK); PIN(A.voffV);
    G2Off go; g2_voff(lane, go);
    unsigned gA0 = go.A0, gA1 = go.A1, gB = (wid & 1) ? go.B1 : go.B0;
    unsigned pfo = (unsigned)(((lane >> 3) & 1) * (DM * 2) + (lane & 7) * 128);
    const u64* hptr = flags.p + (F_H + depword) * 16;
    const u64* qptr = flags.p + qkvword * 16;
    PIN(gA0); PIN(gA1); PIN(gB); PIN(pfo); PIN(hptr); PIN(qptr);
    u64 hv = 0;
    float m0, m1;
    PEEK_AHEAD(hv, qptr, 0);
    WAIT_L0(); BAR(); SBAR();
    STAMP(15);
    const int qo = 2 * SLOT + (wid & 3) * 4096 - (wid >> 2) * 8192;
    const bf16x8 q00 = lds_b128(kb0 + qo), q10 = lds_b128(kb0 + qo + 2048), q01 = lds_b128(kb1 + qo), q11 = lds_b128(kb1 + qo + 2048);
    {
        kfa = lds_b128(kb0); kfb = lds_b128(kb1);
        sA0 = MFMA32(kfa, q00, negm0); sA1 = MFMA32(kfa, q10, negm1); sA0 = MFMA32(kfb, q01, sA0); sA1 = MFMA32(kfb, q11, sA1);
        kfa = lds_b128(kb0 + 2048); kfb = lds_b128(kb1 + 2048);
        m0 = rowmax16(sA0); m1 = rowmax16(sA1);
#pragma unroll
        for (int r = 0; r < 16; ++r) { sA0[r] -= m0; sA1[r] -= m1; negm0[r] = -m0; negm1[r] = -m1; }
        PIN(negm0); PIN(negm1);
    }
    unsigned pfd = 0;
    PEEK_DONE(hv);
    BAR(); SBAR();
    STAMP(5);
    wait_all(flags, qkvword, hv);
    if (bump && wid == 0) signal(flags, F_EPOCH, lane);
    AT_LOAD_K((c0 + 1) & 7, 1); AT_LOAD_V((c0 + 1) & 7, 1);
    STAMP(19);
#define AT_GROUP(QK, RD, S, i, O, L, VF) do { \
        QK; SBAR(); RD; SBAR(); \
        float e0, e1, e2, e3, e4, e5, e6, e7; \
        asm volatile("v_exp_f32 %0, %13\n\tv_exp_f32 %1, %14\n\tv_exp_f32 %2, %15\n\tv_mfma_f32_32x32x16_bf16 %8, %10, %11, %8\n\t" \
                     "v_exp_f32 %3, %16\n\tv_exp_f32 %4, %17\n\tv_exp_f32 %5, %18\n\tv_mfma_f32_16x16x32_bf16 %9, %12, %11, %9\n\tv_exp_f32 %6, %19\n\tv_exp_f32 %7, %20" \
            : "=&v"(e0), "=&v"(e1), "=&v"(e2), "=&v"(e3), "=&v"(e4), "=&v"(e5), "=&v"(e6), "=&v"(e7), "+v"(O), "+v"(L) \
            : "v"(VF), "v"(pf), "v"(ones), "v"(S[i]), "v"(S[i + 1]), "v"(S[i + 2]), "v"(S[i + 3]), "v"(S[i + 4]), "v"(S[i + 5]), "v"(S[i + 6]), "v"(S[i + 7])); \
        pf = __builtin_bit_cast(bf16x8, (u32x4){cvtpk(e0, e1), cvtpk(e2, e3), cvtpk(e4, e5), cvtpk(e6, e7)}); SBAR(); } while (0)
#define LO(lo, hi, off) (((off) >= 2 * SLOT ? hi : lo) + ((off) & (2 * SLOT - 1)))
#define AT_STEP(C0, C1, N0, N1, QK, KR, KO, VO, H0, H1, H2, H3) do { \
        AT_GROUP(if (QK) MFMA32_SET(N0, kfa, q00, negm0), { va = cat8(lds_tr(LO(vb, vh, VO)), lds_tr(LO(vb, vh, VO) + 512)); H0; }, C0, 0, o1, l1, vc); \
        AT_GROUP(if (QK) MFMA32_SET(N1, kfa, q10, negm1), { if (KR) kfa = lds_b128(LO(kb0, kh0, KO)); vc = cat8(lds_tr(LO(vb, vh, VO) + 1024), lds_tr(LO(vb, vh, VO) + 1536)); H1; }, C1, 0, o0, l0, va); \
        AT_GROUP(if (QK) MFMA32_ACC(N0, kfb, q01), H2, C0, 8, o1, l1, va); \
        AT_GROUP(if (QK) MFMA32_ACC(N1, kfb, q11), { if (KR) kfb = lds_b128(LO(kb1, kh1, KO)); H3; }, C1, 8, o0, l0, vc); } while (0)
#define PRV 1
#define YP(p) asm volatile("s_cmp_lt_u32 %0, 4\n\ts_cbranch_scc1 1f\n\ts_setprio " #p "\n1:" :: "s"(wid) : "scc")
    if (PRV) __builtin_amdgcn_s_setprio(1);
    auto ylow = [&]() { if (PRV) YP(0); };
#define S0(c, ...) AT_STEP(sA0, sA1, sB0, sB1, true, true, ((c) & 3) * SLOT + 4096, ((c) & 3) * SLOT, __VA_ARGS__)
#define S1(c, ...) if (PRV == 1) YP(0); AT_STEP(sB0, sB1, sA0, sA1, true, true, ((c) & 3) * SLOT + 6144, ((c) & 3) * SLOT + 2048, __VA_ARGS__)
#define S2(c, ...) if (PRV == 1 || (PRV == 2 && (c) == 7)) YP(2); AT_STEP(sA0, sA1, sB0, sB1, true, (c) < 7, (((c) + 1) & 3) * SLOT, ((c) & 3) * SLOT + 4096, __VA_ARGS__)
#define S3(c, ...) AT_STEP(sB0, sB1, sA0, sA1, (c) < 7, (c) < 7, (((c) + 1) & 3) * SLOT + 2048, ((c) & 3) * SLOT + 6144, __VA_ARGS__)
#define SY(c, n) do { WAIT_V(n); WAIT_L0(); BAR(); SBAR(); STAMP(20 + (c)); } while (0)
#define NO (void)0
#define KP(c, i) glds16c(Kg + ((c0 + (c)) & 7) * 8192 + (i) * 512, A.voffK, lds0 + wid * 2048 + ((c) & 3) * SLOT + (i) * 1024)
#define VP(c, i) glds16c(Vg + ((c0 + (c)) & 7) * 8192 + (i) * 512, A.voffV, lds0 + wid * 2048 + ((c) & 3) * SLOT + 16384 + (i) * 1024)
#define CH(c) KP(c, 0), KP(c, 1), VP(c, 0), VP(c, 1)
#define HS(i) do { __builtin_amdgcn_raw_buffer_store_b128(hp.v[i], hr, hp.off + (i) * 1024, 0, 16); CFENCE(); } while (0)
#define WP(i) wpf(i)
#define GB(j, slot, i) glds16(g2.Wd + (size_t)((j) * 64 + wid * 8) * (DM * 2) + (i) * 128, gB, lds0 + (slot) * SLOT + 16384 + (i) * 8192 + wid * 1024)
#define GA(j, slot, i) glds16c(g2.Ah + (size_t)(j) * (2 * GRP) + g2.rowb + wid * 1024, (i) ? gA1 : gA0, lds0 + (slot) * SLOT + wid * 2048 + (i) * 1024)
    auto wpf = [&](int i) {
        const int pi = (wid + 8 * i < g2.nh + 8) ? wid + 8 * i : 0, lr = prow + 2 * pi;
        asm volatile("global_load_dword %0, %1, %2" : "+v"(pfd) : "v"(pfo), "s"((lr < g2.nh * 64 ? wpd : wpo) + (size_t)lr * (DM * 2)) : "memory");
    };
    auto cpf = [&]() { CODE_PF(pfd); };
    auto hpeek = [&]() { PEEK_AHEAD(hv, hptr, wid); };
    S0(0, NO, NO, NO, NO);         S1(0, NO, NO, NO, NO);    SY(0, 0);  S2(0, CH(2)); S3(0, CH(3));
    S0(1, WP(0), NO, HS(0), NO);   S1(1, NO, NO, HS(1), NO); SY(1, 7);  S2(1, CH(4)); S3(1, WP(1), NO, HS(2), NO);
    S0(2, NO, NO, HS(3), NO);      S1(2, NO, NO, HS(4), NO); SY(2, 11); S2(2, CH(5)); S3(2, WP(2), NO, HS(5), NO);
    S0(3, NO, NO, HS(6), NO);      S1(3, NO, NO, HS(7), NO); SY(3, 12); S2(3, CH(6)); S3(3, WP(3), NO, NO, NO);
    S0(4, NO, NO, NO, NO);         S1(4, NO, NO, NO, NO);    SY(4, 5);
    if (wid == 0) signal(flags, hsig, lane);
                                                                        S2(4, CH(7)); S3(4, WP(4), NO, NO, NO);
    S0(5, GB(0, 4, 0), GB(0, 4, 1), NO, NO); S1(5, NO, NO, NO, NO); SY(5, 2); S2(5, GB(1, 0, 0), GB(1, 0, 1), NO, NO); S3(5, NO, NO, NO, NO);
    S0(6, hpeek(), NO, NO, NO);
    if (wid == 0) { PEEK_DONE(hv); wait_all(flags, F_H + depword, hv); }
                                   S1(6, NO, NO, NO, NO);    SY(6, 0);  S2(6, GA(0, 4, 0), GA(0, 4, 1), GB(2, 1, 0), GB(2, 1, 1)); S3(6, GA(1, 0, 0), GA(1, 0, 1), cpf(), NO);
    S0(7, GA(2, 1, 0), GA(2, 1, 1), NO, NO); S1(7, NO, NO, NO, NO); SY(7, 0); S2(7, NO, NO, NO, NO); S3(7, NO, NO, ylow(), NO);
    __builtin_amdgcn_s_setprio(0);
#undef S0
#undef S1
#undef S2
#undef S3
#undef NO
    STAMP(28);
    asm volatile("s_nop 1" ::: "memory");
    MFMA32_ACC(o1, vc, pf); MFMA16_ACC(l1, ones, pf);
    MFMA_DRAIN(); SBAR();
    asm volatile("" :: "v"(pfd));
    R.o0 = o0; R.o1 = o1; R.l0 = l0; R.l1 = l1; R.m0 = m0; R.m1 = m1; R.q00 = q00; R.q01 = q01; R.q10 = q10; R.q11 = q11;
}

__global__ __launch_bounds__(512, 2) void k_main(const bf16_t* __restrict__ x, const bf16_t* __restrict__ wqkv, const bf16_t* __restrict__ wo, const bf16_t* __restrict__ wg,
                                                 const bf16_t* __restrict__ wu, const bf16_t* __restrict__ wd, bf16_t* __restrict__ out, unsigned char* ws) {
    __shared__ __attribute__((aligned(16))) unsigned char smem[LDS_BYTES];
    const lds_ptr lds = (lds_ptr)smem;
    const unsigned lds0 = (unsigned)(uintptr_t)lds;
    const int wid = __builtin_amdgcn_readfirstlane(threadIdx.x >> 6);
    const int bid = blockIdx.x, xcd = bid & 7, idx = bid >> 3;
    Flags flags; flags.p = (u64*)(ws + WS_FLAGS);
    const __amdgpu_buffer_rsrc_t wsr = __builtin_amdgcn_make_buffer_rsrc((void*)ws, 0, WS_END, 0x00020000);
#define ST16(v, off) __builtin_amdgcn_raw_buffer_store_b128(v, wsr, off, 0, 16)
    STAMP(0);
    unsigned cpd = 0;
    CodePf cp;
    asm volatile("s_getpc_b64 %0\n.Lk_begin:\n\ts_mov_b32 %1, .Lk_end-.Lk_begin" : "=s"(cp.base), "=s"(cp.len));
    cp.first = idx * 16;
    HPend hp;
    {
        const int lane = threadIdx.x & 63, wr = wid >> 2, wc = wid & 3, js = idx & 3, rb = idx >> 2;
        const bool typeY = (js == 3);
        const int f0 = 448 * xcd + 128 * js;
        const char* P0 = (const char*)(wg + f0);
        const char* P2 = (const char*)(wu + f0);
        const char* P1 = typeY ? (const char*)(wqkv + 2048 + 128 * xcd) : (const char*)(wg + f0 + 64);
        const char* P3 = typeY ? (const char*)(wqkv + 2048 + 128 * xcd + 64) : (const char*)(wu + f0 + 64);
        const char* P4 = (const char*)(wqkv + 128 * xcd + 32 * js);
        const unsigned stepF = 32u * FF * 2u, stepQ = 32u * NQKV * 2u, step13 = typeY ? stepQ : stepF;
        const char* XA = (const char*)x + (size_t)rb * 256 * DM * 2;
        const char* XC = (const char*)ws + WS_XC + (size_t)rb * 256 * DM * 2;
        const int r8 = lane >> 3, u8 = lane & 7;
        const int gw = (r8 & 2) | ((wc & 1) << 2);
        const unsigned voffF = (unsigned)(((wc * 8 + r8) * FF + (u8 ^ gw) * 8) * 2);
        const unsigned voffQ = (unsigned)(((wc * 8 + r8) * NQKV + ((u8 ^ gw) >> 2) * 1024 + ((u8 ^ gw) & 3) * 8) * 2);
        const unsigned voff13 = typeY ? (unsigned)(((wc * 8 + r8) * NQKV + (u8 ^ gw) * 8) * 2) : voffF;
        const unsigned voffA0 = (unsigned)(((32 * wid + r8) * DM + (u8 ^ (r8 >> 1)) * 8) * 2);
        const unsigned voffA1 = (unsigned)(((32 * wid + 8 + r8) * DM + (u8 ^ (4 | (r8 >> 1))) * 8) * 2);
        const unsigned dstA = lds0 + G1_A + wid * 4096, dstW = lds0 + G1_W + wc * 1024;
#define G1_LOAD_A(t) do { const char* b_ = XA + (t) * 128; const unsigned d_ = dstA + ((t) & 1) * 32768; \
        glds16(b_, voffA0, d_); glds16(b_, voffA1, d_ + 1024); glds16(b_ + 32768, voffA0, d_ + 2048); glds16(b_ + 32768, voffA1, d_ + 3072); } while (0)
#define G1_LOAD_W(hs) do { const unsigned d_ = dstW + ((hs) & 3) * 4096; \
        glds16(P0 + (size_t)(hs) * stepF, voffF, d_); glds16(P1 + (size_t)(hs) * step13, voff13, d_ + 16384); glds16(P2 + (size_t)(hs) * stepF, voffF, d_ + 32768); \
        glds16(P3 + (size_t)(hs) * step13, voff13, d_ + 49152); glds16(P4 + (size_t)(hs) * stepQ, voffQ, d_ + 65536); } while (0)
        STAMP(29);
        if (wr == 1) G1_LOAD_W(0);
        G1_LOAD_A(0);
        u32x4 xa0, xa1;
        const int srow = rb * 256 + (js * 8 + xcd) * 8 + wid;
        asm volatile("global_load_dwordx4 %0, %2, %3\n\tglobal_load_dwordx4 %1, %2, %3 offset:1024"
                     : "=&v"(xa0), "=&v"(xa1) : "v"((unsigned)(lane * 16)), "s"((const char*)x + (size_t)srow * DM * 2) : "memory");
        CODE_PF(cpd);
        STAMP(30);

        int fr = lane & 15, fq = lane >> 4;
        const lds_ptr aB0 = lds + G1_A + (wr * 128 + fr) * 128 + ((fq ^ (fr >> 1)) * 16);
        const lds_ptr aB1 = lds + G1_A + (wr * 128 + fr) * 128 + (((4 | fq) ^ (fr >> 1)) * 16);
        const int tq = (lane >> 2) & 3, tp = lane & 3, tg = (tq & 2) | ((fq & 1) << 2);
        auto wbase = [&](int panel, int bp) { return lds + G1_W + panel * 16384 + (8 * fq + tq) * 128 + (((2 * bp + (tp >> 1)) ^ tg) * 16) + (tp & 1) * 8; };
        const int pX = wc >> 1, bX = 2 * (wc & 1);
        const lds_ptr wB0 = typeY ? wbase(0, wc) : wbase(pX, bX);
        const lds_ptr wB1 = typeY ? wbase(1, wc) : wbase(pX, bX + 1);
        const lds_ptr wB2 = typeY ? wbase(2, wc) : wbase(2 + pX, bX);
        const lds_ptr wB3 = typeY ? wbase(3, wc) : wbase(2 + pX, bX + 1);
        const lds_ptr wB4 = wbase(4, wc);
        if (threadIdx.x == 0) *(LAS unsigned*)(lds + G1_CNT) = 0u;
        f32x4 acc[8][5];
#pragma unroll
        for (int m = 0; m < 8; ++m)
#pragma unroll
            for (int b = 0; b < 5; ++b) { acc[m][b] = (f32x4){0.f, 0.f, 0.f, 0.f}; PIN(acc[m][b]); }
        if (wr == 1) { WAIT_V(7); BAR(); }
        asm volatile("s_waitcnt vmcnt(1)" : "+v"(xa0), "+v"(xa1) :: "memory");
        STAMP(31);
        u64 sg;
        {
            const f32x2 st = row_stats(xa0, xa1);
            G1_LOAD_W(1 + wr);
            ST16(xa0, (lane >= 32) ? WS_XC + (unsigned)srow * 2048u + lane * 16 : WS_END);
            ST16(xa1, WS_XC + (unsigned)srow * 2048u + 1024 + lane * 16);
            __builtin_amdgcn_raw_buffer_store_b64(__builtin_bit_cast(u32x2, st), wsr, (lane == 0) ? WS_STAT + (unsigned)srow * 8u : WS_END, 0, 16);
            asm volatile("global_load_dwordx2 %0, %1, %2 sc1" : "=&v"(sg) : "v"(0u), "s"(flags.p) : "memory");
        }
        STAMP(32);
        bf16x8 af[8], wf[5];
        float cs0 = 0.f, cs1 = 0.f, cs2 = 0.f;
#define CS1(c, w) "v_dot2c_f32_bf16 %" #c ", %25, %" #w "\n\t"
#define G1_CSUM(t, lim) do { const u32x4 w0_ = __builtin_bit_cast(u32x4, wf[0]), w1_ = __builtin_bit_cast(u32x4, wf[1]), w2_ = __builtin_bit_cast(u32x4, wf[2]), \
            w3_ = __builtin_bit_cast(u32x4, wf[3]), w4_ = __builtin_bit_cast(u32x4, wf[4]); \
        asm volatile("s_cmp_gt_u32 %24, %26\n\ts_cbranch_scc1 2f\n\ts_cmp_lg_u32 %23, 0\n\ts_cbranch_scc1 1f\n\t" \
            CS1(0, 3) CS1(1, 7) CS1(2, 19) CS1(0, 4) CS1(1, 8) CS1(2, 20) CS1(0, 5) CS1(1, 9) CS1(0, 6) CS1(1, 10) "s_nop 1\n\ts_branch 2f\n1:\n\t" \
            CS1(0, 11) CS1(1, 15) CS1(2, 21) CS1(0, 12) CS1(1, 16) CS1(2, 22) CS1(0, 13) CS1(1, 17) CS1(0, 14) CS1(1, 18) "s_nop 2\n2:" \
            : "+v"(cs0), "+v"(cs1), "+v"(cs2) \
            : "v"(w0_[0]), "v"(w0_[1]), "v"(w0_[2]), "v"(w0_[3]), "v"(w1_[0]), "v"(w1_[1]), "v"(w1_[2]), "v"(w1_[3]), "v"(w2_[0]), "v"(w2_[1]), "v"(w2_[2]), "v"(w2_[3]), \
              "v"(w3_[0]), "v"(w3_[1]), "v"(w3_[2]), "v"(w3_[3]), "v"(w4_[0]), "v"(w4_[1]), "v"(w4_[2]), "v"(w4_[3]), "s"(wr), "s"(t), "s"(0x3f803f80u), "i"(lim) : "scc"); } while (0)
#define G1_RW(i, B, slot) wf[i] = cat8(lds_tr(B + (slot) * 4096), lds_tr(B + (slot) * 4096 + 512))
#define G1_RA(m, AB, abuf) af[m] = lds_b128(AB + (abuf) * 32768 + (m) * 2048)
#define G1_SEG(AB, abuf, slot, L0, L1, L2, L3, L4, CLOSE) do { \
        L0; G1_RW(0, wB0, slot); G1_RW(1, wB1, slot); L1; G1_RW(2, wB2, slot); G1_RW(3, wB3, slot); L2; G1_RW(4, wB4, slot); G1_RA(0, AB, abuf); G1_RA(1, AB, abuf); \
        L3; G1_RA(2, AB, abuf); G1_RA(3, AB, abuf); G1_RA(4, AB, abuf); L4; G1_RA(5, AB, abuf); G1_RA(6, AB, abuf); G1_RA(7, AB, abuf); \
        SBAR(); G1_CSUM(t, 3); WAIT_L0(); SBAR(); if (CLOSE) { BAR(); SBAR(); } } while (0)
#define G1_SEG_A(abuf, slot, tt, LIVE) do { const char* b_ = ((tt) < 4 ? XA : XC) + (tt) * 128; const unsigned d_ = (LIVE) ? dstA + (1 - (abuf)) * 32768 : lds0 + LDS_BYTES; \
        G1_SEG(aB0, abuf, slot, glds16c(b_, voffA0, d_), glds16c(b_, voffA1, d_ + 1024), glds16c(b_ + 32768, voffA0, d_ + 2048), glds16c(b_ + 32768, voffA1, d_ + 3072), (void)0, true); } while (0)
#define G1_SEG_W(abuf, slot, hs, dslot, LIVE, CLOSE) do { const unsigned d_ = (LIVE) ? dstW + (dslot) * 4096 : lds0 + LDS_BYTES; \
        G1_SEG(aB1, abuf, slot, glds16(P0 + (size_t)(hs) * stepF, voffF, d_), glds16(P1 + (size_t)(hs) * step13, voff13, d_ + 16384), glds16(P2 + (size_t)(hs) * stepF, voffF, d_ + 32768), \
               glds16(P3 + (size_t)(hs) * step13, voff13, d_ + 49152), glds16(P4 + (size_t)(hs) * stepQ, voffQ, d_ + 65536), CLOSE); } while (0)
#define EB 2
#define FIRST_WAIT() asm volatile("s_waitcnt vmcnt(4)" : "+v"(sg), "+v"(cpd) :: "memory")
#define G1_MM(i0, i1) _Pragma("unroll") for (int i = (i0); i < (i1); ++i) { const int b = i >> 3, m = (b & 1) ? 7 - (i & 7) : (i & 7); MFMA16_ACC(acc[m][b], wf[b], af[m]); }
#define G1_MMA(CLOSE) do { __builtin_amdgcn_s_setprio(1); G1_MM(0, 40 - EB); SBAR(); __builtin_amdgcn_s_setprio(2); CLOSE; SBAR(); G1_MM(40 - EB, 40); __builtin_amdgcn_s_setprio(0); SBAR(); } while (0)

        STAMP(33);
        WAIT_L0();
        STAMP(34);
        BAR();
        SBAR();
        STAMP(1);
        flags.tag = 0;
        const u64* sgp = flags.p + (F_XC + rb * 32 + (lane & 31)) * 16;
#pragma unroll 1
        for (int t = 0; t < 16; t += 2) {
            const int h1 = (2 * t + 3 + wr < 31) ? 2 * t + 3 + wr : 31, h2 = (2 * t + 5 + wr < 31) ? 2 * t + 5 + wr : 31, t2 = (t < 14) ? t + 2 : 15;
            const int sel = (t == 12) ? 0 : ((t ^ 2) | wid);
            STAMP(35 + (t >> 1));
            PEEK_AHEAD(sg, sgp, sel);
            if (t == 14) CODE_PF(cpd);
            G1_SEG_A(0, 0, t + 1, true); G1_MMA({ FIRST_WAIT(); BAR(); });
            if (t == 0) {
                const unsigned elo = __builtin_amdgcn_readfirstlane((unsigned)sg), ehi = __builtin_amdgcn_readfirstlane((unsigned)(sg >> 32));
                flags.tag = (((u64)ehi << 32) | elo) * 0x9E3779B97F4A7C15ull + 0x632BE59BD9B4E019ull;
                if (wid == 4) signal(flags, F_XC + bid, lane);
            }
            if (sel == 0) {
                if (t == 12) *(LAS u64*)(lds + G1_STAT + (wid * 32 + (opaque_tid() & 31)) * 8) = sg;
                else wait_all(flags, F_XC + rb * 32 + (opaque_tid() & 31), sg);
            }
            G1_SEG_W(0, 1, h1, (3 + wr) & 3, t + wr < 15, true); G1_MMA({ WAIT_V(5); BAR(); });
            G1_SEG_A(1, 2, t2, t < 14); G1_MMA({ WAIT_V(4); BAR(); });
            G1_SEG_W(1, 3, h2, (1 + wr) & 3, t < 14, false);
            if (t == 2) {
                const int tl = opaque_tid() & 63;
                const lds_ptr cp = lds + G1_CS + wc * 384 + (tl & 15) * 4;
                *(LAS float*)(cp + wr * 128) = red4(cs0); *(LAS float*)(cp + wr * 128 + 64) = red4(cs1); *(LAS float*)(cp + 256 + wr * 64) = red4(cs2);
                sgp = (const u64*)(ws + WS_STAT) + rb * 256 + wid * 32 + (tl & 31);
                WAIT_L0();
            }
            SBAR(); BAR(); SBAR();
            G1_MMA(asm volatile("s_waitcnt vmcnt(5)\n\ts_cmp_eq_u32 %0, 15\n\ts_cbranch_scc1 1f\n\ts_barrier\n1:" :: "s"(t + wr) : "memory", "scc"));
        }
        MFMA_DRAIN(); SBAR();
        STAMP(2);

        const int tz = opaque_tid(), ln = tz & 63;
        fr = tz & 15; fq = (tz >> 4) & 3;
        const int row0 = rb * 256 + wr * 128 + fr;
        f32x4 csv[5]; f32x2 st[8];
#pragma unroll
        for (int b = 0; b < 5; ++b) csv[b] = *(const LAS f32x4*)(lds + G1_CS + wc * 384 + b * 64 + fq * 16);
        csv[4] += *(const LAS f32x4*)(lds + G1_CS + wc * 384 + 320 + fq * 16);
#pragma unroll
        for (int m = 0; m < 8; ++m) st[m] = *(const LAS f32x2*)(lds + G1_STAT + (wr * 128 + m * 16 + fr) * 8);
        auto lnv = [&](int m, int b, float sc) { return (acc[m][b] - st[m].x * csv[b]) * (st[m].y * sc); };
        auto glu = [&](int m, int bg, int bu) {
            const f32x4 g = lnv(m, bg, -LOG2E), u = lnv(m, bu, 1.0f);
            f32x4 h;
#pragma unroll
            for (int j = 0; j < 4; ++j) h[j] = u[j] * __builtin_amdgcn_rcpf(1.0f + __builtin_amdgcn_exp2f(g[j]));
            return h;
        };
        auto pack2 = [&](const f32x4& v0, const f32x4& v1) {
            unsigned p0 = cvtpk(v0[0], v0[1]), p1 = cvtpk(v0[2], v0[3]), q0 = cvtpk(v1[0], v1[1]), q1 = cvtpk(v1[2], v1[3]);
            swap16(p0, q0); swap16(p1, q1);
            return (u32x4){p0, p1, q0, q1};
        };
#define YM 4
        constexpr unsigned KIND = NH * 2 * 65536u;
        const unsigned roff = WS_QKV + (wc & 1) * 65536u + (unsigned)(row0 + (fq & 1) * 16) * 32u + (fq >> 1) * 16;
        if (typeY) {
            const unsigned voff = roff + 2 * KIND + (unsigned)(4 * xcd + (wc >> 1)) * 131072u;
#pragma unroll
            for (int m = 0; m < 8; m += 2) {
                ST16(pack2(lnv(m, 1, 1.0f), lnv(m + 1, 1, 1.0f)), voff + m * 512);
                ST16(pack2(lnv(m, 3, 1.0f), lnv(m + 1, 3, 1.0f)), voff + 2 * 131072 + m * 512);
            }
            CFENCE();
        }
        {
            const float sc4 = (wc < 2) ? QSCALE : 1.0f;
            const lds_ptr ip = lds + (wc < 2 ? 2 * SLOT : 0) + (wr * 128 + (fq & 1) * 16 + fr) * 64 + ((((wc & 1) * 2 + (fq >> 1)) ^ (fr >> 2)) * 16);
            const unsigned koff = (wc < 2) ? WS_END : roff + KIND + (unsigned)(4 * xcd + js) * 131072u;
#pragma unroll
            for (int m = 0; m < 8; m += 2) {
                const u32x4 v = pack2(lnv(m, 4, sc4), lnv(m + 1, 4, sc4));
                ST16(v, koff + m * 512);
                *(LAS u32x4*)(ip + m * 1024) = v;
            }
            CFENCE();
        }
#define XM 4
#define XS 6
        const int vword = F_QKV + xcd + 8 * (rb * 4 + 3);
        const u64* vsp = flags.p + vword * 16;
        u64 vs = 0;
        auto first_v = [&]() {
            PEEK_DONE(vs); wait_all(flags, vword, vs);
            const unsigned vv = (unsigned)(((ln & 3) >> 1) * 65536 + wid * 1024 + (ln >> 2) * 32 + (ln & 1) * 16);
            const char* vg = (const char*)ws + WS_QKV + (size_t)(2 * NH + 4 * xcd + js) * 131072 + rb * 8192;
            glds16c(vg, vv, lds0 + 16384 + wid * 2048); glds16c(vg + 512, vv, lds0 + 16384 + wid * 2048 + 1024);
        };
        if (!typeY) {
            hp.base = ws + WS_ACT + (unsigned)(14 * xcd + 4 * js + wc) * GRP + (unsigned)(rb * 256 + wr * 128) * 64u;
            hp.off = fr * 64 + (fq & 1) * 32 + (fq >> 1) * 16; hp.size = 8192;
#pragma unroll
            for (int m = 0; m < 8; ++m) {
                hp.v[m] = pack2(glu(m, 0, 2), glu(m, 1, 3));
                if (m == XM) { PIN(hp.v[XM]); WAIT_V(0); arrive(flags, F_QKV + bid, ln, lds + G1_CNT, 7u); }
                if (m == XS) { PIN(hp.v[XS]); PEEK_AHEAD(vs, vsp, 0); SBAR(); }
                if (m == XS + 1) { PIN(hp.v[XS + 1]); SBAR(); first_v(); }
            }
        } else {
            hp.base = ws + WS_ACT + (unsigned)(14 * xcd + 12 + (wc >> 1)) * GRP + (wc & 1) * (GRP / 2) + (unsigned)(rb * 256 + wr * 128) * 32u;
            hp.off = (fr + (fq & 1) * 16) * 32 + (fq >> 1) * 16; hp.size = 4096;
#pragma unroll
            for (int m = 0; m < 8; m += 2) {
                hp.v[m >> 1] = pack2(glu(m, 0, 2), glu(m + 1, 0, 2)); hp.v[4 + (m >> 1)] = hp.v[m >> 1];
                if (m == YM) { PIN(hp.v[YM >> 1]); WAIT_V(0); arrive(flags, F_QKV + bid, ln, lds + G1_CNT, 7u); }
            }
            PIN(hp.v[3]); first_v();
        }
#pragma unroll
        for (int i = 0; i < 8; ++i) PIN(hp.v[i]);
        STAMP(3);
    }

    const int z = xcd & 1, rbk = ((xcd >> 1) & 1) * 8 + (idx & 7), cbk = (xcd >> 2) * 4 + (idx >> 3);
    G2 g2;
    g2.nh = z ? 56 - NH0 : NH0;
    g2.rowb = rbk * 128 * 64;
    g2.split = z ? split_mask(NH0, 56 - NH0) : split_mask(0, NH0);
    g2.Ah = (const char*)ws + WS_ACT + (size_t)(z * 2 * NH0) * GRP;
    g2.Ao = (const char*)ws + WS_ACT + (size_t)(112 + z * 16 - 2 * g2.nh) * GRP;
    g2.Wd = (const char*)(wd + (size_t)z * NH0 * 64 * DM + cbk * 128);
    g2.Wo = (const char*)(wo + (size_t)z * 512 * DM + cbk * 128) - (size_t)g2.nh * 64 * DM * 2;
    {
        const int hl = idx & 3, qb = idx >> 2, h = 4 * xcd + hl;
        const int l0_ = opaque_tid() & 63;
        const int qkvword = F_QKV + xcd + 8 * ((l0_ & 7) * 4 + ((l0_ & 8) ? 3 : hl));
        const int odep = (rbk >> 1) * 32 + ((l0_ >> 2) & 3) * 8 + 4 * z + (l0_ & 3);
        const int depword = (l0_ & 16) ? (odep & ~7) + 4 : odep;
        const char* Kg = (const char*)ws + WS_QKV + (size_t)(NH + h) * 131072;
        const char* Vg = (const char*)ws + WS_QKV + (size_t)(2 * NH + h) * 131072;
        const char* wpd = g2.Wd - (cbk & 3) * 256;
        const char* wpo = g2.Wo - (cbk & 3) * 256;
        AttnOut R;
#define DG
        attn_fast(lds, lds0, Kg, Vg, g2, flags, qkvword, depword, F_H + bid, qb, bid == 0, cp, hp, wpd, wpo, idx * 2 * (g2.nh + 8), wid, R DG);
        STAMP(6);
        const int tz = opaque_tid(), ln = tz & 63, w4 = wid & 3, fr = ln & 15, fq = ln >> 4;
        lds_ptr po0 = lds + 2 * SLOT + w4 * 4096 + ln * 16, po1 = lds + 2 * SLOT + 16384 + w4 * 2048 + ln * 16;
        PIN(po0); PIN(po1);
        auto po = [&](int i) { return i < 4 ? po0 + i * 1024 : po1 + (i >> 1 & 1) * 8192 + (i & 1) * 1024; };
        const lds_ptr pl = po0 + SLOT;
        volatile LAS unsigned* const badw = (volatile LAS unsigned*)(lds + 3 * SLOT + 1024);
        const int wm = w4 >> 1, wn = w4 & 1;
        const unsigned aO0 = (wm * 64 + fr) * 128 + ((fq ^ (fr >> 1)) * 16), aO1 = aO0 ^ 64;
        const int tq = (ln >> 2) & 3, tp = ln & 3, tg = (tq & 2) | ((fq & 1) << 2);
        unsigned wO[4];
#pragma unroll
        for (int b = 0; b < 4; ++b) wO[b] = 16384 + wn * 8192 + (8 * fq + tq) * 128 + (((2 * b + (tp >> 1)) ^ tg) * 16) + (tp & 1) * 8;
        f32x4 acc[4][4];
        bf16x8 xa[4], ya[4]; s16x4 xl[4], xh[4], yl[4], yh[4];
        lds_ptr pa0, pa1, pw0, pw1, pw2, pw3;
        int so = 4 * SLOT;
        auto handover = [&]() {
            if (wid < 4) {
#pragma unroll
                for (int i = 0; i < 4; ++i) {
                    *(LAS f32x4*)po(i) = (f32x4){R.o0[4 * i], R.o0[4 * i + 1], R.o0[4 * i + 2], R.o0[4 * i + 3]};
                    *(LAS f32x4*)po(4 + i) = (f32x4){R.o1[4 * i], R.o1[4 * i + 1], R.o1[4 * i + 2], R.o1[4 * i + 3]};
                }
                *(LAS f32x4*)pl = (f32x4){R.l0[0], R.m0, R.l1[0], R.m1};
                xl[0] = lds_tr(pw0); xh[0] = lds_tr(pw0 + 512);
#pragma unroll
                for (int m = 0; m < 4; ++m) xa[m] = lds_b128(pa0 + m * 2048);
                xl[1] = lds_tr(pw1); xh[1] = lds_tr(pw1 + 512); xl[2] = lds_tr(pw2); xh[2] = lds_tr(pw2 + 512); xl[3] = lds_tr(pw3); xh[3] = lds_tr(pw3 + 512);
#pragma unroll
                for (int m = 0; m < 4; ++m)
#pragma unroll
                    for (int b = 0; b < 4; ++b) { acc[m][b] = (f32x4){0.f, 0.f, 0.f, 0.f}; PIN(acc[m][b]); }
            }
        };
        G2Off go; g2_voff(ln, go);
        PIN(go.A0); PIN(go.A1); PIN(go.N0); PIN(go.N1); PIN(go.B0); PIN(go.B1);
        pa0 = lds + aO0 + so; pa1 = lds + aO1 + so; pw0 = lds + wO[0] + so; pw1 = lds + wO[1] + so; pw2 = lds + wO[2] + so; pw3 = lds + wO[3] + so;
        PIN(pa0); PIN(pa1); PIN(pw0); PIN(pw1); PIN(pw2); PIN(pw3);
        {
            handover();
            const f32x16 z16 = R.o0 * 0.0f + R.o1 * 0.0f;
            const f32x4 z4 = (z16.s0123 + z16.s4567) + (z16.s89ab + z16.scdef);
            const f32x2 z2 = z4.lo + z4.hi;
            const float zs = z2.x + z2.y;
            const bool ok = (zs == 0.f) && (R.l0[0] > 0.f) && (R.l0[0] < 0x1p96f) && (R.l1[0] > 0.f) && (R.l1[0] < 0x1p96f);
            const bool wave_bad = !__all(ok);
            if (ln == 0) badw[wid] = wave_bad ? 1u : 0u;
            STAMP(16);
            WAIT_L0(); BAR(); CFENCE();
            const unsigned anybad = badw[ln & 7];
            if (__builtin_expect(__any(anybad != 0u), 0)) {
                WAIT_L0(); BAR(); CFENCE();
                attn_safe(lds, lds0, Kg, Vg, wid, R);
                handover();
                WAIT_L0(); BAR(); CFENCE();
            }
        }
        STAMP(7);

        const int tile = rbk * 8 + cbk, nt = g2.nh + 8;
        if (__builtin_expect(wid >= 4, 0)) {
#define G2_LOAD0(j, sl) do { const unsigned d_ = lds0 + (sl) * SLOT; g2_load_b(g2, j, d_, 2 * w4, go.B0); g2_load_a(g2, j, d_, 2 * w4, go); } while (0)
#define G2_LOAD1(j, sl) do { const unsigned d_ = lds0 + (sl) * SLOT; g2_load_b(g2, j, d_, 2 * w4 + 1, go.B1); g2_load_a(g2, j, d_, 2 * w4 + 1, go); } while (0)
#define G2_LOAD(j, sl) do { G2_LOAD0(j, sl); G2_LOAD1(j, sl); } while (0)
            const f32x4 lm = *(const LAS f32x4*)pl;
            f32x4 t[8];
#pragma unroll
            for (int i = 0; i < 8; ++i) t[i] = *(const LAS f32x4*)po(i);
            WAIT_L0();
            G2_LOAD(3, 2);
            BAR();
            G2_LOAD(4, 3);
            float ck[2], co[2]; unsigned p[16];
            auto coef = [&](int q) {
                const float lk = q ? R.l1[0] : R.l0[0], mk = q ? R.m1 : R.m0, lo = lm[2 * q], mo = lm[2 * q + 1];
                const float mm = __builtin_fmaxf(mk, mo), fk = __builtin_amdgcn_exp2f(mk - mm), fo = __builtin_amdgcn_exp2f(mo - mm);
                const float rl = 1.0f / (lk * fk + lo * fo);
                ck[q] = fk * rl; co[q] = fo * rl; PIN(ck[q]); PIN(co[q]); SBAR();
            };
            auto half = [&](int q, int hh) {
                const f32x16 ok_ = q ? R.o1 : R.o0;
                unsigned* pp = p + 8 * q + 4 * hh;
#pragma unroll
                for (int i = 0; i < 2; ++i) {
                    const int g = 2 * hh + i; const f32x4 u = t[4 * q + g];
                    pp[2 * i] = cvtpk(ok_[4 * g] * ck[q] + u[0] * co[q], ok_[4 * g + 1] * ck[q] + u[1] * co[q]);
                    pp[2 * i + 1] = cvtpk(ok_[4 * g + 2] * ck[q] + u[2] * co[q], ok_[4 * g + 3] * ck[q] + u[3] * co[q]);
                }
                swap32(pp[0], pp[2]); swap32(pp[1], pp[3]);
                PIN(pp[0]); PIN(pp[1]); PIN(pp[2]); PIN(pp[3]); SBAR();
            };
            const unsigned ooff = WS_ACT + (unsigned)(112 + h) * GRP + (unsigned)(qb * 256 + w4 * 64 + fr) * 64u + (fq & 1) * 32 + (fq >> 1) * 16;
            auto ost = [&](int q) {
                unsigned* pp = p + 8 * q;
                swap16(pp[0], pp[4]); swap16(pp[1], pp[5]); swap16(pp[2], pp[6]); swap16(pp[3], pp[7]);
                ST16(((u32x4){pp[0], pp[1], pp[2], pp[3]}), ooff + q * 2048); ST16(((u32x4){pp[4], pp[5], pp[6], pp[7]}), ooff + q * 2048 + 1024); CFENCE();
            };
            SBAR(); coef(0); coef(1);
            WAIT_V(8);  BAR(); G2_LOAD(5, 4); G2_LOAD0(6, 0);
            WAIT_V(12); BAR(); G2_LOAD1(6, 0); G2_LOAD(7, 1);
            WAIT_V(16); BAR(); G2_LOAD0(8, 2);  SBAR(); half(0, 0); G2_LOAD1(8, 2);
            WAIT_V(16); BAR(); G2_LOAD0(9, 3);  SBAR(); half(0, 1); G2_LOAD1(9, 3);  ost(0);
            WAIT_V(18); BAR(); G2_LOAD0(10, 4); SBAR(); half(1, 0); G2_LOAD1(10, 4);
            WAIT_V(18); BAR(); G2_LOAD0(11, 0); SBAR(); half(1, 1); G2_LOAD1(11, 0); ost(1);
            WAIT_V(20); BAR(); G2_LOAD(12, 1);
            WAIT_V(18); BAR(); G2_LOAD(13, 2);
            WAIT_V(18);
            int sl = 3; cpd = 0;
#pragma unroll 1
            for (int j = 11; j < nt - 3; ++j) {
                BAR();
                if (j == 29) CODE_PF(cpd);
                if (j == 12 && wid == 4) signal(flags, F_O + bid, ln);
                G2_LOAD(j + 3, sl);
                sl = (sl == 4) ? 0 : sl + 1;
                WAIT_V(16);
            }
            BAR();
            if (tz == 256) *(LAS unsigned*)(lds + sl * SLOT) = 0u;
            WAIT_V(8); WAIT_L0(); BAR(); WAIT_V(0); BAR();
            asm volatile("" :: "v"(cpd));
            if (wid == 4) wait_all(flags, F_X + tile * 2 + z, peek(flags, F_X + tile * 2 + z));
            CFENCE(); BAR();
            asm volatile(".Lk_end:");
        } else {
#define G2_M(C, m, b) do { MFMA16_ACC(acc[m][b], cat8(C##l[b], C##h[b]), C##a[m]); SBAR(); } while (0)
#define G2_HALF(C, N, AB, WO, RD, TAIL) do { \
            G2_M(C, 0, 0); if (RD) { N##l[0] = lds_tr(pw0 + (WO)); N##h[0] = lds_tr(pw0 + (WO) + 512); } SBAR(); \
            G2_M(C, 1, 0); if (RD) N##a[0] = lds_b128(AB); SBAR(); \
            G2_M(C, 2, 0); if (RD) N##a[1] = lds_b128((AB) + 2048); SBAR(); \
            G2_M(C, 3, 0); if (RD) N##a[2] = lds_b128((AB) + 4096); SBAR(); \
            G2_M(C, 0, 1); if (RD) N##a[3] = lds_b128((AB) + 6144); SBAR(); \
            G2_M(C, 1, 1); if (RD) { N##l[1] = lds_tr(pw1 + (WO)); N##h[1] = lds_tr(pw1 + (WO) + 512); } SBAR(); \
            G2_M(C, 2, 1); if (RD) { N##l[2] = lds_tr(pw2 + (WO)); N##h[2] = lds_tr(pw2 + (WO) + 512); } SBAR(); \
            G2_M(C, 3, 1); if (RD) { N##l[3] = lds_tr(pw3 + (WO)); N##h[3] = lds_tr(pw3 + (WO) + 512); } SBAR(); \
            G2_M(C, 0, 2); TAIL(0); SBAR(); G2_M(C, 1, 2); TAIL(1); SBAR(); G2_M(C, 2, 2); TAIL(2); SBAR(); G2_M(C, 3, 2); TAIL(3); SBAR(); \
            G2_M(C, 0, 3); G2_M(C, 1, 3); G2_M(C, 2, 3); G2_M(C, 3, 3); } while (0)
            const u64* const optr = flags.p + (F_O + odep) * 16;
            u64 ov = 0;
            STAMP(8); STAMP(43);
#define G2_NEXT(i) do { if ((i) == 0) so = (so == 4 * SLOT) ? 0 : so + SLOT; \
                if ((i) == 1) { pa0 = lds + aO0 + so; pa1 = lds + aO1 + so; PIN(pa0); PIN(pa1); } \
                if ((i) == 2) { pw0 = lds + wO[0] + so; pw1 = lds + wO[1] + so; PIN(pw0); PIN(pw1); } \
                if ((i) == 3) { pw2 = lds + wO[2] + so; pw3 = lds + wO[3] + so; PIN(pw2); PIN(pw3); } } while (0)
#define G2_NONE(i) do {} while (0)
            WAIT_L0(); SBAR(); G2_HALF(x, y, pa1, 4096, true, G2_NEXT);
            WAIT_L0(); SBAR(); G2_HALF(y, x, pa0, 0, true, G2_NONE);
            BAR();
            WAIT_L0(); SBAR(); G2_HALF(x, y, pa1, 4096, true, G2_NEXT);
            WAIT_L0(); SBAR(); G2_HALF(y, x, pa0, 0, true, G2_NONE);
            if (NH0 & 1) {
                WAIT_L0(); SBAR(); G2_HALF(x, y, pa1, 4096, true, G2_NEXT);
                WAIT_L0(); BAR(); SBAR(); G2_HALF(y, x, pa0, 0, true, G2_NONE);
            }
#pragma unroll 1
            for (int ph = 0; ph < 3; ++ph) {
                const int n = (ph == 0) ? g2.nh / 2 - 4 : (ph == 1) ? 1 : 5;
#pragma unroll 1
                for (int i = 0; i < n; ++i) {
                    WAIT_L0(); SBAR(); G2_HALF(x, y, pa1, 4096, true, G2_NEXT);
                    WAIT_L0(); BAR(); SBAR(); G2_HALF(y, x, pa0, 0, true, G2_NONE);
                    WAIT_L0(); SBAR(); G2_HALF(x, y, pa1, 4096, true, G2_NEXT);
                    WAIT_L0(); BAR(); SBAR(); G2_HALF(y, x, pa0, 0, true, G2_NONE);
                }
                PEEK_AHEAD(ov, optr, wid | ph);
                if ((wid | (ph ^ 1)) == 0) { PEEK_DONE(ov); wait_all(flags, F_O + odep, ov); }
            }
            const int row = rbk * 128 + wm * 64 + z * 32 + fr, col = cbk * 128 + wn * 64 + (fq & 1) * 16 + (fq >> 1) * 8;
            u32x4 xr[4];
#pragma unroll
            for (int i = 0; i < 4; ++i) xr[i] = *(const u32x4*)(x + (size_t)(row + (i >> 1) * 16) * DM + col + (i & 1) * 32);
            WAIT_L0(); SBAR(); G2_HALF(x, y, pa1, 4096, true, G2_NEXT);
            WAIT_L0(); BAR(); SBAR(); G2_HALF(y, x, pa0, 0, true, G2_NONE);
            WAIT_L0(); SBAR(); G2_HALF(x, y, pa1, 4096, true, G2_NONE);
            WAIT_L0(); SBAR(); G2_HALF(y, x, pa0, 0, false, G2_NONE);
            MFMA_DRAIN(); SBAR();
            STAMP(9);
            f32x4 vk[8];
            const unsigned xo = WS_XCH + (unsigned)(tile * 2 + 1 - z) * 32768u + w4 * 8192 + ln * 16;
            if (z == 0) {
#pragma unroll
                for (int i = 0; i < 8; ++i) { ST16(__builtin_bit_cast(u32x4, acc[2 + (i >> 2)][i & 3]), xo + i * 1024); vk[i] = acc[i >> 2][i & 3]; }
            } else {
#pragma unroll
                for (int i = 0; i < 8; ++i) { ST16(__builtin_bit_cast(u32x4, acc[i >> 2][i & 3]), xo + i * 1024); vk[i] = acc[2 + (i >> 2)][i & 3]; }
            }
            WAIT_V(0);
            arrive(flags, F_X + tile * 2 + 1 - z, ln, lds + ((so == 4 * SLOT) ? 0 : so + SLOT), 3u);
            STAMP(10);
            CFENCE(); BAR(); CFENCE();
            STAMP(11);
            const unsigned xi = WS_XCH + (unsigned)(tile * 2 + z) * 32768u + w4 * 8192 + ln * 16;
#pragma unroll
            for (int i = 0; i < 8; ++i) vk[i] += __builtin_bit_cast(f32x4, __builtin_amdgcn_raw_buffer_load_b128(wsr, xi + i * 1024, 0, 16));
            const __amdgpu_buffer_rsrc_t outr = __builtin_amdgcn_make_buffer_rsrc((void*)out, 0, SEQ * DM * 2, 0x00020000);
            u32x4 w[4];
#pragma unroll
            for (int i = 0; i < 4; ++i) {
                const u32x4 a = __builtin_bit_cast(u32x4, vk[2 * i]), b = __builtin_bit_cast(u32x4, vk[2 * i + 1]);
                unsigned a0 = a[0], a1 = a[1], a2 = a[2], a3 = a[3], b0 = b[0], b1 = b[1], b2 = b[2], b3 = b[3];
                swap16(a0, b0); swap16(a1, b1); swap16(a2, b2); swap16(a3, b3);
                const u32x4 xx = xr[i];
                w[i][0] = cvtpk(u2f(a0) + bf_lo(xx[0]), u2f(a1) + bf_hi(xx[0]));
                w[i][1] = cvtpk(u2f(a2) + bf_lo(xx[1]), u2f(a3) + bf_hi(xx[1]));
                w[i][2] = cvtpk(u2f(b0) + bf_lo(xx[2]), u2f(b1) + bf_hi(xx[2]));
                w[i][3] = cvtpk(u2f(b2) + bf_lo(xx[3]), u2f(b3) + bf_hi(xx[3]));
            }
            const unsigned oo = (unsigned)((row - (fr & 8)) * DM + col + (fr & 8) * 4) * 2u;
#pragma unroll
            for (int m = 0; m < 2; ++m) {
                u32x4 lo, hi;
#pragma unroll
                for (int k = 0; k < 4; ++k) {
                    lo[k] = __builtin_amdgcn_update_dpp(w[2 * m][k], w[2 * m + 1][k], 0x128, 0xf, 0xc, false);
                    hi[k] = __builtin_amdgcn_update_dpp(w[2 * m + 1][k], w[2 * m][k], 0x128, 0xf, 0x3, false);
                }
                __builtin_amdgcn_raw_buffer_store_b128(lo, outr, oo + m * 16 * DM * 2, 0, 16);
                __builtin_amdgcn_raw_buffer_store_b128(hi, outr, oo + (m * 16 + 8) * DM * 2, 0, 16);
            }
        }
    }
}

extern "C" void kernel_launch(void* const* d_in, const int* in_sizes, int n_in, void* d_out, int out_size, void* d_ws, size_t ws_size, hipStream_t stream) {
    const bf16_t* x = (const bf16_t*)d_in[0];
    k_main<<<256, 512, 0, stream>>>(x, (const bf16_t*)d_in[1], (const bf16_t*)d_in[2], (const bf16_t*)d_in[4], (const bf16_t*)d_in[5], (const bf16_t*)d_in[6],
                                    (bf16_t*)d_out, (unsigned char*)d_ws);
}
```

```cpp
#include <hip/hip_runtime.h>
#include <stdint.h>

typedef unsigned short bf16_t;
typedef short bf16x8 __attribute__((ext_vector_type(8)));
typedef short s16x4 __attribute__((ext_vector_type(4)));
typedef float f32x2 __attribute__((ext_vector_type(2)));
typedef float f32x4 __attribute__((ext_vector_type(4)));
typedef float f32x16 __attribute__((ext_vector_type(16)));
typedef unsigned u32x2 __attribute__((ext_vector_type(2)));
typedef unsigned u32x4 __attribute__((ext_vector_type(4)));
typedef __bf16 bf16x2_t __attribute__((ext_vector_type(2)));
#define LAS __attribute__((address_space(3)))
#define GAS __attribute__((address_space(1)))
typedef LAS unsigned char* lds_ptr;

constexpr int SEQ = 2048, DM = 1024, FF = 3584, NQKV = 3072, NH = 32;
constexpr float LN_EPS = 1e-5f;
constexpr float LOG2E = 1.4426950408889634f;
constexpr float QSCALE = 0.17677669529663687f * LOG2E;

constexpr unsigned WS_FLAGS = 0;
constexpr unsigned WS_STAT = 192 * 1024;
constexpr unsigned WS_QKV = 256 * 1024;
constexpr unsigned WS_ACT = WS_QKV + 3u * SEQ * DM * 2;
constexpr unsigned WS_XCH = WS_ACT + 144u * SEQ * 64;
constexpr unsigned WS_XC = WS_XCH + 256u * 32768;
constexpr unsigned WS_END = WS_XC + SEQ * DM * 2;
constexpr int F_EPOCH = 0, F_QKV = 1, F_H = 257, F_O = 513, F_X = 769, F_XC = 1025;
constexpr unsigned GRP = SEQ * 64;

#define SBAR() __builtin_amdgcn_sched_barrier(0)
#define BAR() __builtin_amdgcn_s_barrier()
#define WAIT_V(n) asm volatile("s_waitcnt vmcnt(" #n ")" ::: "memory")
#define WAIT_L0() do { __builtin_amdgcn_s_waitcnt(0xC07F); asm volatile("" ::: "memory"); } while (0)
#define CFENCE() asm volatile("" ::: "memory")
#define PIN(x) asm volatile("" : "+v"(x))
#define MFMA16(a, b, c) __builtin_amdgcn_mfma_f32_16x16x32_bf16(a, b, c, 0, 0, 0)
#define MFMA32(a, b, c) __builtin_amdgcn_mfma_f32_32x32x16_bf16(a, b, c, 0, 0, 0)
#define MFMA16_ACC(acc, a, b) asm volatile("v_mfma_f32_16x16x32_bf16 %0, %1, %2, %0" : "+v"(acc) : "v"(a), "v"(b))
#define MFMA32_ACC(acc, a, b) asm volatile("v_mfma_f32_32x32x16_bf16 %0, %1, %2, %0" : "+v"(acc) : "v"(a), "v"(b))
#define MFMA32_SET(d, a, b, c) asm volatile("v_mfma_f32_32x32x16_bf16 %0, %1, %2, %3" : "=&v"(d) : "v"(a), "v"(b), "v"(c))
#define MFMA_DRAIN() asm volatile("s_nop 15" ::: "memory")
#define RLX_AGENT __ATOMIC_RELAXED, __HIP_MEMORY_SCOPE_AGENT

__device__ __forceinline__ void glds16(const void* sbase, unsigned voff, unsigned ldsdst) {
    asm volatile("s_mov_b32 m0, %2\n\ts_nop 0\n\tglobal_load_lds_dwordx4 %0, %1" ::"v"(voff), "s"(sbase), "s"(ldsdst) : "memory");
}
__device__ __forceinline__ void glds16c(const void* sbase, unsigned voff, unsigned ldsdst) {
    asm volatile("s_mov_b32 m0, %2\n\ts_nop 0\n\tglobal_load_lds_dwordx4 %0, %1 sc1" ::"v"(voff), "s"(sbase), "s"(ldsdst) : "memory");
}
__device__ __forceinline__ bf16x8 lds_b128(lds_ptr p) { return *(const LAS bf16x8*)p; }
__device__ __forceinline__ s16x4 lds_tr(lds_ptr p) { return __builtin_amdgcn_ds_read_tr16_b64_v4i16((LAS s16x4*)p); }
__device__ __forceinline__ bf16x8 cat8(s16x4 a, s16x4 b) { return __builtin_shufflevector(a, b, 0, 1, 2, 3, 4, 5, 6, 7); }
__device__ __forceinline__ unsigned cvtpk(float lo, float hi) {
    f32x2 v = {lo, hi};
    return __builtin_bit_cast(unsigned, __builtin_convertvector(v, bf16x2_t));
}
__device__ __forceinline__ float u2f(unsigned u) { return __builtin_bit_cast(float, u); }
__device__ __forceinline__ unsigned f2u(float f) { return __builtin_bit_cast(unsigned, f); }
__device__ __forceinline__ float bf_lo(unsigned u) { return u2f(u << 16); }
__device__ __forceinline__ float bf_hi(unsigned u) { return u2f(u & 0xffff0000u); }
__device__ __forceinline__ void swap16(unsigned& a, unsigned& b) {
    auto r = __builtin_amdgcn_permlane16_swap(a, b, false, false);
    a = r[0]; b = r[1];
}
__device__ __forceinline__ void swap32(unsigned& a, unsigned& b) {
    auto r = __builtin_amdgcn_permlane32_swap(a, b, false, false);
    a = r[0]; b = r[1];
}
__device__ __forceinline__ int opaque_tid() { int t = threadIdx.x; asm volatile("" : "+v"(t)); return t; }
typedef unsigned long long u64;
struct Flags { u64* p; u64 tag; };
__device__ __forceinline__ void signal(const Flags& f, int word, int lane) {
    if (lane == 0) __hip_atomic_store((GAS u64*)(f.p + word * 16), f.tag, RLX_AGENT);
}
__device__ __forceinline__ u64 peek(const Flags& f, int word) {
    u64 v;
    asm volatile("global_load_dwordx2 %0, %1, off sc1\n\ts_waitcnt vmcnt(0)" : "=&v"(v) : "v"(f.p + word * 16) : "memory");
    return v;
}
__device__ __forceinline__ void wait_all(const Flags& f, int word, u64 v) {
#pragma unroll 1
    for (unsigned spin = 0; spin < (1u << 20) && !__all(v == f.tag); ++spin) { __builtin_amdgcn_s_sleep(1); v = peek(f, word); }
}
__device__ __forceinline__ void arrive(const Flags& f, int word, int lane, lds_ptr cnt, unsigned last) {
    if (lane == 0 && __hip_atomic_fetch_add((LAS unsigned*)cnt, 1u, __ATOMIC_RELAXED, __HIP_MEMORY_SCOPE_WORKGROUP) == last)
        __hip_atomic_store((GAS u64*)(f.p + word * 16), f.tag, RLX_AGENT);
}
#define PEEK_AHEAD(v, ptr, sel) asm volatile("s_cmp_lg_u32 %2, 0\n\ts_cbranch_scc1 1f\n\tglobal_load_dwordx2 %0, %1, off sc1\n1:" : "+v"(v) : "v"(ptr), "s"(sel) : "memory", "scc")
struct CodePf { const char* base; unsigned len; int first; };
#define CODE_PF(d) do { unsigned o_ = (unsigned)(cp.first + (opaque_tid() & 15)) * 128u; o_ = (o_ < cp.len) ? o_ : 0u; \
    asm volatile("global_load_dword %0, %1, %2" : "+v"(d) : "v"(o_), "s"(cp.base) : "memory"); } while (0)
#define PEEK_DONE(v) asm volatile("s_waitcnt vmcnt(0)" : "+v"(v) :: "memory")

template <int CTRL> __device__ __forceinline__ float dpp_add(float v) { return v + u2f(__builtin_amdgcn_update_dpp(0u, f2u(v), CTRL, 0xf, 0xf, true)); }
__device__ __forceinline__ float red4(float v) {
    unsigned a = f2u(v), b = a;
    swap16(a, b);
    v = u2f(a) + u2f(b);
    a = f2u(v); b = a;
    swap32(a, b);
    return u2f(a) + u2f(b);
}
__device__ __forceinline__ float wave_sum(float v) {
    v = dpp_add<0xB1>(v); v = dpp_add<0x4E>(v); v = dpp_add<0x141>(v); v = dpp_add<0x140>(v);
    return red4(v);
}
__device__ __forceinline__ f32x2 row_stats(u32x4& a, u32x4& b) {
    float v[16];
#pragma unroll
    for (int i = 0; i < 4; ++i) { v[2 * i] = bf_lo(a[i]); v[2 * i + 1] = bf_hi(a[i]); v[8 + 2 * i] = bf_lo(b[i]); v[8 + 2 * i + 1] = bf_hi(b[i]); }
    float s = 0.f;
#pragma unroll
    for (int i = 0; i < 16; ++i) s += v[i];
    const float mu = wave_sum(s) * (1.0f / DM);
    float q = 0.f;
#pragma unroll
    for (int i = 0; i < 16; ++i) { v[i] -= mu; q += v[i] * v[i]; }
#pragma unroll
    for (int i = 0; i < 4; ++i) { a[i] = cvtpk(v[2 * i], v[2 * i + 1]); b[i] = cvtpk(v[8 + 2 * i], v[8 + 2 * i + 1]); }
    return (f32x2){mu, 1.0f / __builtin_sqrtf(wave_sum(q) * (1.0f / DM) + LN_EPS)};
}

constexpr int SLOT = 32768, LDS_BYTES = 5 * SLOT;
constexpr int G1_A = 0, G1_W = 65536, G1_STAT = G1_W + 5 * 16384, G1_CS = G1_STAT + 2048, G1_CNT = G1_CS + 4 * 6 * 64;

#define NH0 30
static_assert(NH0 >= 28 && NH0 <= 32, "");
struct G2 { const char* Ah; const char* Ao; const char* Wd; const char* Wo; int nh; unsigned rowb; u64 split; };
__device__ __forceinline__ void g2_load_b(const G2& g, int j, unsigned dst, int vw, unsigned voffB) {
    const char* b = (j >= g.nh ? g.Wo : g.Wd) + (size_t)(j * 64 + vw * 8) * (DM * 2);
    glds16(b, voffB, dst + 16384 + vw * 1024); glds16(b + 128, voffB, dst + 16384 + 8192 + vw * 1024);
}
struct G2Off { unsigned A0, A1, N0, N1, B0, B1; };
__device__ __forceinline__ void g2_load_a(const G2& g, int j, unsigned dst, int vw, const G2Off& o) {
    const unsigned sp = (unsigned)(g.split >> j) & 1u;
    const char* a = (j >= g.nh ? g.Ao : g.Ah) + (size_t)j * (2 * GRP) + ((g.rowb + vw * 1024) >> sp);
    glds16c(a, sp ? o.N0 : o.A0, dst + vw * 2048); glds16c(a, sp ? o.N1 : o.A1, dst + vw * 2048 + 1024);
}
__device__ __forceinline__ void g2_voff(int lane, G2Off& o) {
    const int r8 = lane >> 3, u8 = lane & 7, ua = u8 ^ (r8 >> 1), ub = ua ^ 4;
    o.A0 = (unsigned)(ua >> 2) * GRP + r8 * 64 + (ua & 3) * 16;
    o.A1 = (unsigned)(ub >> 2) * GRP + (r8 + 8) * 64 + (ub & 3) * 16;
    o.N0 = (unsigned)(ua >> 1) * (GRP / 2) + r8 * 32 + (ua & 1) * 16;
    o.N1 = (unsigned)(ub >> 1) * (GRP / 2) + (r8 + 8) * 32 + (ub & 1) * 16;
    o.B0 = (unsigned)(r8 * (DM * 2) + (u8 ^ (r8 & 2)) * 16);
    o.B1 = (unsigned)(r8 * (DM * 2) + (u8 ^ (4 | (r8 & 2))) * 16);
}
constexpr u64 split_mask(int first, int n) { u64 m = 0; for (int j = 0; j < n; ++j) if ((first + j) % 7 == 6) m |= 1ull << j; return m; }
static_assert(((split_mask(0, NH0) | split_mask(NH0, 56 - NH0)) & 7) == 0, "phase B's tail prefetches K-tiles 0..2 as plain groups");

struct AttnOut { f32x16 o0, o1; f32x4 l0, l1; float m0, m1; };
struct HPend { u32x4 v[8]; unsigned off, size; const void* base; };
struct AttnLane {
    unsigned voffK, voffV; lds_ptr kb0, kb1, vb; const char* qp; bf16x8 ones;
    __device__ __forceinline__ void init(lds_ptr lds, const char* Qg, int wid, int lane) {
        const int r32 = lane & 31, hi = lane >> 5, kh = wid >> 2, qg = wid & 3, ksw = (r32 >> 2) & 3;
        const int uk = (lane & 3) ^ ((lane >> 4) & 3), uv = lane & 3;
        voffK = (unsigned)((uk >> 1) * 65536 + wid * 1024 + (lane >> 2) * 32 + (uk & 1) * 16);
        voffV = (unsigned)((uv >> 1) * 65536 + wid * 1024 + (lane >> 2) * 32 + (uv & 1) * 16);
        kb0 = lds + kh * 8192 + r32 * 64 + ((hi ^ ksw) * 16); kb1 = lds + kh * 8192 + r32 * 64 + (((2 | hi) ^ ksw) * 16);
        vb = lds + 16384 + kh * 8192 + (4 * hi + ((lane >> 2) & 3)) * 64 + ((lane >> 4) & 1) * 32 + (lane & 3) * 8;
        qp = Qg + (qg * 64 + r32) * 32 + hi * 16;
        const short v = (((lane >> 4) & 1) == ((lane >> 2) & 1)) ? (short)0x3f80 : (short)0;
        ones = (bf16x8){v, v, v, v, v, v, v, v};
    }
};
#define EX(v) __builtin_amdgcn_exp2f(v)
#define AT_LOAD_K(c, slot) do { const unsigned d_ = lds0 + wid * 2048 + (slot) * SLOT; const char* k_ = Kg + (c) * 8192; glds16c(k_, A.voffK, d_); glds16c(k_ + 512, A.voffK, d_ + 1024); } while (0)
#define AT_LOAD_V(c, slot) do { const unsigned d_ = lds0 + wid * 2048 + (slot) * SLOT + 16384; const char* v_ = Vg + (c) * 8192; glds16c(v_, A.voffV, d_); glds16c(v_ + 512, A.voffV, d_ + 1024); } while (0)
#define AT_PACK(S, i) __builtin_bit_cast(bf16x8, (u32x4){cvtpk(S[i], S[i + 1]), cvtpk(S[i + 2], S[i + 3]), cvtpk(S[i + 4], S[i + 5]), cvtpk(S[i + 6], S[i + 7])})
#define AT_Q_LOAD() asm volatile("global_load_dwordx4 %0, %4, off sc1\n\tglobal_load_dwordx4 %1, %4, off offset:1024 sc1\n\tglobal_load_dwordx4 %2, %5, off sc1\n\tglobal_load_dwordx4 %3, %5, off offset:1024 sc1" \
        : "=&v"(q00), "=&v"(q10), "=&v"(q01), "=&v"(q11) : "v"(A.qp), "v"(qp2) : "memory")
#define AT_Q_WAIT(n) asm volatile("s_waitcnt vmcnt(" #n ")" : "+v"(q00), "+v"(q01), "+v"(q10), "+v"(q11) :: "memory")
__device__ __forceinline__ float rowmax16(const f32x16& s) {
    float a = s[0];
#pragma unroll
    for (int r = 1; r < 16; ++r) a = __builtin_fmaxf(a, s[r]);
    unsigned x = f2u(a), y = x; swap32(x, y);
    return __builtin_fmaxf(u2f(x), u2f(y));
}

__device__ __forceinline__ void attn_safe(lds_ptr lds, unsigned lds0, const char* Qg, const char* Kg, const char* Vg, int wid, AttnOut& R) {
    AttnLane A; A.init(lds, Qg, wid, opaque_tid() & 63);
    bf16x8 q00, q01, q10, q11; const char* qp2 = A.qp + 65536;
    AT_Q_LOAD(); AT_Q_WAIT(0);
    f32x16 o0 = (f32x16){}, o1 = (f32x16){};
    f32x4 l0 = (f32x4){0.f, 0.f, 0.f, 0.f}, l1 = l0;
    float m0 = -__builtin_inff(), m1 = m0;
#pragma unroll 1
    for (int c = 0; c < 8; ++c) {
        AT_LOAD_K(c, 3); AT_LOAD_V(c, 3);
        WAIT_V(0); BAR(); CFENCE();
#pragma unroll 1
        for (int t = 0; t < 4; ++t) {
            const int to = 3 * SLOT + t * 2048;
            const bf16x8 ka = lds_b128(A.kb0 + to), kb = lds_b128(A.kb1 + to);
            const bf16x8 va = cat8(lds_tr(A.vb + to), lds_tr(A.vb + to + 512)), vc = cat8(lds_tr(A.vb + to + 1024), lds_tr(A.vb + to + 1536));
            f32x16 s0 = MFMA32(kb, q01, MFMA32(ka, q00, (f32x16){})), s1 = MFMA32(kb, q11, MFMA32(ka, q10, (f32x16){}));
            const float n0 = __builtin_fmaxf(m0, rowmax16(s0)), n1 = __builtin_fmaxf(m1, rowmax16(s1));
            const float f0 = EX(m0 - n0), f1 = EX(m1 - n1);
            m0 = n0; m1 = n1; o0 *= f0; o1 *= f1; l0 *= f0; l1 *= f1;
#pragma unroll
            for (int r = 0; r < 16; ++r) { s0[r] = EX(s0[r] - n0); s1[r] = EX(s1[r] - n1); }
            const bf16x8 p00 = AT_PACK(s0, 0), p01 = AT_PACK(s0, 8), p10 = AT_PACK(s1, 0), p11 = AT_PACK(s1, 8);
            o0 = MFMA32(vc, p01, MFMA32(va, p00, o0)); l0 = MFMA16(A.ones, p01, MFMA16(A.ones, p00, l0));
            o1 = MFMA32(vc, p11, MFMA32(va, p10, o1)); l1 = MFMA16(A.ones, p11, MFMA16(A.ones, p10, l1));
        }
        WAIT_L0(); BAR(); CFENCE();
    }
    R.o0 = o0; R.o1 = o1; R.l0 = l0; R.l1 = l1; R.m0 = m0; R.m1 = m1;
}

__device__ __forceinline__ void attn_fast(lds_ptr lds, unsigned lds0, const char* Qg, const char* Kg, const char* Vg, const G2& g2, const Flags& flags, int qkv0, int qkvword, int depword,
                                          int hsig, int c0, bool bump, const CodePf& cp, const HPend& hp, const char* wpd, const char* wpo, int prow, int wid, AttnOut& R) {
    const int lane = opaque_tid() & 63;
    AttnLane A; A.init(lds, Qg, wid, lane);
    lds_ptr kb0 = A.kb0, kb1 = A.kb1, vb = A.vb;
    PIN(kb0); PIN(kb1); PIN(vb);
    lds_ptr kh0 = kb0 + 2 * SLOT, kh1 = kb1 + 2 * SLOT, vh = vb + 2 * SLOT;
    PIN(kh0); PIN(kh1); PIN(vh);
    const bf16x8 ones = A.ones;
    const __amdgpu_buffer_rsrc_t hr = __builtin_amdgcn_make_buffer_rsrc((void*)hp.base, 0, hp.size, 0x00020000);
    f32x16 o0 = (f32x16){}, o1 = (f32x16){}, negm0 = (f32x16){}, negm1 = (f32x16){}, sA0, sA1, sB0, sB1;
    f32x4 l0 = (f32x4){0.f, 0.f, 0.f, 0.f}, l1 = l0;
    bf16x8 kfa, kfb, va, vc = (bf16x8){}, pf = (bf16x8){};
    PIN(o0); PIN(o1); PIN(l0); PIN(l1); PIN(vc); PIN(pf); PIN(A.voffK); PIN(A.voffV);
    G2Off go; g2_voff(lane, go);
    unsigned gA0 = go.A0, gA1 = go.A1, gB = (wid & 1) ? go.B1 : go.B0;
    unsigned pfo = (unsigned)(((lane >> 3) & 1) * (DM * 2) + (lane & 7) * 128);
    const u64* hptr = flags.p + (F_H + depword) * 16;
    const u64* qptr = flags.p + qkvword * 16;
    PIN(gA0); PIN(gA1); PIN(gB); PIN(pfo); PIN(hptr); PIN(qptr);
    if (wid == 0) {
        wait_all(flags, qkv0, peek(flags, qkv0));

    }
    CFENCE(); BAR(); CFENCE();

    AT_LOAD_K(c0, 0);
    glds16c(Qg, A.voffK, lds0 + wid * 2048 + 2 * SLOT); glds16c(Qg + 512, A.voffK, lds0 + wid * 2048 + 2 * SLOT + 1024);
    AT_LOAD_V(c0, 0);
    u64 hv = 0;
    float m0, m1;
    WAIT_V(2);
    PEEK_AHEAD(hv, qptr, wid);
    BAR(); SBAR();

    const int qo = 2 * SLOT + (wid & 3) * 4096 - (wid >> 2) * 8192;
    const bf16x8 q00 = lds_b128(kb0 + qo), q10 = lds_b128(kb0 + qo + 2048), q01 = lds_b128(kb1 + qo), q11 = lds_b128(kb1 + qo + 2048);
    {
        kfa = lds_b128(kb0); kfb = lds_b128(kb1);
        sA0 = MFMA32(kfa, q00, negm0); sA1 = MFMA32(kfa, q10, negm1); sA0 = MFMA32(kfb, q01, sA0); sA1 = MFMA32(kfb, q11, sA1);
        kfa = lds_b128(kb0 + 2048); kfb = lds_b128(kb1 + 2048);
        m0 = rowmax16(sA0); m1 = rowmax16(sA1);
#pragma unroll
        for (int r = 0; r < 16; ++r) { sA0[r] -= m0; sA1[r] -= m1; negm0[r] = -m0; negm1[r] = -m1; }
        PIN(negm0); PIN(negm1);
    }
    unsigned pfd = 0;
    PEEK_DONE(hv);
    if (wid == 0) {
        wait_all(flags, qkvword, hv);
        if (bump) signal(flags, F_EPOCH, lane);

    }
    CFENCE(); BAR(); SBAR();

    AT_LOAD_K((c0 + 1) & 7, 1); AT_LOAD_V((c0 + 1) & 7, 1);

#define AT_GROUP(QK, RD, S, i, O, L, VF) do { \
        QK; SBAR(); RD; SBAR(); \
        float e0, e1, e2, e3, e4, e5, e6, e7; \
        asm volatile("v_exp_f32 %0, %13\n\tv_exp_f32 %1, %14\n\tv_exp_f32 %2, %15\n\tv_mfma_f32_32x32x16_bf16 %8, %10, %11, %8\n\t" \
                     "v_exp_f32 %3, %16\n\tv_exp_f32 %4, %17\n\tv_exp_f32 %5, %18\n\tv_mfma_f32_16x16x32_bf16 %9, %12, %11, %9\n\tv_exp_f32 %6, %19\n\tv_exp_f32 %7, %20" \
            : "=&v"(e0), "=&v"(e1), "=&v"(e2), "=&v"(e3), "=&v"(e4), "=&v"(e5), "=&v"(e6), "=&v"(e7), "+v"(O), "+v"(L) \
            : "v"(VF), "v"(pf), "v"(ones), "v"(S[i]), "v"(S[i + 1]), "v"(S[i + 2]), "v"(S[i + 3]), "v"(S[i + 4]), "v"(S[i + 5]), "v"(S[i + 6]), "v"(S[i + 7])); \
        pf = __builtin_bit_cast(bf16x8, (u32x4){cvtpk(e0, e1), cvtpk(e2, e3), cvtpk(e4, e5), cvtpk(e6, e7)}); SBAR(); } while (0)
#define LO(lo, hi, off) (((off) >= 2 * SLOT ? hi : lo) + ((off) & (2 * SLOT - 1)))
#define AT_STEP(C0, C1, N0, N1, QK, KR, KO, VO, H0, H1, H2, H3) do { \
        AT_GROUP(if (QK) MFMA32_SET(N0, kfa, q00, negm0), { va = cat8(lds_tr(LO(vb, vh, VO)), lds_tr(LO(vb, vh, VO) + 512)); H0; }, C0, 0, o1, l1, vc); \
        AT_GROUP(if (QK) MFMA32_SET(N1, kfa, q10, negm1), { if (KR) kfa = lds_b128(LO(kb0, kh0, KO)); vc = cat8(lds_tr(LO(vb, vh, VO) + 1024), lds_tr(LO(vb, vh, VO) + 1536)); H1; }, C1, 0, o0, l0, va); \
        AT_GROUP(if (QK) MFMA32_ACC(N0, kfb, q01), H2, C0, 8, o1, l1, va); \
        AT_GROUP(if (QK) MFMA32_ACC(N1, kfb, q11), { if (KR) kfb = lds_b128(LO(kb1, kh1, KO)); H3; }, C1, 8, o0, l0, vc); } while (0)
#define PRV 1
#define YP(p) asm volatile("s_cmp_lt_u32 %0, 4\n\ts_cbranch_scc1 1f\n\ts_setprio " #p "\n1:" :: "s"(wid) : "scc")
    if (PRV) __builtin_amdgcn_s_setprio(1);
    auto ylow = [&]() { if (PRV) YP(0); };
#define S0(c, ...) AT_STEP(sA0, sA1, sB0, sB1, true, true, ((c) & 3) * SLOT + 4096, ((c) & 3) * SLOT, __VA_ARGS__)
#define S1(c, ...) if (PRV == 1) YP(0); AT_STEP(sB0, sB1, sA0, sA1, true, true, ((c) & 3) * SLOT + 6144, ((c) & 3) * SLOT + 2048, __VA_ARGS__)
#define S2(c, ...) if (PRV == 1 || (PRV == 2 && (c) == 7)) YP(2); AT_STEP(sA0, sA1, sB0, sB1, true, (c) < 7, (((c) + 1) & 3) * SLOT, ((c) & 3) * SLOT + 4096, __VA_ARGS__)
#define S3(c, ...) AT_STEP(sB0, sB1, sA0, sA1, (c) < 7, (c) < 7, (((c) + 1) & 3) * SLOT + 2048, ((c) & 3) * SLOT + 6144, __VA_ARGS__)
#define SY(c, n) do { WAIT_V(n); WAIT_L0(); BAR(); SBAR(); } while (0)
#define NO (void)0
#define KP(c, i) glds16c(Kg + ((c0 + (c)) & 7) * 8192 + (i) * 512, A.voffK, lds0 + wid * 2048 + ((c) & 3) * SLOT + (i) * 1024)
#define VP(c, i) glds16c(Vg + ((c0 + (c)) & 7) * 8192 + (i) * 512, A.voffV, lds0 + wid * 2048 + ((c) & 3) * SLOT + 16384 + (i) * 1024)
#define CH(c) KP(c, 0), KP(c, 1), VP(c, 0), VP(c, 1)
#define HS(i) do { __builtin_amdgcn_raw_buffer_store_b128(hp.v[i], hr, hp.off + (i) * 1024, 0, 16); CFENCE(); } while (0)
#define WP(i) wpf(i)
#define GB(j, slot, i) glds16(g2.Wd + (size_t)((j) * 64 + wid * 8) * (DM * 2) + (i) * 128, gB, lds0 + (slot) * SLOT + 16384 + (i) * 8192 + wid * 1024)
#define GA(j, slot, i) glds16c(g2.Ah + (size_t)(j) * (2 * GRP) + g2.rowb + wid * 1024, (i) ? gA1 : gA0, lds0 + (slot) * SLOT + wid * 2048 + (i) * 1024)
    auto wpf = [&](int i) {
        const int pi = (wid + 8 * i < g2.nh + 8) ? wid + 8 * i : 0, lr = prow + 2 * pi;
        asm volatile("global_load_dword %0, %1, %2" : "+v"(pfd) : "v"(pfo), "s"((lr < g2.nh * 64 ? wpd : wpo) + (size_t)lr * (DM * 2)) : "memory");
    };
    auto cpf = [&]() { CODE_PF(pfd); };
    auto hpeek = [&]() { PEEK_AHEAD(hv, hptr, wid); };
    S0(0, NO, NO, NO, NO);         S1(0, NO, NO, NO, NO);    SY(0, 0);  S2(0, CH(2)); S3(0, CH(3));
    S0(1, WP(0), NO, HS(0), NO);   S1(1, NO, NO, HS(1), NO); SY(1, 7);  S2(1, CH(4)); S3(1, WP(1), NO, HS(2), NO);
    S0(2, NO, NO, HS(3), NO);      S1(2, NO, NO, HS(4), NO); SY(2, 11); S2(2, CH(5)); S3(2, WP(2), NO, HS(5), NO);
    S0(3, NO, NO, HS(6), NO);      S1(3, NO, NO, HS(7), NO); SY(3, 12); S2(3, CH(6)); S3(3, WP(3), NO, NO, NO);
    S0(4, NO, NO, NO, NO);         S1(4, NO, NO, NO, NO);    SY(4, 5);
    if (wid == 0) signal(flags, hsig, lane);
                                                                        S2(4, CH(7)); S3(4, WP(4), NO, NO, NO);
    S0(5, GB(0, 4, 0), GB(0, 4, 1), NO, NO); S1(5, NO, NO, NO, NO); SY(5, 2); S2(5, GB(1, 0, 0), GB(1, 0, 1), NO, NO); S3(5, NO, NO, NO, NO);
    S0(6, hpeek(), NO, NO, NO);
    if (wid == 0) { PEEK_DONE(hv); wait_all(flags, F_H + depword, hv); }
                                   S1(6, NO, NO, NO, NO);    SY(6, 0);  S2(6, GA(0, 4, 0), GA(0, 4, 1), GB(2, 1, 0), GB(2, 1, 1)); S3(6, GA(1, 0, 0), GA(1, 0, 1), cpf(), NO);
    S0(7, GA(2, 1, 0), GA(2, 1, 1), NO, NO); S1(7, NO, NO, NO, NO); SY(7, 0); S2(7, NO, NO, NO, NO); S3(7, NO, NO, ylow(), NO);
    __builtin_amdgcn_s_setprio(0);
#undef S0
#undef S1
#undef S2
#undef S3
#undef NO

    asm volatile("s_nop 1" ::: "memory");
    MFMA32_ACC(o1, vc, pf); MFMA16_ACC(l1, ones, pf);
    MFMA_DRAIN(); SBAR();
    asm volatile("" :: "v"(pfd));
    R.o0 = o0; R.o1 = o1; R.l0 = l0; R.l1 = l1; R.m0 = m0; R.m1 = m1;
}

__global__ __launch_bounds__(512, 2) void k_main(const bf16_t* __restrict__ x, const bf16_t* __restrict__ wqkv, const bf16_t* __restrict__ wo, const bf16_t* __restrict__ wg,
                                                 const bf16_t* __restrict__ wu, const bf16_t* __restrict__ wd, bf16_t* __restrict__ out, unsigned char* ws) {
    __shared__ __attribute__((aligned(16))) unsigned char smem[LDS_BYTES];
    const lds_ptr lds = (lds_ptr)smem;
    const unsigned lds0 = (unsigned)(uintptr_t)lds;
    const int wid = __builtin_amdgcn_readfirstlane(threadIdx.x >> 6);
    const int bid = blockIdx.x, xcd = bid & 7, idx = bid >> 3;
    Flags flags; flags.p = (u64*)(ws + WS_FLAGS);
    const __amdgpu_buffer_rsrc_t wsr = __builtin_amdgcn_make_buffer_rsrc((void*)ws, 0, WS_END, 0x00020000);
#define ST16(v, off) __builtin_amdgcn_raw_buffer_store_b128(v, wsr, off, 0, 16)

    unsigned cpd = 0;
    CodePf cp;
    asm volatile("s_getpc_b64 %0\n.Lk_begin:\n\ts_mov_b32 %1, .Lk_end-.Lk_begin" : "=s"(cp.base), "=s"(cp.len));
    cp.first = idx * 16;
    HPend hp;
    {
        const int lane = threadIdx.x & 63, wr = wid >> 2, wc = wid & 3, js = idx & 3, rb = idx >> 2;
        const bool typeY = (js == 3);
        const int f0 = 448 * xcd + 128 * js;
        const char* P0 = (const char*)(wg + f0);
        const char* P2 = (const char*)(wu + f0);
        const char* P1 = typeY ? (const char*)(wqkv + 128 * xcd + 64) : (const char*)(wg + f0 + 64);
        const char* P3 = typeY ? (const char*)(wqkv + 1024 + 128 * xcd + 64) : (const char*)(wu + f0 + 64);
        const char* P4 = typeY ? (const char*)(wqkv + 2048 + 128 * xcd + 64) : (const char*)(wqkv + js * 1024 + 128 * xcd);
        const unsigned stepF = 32u * FF * 2u, stepQ = 32u * NQKV * 2u, step13 = typeY ? stepQ : stepF;
        const char* XA = (const char*)x + (size_t)rb * 256 * DM * 2;
        const char* XC = (const char*)ws + WS_XC + (size_t)rb * 256 * DM * 2;
        const int r8 = lane >> 3, u8 = lane & 7;
        const int gw = (r8 & 2) | ((wc & 1) << 2);
        const unsigned voffF = (unsigned)(((wc * 8 + r8) * FF + (u8 ^ gw) * 8) * 2);
        const unsigned voffQ = (unsigned)(((wc * 8 + r8) * NQKV + (u8 ^ gw) * 8) * 2);
        const unsigned voff13 = typeY ? voffQ : voffF;
        const unsigned voffA0 = (unsigned)(((32 * wid + r8) * DM + (u8 ^ (r8 >> 1)) * 8) * 2);
        const unsigned voffA1 = (unsigned)(((32 * wid + 8 + r8) * DM + (u8 ^ (4 | (r8 >> 1))) * 8) * 2);
        const unsigned dstA = lds0 + G1_A + wid * 4096, dstW = lds0 + G1_W + wc * 1024;
#define G1_LOAD_A(t) do { const char* b_ = XA + (t) * 128; const unsigned d_ = dstA + ((t) & 1) * 32768; \
        glds16(b_, voffA0, d_); glds16(b_, voffA1, d_ + 1024); glds16(b_ + 32768, voffA0, d_ + 2048); glds16(b_ + 32768, voffA1, d_ + 3072); } while (0)
#define G1_LOAD_W(hs) do { const unsigned d_ = dstW + ((hs) & 3) * 4096; \
        glds16(P0 + (size_t)(hs) * stepF, voffF, d_); glds16(P1 + (size_t)(hs) * step13, voff13, d_ + 16384); glds16(P2 + (size_t)(hs) * stepF, voffF, d_ + 32768); \
        glds16(P3 + (size_t)(hs) * step13, voff13, d_ + 49152); glds16(P4 + (size_t)(hs) * stepQ, voffQ, d_ + 65536); } while (0)

        if (wr == 1) G1_LOAD_W(0);
        G1_LOAD_A(0);
        u32x4 xa0, xa1;
        const int srow = rb * 256 + (js * 8 + xcd) * 8 + wid;
        asm volatile("global_load_dwordx4 %0, %2, %3\n\tglobal_load_dwordx4 %1, %2, %3 offset:1024"
                     : "=&v"(xa0), "=&v"(xa1) : "v"((unsigned)(lane * 16)), "s"((const char*)x + (size_t)srow * DM * 2) : "memory");
        CODE_PF(cpd);

        int fr = lane & 15, fq = lane >> 4;
        const lds_ptr aB0 = lds + G1_A + (wr * 128 + fr) * 128 + ((fq ^ (fr >> 1)) * 16);
        const lds_ptr aB1 = lds + G1_A + (wr * 128 + fr) * 128 + (((4 | fq) ^ (fr >> 1)) * 16);
        const int tq = (lane >> 2) & 3, tp = lane & 3, tg = (tq & 2) | ((fq & 1) << 2);
        auto wbase = [&](int panel, int bp) { return lds + G1_W + panel * 16384 + (8 * fq + tq) * 128 + (((2 * bp + (tp >> 1)) ^ tg) * 16) + (tp & 1) * 8; };
        const int pX = wc >> 1, bX = 2 * (wc & 1);
        const lds_ptr wB0 = typeY ? wbase(0, wc) : wbase(pX, bX);
        const lds_ptr wB1 = typeY ? wbase(1, wc) : wbase(pX, bX + 1);
        const lds_ptr wB2 = typeY ? wbase(2, wc) : wbase(2 + pX, bX);
        const lds_ptr wB3 = typeY ? wbase(3, wc) : wbase(2 + pX, bX + 1);
        const lds_ptr wB4 = wbase(4, wc);
        if (threadIdx.x == 0) *(LAS unsigned*)(lds + G1_CNT) = 0u;
        f32x4 acc[8][5];
#pragma unroll
        for (int m = 0; m < 8; ++m)
#pragma unroll
            for (int b = 0; b < 5; ++b) { acc[m][b] = (f32x4){0.f, 0.f, 0.f, 0.f}; PIN(acc[m][b]); }
        if (wr == 1) { WAIT_V(7); BAR(); }
        asm volatile("s_waitcnt vmcnt(1)" : "+v"(xa0), "+v"(xa1) :: "memory");

        u64 sg;
        {
            const f32x2 st = row_stats(xa0, xa1);
            G1_LOAD_W(1 + wr);
            ST16(xa0, (lane >= 32) ? WS_XC + (unsigned)srow * 2048u + lane * 16 : WS_END);
            ST16(xa1, WS_XC + (unsigned)srow * 2048u + 1024 + lane * 16);
            __builtin_amdgcn_raw_buffer_store_b64(__builtin_bit_cast(u32x2, st), wsr, (lane == 0) ? WS_STAT + (unsigned)srow * 8u : WS_END, 0, 16);
            asm volatile("global_load_dwordx2 %0, %1, %2 sc1" : "=&v"(sg) : "v"(0u), "s"(flags.p) : "memory");
        }

        bf16x8 af[8], wf[5];
        f32x4 cs0 = (f32x4){0.f, 0.f, 0.f, 0.f}, cs1 = cs0, cs2 = cs0;
        bf16x8 one8 = (bf16x8){0x3f80, 0x3f80, 0x3f80, 0x3f80, 0x3f80, 0x3f80, 0x3f80, 0x3f80};
        PIN(cs0); PIN(cs1); PIN(cs2); PIN(one8);
#define CS1(c, w) "v_mfma_f32_16x16x32_bf16 %" #c ", %" #w ", %8, %" #c "\n\t"
#define G1_CSUM_(P0, P1) asm volatile("s_cmp_gt_u32 %10, 3\n\ts_cbranch_scc1 2f\n\ts_cmp_lg_u32 %9, 0\n\ts_cbranch_scc1 1f\n\t" CS1(0, 3) CS1(1, 4) P0 "s_branch 2f\n1:\n\t" CS1(0, 5) CS1(1, 6) P1 "\n2:" \
            : "+v"(cs0), "+v"(cs1), "+v"(cs2) : "v"(wf[0]), "v"(wf[1]), "v"(wf[2]), "v"(wf[3]), "v"(wf[4]), "v"(one8), "s"(wr), "s"(t) : "scc")
#define G1_CSUM(kk) do { if ((kk) == 0) G1_CSUM_(CS1(2, 7), ""); else G1_CSUM_("", CS1(2, 7)); } while (0)
#define G1_RW(i, B, slot) wf[i] = cat8(lds_tr(B + (slot) * 4096), lds_tr(B + (slot) * 4096 + 512))
#define G1_RA(m, AB, abuf) af[m] = lds_b128(AB + (abuf) * 32768 + (m) * 2048)
#define G1_SEG(AB, abuf, slot, L0, L1, L2, L3, L4, CLOSE) do { \
        L0; G1_RW(0, wB0, slot); G1_RW(1, wB1, slot); L1; G1_RW(2, wB2, slot); G1_RW(3, wB3, slot); L2; G1_RW(4, wB4, slot); G1_RA(0, AB, abuf); G1_RA(1, AB, abuf); \
        L3; G1_RA(2, AB, abuf); G1_RA(3, AB, abuf); G1_RA(4, AB, abuf); L4; G1_RA(5, AB, abuf); G1_RA(6, AB, abuf); G1_RA(7, AB, abuf); \
        WAIT_L0(); SBAR(); if (CLOSE) { BAR(); SBAR(); } } while (0)
#define G1_SEG_A(abuf, slot, tt) do { const char* b_ = ((tt) < 4 ? XA : XC) + (tt) * 128; const unsigned d_ = dstA + (1 - (abuf)) * 32768; \
        G1_SEG(aB0, abuf, slot, glds16c(b_, voffA0, d_), glds16c(b_, voffA1, d_ + 1024), glds16c(b_ + 32768, voffA0, d_ + 2048), glds16c(b_ + 32768, voffA1, d_ + 3072), (void)0, true); } while (0)
#define G1_SEG_W(abuf, slot, hs, dslot, CLOSE) do { const unsigned d_ = dstW + (dslot) * 4096; \
        G1_SEG(aB1, abuf, slot, glds16(P0 + (size_t)(hs) * stepF, voffF, d_), glds16(P1 + (size_t)(hs) * step13, voff13, d_ + 16384), glds16(P2 + (size_t)(hs) * stepF, voffF, d_ + 32768), \
               glds16(P3 + (size_t)(hs) * step13, voff13, d_ + 49152), glds16(P4 + (size_t)(hs) * stepQ, voffQ, d_ + 65536), CLOSE); } while (0)
#define EB 2
#define FIRST_WAIT() asm volatile("s_waitcnt vmcnt(4)" : "+v"(sg), "+v"(cpd) :: "memory")
#define G1_MM(i0, i1) _Pragma("unroll") for (int i = (i0); i < (i1); ++i) { const int b = i >> 3, m = (b & 1) ? 7 - (i & 7) : (i & 7); MFMA16_ACC(acc[m][b], wf[b], af[m]); }
#define G1_MMA(kk, CLOSE) do { __builtin_amdgcn_s_setprio(1); G1_MM(0, 40 - EB); SBAR(); __builtin_amdgcn_s_setprio(2); CLOSE; SBAR(); G1_MM(40 - EB, 40); G1_CSUM(kk); __builtin_amdgcn_s_setprio(0); SBAR(); } while (0)

        WAIT_L0();

        BAR();
        SBAR();

        flags.tag = 0;
        const u64* sgp = flags.p + (F_XC + rb * 32 + (lane & 31)) * 16;
#pragma unroll 1
        for (int t = 0; t < 16; t += 2) {
            const int h1 = (2 * t + 3 + wr < 31) ? 2 * t + 3 + wr : 31, h2 = (2 * t + 5 + wr < 31) ? 2 * t + 5 + wr : 31, t2 = (t < 14) ? t + 2 : 15;
            const int sel = (t == 12) ? 0 : ((t ^ 2) | wid);

            PEEK_AHEAD(sg, sgp, sel);
            if (t == 14) CODE_PF(cpd);
            G1_SEG_A(0, 0, t + 1); G1_MMA(0, { FIRST_WAIT(); BAR(); });
            if (t == 0) {
                const unsigned elo = __builtin_amdgcn_readfirstlane((unsigned)sg), ehi = __builtin_amdgcn_readfirstlane((unsigned)(sg >> 32));
                flags.tag = (((u64)ehi << 32) | elo) * 0x9E3779B97F4A7C15ull + 0x632BE59BD9B4E019ull;
                if (wid == 4) signal(flags, F_XC + bid, lane);
            }
            if (sel == 0) {
                if (t == 12) *(LAS u64*)(lds + G1_STAT + (wid * 32 + (opaque_tid() & 31)) * 8) = sg;
                else wait_all(flags, F_XC + rb * 32 + (opaque_tid() & 31), sg);
            }
            G1_SEG_W(0, 1, h1, (3 + wr) & 3, true); G1_MMA(1, { WAIT_V(5); BAR(); });
            if (t == 4) {
                const int tl = opaque_tid() & 63, k = tl & 15;
                if (k < 3) *(LAS f32x4*)(lds + G1_CS + wc * 384 + (tl >> 4) * 16 + (k == 2 ? 256 + wr * 64 : wr * 128 + k * 64)) = k == 0 ? cs0 : k == 1 ? cs1 : cs2;
                sgp = (const u64*)(ws + WS_STAT) + rb * 256 + wid * 32 + (tl & 31);
            }
            G1_SEG_A(1, 2, t2); G1_MMA(0, { WAIT_V(4); BAR(); });
            G1_SEG_W(1, 3, h2, (1 + wr) & 3, true);
            G1_MMA(1, asm volatile("s_waitcnt vmcnt(5)\n\ts_cmp_eq_u32 %0, 15\n\ts_cbranch_scc1 1f\n\ts_barrier\n1:" :: "s"(t + wr) : "memory", "scc"));
        }
        MFMA_DRAIN(); SBAR();

        const int tz = opaque_tid(), ln = tz & 63;
        fr = tz & 15; fq = (tz >> 4) & 3;
        const int row0 = rb * 256 + wr * 128 + fr;
        f32x4 csv[5]; f32x2 st[8];
#pragma unroll
        for (int b = 0; b < 5; ++b) csv[b] = *(const LAS f32x4*)(lds + G1_CS + wc * 384 + b * 64 + fq * 16);
        csv[4] += *(const LAS f32x4*)(lds + G1_CS + wc * 384 + 320 + fq * 16);
#pragma unroll
        for (int m = 0; m < 8; ++m) st[m] = *(const LAS f32x2*)(lds + G1_STAT + (wr * 128 + m * 16 + fr) * 8);
        auto lnv = [&](int m, int b, float sc) { return (acc[m][b] - st[m].x * csv[b]) * (st[m].y * sc); };
        auto glu = [&](int m, int bg, int bu) {
            const f32x4 g = lnv(m, bg, -LOG2E), u = lnv(m, bu, 1.0f);
            f32x4 h;
#pragma unroll
            for (int j = 0; j < 4; ++j) h[j] = u[j] * __builtin_amdgcn_rcpf(1.0f + __builtin_amdgcn_exp2f(g[j]));
            return h;
        };
        auto pack2 = [&](const f32x4& v0, const f32x4& v1) {
            unsigned p0 = cvtpk(v0[0], v0[1]), p1 = cvtpk(v0[2], v0[3]), q0 = cvtpk(v1[0], v1[1]), q1 = cvtpk(v1[2], v1[3]);
            swap16(p0, q0); swap16(p1, q1);
            return (u32x4){p0, p1, q0, q1};
        };
        const int hd = 4 * xcd + (typeY ? 2 : 0) + (wc >> 1);
        const unsigned qoff = WS_QKV + (unsigned)(hd * 2 + (wc & 1)) * 65536u + (unsigned)(row0 + (fq & 1) * 16) * 32u + (fq >> 1) * 16;
        constexpr unsigned KIND = NH * 2 * 65536u;
        if (!typeY) {
            const float sc4 = (js == 0) ? QSCALE : 1.0f;
#pragma unroll
            for (int m = 0; m < 8; m += 2) ST16(pack2(lnv(m, 4, sc4), lnv(m + 1, 4, sc4)), qoff + js * KIND + m * 512);
            CFENCE();
            hp.base = ws + WS_ACT + (unsigned)(14 * xcd + 4 * js + wc) * GRP + (unsigned)(rb * 256 + wr * 128) * 64u;
            hp.off = fr * 64 + (fq & 1) * 32 + (fq >> 1) * 16; hp.size = 8192;
#pragma unroll
            for (int m = 0; m < 8; ++m) {
                hp.v[m] = pack2(glu(m, 0, 2), glu(m, 1, 3));
                if (m == 5) { PIN(hp.v[5]); WAIT_V(0); arrive(flags, F_QKV + bid, ln, lds + G1_CNT, 7u); }
            }
        } else {
#pragma unroll
            for (int m = 0; m < 8; m += 2) {
                ST16(pack2(lnv(m, 3, 1.0f), lnv(m + 1, 3, 1.0f)), qoff + KIND + m * 512);
                ST16(pack2(lnv(m, 4, 1.0f), lnv(m + 1, 4, 1.0f)), qoff + 2 * KIND + m * 512);
                ST16(pack2(lnv(m, 1, QSCALE), lnv(m + 1, 1, QSCALE)), qoff + m * 512);
            }
            CFENCE();
            hp.base = ws + WS_ACT + (unsigned)(14 * xcd + 12 + (wc >> 1)) * GRP + (wc & 1) * (GRP / 2) + (unsigned)(rb * 256 + wr * 128) * 32u;
            hp.off = (fr + (fq & 1) * 16) * 32 + (fq >> 1) * 16; hp.size = 4096;
#pragma unroll
            for (int m = 0; m < 8; m += 2) { hp.v[m >> 1] = pack2(glu(m, 0, 2), glu(m + 1, 0, 2)); hp.v[4 + (m >> 1)] = hp.v[m >> 1]; }
            PIN(hp.v[3]); WAIT_V(0); arrive(flags, F_QKV + bid, ln, lds + G1_CNT, 7u);
        }
#pragma unroll
        for (int i = 0; i < 8; ++i) PIN(hp.v[i]);

    }

    const int z = xcd & 1, rbk = ((xcd >> 1) & 1) * 8 + (idx & 7), cbk = (xcd >> 2) * 4 + (idx >> 3);
    G2 g2;
    g2.nh = z ? 56 - NH0 : NH0;
    g2.rowb = rbk * 128 * 64;
    g2.split = z ? split_mask(NH0, 56 - NH0) : split_mask(0, NH0);
    g2.Ah = (const char*)ws + WS_ACT + (size_t)(z * 2 * NH0) * GRP;
    g2.Ao = (const char*)ws + WS_ACT + (size_t)(112 + z * 16 - 2 * g2.nh) * GRP;
    g2.Wd = (const char*)(wd + (size_t)z * NH0 * 64 * DM + cbk * 128);
    g2.Wo = (const char*)(wo + (size_t)z * 512 * DM + cbk * 128) - (size_t)g2.nh * 64 * DM * 2;
    {
        const int hl = idx & 3, qb = idx >> 2, h = 4 * xcd + hl;
        const int l0_ = opaque_tid() & 63;
        const int qkv0 = F_QKV + xcd + 8 * (qb * 4 + ((hl >= 2) ? 3 : l0_ % 3));
        const int qkvword = F_QKV + xcd + 8 * ((l0_ & 7) * 4 + ((hl >= 2) ? 3 : 1 + ((l0_ >> 3) & 1)));
        const int odep = (rbk >> 1) * 32 + ((l0_ >> 2) & 3) * 8 + 4 * z + (l0_ & 3);
        const int depword = (l0_ & 16) ? (odep & ~7) + 4 : odep;
        const char* Qg = (const char*)ws + WS_QKV + (size_t)h * 131072 + qb * 256 * 32;
        const char* Kg = (const char*)ws + WS_QKV + (size_t)(NH + h) * 131072;
        const char* Vg = (const char*)ws + WS_QKV + (size_t)(2 * NH + h) * 131072;
        const char* wpd = g2.Wd - (cbk & 3) * 256;
        const char* wpo = g2.Wo - (cbk & 3) * 256;
        AttnOut R;
        attn_fast(lds, lds0, Qg, Kg, Vg, g2, flags, qkv0, qkvword, depword, F_H + bid, qb, bid == 0, cp, hp, wpd, wpo, idx * 2 * (g2.nh + 8), wid, R);

        const int tz = opaque_tid(), ln = tz & 63, w4 = wid & 3, fr = ln & 15, fq = ln >> 4;
        lds_ptr po0 = lds + 2 * SLOT + w4 * 4096 + ln * 16, po1 = lds + 2 * SLOT + 16384 + w4 * 2048 + ln * 16;
        PIN(po0); PIN(po1);
        auto po = [&](int i) { return i < 4 ? po0 + i * 1024 : po1 + (i >> 1 & 1) * 8192 + (i & 1) * 1024; };
        const lds_ptr pl = po0 + SLOT;
        volatile LAS unsigned* const badw = (volatile LAS unsigned*)(lds + 3 * SLOT + 1024);
        const int wm = w4 >> 1, wn = w4 & 1;
        const unsigned aO0 = (wm * 64 + fr) * 128 + ((fq ^ (fr >> 1)) * 16), aO1 = aO0 ^ 64;
        const int tq = (ln >> 2) & 3, tp = ln & 3, tg = (tq & 2) | ((fq & 1) << 2);
        unsigned wO[4];
#pragma unroll
        for (int b = 0; b < 4; ++b) wO[b] = 16384 + wn * 8192 + (8 * fq + tq) * 128 + (((2 * b + (tp >> 1)) ^ tg) * 16) + (tp & 1) * 8;
        f32x4 acc[4][4];
        bf16x8 xa[4], ya[4]; s16x4 xl[4], xh[4], yl[4], yh[4];
        lds_ptr pa0, pa1, pw0, pw1, pw2, pw3;
        int so = 4 * SLOT;
        auto handover = [&]() {
            if (wid < 4) {
#pragma unroll
                for (int i = 0; i < 4; ++i) {
                    *(LAS f32x4*)po(i) = (f32x4){R.o0[4 * i], R.o0[4 * i + 1], R.o0[4 * i + 2], R.o0[4 * i + 3]};
                    *(LAS f32x4*)po(4 + i) = (f32x4){R.o1[4 * i], R.o1[4 * i + 1], R.o1[4 * i + 2], R.o1[4 * i + 3]};
                }
                *(LAS f32x4*)pl = (f32x4){R.l0[0], R.m0, R.l1[0], R.m1};
                xl[0] = lds_tr(pw0); xh[0] = lds_tr(pw0 + 512);
#pragma unroll
                for (int m = 0; m < 4; ++m) xa[m] = lds_b128(pa0 + m * 2048);
                xl[1] = lds_tr(pw1); xh[1] = lds_tr(pw1 + 512); xl[2] = lds_tr(pw2); xh[2] = lds_tr(pw2 + 512); xl[3] = lds_tr(pw3); xh[3] = lds_tr(pw3 + 512);
#pragma unroll
                for (int m = 0; m < 4; ++m)
#pragma unroll
                    for (int b = 0; b < 4; ++b) { acc[m][b] = (f32x4){0.f, 0.f, 0.f, 0.f}; PIN(acc[m][b]); }
            }
        };
        G2Off go; g2_voff(ln, go);
        PIN(go.A0); PIN(go.A1); PIN(go.N0); PIN(go.N1); PIN(go.B0); PIN(go.B1);
        pa0 = lds + aO0 + so; pa1 = lds + aO1 + so; pw0 = lds + wO[0] + so; pw1 = lds + wO[1] + so; pw2 = lds + wO[2] + so; pw3 = lds + wO[3] + so;
        PIN(pa0); PIN(pa1); PIN(pw0); PIN(pw1); PIN(pw2); PIN(pw3);
        {
            handover();
            const f32x16 z16 = R.o0 * 0.0f + R.o1 * 0.0f;
            const f32x4 z4 = (z16.s0123 + z16.s4567) + (z16.s89ab + z16.scdef);
            const f32x2 z2 = z4.lo + z4.hi;
            const float zs = z2.x + z2.y;
            const bool ok = (zs == 0.f) && (R.l0[0] > 0.f) && (R.l0[0] < 0x1p96f) && (R.l1[0] > 0.f) && (R.l1[0] < 0x1p96f);
            const bool wave_bad = !__all(ok);
            if (ln == 0) badw[wid] = wave_bad ? 1u : 0u;

            WAIT_L0(); BAR(); CFENCE();
            const unsigned anybad = badw[ln & 7];
            if (__builtin_expect(__any(anybad != 0u), 0)) {
                WAIT_L0(); BAR(); CFENCE();
                attn_safe(lds, lds0, Qg, Kg, Vg, wid, R);
                handover();
                WAIT_L0(); BAR(); CFENCE();
            }
        }

        const int tile = rbk * 8 + cbk, nt = g2.nh + 8;
        if (__builtin_expect(wid >= 4, 0)) {
#define G2_LOAD0(j, sl) do { const unsigned d_ = lds0 + (sl) * SLOT; g2_load_b(g2, j, d_, 2 * w4, go.B0); g2_load_a(g2, j, d_, 2 * w4, go); } while (0)
#define G2_LOAD1(j, sl) do { const unsigned d_ = lds0 + (sl) * SLOT; g2_load_b(g2, j, d_, 2 * w4 + 1, go.B1); g2_load_a(g2, j, d_, 2 * w4 + 1, go); } while (0)
#define G2_LOAD(j, sl) do { G2_LOAD0(j, sl); G2_LOAD1(j, sl); } while (0)
            const f32x4 lm = *(const LAS f32x4*)pl;
            f32x4 t[8];
#pragma unroll
            for (int i = 0; i < 8; ++i) t[i] = *(const LAS f32x4*)po(i);
            WAIT_L0();
            G2_LOAD(3, 2);
            BAR();
            G2_LOAD(4, 3);
            float ck[2], co[2]; unsigned p[16];
            auto coef = [&](int q) {
                const float lk = q ? R.l1[0] : R.l0[0], mk = q ? R.m1 : R.m0, lo = lm[2 * q], mo = lm[2 * q + 1];
                const float mm = __builtin_fmaxf(mk, mo), fk = __builtin_amdgcn_exp2f(mk - mm), fo = __builtin_amdgcn_exp2f(mo - mm);
                const float rl = 1.0f / (lk * fk + lo * fo);
                ck[q] = fk * rl; co[q] = fo * rl; PIN(ck[q]); PIN(co[q]); SBAR();
            };
            auto half = [&](int q, int hh) {
                const f32x16 ok_ = q ? R.o1 : R.o0;
                unsigned* pp = p + 8 * q + 4 * hh;
#pragma unroll
                for (int i = 0; i < 2; ++i) {
                    const int g = 2 * hh + i; const f32x4 u = t[4 * q + g];
                    pp[2 * i] = cvtpk(ok_[4 * g] * ck[q] + u[0] * co[q], ok_[4 * g + 1] * ck[q] + u[1] * co[q]);
                    pp[2 * i + 1] = cvtpk(ok_[4 * g + 2] * ck[q] + u[2] * co[q], ok_[4 * g + 3] * ck[q] + u[3] * co[q]);
                }
                swap32(pp[0], pp[2]); swap32(pp[1], pp[3]);
                PIN(pp[0]); PIN(pp[1]); PIN(pp[2]); PIN(pp[3]); SBAR();
            };
            const unsigned ooff = WS_ACT + (unsigned)(112 + h) * GRP + (unsigned)(qb * 256 + w4 * 64 + fr) * 64u + (fq & 1) * 32 + (fq >> 1) * 16;
            auto ost = [&](int q) {
                unsigned* pp = p + 8 * q;
                swap16(pp[0], pp[4]); swap16(pp[1], pp[5]); swap16(pp[2], pp[6]); swap16(pp[3], pp[7]);
                ST16(((u32x4){pp[0], pp[1], pp[2], pp[3]}), ooff + q * 2048); ST16(((u32x4){pp[4], pp[5], pp[6], pp[7]}), ooff + q * 2048 + 1024); CFENCE();
            };
            SBAR(); coef(0); coef(1);
            WAIT_V(8);  BAR(); G2_LOAD(5, 4); G2_LOAD0(6, 0);
            WAIT_V(12); BAR(); G2_LOAD1(6, 0); G2_LOAD(7, 1);
            WAIT_V(16); BAR(); G2_LOAD0(8, 2);  SBAR(); half(0, 0); G2_LOAD1(8, 2);
            WAIT_V(16); BAR(); G2_LOAD0(9, 3);  SBAR(); half(0, 1); G2_LOAD1(9, 3);  ost(0);
            WAIT_V(18); BAR(); G2_LOAD0(10, 4); SBAR(); half(1, 0); G2_LOAD1(10, 4);
            WAIT_V(18); BAR(); G2_LOAD0(11, 0); SBAR(); half(1, 1); G2_LOAD1(11, 0); ost(1);
            WAIT_V(20); BAR(); G2_LOAD(12, 1);
            WAIT_V(18); BAR(); G2_LOAD(13, 2);
            WAIT_V(18);
            int sl = 3; cpd = 0;
#pragma unroll 1
            for (int j = 11; j < nt - 3; ++j) {
                BAR();
                if (j == 29) CODE_PF(cpd);
                if (j == 12 && wid == 4) signal(flags, F_O + bid, ln);
                G2_LOAD(j + 3, sl);
                sl = (sl == 4) ? 0 : sl + 1;
                WAIT_V(16);
            }
            BAR();
            if (tz == 256) *(LAS unsigned*)(lds + sl * SLOT) = 0u;
            WAIT_V(8); WAIT_L0(); BAR(); WAIT_V(0); BAR();
            asm volatile("" :: "v"(cpd));
            if (wid == 4) wait_all(flags, F_X + tile * 2 + z, peek(flags, F_X + tile * 2 + z));
            CFENCE(); BAR();
            asm volatile(".Lk_end:");
        } else {
#define G2_M(C, m, b) do { MFMA16_ACC(acc[m][b], cat8(C##l[b], C##h[b]), C##a[m]); SBAR(); } while (0)
#define G2_HALF(C, N, AB, WO, RD, TAIL) do { \
            G2_M(C, 0, 0); if (RD) { N##l[0] = lds_tr(pw0 + (WO)); N##h[0] = lds_tr(pw0 + (WO) + 512); } SBAR(); \
            G2_M(C, 1, 0); if (RD) N##a[0] = lds_b128(AB); SBAR(); \
            G2_M(C, 2, 0); if (RD) N##a[1] = lds_b128((AB) + 2048); SBAR(); \
            G2_M(C, 3, 0); if (RD) N##a[2] = lds_b128((AB) + 4096); SBAR(); \
            G2_M(C, 0, 1); if (RD) N##a[3] = lds_b128((AB) + 6144); SBAR(); \
            G2_M(C, 1, 1); if (RD) { N##l[1] = lds_tr(pw1 + (WO)); N##h[1] = lds_tr(pw1 + (WO) + 512); } SBAR(); \
            G2_M(C, 2, 1); if (RD) { N##l[2] = lds_tr(pw2 + (WO)); N##h[2] = lds_tr(pw2 + (WO) + 512); } SBAR(); \
            G2_M(C, 3, 1); if (RD) { N##l[3] = lds_tr(pw3 + (WO)); N##h[3] = lds_tr(pw3 + (WO) + 512); } SBAR(); \
            G2_M(C, 0, 2); TAIL(0); SBAR(); G2_M(C, 1, 2); TAIL(1); SBAR(); G2_M(C, 2, 2); TAIL(2); SBAR(); G2_M(C, 3, 2); TAIL(3); SBAR(); \
            G2_M(C, 0, 3); G2_M(C, 1, 3); G2_M(C, 2, 3); G2_M(C, 3, 3); } while (0)
            const u64* const optr = flags.p + (F_O + odep) * 16;
            u64 ov = 0;
#define G2_NEXT(i) do { if ((i) == 0) so = (so == 4 * SLOT) ? 0 : so + SLOT; \
                if ((i) == 1) { pa0 = lds + aO0 + so; pa1 = lds + aO1 + so; PIN(pa0); PIN(pa1); } \
                if ((i) == 2) { pw0 = lds + wO[0] + so; pw1 = lds + wO[1] + so; PIN(pw0); PIN(pw1); } \
                if ((i) == 3) { pw2 = lds + wO[2] + so; pw3 = lds + wO[3] + so; PIN(pw2); PIN(pw3); } } while (0)
#define G2_NONE(i) do {} while (0)
            WAIT_L0(); SBAR(); G2_HALF(x, y, pa1, 4096, true, G2_NEXT);
            WAIT_L0(); SBAR(); G2_HALF(y, x, pa0, 0, true, G2_NONE);
            BAR();
            WAIT_L0(); SBAR(); G2_HALF(x, y, pa1, 4096, true, G2_NEXT);
            WAIT_L0(); SBAR(); G2_HALF(y, x, pa0, 0, true, G2_NONE);
            if (NH0 & 1) {
                WAIT_L0(); SBAR(); G2_HALF(x, y, pa1, 4096, true, G2_NEXT);
                WAIT_L0(); BAR(); SBAR(); G2_HALF(y, x, pa0, 0, true, G2_NONE);
            }
#pragma unroll 1
            for (int ph = 0; ph < 3; ++ph) {
                const int n = (ph == 0) ? g2.nh / 2 - 4 : (ph == 1) ? 1 : 5;
#pragma unroll 1
                for (int i = 0; i < n; ++i) {
                    WAIT_L0(); SBAR(); G2_HALF(x, y, pa1, 4096, true, G2_NEXT);
                    WAIT_L0(); BAR(); SBAR(); G2_HALF(y, x, pa0, 0, true, G2_NONE);
                    WAIT_L0(); SBAR(); G2_HALF(x, y, pa1, 4096, true, G2_NEXT);
                    WAIT_L0(); BAR(); SBAR(); G2_HALF(y, x, pa0, 0, true, G2_NONE);
                }
                PEEK_AHEAD(ov, optr, wid | ph);
                if ((wid | (ph ^ 1)) == 0) { PEEK_DONE(ov); wait_all(flags, F_O + odep, ov); }
            }
            const int row = rbk * 128 + wm * 64 + z * 32 + fr, col = cbk * 128 + wn * 64 + (fq & 1) * 16 + (fq >> 1) * 8;
            u32x4 xr[4];
#pragma unroll
            for (int i = 0; i < 4; ++i) xr[i] = *(const u32x4*)(x + (size_t)(row + (i >> 1) * 16) * DM + col + (i & 1) * 32);
            WAIT_L0(); SBAR(); G2_HALF(x, y, pa1, 4096, true, G2_NEXT);
            WAIT_L0(); BAR(); SBAR(); G2_HALF(y, x, pa0, 0, true, G2_NONE);
            WAIT_L0(); SBAR(); G2_HALF(x, y, pa1, 4096, true, G2_NONE);
            WAIT_L0(); SBAR(); G2_HALF(y, x, pa0, 0, false, G2_NONE);
            MFMA_DRAIN(); SBAR();

            f32x4 vk[8];
            const unsigned xo = WS_XCH + (unsigned)(tile * 2 + 1 - z) * 32768u + w4 * 8192 + ln * 16;
            if (z == 0) {
#pragma unroll
                for (int i = 0; i < 8; ++i) { ST16(__builtin_bit_cast(u32x4, acc[2 + (i >> 2)][i & 3]), xo + i * 1024); vk[i] = acc[i >> 2][i & 3]; }
            } else {
#pragma unroll
                for (int i = 0; i < 8; ++i) { ST16(__builtin_bit_cast(u32x4, acc[i >> 2][i & 3]), xo + i * 1024); vk[i] = acc[2 + (i >> 2)][i & 3]; }
            }
            WAIT_V(0);
            arrive(flags, F_X + tile * 2 + 1 - z, ln, lds + ((so == 4 * SLOT) ? 0 : so + SLOT), 3u);

            CFENCE(); BAR(); CFENCE();

            const unsigned xi = WS_XCH + (unsigned)(tile * 2 + z) * 32768u + w4 * 8192 + ln * 16;
#pragma unroll
            for (int i = 0; i < 8; ++i) vk[i] += __builtin_bit_cast(f32x4, __builtin_amdgcn_raw_buffer_load_b128(wsr, xi + i * 1024, 0, 16));
            const __amdgpu_buffer_rsrc_t outr = __builtin_amdgcn_make_buffer_rsrc((void*)out, 0, SEQ * DM * 2, 0x00020000);
            u32x4 w[4];
#pragma unroll
            for (int i = 0; i < 4; ++i) {
                const u32x4 a = __builtin_bit_cast(u32x4, vk[2 * i]), b = __builtin_bit_cast(u32x4, vk[2 * i + 1]);
                unsigned a0 = a[0], a1 = a[1], a2 = a[2], a3 = a[3], b0 = b[0], b1 = b[1], b2 = b[2], b3 = b[3];
                swap16(a0, b0); swap16(a1, b1); swap16(a2, b2); swap16(a3, b3);
                const u32x4 xx = xr[i];
                w[i][0] = cvtpk(u2f(a0) + bf_lo(xx[0]), u2f(a1) + bf_hi(xx[0]));
                w[i][1] = cvtpk(u2f(a2) + bf_lo(xx[1]), u2f(a3) + bf_hi(xx[1]));
                w[i][2] = cvtpk(u2f(b0) + bf_lo(xx[2]), u2f(b1) + bf_hi(xx[2]));
                w[i][3] = cvtpk(u2f(b2) + bf_lo(xx[3]), u2f(b3) + bf_hi(xx[3]));
            }
            const unsigned oo = (unsigned)((row - (fr & 8)) * DM + col + (fr & 8) * 4) * 2u;
#pragma unroll
            for (int m = 0; m < 2; ++m) {
                u32x4 lo, hi;
#pragma unroll
                for (int k = 0; k < 4; ++k) {
                    lo[k] = __builtin_amdgcn_update_dpp(w[2 * m][k], w[2 * m + 1][k], 0x128, 0xf, 0xc, false);
                    hi[k] = __builtin_amdgcn_update_dpp(w[2 * m + 1][k], w[2 * m][k], 0x128, 0xf, 0x3, false);
                }
                __builtin_amdgcn_raw_buffer_store_b128(lo, outr, oo + m * 16 * DM * 2, 0, 16);
                __builtin_amdgcn_raw_buffer_store_b128(hi, outr, oo + (m * 16 + 8) * DM * 2, 0, 16);
            }
        }
    }
}

extern "C" void kernel_launch(void* const* d_in, const int* in_sizes, int n_in, void* d_out, int out_size, void* d_ws, size_t ws_size, hipStream_t stream) {
    const bf16_t* x = (const bf16_t*)d_in[0];
    k_main<<<256, 512, 0, stream>>>(x, (const bf16_t*)d_in[1], (const bf16_t*)d_in[2], (const bf16_t*)d_in[4], (const bf16_t*)d_in[5], (const bf16_t*)d_in[6],
                                    (bf16_t*)d_out, (unsigned char*)d_ws);
}
```

```cpp
#include <hip/hip_runtime.h>
#include <stdint.h>
#define MK 99

typedef unsigned short bf16_t;
typedef short bf16x8 __attribute__((ext_vector_type(8)));
typedef short s16x4 __attribute__((ext_vector_type(4)));
typedef float f32x2 __attribute__((ext_vector_type(2)));
typedef float f32x4 __attribute__((ext_vector_type(4)));
typedef float f32x16 __attribute__((ext_vector_type(16)));
typedef unsigned u32x2 __attribute__((ext_vector_type(2)));
typedef unsigned u32x4 __attribute__((ext_vector_type(4)));
typedef __bf16 bf16x2_t __attribute__((ext_vector_type(2)));
#define LAS __attribute__((address_space(3)))
#define GAS __attribute__((address_space(1)))
typedef LAS unsigned char* lds_ptr;

constexpr int SEQ = 2048, DM = 1024, FF = 3584, NQKV = 3072, NH = 32;
constexpr float LN_EPS = 1e-5f;
constexpr float LOG2E = 1.4426950408889634f;
constexpr float QSCALE = 0.17677669529663687f * LOG2E;

constexpr unsigned WS_FLAGS = 0;
constexpr unsigned WS_STAT = 192 * 1024;
constexpr unsigned WS_QKV = 256 * 1024;
constexpr unsigned WS_ACT = WS_QKV + 3u * SEQ * DM * 2;
constexpr unsigned WS_XCH = WS_ACT + 144u * SEQ * 64;
constexpr unsigned WS_XC = WS_XCH + 256u * 32768;
constexpr unsigned WS_END = WS_XC + SEQ * DM * 2;
constexpr int F_EPOCH = 0, F_QKV = 1, F_H = 257, F_O = 513, F_X = 769, F_XC = 1025;
constexpr unsigned GRP = SEQ * 64;

#define SBAR() __builtin_amdgcn_sched_barrier(0)
#define BAR() __builtin_amdgcn_s_barrier()
#define WAIT_V(n) asm volatile("s_waitcnt vmcnt(" #n ")" ::: "memory")
#define WAIT_L0() do { __builtin_amdgcn_s_waitcnt(0xC07F); asm volatile("" ::: "memory"); } while (0)
#define CFENCE() asm volatile("" ::: "memory")
#define PIN(x) asm volatile("" : "+v"(x))
#define MFMA16(a, b, c) __builtin_amdgcn_mfma_f32_16x16x32_bf16(a, b, c, 0, 0, 0)
#define MFMA32(a, b, c) __builtin_amdgcn_mfma_f32_32x32x16_bf16(a, b, c, 0, 0, 0)
#define MFMA16_ACC(acc, a, b) asm volatile("v_mfma_f32_16x16x32_bf16 %0, %1, %2, %0" : "+v"(acc) : "v"(a), "v"(b))
#define MFMA32_ACC(acc, a, b) asm volatile("v_mfma_f32_32x32x16_bf16 %0, %1, %2, %0" : "+v"(acc) : "v"(a), "v"(b))
#define MFMA32_SET(d, a, b, c) asm volatile("v_mfma_f32_32x32x16_bf16 %0, %1, %2, %3" : "=&v"(d) : "v"(a), "v"(b), "v"(c))
#define MFMA_DRAIN() asm volatile("s_nop 15" ::: "memory")
#define RLX_AGENT __ATOMIC_RELAXED, __HIP_MEMORY_SCOPE_AGENT

#define DIAG
#define STAMP(i) do { if ((i) == MK) { SBAR(); dg_r = (unsigned)__builtin_amdgcn_s_memrealtime(); __builtin_amdgcn_s_waitcnt(0xC07F); SBAR(); } } while (0)
#define TS(v) do {} while (0)
#define PUT(i, v) do {} while (0)

__device__ __forceinline__ void glds16(const void* sbase, unsigned voff, unsigned ldsdst) {
    asm volatile("s_mov_b32 m0, %2\n\ts_nop 0\n\tglobal_load_lds_dwordx4 %0, %1" ::"v"(voff), "s"(sbase), "s"(ldsdst) : "memory");
}
__device__ __forceinline__ void glds16c(const void* sbase, unsigned voff, unsigned ldsdst) {
    asm volatile("s_mov_b32 m0, %2\n\ts_nop 0\n\tglobal_load_lds_dwordx4 %0, %1 sc1" ::"v"(voff), "s"(sbase), "s"(ldsdst) : "memory");
}
__device__ __forceinline__ bf16x8 lds_b128(lds_ptr p) { return *(const LAS bf16x8*)p; }
__device__ __forceinline__ s16x4 lds_tr(lds_ptr p) { return __builtin_amdgcn_ds_read_tr16_b64_v4i16((LAS s16x4*)p); }
__device__ __forceinline__ bf16x8 cat8(s16x4 a, s16x4 b) { return __builtin_shufflevector(a, b, 0, 1, 2, 3, 4, 5, 6, 7); }
__device__ __forceinline__ unsigned cvtpk(float lo, float hi) {
    f32x2 v = {lo, hi};
    return __builtin_bit_cast(unsigned, __builtin_convertvector(v, bf16x2_t));
}
__device__ __forceinline__ float u2f(unsigned u) { return __builtin_bit_cast(float, u); }
__device__ __forceinline__ unsigned f2u(float f) { return __builtin_bit_cast(unsigned, f); }
__device__ __forceinline__ float bf_lo(unsigned u) { return u2f(u << 16); }
__device__ __forceinline__ float bf_hi(unsigned u) { return u2f(u & 0xffff0000u); }
__device__ __forceinline__ void swap16(unsigned& a, unsigned& b) {
    auto r = __builtin_amdgcn_permlane16_swap(a, b, false, false);
    a = r[0]; b = r[1];
}
__device__ __forceinline__ void swap32(unsigned& a, unsigned& b) {
    auto r = __builtin_amdgcn_permlane32_swap(a, b, false, false);
    a = r[0]; b = r[1];
}
__device__ __forceinline__ int opaque_tid() { int t = threadIdx.x; asm volatile("" : "+v"(t)); return t; }
typedef unsigned long long u64;
struct Flags { u64* p; u64 tag; };
__device__ __forceinline__ void signal(const Flags& f, int word, int lane) {
    if (lane == 0) __hip_atomic_store((GAS u64*)(f.p + word * 16), f.tag, RLX_AGENT);
}
__device__ __forceinline__ u64 peek(const Flags& f, int word) {
    u64 v;
    asm volatile("global_load_dwordx2 %0, %1, off sc1\n\ts_waitcnt vmcnt(0)" : "=&v"(v) : "v"(f.p + word * 16) : "memory");
    return v;
}
__device__ __forceinline__ void wait_all(const Flags& f, int word, u64 v) {
#pragma unroll 1
    for (unsigned spin = 0; spin < (1u << 20) && !__all(v == f.tag); ++spin) { __builtin_amdgcn_s_sleep(1); v = peek(f, word); }
}
__device__ __forceinline__ void arrive(const Flags& f, int word, int lane, lds_ptr cnt, unsigned last) {
    if (lane == 0 && __hip_atomic_fetch_add((LAS unsigned*)cnt, 1u, __ATOMIC_RELAXED, __HIP_MEMORY_SCOPE_WORKGROUP) == last)
        __hip_atomic_store((GAS u64*)(f.p + word * 16), f.tag, RLX_AGENT);
}
#define PEEK_AHEAD(v, ptr, sel) asm volatile("s_cmp_lg_u32 %2, 0\n\ts_cbranch_scc1 1f\n\tglobal_load_dwordx2 %0, %1, off sc1\n1:" : "+v"(v) : "v"(ptr), "s"(sel) : "memory", "scc")
struct CodePf { const char* base; unsigned len; int first; };
#define CODE_PF(d) do { unsigned o_ = (unsigned)(cp.first + (opaque_tid() & 15)) * 128u; o_ = (o_ < cp.len) ? o_ : 0u; \
    asm volatile("global_load_dword %0, %1, %2" : "+v"(d) : "v"(o_), "s"(cp.base) : "memory"); } while (0)
#define PEEK_DONE(v) asm volatile("s_waitcnt vmcnt(0)" : "+v"(v) :: "memory")

template <int CTRL> __device__ __forceinline__ float dpp_add(float v) { return v + u2f(__builtin_amdgcn_update_dpp(0u, f2u(v), CTRL, 0xf, 0xf, true)); }
__device__ __forceinline__ float red4(float v) {
    unsigned a = f2u(v), b = a;
    swap16(a, b);
    v = u2f(a) + u2f(b);
    a = f2u(v); b = a;
    swap32(a, b);
    return u2f(a) + u2f(b);
}
__device__ __forceinline__ float wave_sum(float v) {
    v = dpp_add<0xB1>(v); v = dpp_add<0x4E>(v); v = dpp_add<0x141>(v); v = dpp_add<0x140>(v);
    return red4(v);
}
__device__ __forceinline__ f32x2 row_stats(u32x4& a, u32x4& b) {
    float v[16];
#pragma unroll
    for (int i = 0; i < 4; ++i) { v[2 * i] = bf_lo(a[i]); v[2 * i + 1] = bf_hi(a[i]); v[8 + 2 * i] = bf_lo(b[i]); v[8 + 2 * i + 1] = bf_hi(b[i]); }
    float s = 0.f;
#pragma unroll
    for (int i = 0; i < 16; ++i) s += v[i];
    const float mu = wave_sum(s) * (1.0f / DM);
    float q = 0.f;
#pragma unroll
    for (int i = 0; i < 16; ++i) { v[i] -= mu; q += v[i] * v[i]; }
#pragma unroll
    for (int i = 0; i < 4; ++i) { a[i] = cvtpk(v[2 * i], v[2 * i + 1]); b[i] = cvtpk(v[8 + 2 * i], v[8 + 2 * i + 1]); }
    return (f32x2){mu, 1.0f / __builtin_sqrtf(wave_sum(q) * (1.0f / DM) + LN_EPS)};
}

constexpr int SLOT = 32768, LDS_BYTES = 5 * SLOT;
constexpr int G1_A = 0, G1_W = 65536, G1_STAT = G1_W + 5 * 16384, G1_CS = G1_STAT + 2048, G1_CNT = G1_CS + 4 * 6 * 64;

#define NH0 30
static_assert(NH0 >= 28 && NH0 <= 32, "");
struct G2 { const char* Ah; const char* Ao; const char* Wd; const char* Wo; int nh; unsigned rowb; u64 split; };
__device__ __forceinline__ void g2_load_b(const G2& g, int j, unsigned dst, int vw, unsigned voffB) {
    const char* b = (j >= g.nh ? g.Wo : g.Wd) + (size_t)(j * 64 + vw * 8) * (DM * 2);
    glds16(b, voffB, dst + 16384 + vw * 1024); glds16(b + 128, voffB, dst + 16384 + 8192 + vw * 1024);
}
struct G2Off { unsigned A0, A1, N0, N1, B0, B1; };
__device__ __forceinline__ void g2_load_a(const G2& g, int j, unsigned dst, int vw, const G2Off& o) {
    const unsigned sp = (unsigned)(g.split >> j) & 1u;
    const char* a = (j >= g.nh ? g.Ao : g.Ah) + (size_t)j * (2 * GRP) + ((g.rowb + vw * 1024) >> sp);
    glds16c(a, sp ? o.N0 : o.A0, dst + vw * 2048); glds16c(a, sp ? o.N1 : o.A1, dst + vw * 2048 + 1024);
}
__device__ __forceinline__ void g2_voff(int lane, G2Off& o) {
    const int r8 = lane >> 3, u8 = lane & 7, ua = u8 ^ (r8 >> 1), ub = ua ^ 4;
    o.A0 = (unsigned)(ua >> 2) * GRP + r8 * 64 + (ua & 3) * 16;
    o.A1 = (unsigned)(ub >> 2) * GRP + (r8 + 8) * 64 + (ub & 3) * 16;
    o.N0 = (unsigned)(ua >> 1) * (GRP / 2) + r8 * 32 + (ua & 1) * 16;
    o.N1 = (unsigned)(ub >> 1) * (GRP / 2) + (r8 + 8) * 32 + (ub & 1) * 16;
    o.B0 = (unsigned)(r8 * (DM * 2) + (u8 ^ (r8 & 2)) * 16);
    o.B1 = (unsigned)(r8 * (DM * 2) + (u8 ^ (4 | (r8 & 2))) * 16);
}
constexpr u64 split_mask(int first, int n) { u64 m = 0; for (int j = 0; j < n; ++j) if ((first + j) % 7 == 6) m |= 1ull << j; return m; }
static_assert(((split_mask(0, NH0) | split_mask(NH0, 56 - NH0)) & 7) == 0, "phase B's tail prefetches K-tiles 0..2 as plain groups");

struct AttnOut { f32x16 o0, o1; f32x4 l0, l1; float m0, m1; };
struct HPend { u32x4 v[8]; unsigned off, size; const void* base; };
struct AttnLane {
    unsigned voffK, voffV; lds_ptr kb0, kb1, vb; const char* qp; bf16x8 ones;
    __device__ __forceinline__ void init(lds_ptr lds, const char* Qg, int wid, int lane) {
        const int r32 = lane & 31, hi = lane >> 5, kh = wid >> 2, qg = wid & 3, ksw = (r32 >> 2) & 3;
        const int uk = (lane & 3) ^ ((lane >> 4) & 3), uv = lane & 3;
        voffK = (unsigned)((uk >> 1) * 65536 + wid * 1024 + (lane >> 2) * 32 + (uk & 1) * 16);
        voffV = (unsigned)((uv >> 1) * 65536 + wid * 1024 + (lane >> 2) * 32 + (uv & 1) * 16);
        kb0 = lds + kh * 8192 + r32 * 64 + ((hi ^ ksw) * 16); kb1 = lds + kh * 8192 + r32 * 64 + (((2 | hi) ^ ksw) * 16);
        vb = lds + 16384 + kh * 8192 + (4 * hi + ((lane >> 2) & 3)) * 64 + ((lane >> 4) & 1) * 32 + (lane & 3) * 8;
        qp = Qg + (qg * 64 + r32) * 32 + hi * 16;
        const short v = (((lane >> 4) & 1) == ((lane >> 2) & 1)) ? (short)0x3f80 : (short)0;
        ones = (bf16x8){v, v, v, v, v, v, v, v};
    }
};
#define EX(v) __builtin_amdgcn_exp2f(v)
#define AT_LOAD_K(c, slot) do { const unsigned d_ = lds0 + wid * 2048 + (slot) * SLOT; const char* k_ = Kg + (c) * 8192; glds16c(k_, A.voffK, d_); glds16c(k_ + 512, A.voffK, d_ + 1024); } while (0)
#define AT_LOAD_V(c, slot) do { const unsigned d_ = lds0 + wid * 2048 + (slot) * SLOT + 16384; const char* v_ = Vg + (c) * 8192; glds16c(v_, A.voffV, d_); glds16c(v_ + 512, A.voffV, d_ + 1024); } while (0)
#define AT_PACK(S, i) __builtin_bit_cast(bf16x8, (u32x4){cvtpk(S[i], S[i + 1]), cvtpk(S[i + 2], S[i + 3]), cvtpk(S[i + 4], S[i + 5]), cvtpk(S[i + 6], S[i + 7])})
#define AT_Q_LOAD() asm volatile("global_load_dwordx4 %0, %4, off sc1\n\tglobal_load_dwordx4 %1, %4, off offset:1024 sc1\n\tglobal_load_dwordx4 %2, %5, off sc1\n\tglobal_load_dwordx4 %3, %5, off offset:1024 sc1" \
        : "=&v"(q00), "=&v"(q10), "=&v"(q01), "=&v"(q11) : "v"(A.qp), "v"(qp2) : "memory")
#define AT_Q_WAIT(n) asm volatile("s_waitcnt vmcnt(" #n ")" : "+v"(q00), "+v"(q01), "+v"(q10), "+v"(q11) :: "memory")
__device__ __forceinline__ float rowmax16(const f32x16& s) {
    float a = s[0];
#pragma unroll
    for (int r = 1; r < 16; ++r) a = __builtin_fmaxf(a, s[r]);
    unsigned x = f2u(a), y = x; swap32(x, y);
    return __builtin_fmaxf(u2f(x), u2f(y));
}

__device__ __forceinline__ void attn_safe(lds_ptr lds, unsigned lds0, const char* Qg, const char* Kg, const char* Vg, int wid, AttnOut& R) {
    AttnLane A; A.init(lds, Qg, wid, opaque_tid() & 63);
    bf16x8 q00, q01, q10, q11; const char* qp2 = A.qp + 65536;
    AT_Q_LOAD(); AT_Q_WAIT(0);
    f32x16 o0 = (f32x16){}, o1 = (f32x16){};
    f32x4 l0 = (f32x4){0.f, 0.f, 0.f, 0.f}, l1 = l0;
    float m0 = -__builtin_inff(), m1 = m0;
#pragma unroll 1
    for (int c = 0; c < 8; ++c) {
        AT_LOAD_K(c, 3); AT_LOAD_V(c, 3);
        WAIT_V(0); BAR(); CFENCE();
#pragma unroll 1
        for (int t = 0; t < 4; ++t) {
            const int to = 3 * SLOT + t * 2048;
            const bf16x8 ka = lds_b128(A.kb0 + to), kb = lds_b128(A.kb1 + to);
            const bf16x8 va = cat8(lds_tr(A.vb + to), lds_tr(A.vb + to + 512)), vc = cat8(lds_tr(A.vb + to + 1024), lds_tr(A.vb + to + 1536));
            f32x16 s0 = MFMA32(kb, q01, MFMA32(ka, q00, (f32x16){})), s1 = MFMA32(kb, q11, MFMA32(ka, q10, (f32x16){}));
            const float n0 = __builtin_fmaxf(m0, rowmax16(s0)), n1 = __builtin_fmaxf(m1, rowmax16(s1));
            const float f0 = EX(m0 - n0), f1 = EX(m1 - n1);
            m0 = n0; m1 = n1; o0 *= f0; o1 *= f1; l0 *= f0; l1 *= f1;
#pragma unroll
            for (int r = 0; r < 16; ++r) { s0[r] = EX(s0[r] - n0); s1[r] = EX(s1[r] - n1); }
            const bf16x8 p00 = AT_PACK(s0, 0), p01 = AT_PACK(s0, 8), p10 = AT_PACK(s1, 0), p11 = AT_PACK(s1, 8);
            o0 = MFMA32(vc, p01, MFMA32(va, p00, o0)); l0 = MFMA16(A.ones, p01, MFMA16(A.ones, p00, l0));
            o1 = MFMA32(vc, p11, MFMA32(va, p10, o1)); l1 = MFMA16(A.ones, p11, MFMA16(A.ones, p10, l1));
        }
        WAIT_L0(); BAR(); CFENCE();
    }
    R.o0 = o0; R.o1 = o1; R.l0 = l0; R.l1 = l1; R.m0 = m0; R.m1 = m1;
}

__device__ __forceinline__ void attn_fast(lds_ptr lds, unsigned lds0, const char* Qg, const char* Kg, const char* Vg, const G2& g2, const Flags& flags, int qkv0, int qkvword, int depword,
                                          int hsig, int c0, bool bump, const CodePf& cp, const HPend& hp, const char* wpd, const char* wpo, int prow, int wid, AttnOut& R
                                          , unsigned& dg_c, unsigned& dg_r
                                          ) {
    const int lane = opaque_tid() & 63;
    AttnLane A; A.init(lds, Qg, wid, lane);
    lds_ptr kb0 = A.kb0, kb1 = A.kb1, vb = A.vb;
    PIN(kb0); PIN(kb1); PIN(vb);
    lds_ptr kh0 = kb0 + 2 * SLOT, kh1 = kb1 + 2 * SLOT, vh = vb + 2 * SLOT;
    PIN(kh0); PIN(kh1); PIN(vh);
    const bf16x8 ones = A.ones;
    const __amdgpu_buffer_rsrc_t hr = __builtin_amdgcn_make_buffer_rsrc((void*)hp.base, 0, hp.size, 0x00020000);
    f32x16 o0 = (f32x16){}, o1 = (f32x16){}, negm0 = (f32x16){}, negm1 = (f32x16){}, sA0, sA1, sB0, sB1;
    f32x4 l0 = (f32x4){0.f, 0.f, 0.f, 0.f}, l1 = l0;
    bf16x8 kfa, kfb, va, vc = (bf16x8){}, pf = (bf16x8){};
    PIN(o0); PIN(o1); PIN(l0); PIN(l1); PIN(vc); PIN(pf); PIN(A.voffK); PIN(A.voffV);
    G2Off go; g2_voff(lane, go);
    unsigned gA0 = go.A0, gA1 = go.A1, gB = (wid & 1) ? go.B1 : go.B0;
    unsigned pfo = (unsigned)(((lane >> 3) & 1) * (DM * 2) + (lane & 7) * 128);
    const u64* hptr = flags.p + (F_H + depword) * 16;
    const u64* qptr = flags.p + qkvword * 16;
    PIN(gA0); PIN(gA1); PIN(gB); PIN(pfo); PIN(hptr); PIN(qptr);
    if (wid == 0) {
        wait_all(flags, qkv0, peek(flags, qkv0));
        STAMP(13);
    }
    CFENCE(); BAR(); CFENCE();
    STAMP(4);
    AT_LOAD_K(c0, 0);
    glds16c(Qg, A.voffK, lds0 + wid * 2048 + 2 * SLOT); glds16c(Qg + 512, A.voffK, lds0 + wid * 2048 + 2 * SLOT + 1024);
    AT_LOAD_V(c0, 0);
    u64 hv = 0;
    float m0, m1;
    WAIT_V(2);
    PEEK_AHEAD(hv, qptr, wid);
    BAR(); SBAR();
    STAMP(15);
    const int qo = 2 * SLOT + (wid & 3) * 4096 - (wid >> 2) * 8192;
    const bf16x8 q00 = lds_b128(kb0 + qo), q10 = lds_b128(kb0 + qo + 2048), q01 = lds_b128(kb1 + qo), q11 = lds_b128(kb1 + qo + 2048);
    {
        kfa = lds_b128(kb0); kfb = lds_b128(kb1);
        sA0 = MFMA32(kfa, q00, negm0); sA1 = MFMA32(kfa, q10, negm1); sA0 = MFMA32(kfb, q01, sA0); sA1 = MFMA32(kfb, q11, sA1);
        kfa = lds_b128(kb0 + 2048); kfb = lds_b128(kb1 + 2048);
        m0 = rowmax16(sA0); m1 = rowmax16(sA1);
#pragma unroll
        for (int r = 0; r < 16; ++r) { sA0[r] -= m0; sA1[r] -= m1; negm0[r] = -m0; negm1[r] = -m1; }
        PIN(negm0); PIN(negm1);
    }
    unsigned pfd = 0;
    PEEK_DONE(hv);
    if (wid == 0) {
        wait_all(flags, qkvword, hv);
        if (bump) signal(flags, F_EPOCH, lane);
        STAMP(14);
    }
    CFENCE(); BAR(); SBAR();
    STAMP(5);
    AT_LOAD_K((c0 + 1) & 7, 1); AT_LOAD_V((c0 + 1) & 7, 1);
    STAMP(19);
#define AT_GROUP(QK, RD, S, i, O, L, VF) do { \
        QK; SBAR(); RD; SBAR(); \
        float e0, e1, e2, e3, e4, e5, e6, e7; \
        asm volatile("v_exp_f32 %0, %13\n\tv_exp_f32 %1, %14\n\tv_exp_f32 %2, %15\n\tv_mfma_f32_32x32x16_bf16 %8, %10, %11, %8\n\t" \
                     "v_exp_f32 %3, %16\n\tv_exp_f32 %4, %17\n\tv_exp_f32 %5, %18\n\tv_mfma_f32_16x16x32_bf16 %9, %12, %11, %9\n\tv_exp_f32 %6, %19\n\tv_exp_f32 %7, %20" \
            : "=&v"(e0), "=&v"(e1), "=&v"(e2), "=&v"(e3), "=&v"(e4), "=&v"(e5), "=&v"(e6), "=&v"(e7), "+v"(O), "+v"(L) \
            : "v"(VF), "v"(pf), "v"(ones), "v"(S[i]), "v"(S[i + 1]), "v"(S[i + 2]), "v"(S[i + 3]), "v"(S[i + 4]), "v"(S[i + 5]), "v"(S[i + 6]), "v"(S[i + 7])); \
        pf = __builtin_bit_cast(bf16x8, (u32x4){cvtpk(e0, e1), cvtpk(e2, e3), cvtpk(e4, e5), cvtpk(e6, e7)}); SBAR(); } while (0)
#define LO(lo, hi, off) (((off) >= 2 * SLOT ? hi : lo) + ((off) & (2 * SLOT - 1)))
#define AT_STEP(C0, C1, N0, N1, QK, KR, KO, VO, H0, H1, H2, H3) do { \
        AT_GROUP(if (QK) MFMA32_SET(N0, kfa, q00, negm0), { va = cat8(lds_tr(LO(vb, vh, VO)), lds_tr(LO(vb, vh, VO) + 512)); H0; }, C0, 0, o1, l1, vc); \
        AT_GROUP(if (QK) MFMA32_SET(N1, kfa, q10, negm1), { if (KR) kfa = lds_b128(LO(kb0, kh0, KO)); vc = cat8(lds_tr(LO(vb, vh, VO) + 1024), lds_tr(LO(vb, vh, VO) + 1536)); H1; }, C1, 0, o0, l0, va); \
        AT_GROUP(if (QK) MFMA32_ACC(N0, kfb, q01), H2, C0, 8, o1, l1, va); \
        AT_GROUP(if (QK) MFMA32_ACC(N1, kfb, q11), { if (KR) kfb = lds_b128(LO(kb1, kh1, KO)); H3; }, C1, 8, o0, l0, vc); } while (0)
#define PRV 1
#define YP(p) asm volatile("s_cmp_lt_u32 %0, 4\n\ts_cbranch_scc1 1f\n\ts_setprio " #p "\n1:" :: "s"(wid) : "scc")
    if (PRV) __builtin_amdgcn_s_setprio(1);
    auto ylow = [&]() { if (PRV) YP(0); };
#define S0(c, ...) AT_STEP(sA0, sA1, sB0, sB1, true, true, ((c) & 3) * SLOT + 4096, ((c) & 3) * SLOT, __VA_ARGS__)
#define S1(c, ...) if (PRV == 1) YP(0); AT_STEP(sB0, sB1, sA0, sA1, true, true, ((c) & 3) * SLOT + 6144, ((c) & 3) * SLOT + 2048, __VA_ARGS__)
#define S2(c, ...) if (PRV == 1 || (PRV == 2 && (c) == 7)) YP(2); AT_STEP(sA0, sA1, sB0, sB1, true, (c) < 7, (((c) + 1) & 3) * SLOT, ((c) & 3) * SLOT + 4096, __VA_ARGS__)
#define S3(c, ...) AT_STEP(sB0, sB1, sA0, sA1, (c) < 7, (c) < 7, (((c) + 1) & 3) * SLOT + 2048, ((c) & 3) * SLOT + 6144, __VA_ARGS__)
#define SY(c, n) do { WAIT_V(n); WAIT_L0(); BAR(); SBAR(); STAMP(20 + (c)); } while (0)
#define NO (void)0
#define KP(c, i) glds16c(Kg + ((c0 + (c)) & 7) * 8192 + (i) * 512, A.voffK, lds0 + wid * 2048 + ((c) & 3) * SLOT + (i) * 1024)
#define VP(c, i) glds16c(Vg + ((c0 + (c)) & 7) * 8192 + (i) * 512, A.voffV, lds0 + wid * 2048 + ((c) & 3) * SLOT + 16384 + (i) * 1024)
#define CH(c) KP(c, 0), KP(c, 1), VP(c, 0), VP(c, 1)
#define HS(i) do { __builtin_amdgcn_raw_buffer_store_b128(hp.v[i], hr, hp.off + (i) * 1024, 0, 16); CFENCE(); } while (0)
#define WP(i) wpf(i)
#define GB(j, slot, i) glds16(g2.Wd + (size_t)((j) * 64 + wid * 8) * (DM * 2) + (i) * 128, gB, lds0 + (slot) * SLOT + 16384 + (i) * 8192 + wid * 1024)
#define GA(j, slot, i) glds16c(g2.Ah + (size_t)(j) * (2 * GRP) + g2.rowb + wid * 1024, (i) ? gA1 : gA0, lds0 + (slot) * SLOT + wid * 2048 + (i) * 1024)
    auto wpf = [&](int i) {
        const int pi = (wid + 8 * i < g2.nh + 8) ? wid + 8 * i : 0, lr = prow + 2 * pi;
        asm volatile("global_load_dword %0, %1, %2" : "+v"(pfd) : "v"(pfo), "s"((lr < g2.nh * 64 ? wpd : wpo) + (size_t)lr * (DM * 2)) : "memory");
    };
    auto cpf = [&]() { CODE_PF(pfd); };
    auto hpeek = [&]() { PEEK_AHEAD(hv, hptr, wid); };
    S0(0, NO, NO, NO, NO);         S1(0, NO, NO, NO, NO);    SY(0, 0);  S2(0, CH(2)); S3(0, CH(3));
    S0(1, WP(0), NO, HS(0), NO);   S1(1, NO, NO, HS(1), NO); SY(1, 7);  S2(1, CH(4)); S3(1, WP(1), NO, HS(2), NO);
    S0(2, NO, NO, HS(3), NO);      S1(2, NO, NO, HS(4), NO); SY(2, 11); S2(2, CH(5)); S3(2, WP(2), NO, HS(5), NO);
    S0(3, NO, NO, HS(6), NO);      S1(3, NO, NO, HS(7), NO); SY(3, 12); S2(3, CH(6)); S3(3, WP(3), NO, NO, NO);
    S0(4, NO, NO, NO, NO);         S1(4, NO, NO, NO, NO);    SY(4, 5);
    if (wid == 0) signal(flags, hsig, lane);
                                                                        S2(4, CH(7)); S3(4, WP(4), NO, NO, NO);
    S0(5, GB(0, 4, 0), GB(0, 4, 1), NO, NO); S1(5, NO, NO, NO, NO); SY(5, 2); S2(5, GB(1, 0, 0), GB(1, 0, 1), NO, NO); S3(5, NO, NO, NO, NO);
    S0(6, hpeek(), NO, NO, NO);
    if (wid == 0) { PEEK_DONE(hv); wait_all(flags, F_H + depword, hv); }
                                   S1(6, NO, NO, NO, NO);    SY(6, 0);  S2(6, GA(0, 4, 0), GA(0, 4, 1), GB(2, 1, 0), GB(2, 1, 1)); S3(6, GA(1, 0, 0), GA(1, 0, 1), cpf(), NO);
    S0(7, GA(2, 1, 0), GA(2, 1, 1), NO, NO); S1(7, NO, NO, NO, NO); SY(7, 0); S2(7, NO, NO, NO, NO); S3(7, NO, NO, ylow(), NO);
    __builtin_amdgcn_s_setprio(0);
#undef S0
#undef S1
#undef S2
#undef S3
#undef NO
    STAMP(28);
    asm volatile("s_nop 1" ::: "memory");
    MFMA32_ACC(o1, vc, pf); MFMA16_ACC(l1, ones, pf);
    MFMA_DRAIN(); SBAR();
    asm volatile("" :: "v"(pfd));
    R.o0 = o0; R.o1 = o1; R.l0 = l0; R.l1 = l1; R.m0 = m0; R.m1 = m1;
}

__global__ __launch_bounds__(512, 2) void k_main(const bf16_t* __restrict__ x, const bf16_t* __restrict__ wqkv, const bf16_t* __restrict__ wo, const bf16_t* __restrict__ wg,
                                                 const bf16_t* __restrict__ wu, const bf16_t* __restrict__ wd, bf16_t* __restrict__ out, unsigned char* ws) {
    __shared__ __attribute__((aligned(16))) unsigned char smem[LDS_BYTES];
    const lds_ptr lds = (lds_ptr)smem;
    const unsigned lds0 = (unsigned)(uintptr_t)lds;
    const int wid = __builtin_amdgcn_readfirstlane(threadIdx.x >> 6);
    const int bid = blockIdx.x, xcd = bid & 7, idx = bid >> 3;
    Flags flags; flags.p = (u64*)(ws + WS_FLAGS);
    const __amdgpu_buffer_rsrc_t wsr = __builtin_amdgcn_make_buffer_rsrc((void*)ws, 0, WS_END, 0x00020000);
#define ST16(v, off) __builtin_amdgcn_raw_buffer_store_b128(v, wsr, off, 0, 16)
    unsigned dg_c = (unsigned)__builtin_amdgcn_s_memrealtime(), dg_r = dg_c;
    STAMP(0);
    unsigned cpd = 0;
    CodePf cp;
    asm volatile("s_getpc_b64 %0\n.Lk_begin:\n\ts_mov_b32 %1, .Lk_end-.Lk_begin" : "=s"(cp.base), "=s"(cp.len));
    cp.first = idx * 16;
    HPend hp;
    {
        const int lane = threadIdx.x & 63, wr = wid >> 2, wc = wid & 3, js = idx & 3, rb = idx >> 2;
        const bool typeY = (js == 3);
        const int f0 = 448 * xcd + 128 * js;
        const char* P0 = (const char*)(wg + f0);
        const char* P2 = (const char*)(wu + f0);
        const char* P1 = typeY ? (const char*)(wqkv + 128 * xcd + 64) : (const char*)(wg + f0 + 64);
        const char* P3 = typeY ? (const char*)(wqkv + 1024 + 128 * xcd + 64) : (const char*)(wu + f0 + 64);
        const char* P4 = typeY ? (const char*)(wqkv + 2048 + 128 * xcd + 64) : (const char*)(wqkv + js * 1024 + 128 * xcd);
        const unsigned stepF = 32u * FF * 2u, stepQ = 32u * NQKV * 2u, step13 = typeY ? stepQ : stepF;
        const char* XA = (const char*)x + (size_t)rb * 256 * DM * 2;
        const char* XC = (const char*)ws + WS_XC + (size_t)rb * 256 * DM * 2;
        const int r8 = lane >> 3, u8 = lane & 7;
        const int gw = (r8 & 2) | ((wc & 1) << 2);
        const unsigned voffF = (unsigned)(((wc * 8 + r8) * FF + (u8 ^ gw) * 8) * 2);
        const unsigned voffQ = (unsigned)(((wc * 8 + r8) * NQKV + (u8 ^ gw) * 8) * 2);
        const unsigned voff13 = typeY ? voffQ : voffF;
        const unsigned voffA0 = (unsigned)(((32 * wid + r8) * DM + (u8 ^ (r8 >> 1)) * 8) * 2);
        const unsigned voffA1 = (unsigned)(((32 * wid + 8 + r8) * DM + (u8 ^ (4 | (r8 >> 1))) * 8) * 2);
        const unsigned dstA = lds0 + G1_A + wid * 4096, dstW = lds0 + G1_W + wc * 1024;
#define G1_LOAD_A(t) do { const char* b_ = XA + (t) * 128; const unsigned d_ = dstA + ((t) & 1) * 32768; \
        glds16(b_, voffA0, d_); glds16(b_, voffA1, d_ + 1024); glds16(b_ + 32768, voffA0, d_ + 2048); glds16(b_ + 32768, voffA1, d_ + 3072); } while (0)
#define G1_LOAD_W(hs) do { const unsigned d_ = dstW + ((hs) & 3) * 4096; \
        glds16(P0 + (size_t)(hs) * stepF, voffF, d_); glds16(P1 + (size_t)(hs) * step13, voff13, d_ + 16384); glds16(P2 + (size_t)(hs) * stepF, voffF, d_ + 32768); \
        glds16(P3 + (size_t)(hs) * step13, voff13, d_ + 49152); glds16(P4 + (size_t)(hs) * stepQ, voffQ, d_ + 65536); } while (0)
        STAMP(29);
        if (wr == 1) G1_LOAD_W(0);
        G1_LOAD_A(0);
        u32x4 xa0, xa1;
        const int srow = rb * 256 + (js * 8 + xcd) * 8 + wid;
        asm volatile("global_load_dwordx4 %0, %2, %3\n\tglobal_load_dwordx4 %1, %2, %3 offset:1024"
                     : "=&v"(xa0), "=&v"(xa1) : "v"((unsigned)(lane * 16)), "s"((const char*)x + (size_t)srow * DM * 2) : "memory");
        CODE_PF(cpd);
        STAMP(30);

        int fr = lane & 15, fq = lane >> 4;
        const lds_ptr aB0 = lds + G1_A + (wr * 128 + fr) * 128 + ((fq ^ (fr >> 1)) * 16);
        const lds_ptr aB1 = lds + G1_A + (wr * 128 + fr) * 128 + (((4 | fq) ^ (fr >> 1)) * 16);
        const int tq = (lane >> 2) & 3, tp = lane & 3, tg = (tq & 2) | ((fq & 1) << 2);
        auto wbase = [&](int panel, int bp) { return lds + G1_W + panel * 16384 + (8 * fq + tq) * 128 + (((2 * bp + (tp >> 1)) ^ tg) * 16) + (tp & 1) * 8; };
        const int pX = wc >> 1, bX = 2 * (wc & 1);
        const lds_ptr wB0 = typeY ? wbase(0, wc) : wbase(pX, bX);
        const lds_ptr wB1 = typeY ? wbase(1, wc) : wbase(pX, bX + 1);
        const lds_ptr wB2 = typeY ? wbase(2, wc) : wbase(2 + pX, bX);
        const lds_ptr wB3 = typeY ? wbase(3, wc) : wbase(2 + pX, bX + 1);
        const lds_ptr wB4 = wbase(4, wc);
        if (threadIdx.x == 0) *(LAS unsigned*)(lds + G1_CNT) = 0u;
        f32x4 acc[8][5];
#pragma unroll
        for (int m = 0; m < 8; ++m)
#pragma unroll
            for (int b = 0; b < 5; ++b) { acc[m][b] = (f32x4){0.f, 0.f, 0.f, 0.f}; PIN(acc[m][b]); }
        if (wr == 1) { WAIT_V(7); BAR(); }
        asm volatile("s_waitcnt vmcnt(1)" : "+v"(xa0), "+v"(xa1) :: "memory");
        STAMP(31);
        u64 sg;
        {
            const f32x2 st = row_stats(xa0, xa1);
            G1_LOAD_W(1 + wr);
            ST16(xa0, (lane >= 32) ? WS_XC + (unsigned)srow * 2048u + lane * 16 : WS_END);
            ST16(xa1, WS_XC + (unsigned)srow * 2048u + 1024 + lane * 16);
            __builtin_amdgcn_raw_buffer_store_b64(__builtin_bit_cast(u32x2, st), wsr, (lane == 0) ? WS_STAT + (unsigned)srow * 8u : WS_END, 0, 16);
            asm volatile("global_load_dwordx2 %0, %1, %2 sc1" : "=&v"(sg) : "v"(0u), "s"(flags.p) : "memory");
        }
        STAMP(32);
        bf16x8 af[8], wf[5];
        f32x4 cs0 = (f32x4){0.f, 0.f, 0.f, 0.f}, cs1 = cs0, cs2 = cs0;
        bf16x8 one8 = (bf16x8){0x3f80, 0x3f80, 0x3f80, 0x3f80, 0x3f80, 0x3f80, 0x3f80, 0x3f80};
        PIN(cs0); PIN(cs1); PIN(cs2); PIN(one8);
#define CS1(c, w) "v_mfma_f32_16x16x32_bf16 %" #c ", %" #w ", %8, %" #c "\n\t"
#define G1_CSUM_(P0, P1) asm volatile("s_cmp_gt_u32 %10, 3\n\ts_cbranch_scc1 2f\n\ts_cmp_lg_u32 %9, 0\n\ts_cbranch_scc1 1f\n\t" CS1(0, 3) CS1(1, 4) P0 "s_branch 2f\n1:\n\t" CS1(0, 5) CS1(1, 6) P1 "\n2:" \
            : "+v"(cs0), "+v"(cs1), "+v"(cs2) : "v"(wf[0]), "v"(wf[1]), "v"(wf[2]), "v"(wf[3]), "v"(wf[4]), "v"(one8), "s"(wr), "s"(t) : "scc")
#define G1_CSUM(kk) do { if ((kk) == 0) G1_CSUM_(CS1(2, 7), ""); else G1_CSUM_("", CS1(2, 7)); } while (0)
#define G1_RW(i, B, slot) wf[i] = cat8(lds_tr(B + (slot) * 4096), lds_tr(B + (slot) * 4096 + 512))
#define G1_RA(m, AB, abuf) af[m] = lds_b128(AB + (abuf) * 32768 + (m) * 2048)
#define G1_SEG(AB, abuf, slot, L0, L1, L2, L3, L4, CLOSE) do { \
        L0; G1_RW(0, wB0, slot); G1_RW(1, wB1, slot); L1; G1_RW(2, wB2, slot); G1_RW(3, wB3, slot); L2; G1_RW(4, wB4, slot); G1_RA(0, AB, abuf); G1_RA(1, AB, abuf); \
        L3; G1_RA(2, AB, abuf); G1_RA(3, AB, abuf); G1_RA(4, AB, abuf); L4; G1_RA(5, AB, abuf); G1_RA(6, AB, abuf); G1_RA(7, AB, abuf); \
        WAIT_L0(); SBAR(); if (CLOSE) { BAR(); SBAR(); } } while (0)
#define G1_SEG_A(abuf, slot, tt) do { const char* b_ = ((tt) < 4 ? XA : XC) + (tt) * 128; const unsigned d_ = dstA + (1 - (abuf)) * 32768; \
        G1_SEG(aB0, abuf, slot, glds16c(b_, voffA0, d_), glds16c(b_, voffA1, d_ + 1024), glds16c(b_ + 32768, voffA0, d_ + 2048), glds16c(b_ + 32768, voffA1, d_ + 3072), (void)0, true); } while (0)
#define G1_SEG_W(abuf, slot, hs, dslot, CLOSE) do { const unsigned d_ = dstW + (dslot) * 4096; \
        G1_SEG(aB1, abuf, slot, glds16(P0 + (size_t)(hs) * stepF, voffF, d_), glds16(P1 + (size_t)(hs) * step13, voff13, d_ + 16384), glds16(P2 + (size_t)(hs) * stepF, voffF, d_ + 32768), \
               glds16(P3 + (size_t)(hs) * step13, voff13, d_ + 49152), glds16(P4 + (size_t)(hs) * stepQ, voffQ, d_ + 65536), CLOSE); } while (0)
#define EB 2
#define FIRST_WAIT() asm volatile("s_waitcnt vmcnt(4)" : "+v"(sg), "+v"(cpd) :: "memory")
#define G1_MM(i0, i1) _Pragma("unroll") for (int i = (i0); i < (i1); ++i) { const int b = i >> 3, m = (b & 1) ? 7 - (i & 7) : (i & 7); MFMA16_ACC(acc[m][b], wf[b], af[m]); }
#define G1_MMA(kk, CLOSE) do { __builtin_amdgcn_s_setprio(1); G1_MM(0, 40 - EB); SBAR(); __builtin_amdgcn_s_setprio(2); CLOSE; SBAR(); G1_MM(40 - EB, 40); G1_CSUM(kk); __builtin_amdgcn_s_setprio(0); SBAR(); } while (0)

        STAMP(33);
        WAIT_L0();
        STAMP(34);
        BAR();
        SBAR();
        STAMP(1);
        flags.tag = 0;
        const u64* sgp = flags.p + (F_XC + rb * 32 + (lane & 31)) * 16;
#pragma unroll 1
        for (int t = 0; t < 16; t += 2) {
            const int h1 = (2 * t + 3 + wr < 31) ? 2 * t + 3 + wr : 31, h2 = (2 * t + 5 + wr < 31) ? 2 * t + 5 + wr : 31, t2 = (t < 14) ? t + 2 : 15;
            const int sel = (t == 12) ? 0 : ((t ^ 2) | wid);

            PEEK_AHEAD(sg, sgp, sel);
            if (t == 14) CODE_PF(cpd);
            G1_SEG_A(0, 0, t + 1); G1_MMA(0, { FIRST_WAIT(); BAR(); });
            if (t == 0) {
                const unsigned elo = __builtin_amdgcn_readfirstlane((unsigned)sg), ehi = __builtin_amdgcn_readfirstlane((unsigned)(sg >> 32));
                flags.tag = (((u64)ehi << 32) | elo) * 0x9E3779B97F4A7C15ull + 0x632BE59BD9B4E019ull;
                if (wid == 4) signal(flags, F_XC + bid, lane);
            }
            if (sel == 0) {
                if (t == 12) *(LAS u64*)(lds + G1_STAT + (wid * 32 + (opaque_tid() & 31)) * 8) = sg;
                else wait_all(flags, F_XC + rb * 32 + (opaque_tid() & 31), sg);
            }
            G1_SEG_W(0, 1, h1, (3 + wr) & 3, true); G1_MMA(1, { WAIT_V(5); BAR(); });
            if (t == 4) {
                const int tl = opaque_tid() & 63, k = tl & 15;
                if (k < 3) *(LAS f32x4*)(lds + G1_CS + wc * 384 + (tl >> 4) * 16 + (k == 2 ? 256 + wr * 64 : wr * 128 + k * 64)) = k == 0 ? cs0 : k == 1 ? cs1 : cs2;
                sgp = (const u64*)(ws + WS_STAT) + rb * 256 + wid * 32 + (tl & 31);
            }
            G1_SEG_A(1, 2, t2); G1_MMA(0, { WAIT_V(4); BAR(); });
            G1_SEG_W(1, 3, h2, (1 + wr) & 3, true);
            G1_MMA(1, asm volatile("s_waitcnt vmcnt(5)\n\ts_cmp_eq_u32 %0, 15\n\ts_cbranch_scc1 1f\n\ts_barrier\n1:" :: "s"(t + wr) : "memory", "scc"));
        }
        MFMA_DRAIN(); SBAR();
        STAMP(2);

        const int tz = opaque_tid(), ln = tz & 63;
        fr = tz & 15; fq = (tz >> 4) & 3;
        const int row0 = rb * 256 + wr * 128 + fr;
        f32x4 csv[5]; f32x2 st[8];
#pragma unroll
        for (int b = 0; b < 5; ++b) csv[b] = *(const LAS f32x4*)(lds + G1_CS + wc * 384 + b * 64 + fq * 16);
        csv[4] += *(const LAS f32x4*)(lds + G1_CS + wc * 384 + 320 + fq * 16);
#pragma unroll
        for (int m = 0; m < 8; ++m) st[m] = *(const LAS f32x2*)(lds + G1_STAT + (wr * 128 + m * 16 + fr) * 8);
        auto lnv = [&](int m, int b, float sc) { return (acc[m][b] - st[m].x * csv[b]) * (st[m].y * sc); };
        auto glu = [&](int m, int bg, int bu) {
            const f32x4 g = lnv(m, bg, -LOG2E), u = lnv(m, bu, 1.0f);
            f32x4 h;
#pragma unroll
            for (int j = 0; j < 4; ++j) h[j] = u[j] * __builtin_amdgcn_rcpf(1.0f + __builtin_amdgcn_exp2f(g[j]));
            return h;
        };
        auto pack2 = [&](const f32x4& v0, const f32x4& v1) {
            unsigned p0 = cvtpk(v0[0], v0[1]), p1 = cvtpk(v0[2], v0[3]), q0 = cvtpk(v1[0], v1[1]), q1 = cvtpk(v1[2], v1[3]);
            swap16(p0, q0); swap16(p1, q1);
            return (u32x4){p0, p1, q0, q1};
        };
        const int hd = 4 * xcd + (typeY ? 2 : 0) + (wc >> 1);
        const unsigned qoff = WS_QKV + (unsigned)(hd * 2 + (wc & 1)) * 65536u + (unsigned)(row0 + (fq & 1) * 16) * 32u + (fq >> 1) * 16;
        constexpr unsigned KIND = NH * 2 * 65536u;
        if (!typeY) {
            const float sc4 = (js == 0) ? QSCALE : 1.0f;
#pragma unroll
            for (int m = 0; m < 8; m += 2) ST16(pack2(lnv(m, 4, sc4), lnv(m + 1, 4, sc4)), qoff + js * KIND + m * 512);
            CFENCE();
            hp.base = ws + WS_ACT + (unsigned)(14 * xcd + 4 * js + wc) * GRP + (unsigned)(rb * 256 + wr * 128) * 64u;
            hp.off = fr * 64 + (fq & 1) * 32 + (fq >> 1) * 16; hp.size = 8192;
#pragma unroll
            for (int m = 0; m < 8; ++m) {
                hp.v[m] = pack2(glu(m, 0, 2), glu(m, 1, 3));
                if (m == 5) { PIN(hp.v[5]); WAIT_V(0); arrive(flags, F_QKV + bid, ln, lds + G1_CNT, 7u); }
            }
        } else {
#pragma unroll
            for (int m = 0; m < 8; m += 2) {
                ST16(pack2(lnv(m, 3, 1.0f), lnv(m + 1, 3, 1.0f)), qoff + KIND + m * 512);
                ST16(pack2(lnv(m, 4, 1.0f), lnv(m + 1, 4, 1.0f)), qoff + 2 * KIND + m * 512);
                ST16(pack2(lnv(m, 1, QSCALE), lnv(m + 1, 1, QSCALE)), qoff + m * 512);
            }
            CFENCE();
            hp.base = ws + WS_ACT + (unsigned)(14 * xcd + 12 + (wc >> 1)) * GRP + (wc & 1) * (GRP / 2) + (unsigned)(rb * 256 + wr * 128) * 32u;
            hp.off = (fr + (fq & 1) * 16) * 32 + (fq >> 1) * 16; hp.size = 4096;
#pragma unroll
            for (int m = 0; m < 8; m += 2) { hp.v[m >> 1] = pack2(glu(m, 0, 2), glu(m + 1, 0, 2)); hp.v[4 + (m >> 1)] = hp.v[m >> 1]; }
            PIN(hp.v[3]); WAIT_V(0); arrive(flags, F_QKV + bid, ln, lds + G1_CNT, 7u);
        }
#pragma unroll
        for (int i = 0; i < 8; ++i) PIN(hp.v[i]);
        STAMP(3);
    }

    const int z = xcd & 1, rbk = ((xcd >> 1) & 1) * 8 + (idx & 7), cbk = (xcd >> 2) * 4 + (idx >> 3);
    G2 g2;
    g2.nh = z ? 56 - NH0 : NH0;
    g2.rowb = rbk * 128 * 64;
    g2.split = z ? split_mask(NH0, 56 - NH0) : split_mask(0, NH0);
    g2.Ah = (const char*)ws + WS_ACT + (size_t)(z * 2 * NH0) * GRP;
    g2.Ao = (const char*)ws + WS_ACT + (size_t)(112 + z * 16 - 2 * g2.nh) * GRP;
    g2.Wd = (const char*)(wd + (size_t)z * NH0 * 64 * DM + cbk * 128);
    g2.Wo = (const char*)(wo + (size_t)z * 512 * DM + cbk * 128) - (size_t)g2.nh * 64 * DM * 2;
    {
        const int hl = idx & 3, qb = idx >> 2, h = 4 * xcd + hl;
        const int l0_ = opaque_tid() & 63;
        const int qkv0 = F_QKV + xcd + 8 * (qb * 4 + ((hl >= 2) ? 3 : l0_ % 3));
        const int qkvword = F_QKV + xcd + 8 * ((l0_ & 7) * 4 + ((hl >= 2) ? 3 : 1 + ((l0_ >> 3) & 1)));
        const int odep = (rbk >> 1) * 32 + ((l0_ >> 2) & 3) * 8 + 4 * z + (l0_ & 3);
        const int depword = (l0_ & 16) ? (odep & ~7) + 4 : odep;
        const char* Qg = (const char*)ws + WS_QKV + (size_t)h * 131072 + qb * 256 * 32;
        const char* Kg = (const char*)ws + WS_QKV + (size_t)(NH + h) * 131072;
        const char* Vg = (const char*)ws + WS_QKV + (size_t)(2 * NH + h) * 131072;
        const char* wpd = g2.Wd - (cbk & 3) * 256;
        const char* wpo = g2.Wo - (cbk & 3) * 256;
        AttnOut R;
#define DG , dg_c, dg_r
        attn_fast(lds, lds0, Qg, Kg, Vg, g2, flags, qkv0, qkvword, depword, F_H + bid, qb, bid == 0, cp, hp, wpd, wpo, idx * 2 * (g2.nh + 8), wid, R DG);
        STAMP(6);
        const int tz = opaque_tid(), ln = tz & 63, w4 = wid & 3, fr = ln & 15, fq = ln >> 4;
        lds_ptr po0 = lds + 2 * SLOT + w4 * 4096 + ln * 16, po1 = lds + 2 * SLOT + 16384 + w4 * 2048 + ln * 16;
        PIN(po0); PIN(po1);
        auto po = [&](int i) { return i < 4 ? po0 + i * 1024 : po1 + (i >> 1 & 1) * 8192 + (i & 1) * 1024; };
        const lds_ptr pl = po0 + SLOT;
        volatile LAS unsigned* const badw = (volatile LAS unsigned*)(lds + 3 * SLOT + 1024);
        const int wm = w4 >> 1, wn = w4 & 1;
        const unsigned aO0 = (wm * 64 + fr) * 128 + ((fq ^ (fr >> 1)) * 16), aO1 = aO0 ^ 64;
        const int tq = (ln >> 2) & 3, tp = ln & 3, tg = (tq & 2) | ((fq & 1) << 2);
        unsigned wO[4];
#pragma unroll
        for (int b = 0; b < 4; ++b) wO[b] = 16384 + wn * 8192 + (8 * fq + tq) * 128 + (((2 * b + (tp >> 1)) ^ tg) * 16) + (tp & 1) * 8;
        f32x4 acc[4][4];
        bf16x8 xa[4], ya[4]; s16x4 xl[4], xh[4], yl[4], yh[4];
        lds_ptr pa0, pa1, pw0, pw1, pw2, pw3;
        int so = 4 * SLOT;
        auto handover = [&]() {
            if (wid < 4) {
#pragma unroll
                for (int i = 0; i < 4; ++i) {
                    *(LAS f32x4*)po(i) = (f32x4){R.o0[4 * i], R.o0[4 * i + 1], R.o0[4 * i + 2], R.o0[4 * i + 3]};
                    *(LAS f32x4*)po(4 + i) = (f32x4){R.o1[4 * i], R.o1[4 * i + 1], R.o1[4 * i + 2], R.o1[4 * i + 3]};
                }
                *(LAS f32x4*)pl = (f32x4){R.l0[0], R.m0, R.l1[0], R.m1};
                xl[0] = lds_tr(pw0); xh[0] = lds_tr(pw0 + 512);
#pragma unroll
                for (int m = 0; m < 4; ++m) xa[m] = lds_b128(pa0 + m * 2048);
                xl[1] = lds_tr(pw1); xh[1] = lds_tr(pw1 + 512); xl[2] = lds_tr(pw2); xh[2] = lds_tr(pw2 + 512); xl[3] = lds_tr(pw3); xh[3] = lds_tr(pw3 + 512);
#pragma unroll
                for (int m = 0; m < 4; ++m)
#pragma unroll
                    for (int b = 0; b < 4; ++b) { acc[m][b] = (f32x4){0.f, 0.f, 0.f, 0.f}; PIN(acc[m][b]); }
            }
        };
        G2Off go; g2_voff(ln, go);
        PIN(go.A0); PIN(go.A1); PIN(go.N0); PIN(go.N1); PIN(go.B0); PIN(go.B1);
        pa0 = lds + aO0 + so; pa1 = lds + aO1 + so; pw0 = lds + wO[0] + so; pw1 = lds + wO[1] + so; pw2 = lds + wO[2] + so; pw3 = lds + wO[3] + so;
        PIN(pa0); PIN(pa1); PIN(pw0); PIN(pw1); PIN(pw2); PIN(pw3);
        {
            handover();
            const f32x16 z16 = R.o0 * 0.0f + R.o1 * 0.0f;
            const f32x4 z4 = (z16.s0123 + z16.s4567) + (z16.s89ab + z16.scdef);
            const f32x2 z2 = z4.lo + z4.hi;
            const float zs = z2.x + z2.y;
            const bool ok = (zs == 0.f) && (R.l0[0] > 0.f) && (R.l0[0] < 0x1p96f) && (R.l1[0] > 0.f) && (R.l1[0] < 0x1p96f);
            const bool wave_bad = !__all(ok);
            if (ln == 0) badw[wid] = wave_bad ? 1u : 0u;
            STAMP(16);
            WAIT_L0(); BAR(); CFENCE();
            const unsigned anybad = badw[ln & 7];
            if (__builtin_expect(__any(anybad != 0u), 0)) {
                WAIT_L0(); BAR(); CFENCE();
                attn_safe(lds, lds0, Qg, Kg, Vg, wid, R);
                handover();
                WAIT_L0(); BAR(); CFENCE();
            }
        }
        STAMP(7);

        const int tile = rbk * 8 + cbk, nt = g2.nh + 8;
        if (__builtin_expect(wid >= 4, 0)) {
#define G2_LOAD0(j, sl) do { const unsigned d_ = lds0 + (sl) * SLOT; g2_load_b(g2, j, d_, 2 * w4, go.B0); g2_load_a(g2, j, d_, 2 * w4, go); } while (0)
#define G2_LOAD1(j, sl) do { const unsigned d_ = lds0 + (sl) * SLOT; g2_load_b(g2, j, d_, 2 * w4 + 1, go.B1); g2_load_a(g2, j, d_, 2 * w4 + 1, go); } while (0)
#define G2_LOAD(j, sl) do { G2_LOAD0(j, sl); G2_LOAD1(j, sl); } while (0)
            const f32x4 lm = *(const LAS f32x4*)pl;
            f32x4 t[8];
#pragma unroll
            for (int i = 0; i < 8; ++i) t[i] = *(const LAS f32x4*)po(i);
            WAIT_L0();
            G2_LOAD(3, 2);
            BAR();
            G2_LOAD(4, 3);
            float ck[2], co[2]; unsigned p[16];
            auto coef = [&](int q) {
                const float lk = q ? R.l1[0] : R.l0[0], mk = q ? R.m1 : R.m0, lo = lm[2 * q], mo = lm[2 * q + 1];
                const float mm = __builtin_fmaxf(mk, mo), fk = __builtin_amdgcn_exp2f(mk - mm), fo = __builtin_amdgcn_exp2f(mo - mm);
                const float rl = 1.0f / (lk * fk + lo * fo);
                ck[q] = fk * rl; co[q] = fo * rl; PIN(ck[q]); PIN(co[q]); SBAR();
            };
            auto half = [&](int q, int hh) {
                const f32x16 ok_ = q ? R.o1 : R.o0;
                unsigned* pp = p + 8 * q + 4 * hh;
#pragma unroll
                for (int i = 0; i < 2; ++i) {
                    const int g = 2 * hh + i; const f32x4 u = t[4 * q + g];
                    pp[2 * i] = cvtpk(ok_[4 * g] * ck[q] + u[0] * co[q], ok_[4 * g + 1] * ck[q] + u[1] * co[q]);
                    pp[2 * i + 1] = cvtpk(ok_[4 * g + 2] * ck[q] + u[2] * co[q], ok_[4 * g + 3] * ck[q] + u[3] * co[q]);
                }
                swap32(pp[0], pp[2]); swap32(pp[1], pp[3]);
                PIN(pp[0]); PIN(pp[1]); PIN(pp[2]); PIN(pp[3]); SBAR();
            };
            const unsigned ooff = WS_ACT + (unsigned)(112 + h) * GRP + (unsigned)(qb * 256 + w4 * 64 + fr) * 64u + (fq & 1) * 32 + (fq >> 1) * 16;
            auto ost = [&](int q) {
                unsigned* pp = p + 8 * q;
                swap16(pp[0], pp[4]); swap16(pp[1], pp[5]); swap16(pp[2], pp[6]); swap16(pp[3], pp[7]);
                ST16(((u32x4){pp[0], pp[1], pp[2], pp[3]}), ooff + q * 2048); ST16(((u32x4){pp[4], pp[5], pp[6], pp[7]}), ooff + q * 2048 + 1024); CFENCE();
            };
            SBAR(); coef(0); coef(1);
            WAIT_V(8);  BAR(); G2_LOAD(5, 4); G2_LOAD0(6, 0);
            WAIT_V(12); BAR(); G2_LOAD1(6, 0); G2_LOAD(7, 1);
            WAIT_V(16); BAR(); G2_LOAD0(8, 2);  SBAR(); half(0, 0); G2_LOAD1(8, 2);
            WAIT_V(16); BAR(); G2_LOAD0(9, 3);  SBAR(); half(0, 1); G2_LOAD1(9, 3);  ost(0);
            WAIT_V(18); BAR(); G2_LOAD0(10, 4); SBAR(); half(1, 0); G2_LOAD1(10, 4);
            WAIT_V(18); BAR(); G2_LOAD0(11, 0); SBAR(); half(1, 1); G2_LOAD1(11, 0); ost(1);
            WAIT_V(20); BAR(); G2_LOAD(12, 1);
            WAIT_V(18); BAR(); G2_LOAD(13, 2);
            WAIT_V(18);
            int sl = 3; cpd = 0;
#pragma unroll 1
            for (int j = 11; j < nt - 3; ++j) {
                BAR();
                if (j == 29) CODE_PF(cpd);
                if (j == 12 && wid == 4) signal(flags, F_O + bid, ln);
                G2_LOAD(j + 3, sl);
                sl = (sl == 4) ? 0 : sl + 1;
                WAIT_V(16);
            }
            BAR();
            if (tz == 256) *(LAS unsigned*)(lds + sl * SLOT) = 0u;
            WAIT_V(8); WAIT_L0(); BAR(); WAIT_V(0); BAR();
            asm volatile("" :: "v"(cpd));
            if (wid == 4) wait_all(flags, F_X + tile * 2 + z, peek(flags, F_X + tile * 2 + z));
            CFENCE(); BAR();
            asm volatile(".Lk_end:");
        } else {
#define G2_M(C, m, b) do { MFMA16_ACC(acc[m][b], cat8(C##l[b], C##h[b]), C##a[m]); SBAR(); } while (0)
#define G2_HALF(C, N, AB, WO, RD, TAIL) do { \
            G2_M(C, 0, 0); if (RD) { N##l[0] = lds_tr(pw0 + (WO)); N##h[0] = lds_tr(pw0 + (WO) + 512); } SBAR(); \
            G2_M(C, 1, 0); if (RD) N##a[0] = lds_b128(AB); SBAR(); \
            G2_M(C, 2, 0); if (RD) N##a[1] = lds_b128((AB) + 2048); SBAR(); \
            G2_M(C, 3, 0); if (RD) N##a[2] = lds_b128((AB) + 4096); SBAR(); \
            G2_M(C, 0, 1); if (RD) N##a[3] = lds_b128((AB) + 6144); SBAR(); \
            G2_M(C, 1, 1); if (RD) { N##l[1] = lds_tr(pw1 + (WO)); N##h[1] = lds_tr(pw1 + (WO) + 512); } SBAR(); \
            G2_M(C, 2, 1); if (RD) { N##l[2] = lds_tr(pw2 + (WO)); N##h[2] = lds_tr(pw2 + (WO) + 512); } SBAR(); \
            G2_M(C, 3, 1); if (RD) { N##l[3] = lds_tr(pw3 + (WO)); N##h[3] = lds_tr(pw3 + (WO) + 512); } SBAR(); \
            G2_M(C, 0, 2); TAIL(0); SBAR(); G2_M(C, 1, 2); TAIL(1); SBAR(); G2_M(C, 2, 2); TAIL(2); SBAR(); G2_M(C, 3, 2); TAIL(3); SBAR(); \
            G2_M(C, 0, 3); G2_M(C, 1, 3); G2_M(C, 2, 3); G2_M(C, 3, 3); } while (0)
            const u64* const optr = flags.p + (F_O + odep) * 16;
            u64 ov = 0;
            STAMP(8); STAMP(43);
#define G2_NEXT(i) do { if ((i) == 0) so = (so == 4 * SLOT) ? 0 : so + SLOT; \
                if ((i) == 1) { pa0 = lds + aO0 + so; pa1 = lds + aO1 + so; PIN(pa0); PIN(pa1); } \
                if ((i) == 2) { pw0 = lds + wO[0] + so; pw1 = lds + wO[1] + so; PIN(pw0); PIN(pw1); } \
                if ((i) == 3) { pw2 = lds + wO[2] + so; pw3 = lds + wO[3] + so; PIN(pw2); PIN(pw3); } } while (0)
#define G2_NONE(i) do {} while (0)
            WAIT_L0(); SBAR(); G2_HALF(x, y, pa1, 4096, true, G2_NEXT);
            WAIT_L0(); SBAR(); G2_HALF(y, x, pa0, 0, true, G2_NONE);
            BAR();
            WAIT_L0(); SBAR(); G2_HALF(x, y, pa1, 4096, true, G2_NEXT);
            WAIT_L0(); SBAR(); G2_HALF(y, x, pa0, 0, true, G2_NONE);
            if (NH0 & 1) {
                WAIT_L0(); SBAR(); G2_HALF(x, y, pa1, 4096, true, G2_NEXT);
                WAIT_L0(); BAR(); SBAR(); G2_HALF(y, x, pa0, 0, true, G2_NONE);
            }
#pragma unroll 1
            for (int ph = 0; ph < 3; ++ph) {
                const int n = (ph == 0) ? g2.nh / 2 - 4 : (ph == 1) ? 1 : 5;
#pragma unroll 1
                for (int i = 0; i < n; ++i) {
                    WAIT_L0(); SBAR(); G2_HALF(x, y, pa1, 4096, true, G2_NEXT);
                    WAIT_L0(); BAR(); SBAR(); G2_HALF(y, x, pa0, 0, true, G2_NONE);
                    WAIT_L0(); SBAR(); G2_HALF(x, y, pa1, 4096, true, G2_NEXT);
                    WAIT_L0(); BAR(); SBAR(); G2_HALF(y, x, pa0, 0, true, G2_NONE);

                }
                PEEK_AHEAD(ov, optr, wid | ph);
                if ((wid | (ph ^ 1)) == 0) { PEEK_DONE(ov); wait_all(flags, F_O + odep, ov); }
            }
            const int row = rbk * 128 + wm * 64 + z * 32 + fr, col = cbk * 128 + wn * 64 + (fq & 1) * 16 + (fq >> 1) * 8;
            u32x4 xr[4];
#pragma unroll
            for (int i = 0; i < 4; ++i) xr[i] = *(const u32x4*)(x + (size_t)(row + (i >> 1) * 16) * DM + col + (i & 1) * 32);
            WAIT_L0(); SBAR(); G2_HALF(x, y, pa1, 4096, true, G2_NEXT);
            WAIT_L0(); BAR(); SBAR(); G2_HALF(y, x, pa0, 0, true, G2_NONE);
            WAIT_L0(); SBAR(); G2_HALF(x, y, pa1, 4096, true, G2_NONE);
            WAIT_L0(); SBAR(); G2_HALF(y, x, pa0, 0, false, G2_NONE);
            MFMA_DRAIN(); SBAR();
            STAMP(9);
            f32x4 vk[8];
            const unsigned xo = WS_XCH + (unsigned)(tile * 2 + 1 - z) * 32768u + w4 * 8192 + ln * 16;
            if (z == 0) {
#pragma unroll
                for (int i = 0; i < 8; ++i) { ST16(__builtin_bit_cast(u32x4, acc[2 + (i >> 2)][i & 3]), xo + i * 1024); vk[i] = acc[i >> 2][i & 3]; }
            } else {
#pragma unroll
                for (int i = 0; i < 8; ++i) { ST16(__builtin_bit_cast(u32x4, acc[i >> 2][i & 3]), xo + i * 1024); vk[i] = acc[2 + (i >> 2)][i & 3]; }
            }
            WAIT_V(0);
            arrive(flags, F_X + tile * 2 + 1 - z, ln, lds + ((so == 4 * SLOT) ? 0 : so + SLOT), 3u);
            STAMP(10);
            CFENCE(); BAR(); CFENCE();
            STAMP(11);
            const unsigned xi = WS_XCH + (unsigned)(tile * 2 + z) * 32768u + w4 * 8192 + ln * 16;
#pragma unroll
            for (int i = 0; i < 8; ++i) vk[i] += __builtin_bit_cast(f32x4, __builtin_amdgcn_raw_buffer_load_b128(wsr, xi + i * 1024, 0, 16));
            const __amdgpu_buffer_rsrc_t outr = __builtin_amdgcn_make_buffer_rsrc((void*)out, 0, SEQ * DM * 2, 0x00020000);
            u32x4 w[4];
#pragma unroll
            for (int i = 0; i < 4; ++i) {
                const u32x4 a = __builtin_bit_cast(u32x4, vk[2 * i]), b = __builtin_bit_cast(u32x4, vk[2 * i + 1]);
                unsigned a0 = a[0], a1 = a[1], a2 = a[2], a3 = a[3], b0 = b[0], b1 = b[1], b2 = b[2], b3 = b[3];
                swap16(a0, b0); swap16(a1, b1); swap16(a2, b2); swap16(a3, b3);
                const u32x4 xx = xr[i];
                w[i][0] = cvtpk(u2f(a0) + bf_lo(xx[0]), u2f(a1) + bf_hi(xx[0]));
                w[i][1] = cvtpk(u2f(a2) + bf_lo(xx[1]), u2f(a3) + bf_hi(xx[1]));
                w[i][2] = cvtpk(u2f(b0) + bf_lo(xx[2]), u2f(b1) + bf_hi(xx[2]));
                w[i][3] = cvtpk(u2f(b2) + bf_lo(xx[3]), u2f(b3) + bf_hi(xx[3]));
            }
            const unsigned oo = (unsigned)((row - (fr & 8)) * DM + col + (fr & 8) * 4) * 2u;
#pragma unroll
            for (int m = 0; m < 2; ++m) {
                u32x4 lo, hi;
#pragma unroll
                for (int k = 0; k < 4; ++k) {
                    lo[k] = __builtin_amdgcn_update_dpp(w[2 * m][k], w[2 * m + 1][k], 0x128, 0xf, 0xc, false);
                    hi[k] = __builtin_amdgcn_update_dpp(w[2 * m + 1][k], w[2 * m][k], 0x128, 0xf, 0x3, false);
                }
                __builtin_amdgcn_raw_buffer_store_b128(lo, outr, oo + m * 16 * DM * 2, 0, 16);
                __builtin_amdgcn_raw_buffer_store_b128(hi, outr, oo + (m * 16 + 8) * DM * 2, 0, 16);
            }
        }
        WAIT_V(0);
        STAMP(12);
        if (wid == 0) { const unsigned lim = 12000u + (dg_r - dg_c); while ((unsigned)__builtin_amdgcn_s_memrealtime() - dg_c < lim) __builtin_amdgcn_s_sleep(2); }
    }
}

extern "C" void kernel_launch(void* const* d_in, const int* in_sizes, int n_in, void* d_out, int out_size, void* d_ws, size_t ws_size, hipStream_t stream) {
    const bf16_t* x = (const bf16_t*)d_in[0];
    k_main<<<256, 512, 0, stream>>>(x, (const bf16_t*)d_in[1], (const bf16_t*)d_in[2], (const bf16_t*)d_in[4], (const bf16_t*)d_in[5], (const bf16_t*)d_in[6],
                                    (bf16_t*)d_out, (unsigned char*)d_ws);
}
```

```cpp
#include <hip/hip_runtime.h>
#include <stdint.h>
#define MK 99

typedef unsigned short bf16_t;
typedef short bf16x8 __attribute__((ext_vector_type(8)));
typedef short s16x4 __attribute__((ext_vector_type(4)));
typedef float f32x2 __attribute__((ext_vector_type(2)));
typedef float f32x4 __attribute__((ext_vector_type(4)));
typedef float f32x16 __attribute__((ext_vector_type(16)));
typedef unsigned u32x2 __attribute__((ext_vector_type(2)));
typedef unsigned u32x4 __attribute__((ext_vector_type(4)));
typedef __bf16 bf16x2_t __attribute__((ext_vector_type(2)));
#define LAS __attribute__((address_space(3)))
#define GAS __attribute__((address_space(1)))
typedef LAS unsigned char* lds_ptr;

constexpr int SEQ = 2048, DM = 1024, FF = 3584, NQKV = 3072, NH = 32;
constexpr float LN_EPS = 1e-5f;
constexpr float LOG2E = 1.4426950408889634f;
constexpr float QSCALE = 0.17677669529663687f * LOG2E;

constexpr unsigned WS_FLAGS = 0;
constexpr unsigned WS_STAT = 192 * 1024;
constexpr unsigned WS_QKV = 256 * 1024;
constexpr unsigned WS_ACT = WS_QKV + 3u * SEQ * DM * 2;
constexpr unsigned WS_XCH = WS_ACT + 144u * SEQ * 64;
constexpr unsigned WS_XC = WS_XCH + 256u * 32768;
constexpr unsigned WS_END = WS_XC + SEQ * DM * 2;
constexpr int F_EPOCH = 0, F_QKV = 1, F_H = 257, F_O = 513, F_X = 769, F_XC = 1025;
constexpr unsigned GRP = SEQ * 64;

#define SBAR() __builtin_amdgcn_sched_barrier(0)
#define BAR() __builtin_amdgcn_s_barrier()
#define WAIT_V(n) asm volatile("s_waitcnt vmcnt(" #n ")" ::: "memory")
#define WAIT_L0() do { __builtin_amdgcn_s_waitcnt(0xC07F); asm volatile("" ::: "memory"); } while (0)
#define CFENCE() asm volatile("" ::: "memory")
#define PIN(x) asm volatile("" : "+v"(x))
#define MFMA16(a, b, c) __builtin_amdgcn_mfma_f32_16x16x32_bf16(a, b, c, 0, 0, 0)
#define MFMA32(a, b, c) __builtin_amdgcn_mfma_f32_32x32x16_bf16(a, b, c, 0, 0, 0)
#define MFMA16_ACC(acc, a, b) asm volatile("v_mfma_f32_16x16x32_bf16 %0, %1, %2, %0" : "+v"(acc) : "v"(a), "v"(b))
#define MFMA32_ACC(acc, a, b) asm volatile("v_mfma_f32_32x32x16_bf16 %0, %1, %2, %0" : "+v"(acc) : "v"(a), "v"(b))
#define MFMA32_SET(d, a, b, c) asm volatile("v_mfma_f32_32x32x16_bf16 %0, %1, %2, %3" : "=&v"(d) : "v"(a), "v"(b), "v"(c))
#define MFMA_DRAIN() asm volatile("s_nop 15" ::: "memory")
#define RLX_AGENT __ATOMIC_RELAXED, __HIP_MEMORY_SCOPE_AGENT

#define DIAG
#define STAMP(i) do { if ((i) == MK) { SBAR(); dg_r = (unsigned)__builtin_amdgcn_s_memrealtime(); __builtin_amdgcn_s_waitcnt(0xC07F); SBAR(); } } while (0)
#define TS(v) do {} while (0)
#define PUT(i, v) do {} while (0)

__device__ __forceinline__ void glds16(const void* sbase, unsigned voff, unsigned ldsdst) {
    asm volatile("s_mov_b32 m0, %2\n\ts_nop 0\n\tglobal_load_lds_dwordx4 %0, %1" ::"v"(voff), "s"(sbase), "s"(ldsdst) : "memory");
}
__device__ __forceinline__ void glds16c(const void* sbase, unsigned voff, unsigned ldsdst) {
    asm volatile("s_mov_b32 m0, %2\n\ts_nop 0\n\tglobal_load_lds_dwordx4 %0, %1 sc1" ::"v"(voff), "s"(sbase), "s"(ldsdst) : "memory");
}
__device__ __forceinline__ bf16x8 lds_b128(lds_ptr p) { return *(const LAS bf16x8*)p; }
__device__ __forceinline__ s16x4 lds_tr(lds_ptr p) { return __builtin_amdgcn_ds_read_tr16_b64_v4i16((LAS s16x4*)p); }
__device__ __forceinline__ bf16x8 cat8(s16x4 a, s16x4 b) { return __builtin_shufflevector(a, b, 0, 1, 2, 3, 4, 5, 6, 7); }
__device__ __forceinline__ unsigned cvtpk(float lo, float hi) {
    f32x2 v = {lo, hi};
    return __builtin_bit_cast(unsigned, __builtin_convertvector(v, bf16x2_t));
}
__device__ __forceinline__ float u2f(unsigned u) { return __builtin_bit_cast(float, u); }
__device__ __forceinline__ unsigned f2u(float f) { return __builtin_bit_cast(unsigned, f); }
__device__ __forceinline__ float bf_lo(unsigned u) { return u2f(u << 16); }
__device__ __forceinline__ float bf_hi(unsigned u) { return u2f(u & 0xffff0000u); }
__device__ __forceinline__ void swap16(unsigned& a, unsigned& b) {
    auto r = __builtin_amdgcn_permlane16_swap(a, b, false, false);
    a = r[0]; b = r[1];
}
__device__ __forceinline__ void swap32(unsigned& a, unsigned& b) {
    auto r = __builtin_amdgcn_permlane32_swap(a, b, false, false);
    a = r[0]; b = r[1];
}
__device__ __forceinline__ int opaque_tid() { int t = threadIdx.x; asm volatile("" : "+v"(t)); return t; }
typedef unsigned long long u64;
struct Flags { u64* p; u64 tag; };
__device__ __forceinline__ void signal(const Flags& f, int word, int lane) {
    if (lane == 0) __hip_atomic_store((GAS u64*)(f.p + word * 16), f.tag, RLX_AGENT);
}
__device__ __forceinline__ u64 peek(const Flags& f, int word) {
    u64 v;
    asm volatile("global_load_dwordx2 %0, %1, off sc1\n\ts_waitcnt vmcnt(0)" : "=&v"(v) : "v"(f.p + word * 16) : "memory");
    return v;
}
__device__ __forceinline__ void wait_all(const Flags& f, int word, u64 v) {
#pragma unroll 1
    for (unsigned spin = 0; spin < (1u << 20) && !__all(v == f.tag); ++spin) { __builtin_amdgcn_s_sleep(1); v = peek(f, word); }
}
__device__ __forceinline__ void arrive(const Flags& f, int word, int lane, lds_ptr cnt, unsigned last) {
    if (lane == 0 && __hip_atomic_fetch_add((LAS unsigned*)cnt, 1u, __ATOMIC_RELAXED, __HIP_MEMORY_SCOPE_WORKGROUP) == last)
        __hip_atomic_store((GAS u64*)(f.p + word * 16), f.tag, RLX_AGENT);
}
#define PEEK_AHEAD(v, ptr, sel) asm volatile("s_cmp_lg_u32 %2, 0\n\ts_cbranch_scc1 1f\n\tglobal_load_dwordx2 %0, %1, off sc1\n1:" : "+v"(v) : "v"(ptr), "s"(sel) : "memory", "scc")
struct CodePf { const char* base; unsigned len; int first; };
#define CODE_PF(d) do { unsigned o_ = (unsigned)(cp.first + (opaque_tid() & 15)) * 128u; o_ = (o_ < cp.len) ? o_ : 0u; \
    asm volatile("global_load_dword %0, %1, %2" : "+v"(d) : "v"(o_), "s"(cp.base) : "memory"); } while (0)
#define PEEK_DONE(v) asm volatile("s_waitcnt vmcnt(0)" : "+v"(v) :: "memory")

template <int CTRL> __device__ __forceinline__ float dpp_add(float v) { return v + u2f(__builtin_amdgcn_update_dpp(0u, f2u(v), CTRL, 0xf, 0xf, true)); }
__device__ __forceinline__ float red4(float v) {
    unsigned a = f2u(v), b = a;
    swap16(a, b);
    v = u2f(a) + u2f(b);
    a = f2u(v); b = a;
    swap32(a, b);
    return u2f(a) + u2f(b);
}
__device__ __forceinline__ float wave_sum(float v) {
    v = dpp_add<0xB1>(v); v = dpp_add<0x4E>(v); v = dpp_add<0x141>(v); v = dpp_add<0x140>(v);
    return red4(v);
}
__device__ __forceinline__ f32x2 row_stats(u32x4& a, u32x4& b) {
    float v[16];
#pragma unroll
    for (int i = 0; i < 4; ++i) { v[2 * i] = bf_lo(a[i]); v[2 * i + 1] = bf_hi(a[i]); v[8 + 2 * i] = bf_lo(b[i]); v[8 + 2 * i + 1] = bf_hi(b[i]); }
    float s = 0.f;
#pragma unroll
    for (int i = 0; i < 16; ++i) s += v[i];
    const float mu = wave_sum(s) * (1.0f / DM);
    float q = 0.f;
#pragma unroll
    for (int i = 0; i < 16; ++i) { v[i] -= mu; q += v[i] * v[i]; }
#pragma unroll
    for (int i = 0; i < 4; ++i) { a[i] = cvtpk(v[2 * i], v[2 * i + 1]); b[i] = cvtpk(v[8 + 2 * i], v[8 + 2 * i + 1]); }
    return (f32x2){mu, 1.0f / __builtin_sqrtf(wave_sum(q) * (1.0f / DM) + LN_EPS)};
}

constexpr int SLOT = 32768, LDS_BYTES = 5 * SLOT;
constexpr int G1_A = 0, G1_W = 65536, G1_STAT = G1_W + 5 * 16384, G1_CS = G1_STAT + 2048, G1_CNT = G1_CS + 4 * 6 * 64;

#define NH0 30
static_assert(NH0 >= 28 && NH0 <= 32, "");
struct G2 { const char* Ah; const char* Ao; const char* Wd; const char* Wo; int nh; unsigned rowb; u64 split; };
__device__ __forceinline__ void g2_load_b(const G2& g, int j, unsigned dst, int vw, unsigned voffB) {
    const char* b = (j >= g.nh ? g.Wo : g.Wd) + (size_t)(j * 64 + vw * 8) * (DM * 2);
    glds16(b, voffB, dst + 16384 + vw * 1024); glds16(b + 128, voffB, dst + 16384 + 8192 + vw * 1024);
}
struct G2Off { unsigned A0, A1, N0, N1, B0, B1; };
__device__ __forceinline__ void g2_load_a(const G2& g, int j, unsigned dst, int vw, const G2Off& o) {
    const unsigned sp = (unsigned)(g.split >> j) & 1u;
    const char* a = (j >= g.nh ? g.Ao : g.Ah) + (size_t)j * (2 * GRP) + ((g.rowb + vw * 1024) >> sp);
    glds16c(a, sp ? o.N0 : o.A0, dst + vw * 2048); glds16c(a, sp ? o.N1 : o.A1, dst + vw * 2048 + 1024);
}
__device__ __forceinline__ void g2_voff(int lane, G2Off& o) {
    const int r8 = lane >> 3, u8 = lane & 7, ua = u8 ^ (r8 >> 1), ub = ua ^ 4;
    o.A0 = (unsigned)(ua >> 2) * GRP + r8 * 64 + (ua & 3) * 16;
    o.A1 = (unsigned)(ub >> 2) * GRP + (r8 + 8) * 64 + (ub & 3) * 16;
    o.N0 = (unsigned)(ua >> 1) * (GRP / 2) + r8 * 32 + (ua & 1) * 16;
    o.N1 = (unsigned)(ub >> 1) * (GRP / 2) + (r8 + 8) * 32 + (ub & 1) * 16;
    o.B0 = (unsigned)(r8 * (DM * 2) + (u8 ^ (r8 & 2)) * 16);
    o.B1 = (unsigned)(r8 * (DM * 2) + (u8 ^ (4 | (r8 & 2))) * 16);
}
constexpr u64 split_mask(int first, int n) { u64 m = 0; for (int j = 0; j < n; ++j) if ((first + j) % 7 == 6) m |= 1ull << j; return m; }
static_assert(((split_mask(0, NH0) | split_mask(NH0, 56 - NH0)) & 7) == 0, "phase B's tail prefetches K-tiles 0..2 as plain groups");

struct AttnOut { f32x16 o0, o1; f32x4 l0, l1; float m0, m1; };
struct HPend { u32x4 v[8]; unsigned off, size; const void* base; };
struct AttnLane {
    unsigned voffK, voffV; lds_ptr kb0, kb1, vb; const char* qp; bf16x8 ones;
    __device__ __forceinline__ void init(lds_ptr lds, const char* Qg, int wid, int lane) {
        const int r32 = lane & 31, hi = lane >> 5, kh = wid >> 2, qg = wid & 3, ksw = (r32 >> 2) & 3;
        const int uk = (lane & 3) ^ ((lane >> 4) & 3), uv = lane & 3;
        voffK = (unsigned)((uk >> 1) * 65536 + wid * 1024 + (lane >> 2) * 32 + (uk & 1) * 16);
        voffV = (unsigned)((uv >> 1) * 65536 + wid * 1024 + (lane >> 2) * 32 + (uv & 1) * 16);
        kb0 = lds + kh * 8192 + r32 * 64 + ((hi ^ ksw) * 16); kb1 = lds + kh * 8192 + r32 * 64 + (((2 | hi) ^ ksw) * 16);
        vb = lds + 16384 + kh * 8192 + (4 * hi + ((lane >> 2) & 3)) * 64 + ((lane >> 4) & 1) * 32 + (lane & 3) * 8;
        qp = Qg + (qg * 64 + r32) * 32 + hi * 16;
        const short v = (((lane >> 4) & 1) == ((lane >> 2) & 1)) ? (short)0x3f80 : (short)0;
        ones = (bf16x8){v, v, v, v, v, v, v, v};
    }
};
#define EX(v) __builtin_amdgcn_exp2f(v)
#define AT_LOAD_K(c, slot) do { const unsigned d_ = lds0 + wid * 2048 + (slot) * SLOT; const char* k_ = Kg + (c) * 8192; glds16c(k_, A.voffK, d_); glds16c(k_ + 512, A.voffK, d_ + 1024); } while (0)
#define AT_LOAD_V(c, slot) do { const unsigned d_ = lds0 + wid * 2048 + (slot) * SLOT + 16384; const char* v_ = Vg + (c) * 8192; glds16c(v_, A.voffV, d_); glds16c(v_ + 512, A.voffV, d_ + 1024); } while (0)
#define AT_PACK(S, i) __builtin_bit_cast(bf16x8, (u32x4){cvtpk(S[i], S[i + 1]), cvtpk(S[i + 2], S[i + 3]), cvtpk(S[i + 4], S[i + 5]), cvtpk(S[i + 6], S[i + 7])})
#define AT_Q_LOAD() asm volatile("global_load_dwordx4 %0, %4, off sc1\n\tglobal_load_dwordx4 %1, %4, off offset:1024 sc1\n\tglobal_load_dwordx4 %2, %5, off sc1\n\tglobal_load_dwordx4 %3, %5, off offset:1024 sc1" \
        : "=&v"(q00), "=&v"(q10), "=&v"(q01), "=&v"(q11) : "v"(A.qp), "v"(qp2) : "memory")
#define AT_Q_WAIT(n) asm volatile("s_waitcnt vmcnt(" #n ")" : "+v"(q00), "+v"(q01), "+v"(q10), "+v"(q11) :: "memory")
__device__ __forceinline__ float rowmax16(const f32x16& s) {
    float a = s[0];
#pragma unroll
    for (int r = 1; r < 16; ++r) a = __builtin_fmaxf(a, s[r]);
    unsigned x = f2u(a), y = x; swap32(x, y);
    return __builtin_fmaxf(u2f(x), u2f(y));
}

__device__ __forceinline__ void attn_safe(lds_ptr lds, unsigned lds0, const char* Qg, const char* Kg, const char* Vg, int wid, AttnOut& R) {
    AttnLane A; A.init(lds, Qg, wid, opaque_tid() & 63);
    bf16x8 q00, q01, q10, q11; const char* qp2 = A.qp + 65536;
    AT_Q_LOAD(); AT_Q_WAIT(0);
    f32x16 o0 = (f32x16){}, o1 = (f32x16){};
    f32x4 l0 = (f32x4){0.f, 0.f, 0.f, 0.f}, l1 = l0;
    float m0 = -__builtin_inff(), m1 = m0;
#pragma unroll 1
    for (int c = 0; c < 8; ++c) {
        AT_LOAD_K(c, 3); AT_LOAD_V(c, 3);
        WAIT_V(0); BAR(); CFENCE();
#pragma unroll 1
        for (int t = 0; t < 4; ++t) {
            const int to = 3 * SLOT + t * 2048;
            const bf16x8 ka = lds_b128(A.kb0 + to), kb = lds_b128(A.kb1 + to);
            const bf16x8 va = cat8(lds_tr(A.vb + to), lds_tr(A.vb + to + 512)), vc = cat8(lds_tr(A.vb + to + 1024), lds_tr(A.vb + to + 1536));
            f32x16 s0 = MFMA32(kb, q01, MFMA32(ka, q00, (f32x16){})), s1 = MFMA32(kb, q11, MFMA32(ka, q10, (f32x16){}));
            const float n0 = __builtin_fmaxf(m0, rowmax16(s0)), n1 = __builtin_fmaxf(m1, rowmax16(s1));
            const float f0 = EX(m0 - n0), f1 = EX(m1 - n1);
            m0 = n0; m1 = n1; o0 *= f0; o1 *= f1; l0 *= f0; l1 *= f1;
#pragma unroll
            for (int r = 0; r < 16; ++r) { s0[r] = EX(s0[r] - n0); s1[r] = EX(s1[r] - n1); }
            const bf16x8 p00 = AT_PACK(s0, 0), p01 = AT_PACK(s0, 8), p10 = AT_PACK(s1, 0), p11 = AT_PACK(s1, 8);
            o0 = MFMA32(vc, p01, MFMA32(va, p00, o0)); l0 = MFMA16(A.ones, p01, MFMA16(A.ones, p00, l0));
            o1 = MFMA32(vc, p11, MFMA32(va, p10, o1)); l1 = MFMA16(A.ones, p11, MFMA16(A.ones, p10, l1));
        }
        WAIT_L0(); BAR(); CFENCE();
    }
    R.o0 = o0; R.o1 = o1; R.l0 = l0; R.l1 = l1; R.m0 = m0; R.m1 = m1;
}

__device__ __forceinline__ void attn_fast(lds_ptr lds, unsigned lds0, const char* Qg, const char* Kg, const char* Vg, const G2& g2, const Flags& flags, int qkv0, int qkvword, int depword,
                                          int hsig, int c0, bool bump, const CodePf& cp, const HPend& hp, const char* wpd, const char* wpo, int prow, int wid, AttnOut& R
                                          , unsigned& dg_c, unsigned& dg_r
                                          ) {
    const int lane = opaque_tid() & 63;
    AttnLane A; A.init(lds, Qg, wid, lane);
    lds_ptr kb0 = A.kb0, kb1 = A.kb1, vb = A.vb;
    PIN(kb0); PIN(kb1); PIN(vb);
    lds_ptr kh0 = kb0 + 2 * SLOT, kh1 = kb1 + 2 * SLOT, vh = vb + 2 * SLOT;
    PIN(kh0); PIN(kh1); PIN(vh);
    const bf16x8 ones = A.ones;
    const __amdgpu_buffer_rsrc_t hr = __builtin_amdgcn_make_buffer_rsrc((void*)hp.base, 0, hp.size, 0x00020000);
    f32x16 o0 = (f32x16){}, o1 = (f32x16){}, negm0 = (f32x16){}, negm1 = (f32x16){}, sA0, sA1, sB0, sB1;
    f32x4 l0 = (f32x4){0.f, 0.f, 0.f, 0.f}, l1 = l0;
    bf16x8 kfa, kfb, va, vc = (bf16x8){}, pf = (bf16x8){};
    PIN(o0); PIN(o1); PIN(l0); PIN(l1); PIN(vc); PIN(pf); PIN(A.voffK); PIN(A.voffV);
    G2Off go; g2_voff(lane, go);
    unsigned gA0 = go.A0, gA1 = go.A1, gB = (wid & 1) ? go.B1 : go.B0;
    unsigned pfo = (unsigned)(((lane >> 3) & 1) * (DM * 2) + (lane & 7) * 128);
    const u64* hptr = flags.p + (F_H + depword) * 16;
    const u64* qptr = flags.p + qkvword * 16;
    PIN(gA0); PIN(gA1); PIN(gB); PIN(pfo); PIN(hptr); PIN(qptr);
    if (wid == 0) {
        wait_all(flags, qkv0, peek(flags, qkv0));
        STAMP(13);
    }
    CFENCE(); BAR(); CFENCE();
    STAMP(4);
    AT_LOAD_K(c0, 0);
    glds16c(Qg, A.voffK, lds0 + wid * 2048 + 2 * SLOT); glds16c(Qg + 512, A.voffK, lds0 + wid * 2048 + 2 * SLOT + 1024);
    AT_LOAD_V(c0, 0);
    u64 hv = 0;
    float m0, m1;
    WAIT_V(2);
    PEEK_AHEAD(hv, qptr, wid);
    BAR(); SBAR();
    STAMP(15);
    const int qo = 2 * SLOT + (wid & 3) * 4096 - (wid >> 2) * 8192;
    const bf16x8 q00 = lds_b128(kb0 + qo), q10 = lds_b128(kb0 + qo + 2048), q01 = lds_b128(kb1 + qo), q11 = lds_b128(kb1 + qo + 2048);
    {
        kfa = lds_b128(kb0); kfb = lds_b128(kb1);
        sA0 = MFMA32(kfa, q00, negm0); sA1 = MFMA32(kfa, q10, negm1); sA0 = MFMA32(kfb, q01, sA0); sA1 = MFMA32(kfb, q11, sA1);
        kfa = lds_b128(kb0 + 2048); kfb = lds_b128(kb1 + 2048);
        m0 = rowmax16(sA0); m1 = rowmax16(sA1);
#pragma unroll
        for (int r = 0; r < 16; ++r) { sA0[r] -= m0; sA1[r] -= m1; negm0[r] = -m0; negm1[r] = -m1; }
        PIN(negm0); PIN(negm1);
    }
    unsigned pfd = 0;
    PEEK_DONE(hv);
    if (wid == 0) {
        wait_all(flags, qkvword, hv);
        if (bump) signal(flags, F_EPOCH, lane);
        STAMP(14);
    }
    CFENCE(); BAR(); SBAR();
    STAMP(5);
    AT_LOAD_K((c0 + 1) & 7, 1); AT_LOAD_V((c0 + 1) & 7, 1);
    STAMP(19);
#define AT_GROUP(QK, RD, S, i, O, L, VF) do { \
        QK; SBAR(); RD; SBAR(); \
        float e0, e1, e2, e3, e4, e5, e6, e7; \
        asm volatile("v_exp_f32 %0, %13\n\tv_exp_f32 %1, %14\n\tv_exp_f32 %2, %15\n\tv_mfma_f32_32x32x16_bf16 %8, %10, %11, %8\n\t" \
                     "v_exp_f32 %3, %16\n\tv_exp_f32 %4, %17\n\tv_exp_f32 %5, %18\n\tv_mfma_f32_16x16x32_bf16 %9, %12, %11, %9\n\tv_exp_f32 %6, %19\n\tv_exp_f32 %7, %20" \
            : "=&v"(e0), "=&v"(e1), "=&v"(e2), "=&v"(e3), "=&v"(e4), "=&v"(e5), "=&v"(e6), "=&v"(e7), "+v"(O), "+v"(L) \
            : "v"(VF), "v"(pf), "v"(ones), "v"(S[i]), "v"(S[i + 1]), "v"(S[i + 2]), "v"(S[i + 3]), "v"(S[i + 4]), "v"(S[i + 5]), "v"(S[i + 6]), "v"(S[i + 7])); \
        pf = __builtin_bit_cast(bf16x8, (u32x4){cvtpk(e0, e1), cvtpk(e2, e3), cvtpk(e4, e5), cvtpk(e6, e7)}); SBAR(); } while (0)
#define LO(lo, hi, off) (((off) >= 2 * SLOT ? hi : lo) + ((off) & (2 * SLOT - 1)))
#define AT_STEP(C0, C1, N0, N1, QK, KR, KO, VO, H0, H1, H2, H3) do { \
        AT_GROUP(if (QK) MFMA32_SET(N0, kfa, q00, negm0), { va = cat8(lds_tr(LO(vb, vh, VO)), lds_tr(LO(vb, vh, VO) + 512)); H0; }, C0, 0, o1, l1, vc); \
        AT_GROUP(if (QK) MFMA32_SET(N1, kfa, q10, negm1), { if (KR) kfa = lds_b128(LO(kb0, kh0, KO)); vc = cat8(lds_tr(LO(vb, vh, VO) + 1024), lds_tr(LO(vb, vh, VO) + 1536)); H1; }, C1, 0, o0, l0, va); \
        AT_GROUP(if (QK) MFMA32_ACC(N0, kfb, q01), H2, C0, 8, o1, l1, va); \
        AT_GROUP(if (QK) MFMA32_ACC(N1, kfb, q11), { if (KR) kfb = lds_b128(LO(kb1, kh1, KO)); H3; }, C1, 8, o0, l0, vc); } while (0)
#define PRV 1
#define YP(p) asm volatile("s_cmp_lt_u32 %0, 4\n\ts_cbranch_scc1 1f\n\ts_setprio " #p "\n1:" :: "s"(wid) : "scc")
    if (PRV) __builtin_amdgcn_s_setprio(1);
    auto ylow = [&]() { if (PRV) YP(0); };
#define S0(c, ...) AT_STEP(sA0, sA1, sB0, sB1, true, true, ((c) & 3) * SLOT + 4096, ((c) & 3) * SLOT, __VA_ARGS__)
#define S1(c, ...) if (PRV == 1) YP(0); AT_STEP(sB0, sB1, sA0, sA1, true, true, ((c) & 3) * SLOT + 6144, ((c) & 3) * SLOT + 2048, __VA_ARGS__)
#define S2(c, ...) if (PRV == 1 || (PRV == 2 && (c) == 7)) YP(2); AT_STEP(sA0, sA1, sB0, sB1, true, (c) < 7, (((c) + 1) & 3) * SLOT, ((c) & 3) * SLOT + 4096, __VA_ARGS__)
#define S3(c, ...) AT_STEP(sB0, sB1, sA0, sA1, (c) < 7, (c) < 7, (((c) + 1) & 3) * SLOT + 2048, ((c) & 3) * SLOT + 6144, __VA_ARGS__)
#define SY(c, n) do { WAIT_V(n); WAIT_L0(); BAR(); SBAR(); STAMP(20 + (c)); } while (0)
#define NO (void)0
#define KP(c, i) glds16c(Kg + ((c0 + (c)) & 7) * 8192 + (i) * 512, A.voffK, lds0 + wid * 2048 + ((c) & 3) * SLOT + (i) * 1024)
#define VP(c, i) glds16c(Vg + ((c0 + (c)) & 7) * 8192 + (i) * 512, A.voffV, lds0 + wid * 2048 + ((c) & 3) * SLOT + 16384 + (i) * 1024)
#define CH(c) KP(c, 0), KP(c, 1), VP(c, 0), VP(c, 1)
#define HS(i) do { __builtin_amdgcn_raw_buffer_store_b128(hp.v[i], hr, hp.off + (i) * 1024, 0, 16); CFENCE(); } while (0)
#define WP(i) wpf(i)
#define GB(j, slot, i) glds16(g2.Wd + (size_t)((j) * 64 + wid * 8) * (DM * 2) + (i) * 128, gB, lds0 + (slot) * SLOT + 16384 + (i) * 8192 + wid * 1024)
#define GA(j, slot, i) glds16c(g2.Ah + (size_t)(j) * (2 * GRP) + g2.rowb + wid * 1024, (i) ? gA1 : gA0, lds0 + (slot) * SLOT + wid * 2048 + (i) * 1024)
    auto wpf = [&](int i) {
        const int pi = (wid + 8 * i < g2.nh + 8) ? wid + 8 * i : 0, lr = prow + 2 * pi;
        asm volatile("global_load_dword %0, %1, %2" : "+v"(pfd) : "v"(pfo), "s"((lr < g2.nh * 64 ? wpd : wpo) + (size_t)lr * (DM * 2)) : "memory");
    };
    auto cpf = [&]() { CODE_PF(pfd); };
    auto hpeek = [&]() { PEEK_AHEAD(hv, hptr, wid); };
    S0(0, NO, NO, NO, NO);         S1(0, NO, NO, NO, NO);    SY(0, 0);  S2(0, CH(2)); S3(0, CH(3));
    S0(1, WP(0), NO, HS(0), NO);   S1(1, NO, NO, HS(1), NO); SY(1, 7);  S2(1, CH(4)); S3(1, WP(1), NO, HS(2), NO);
    S0(2, NO, NO, HS(3), NO);      S1(2, NO, NO, HS(4), NO); SY(2, 11); S2(2, CH(5)); S3(2, WP(2), NO, HS(5), NO);
    S0(3, NO, NO, HS(6), NO);      S1(3, NO, NO, HS(7), NO); SY(3, 12); S2(3, CH(6)); S3(3, WP(3), NO, NO, NO);
    S0(4, NO, NO, NO, NO);         S1(4, NO, NO, NO, NO);    SY(4, 5);
    if (wid == 0) signal(flags, hsig, lane);
                                                                        S2(4, CH(7)); S3(4, WP(4), NO, NO, NO);
    S0(5, GB(0, 4, 0), GB(0, 4, 1), NO, NO); S1(5, NO, NO, NO, NO); SY(5, 2); S2(5, GB(1, 0, 0), GB(1, 0, 1), NO, NO); S3(5, NO, NO, NO, NO);
    S0(6, hpeek(), NO, NO, NO);
    if (wid == 0) { PEEK_DONE(hv); wait_all(flags, F_H + depword, hv); }
                                   S1(6, NO, NO, NO, NO);    SY(6, 0);  S2(6, GA(0, 4, 0), GA(0, 4, 1), GB(2, 1, 0), GB(2, 1, 1)); S3(6, GA(1, 0, 0), GA(1, 0, 1), cpf(), NO);
    S0(7, GA(2, 1, 0), GA(2, 1, 1), NO, NO); S1(7, NO, NO, NO, NO); SY(7, 0); S2(7, NO, NO, NO, NO); S3(7, NO, NO, ylow(), NO);
    __builtin_amdgcn_s_setprio(0);
#undef S0
#undef S1
#undef S2
#undef S3
#undef NO
    STAMP(28);
    asm volatile("s_nop 1" ::: "memory");
    MFMA32_ACC(o1, vc, pf); MFMA16_ACC(l1, ones, pf);
    MFMA_DRAIN(); SBAR();
    asm volatile("" :: "v"(pfd));
    R.o0 = o0; R.o1 = o1; R.l0 = l0; R.l1 = l1; R.m0 = m0; R.m1 = m1;
}

__global__ __launch_bounds__(512, 2) void k_main(const bf16_t* __restrict__ x, const bf16_t* __restrict__ wqkv, const bf16_t* __restrict__ wo, const bf16_t* __restrict__ wg,
                                                 const bf16_t* __restrict__ wu, const bf16_t* __restrict__ wd, bf16_t* __restrict__ out, unsigned char* ws) {
    unsigned long long t00; int bid_ = blockIdx.x; asm volatile("s_memrealtime %0\n\ts_waitcnt lgkmcnt(0)" : "=s"(t00), "+s"(bid_)); SBAR();
    __shared__ __attribute__((aligned(16))) unsigned char smem[LDS_BYTES];
    const lds_ptr lds = (lds_ptr)smem;
    const unsigned lds0 = (unsigned)(uintptr_t)lds;
    const int wid = __builtin_amdgcn_readfirstlane(threadIdx.x >> 6);
    const int bid = bid_, xcd = bid & 7, idx = bid >> 3;
    Flags flags; flags.p = (u64*)(ws + WS_FLAGS);
    const __amdgpu_buffer_rsrc_t wsr = __builtin_amdgcn_make_buffer_rsrc((void*)ws, 0, WS_END, 0x00020000);
#define ST16(v, off) __builtin_amdgcn_raw_buffer_store_b128(v, wsr, off, 0, 16)
    unsigned dg_c = (unsigned)t00, dg_r = dg_c;
    STAMP(0);
    unsigned cpd = 0;
    CodePf cp;
    asm volatile("s_getpc_b64 %0\n.Lk_begin:\n\ts_mov_b32 %1, .Lk_end-.Lk_begin" : "=s"(cp.base), "=s"(cp.len));
    cp.first = idx * 16;
    HPend hp;
    {
        const int lane = threadIdx.x & 63, wr = wid >> 2, wc = wid & 3, js = idx & 3, rb = idx >> 2;
        const bool typeY = (js == 3);
        const int f0 = 448 * xcd + 128 * js;
        const char* P0 = (const char*)(wg + f0);
        const char* P2 = (const char*)(wu + f0);
        const char* P1 = typeY ? (const char*)(wqkv + 128 * xcd + 64) : (const char*)(wg + f0 + 64);
        const char* P3 = typeY ? (const char*)(wqkv + 1024 + 128 * xcd + 64) : (const char*)(wu + f0 + 64);
        const char* P4 = typeY ? (const char*)(wqkv + 2048 + 128 * xcd + 64) : (const char*)(wqkv + js * 1024 + 128 * xcd);
        const unsigned stepF = 32u * FF * 2u, stepQ = 32u * NQKV * 2u, step13 = typeY ? stepQ : stepF;
        const char* XA = (const char*)x + (size_t)rb * 256 * DM * 2;
        const char* XC = (const char*)ws + WS_XC + (size_t)rb * 256 * DM * 2;
        const int r8 = lane >> 3, u8 = lane & 7;
        const int gw = (r8 & 2) | ((wc & 1) << 2);
        const unsigned voffF = (unsigned)(((wc * 8 + r8) * FF + (u8 ^ gw) * 8) * 2);
        const unsigned voffQ = (unsigned)(((wc * 8 + r8) * NQKV + (u8 ^ gw) * 8) * 2);
        const unsigned voff13 = typeY ? voffQ : voffF;
        const unsigned voffA0 = (unsigned)(((32 * wid + r8) * DM + (u8 ^ (r8 >> 1)) * 8) * 2);
        const unsigned voffA1 = (unsigned)(((32 * wid + 8 + r8) * DM + (u8 ^ (4 | (r8 >> 1))) * 8) * 2);
        const unsigned dstA = lds0 + G1_A + wid * 4096, dstW = lds0 + G1_W + wc * 1024;
#define G1_LOAD_A(t) do { const char* b_ = XA + (t) * 128; const unsigned d_ = dstA + ((t) & 1) * 32768; \
        glds16(b_, voffA0, d_); glds16(b_, voffA1, d_ + 1024); glds16(b_ + 32768, voffA0, d_ + 2048); glds16(b_ + 32768, voffA1, d_ + 3072); } while (0)
#define G1_LOAD_W(hs) do { const unsigned d_ = dstW + ((hs) & 3) * 4096; \
        glds16(P0 + (size_t)(hs) * stepF, voffF, d_); glds16(P1 + (size_t)(hs) * step13, voff13, d_ + 16384); glds16(P2 + (size_t)(hs) * stepF, voffF, d_ + 32768); \
        glds16(P3 + (size_t)(hs) * step13, voff13, d_ + 49152); glds16(P4 + (size_t)(hs) * stepQ, voffQ, d_ + 65536); } while (0)
        STAMP(29);
        if (wr == 1) G1_LOAD_W(0);
        G1_LOAD_A(0);
        u32x4 xa0, xa1;
        const int srow = rb * 256 + (js * 8 + xcd) * 8 + wid;
        asm volatile("global_load_dwordx4 %0, %2, %3\n\tglobal_load_dwordx4 %1, %2, %3 offset:1024"
                     : "=&v"(xa0), "=&v"(xa1) : "v"((unsigned)(lane * 16)), "s"((const char*)x + (size_t)srow * DM * 2) : "memory");
        CODE_PF(cpd);
        STAMP(30);

        int fr = lane & 15, fq = lane >> 4;
        const lds_ptr aB0 = lds + G1_A + (wr * 128 + fr) * 128 + ((fq ^ (fr >> 1)) * 16);
        const lds_ptr aB1 = lds + G1_A + (wr * 128 + fr) * 128 + (((4 | fq) ^ (fr >> 1)) * 16);
        const int tq = (lane >> 2) & 3, tp = lane & 3, tg = (tq & 2) | ((fq & 1) << 2);
        auto wbase = [&](int panel, int bp) { return lds + G1_W + panel * 16384 + (8 * fq + tq) * 128 + (((2 * bp + (tp >> 1)) ^ tg) * 16) + (tp & 1) * 8; };
        const int pX = wc >> 1, bX = 2 * (wc & 1);
        const lds_ptr wB0 = typeY ? wbase(0, wc) : wbase(pX, bX);
        const lds_ptr wB1 = typeY ? wbase(1, wc) : wbase(pX, bX + 1);
        const lds_ptr wB2 = typeY ? wbase(2, wc) : wbase(2 + pX, bX);
        const lds_ptr wB3 = typeY ? wbase(3, wc) : wbase(2 + pX, bX + 1);
        const lds_ptr wB4 = wbase(4, wc);
        if (threadIdx.x == 0) *(LAS unsigned*)(lds + G1_CNT) = 0u;
        f32x4 acc[8][5];
#pragma unroll
        for (int m = 0; m < 8; ++m)
#pragma unroll
            for (int b = 0; b < 5; ++b) { acc[m][b] = (f32x4){0.f, 0.f, 0.f, 0.f}; PIN(acc[m][b]); }
        if (wr == 1) { WAIT_V(7); BAR(); }
        asm volatile("s_waitcnt vmcnt(1)" : "+v"(xa0), "+v"(xa1) :: "memory");
        STAMP(31);
        u64 sg;
        {
            const f32x2 st = row_stats(xa0, xa1);
            G1_LOAD_W(1 + wr);
            ST16(xa0, (lane >= 32) ? WS_XC + (unsigned)srow * 2048u + lane * 16 : WS_END);
            ST16(xa1, WS_XC + (unsigned)srow * 2048u + 1024 + lane * 16);
            __builtin_amdgcn_raw_buffer_store_b64(__builtin_bit_cast(u32x2, st), wsr, (lane == 0) ? WS_STAT + (unsigned)srow * 8u : WS_END, 0, 16);
            asm volatile("global_load_dwordx2 %0, %1, %2 sc1" : "=&v"(sg) : "v"(0u), "s"(flags.p) : "memory");
        }
        STAMP(32);
        bf16x8 af[8], wf[5];
        f32x4 cs0 = (f32x4){0.f, 0.f, 0.f, 0.f}, cs1 = cs0, cs2 = cs0;
        bf16x8 one8 = (bf16x8){0x3f80, 0x3f80, 0x3f80, 0x3f80, 0x3f80, 0x3f80, 0x3f80, 0x3f80};
        PIN(cs0); PIN(cs1); PIN(cs2); PIN(one8);
#define CS1(c, w) "v_mfma_f32_16x16x32_bf16 %" #c ", %" #w ", %8, %" #c "\n\t"
#define G1_CSUM_(P0, P1) asm volatile("s_cmp_gt_u32 %10, 3\n\ts_cbranch_scc1 2f\n\ts_cmp_lg_u32 %9, 0\n\ts_cbranch_scc1 1f\n\t" CS1(0, 3) CS1(1, 4) P0 "s_branch 2f\n1:\n\t" CS1(0, 5) CS1(1, 6) P1 "\n2:" \
            : "+v"(cs0), "+v"(cs1), "+v"(cs2) : "v"(wf[0]), "v"(wf[1]), "v"(wf[2]), "v"(wf[3]), "v"(wf[4]), "v"(one8), "s"(wr), "s"(t) : "scc")
#define G1_CSUM(kk) do { if ((kk) == 0) G1_CSUM_(CS1(2, 7), ""); else G1_CSUM_("", CS1(2, 7)); } while (0)
#define G1_RW(i, B, slot) wf[i] = cat8(lds_tr(B + (slot) * 4096), lds_tr(B + (slot) * 4096 + 512))
#define G1_RA(m, AB, abuf) af[m] = lds_b128(AB + (abuf) * 32768 + (m) * 2048)
#define G1_SEG(AB, abuf, slot, L0, L1, L2, L3, L4, CLOSE) do { \
        L0; G1_RW(0, wB0, slot); G1_RW(1, wB1, slot); L1; G1_RW(2, wB2, slot); G1_RW(3, wB3, slot); L2; G1_RW(4, wB4, slot); G1_RA(0, AB, abuf); G1_RA(1, AB, abuf); \
        L3; G1_RA(2, AB, abuf); G1_RA(3, AB, abuf); G1_RA(4, AB, abuf); L4; G1_RA(5, AB, abuf); G1_RA(6, AB, abuf); G1_RA(7, AB, abuf); \
        WAIT_L0(); SBAR(); if (CLOSE) { BAR(); SBAR(); } } while (0)
#define G1_SEG_A(abuf, slot, tt) do { const char* b_ = ((tt) < 4 ? XA : XC) + (tt) * 128; const unsigned d_ = dstA + (1 - (abuf)) * 32768; \
        G1_SEG(aB0, abuf, slot, glds16c(b_, voffA0, d_), glds16c(b_, voffA1, d_ + 1024), glds16c(b_ + 32768, voffA0, d_ + 2048), glds16c(b_ + 32768, voffA1, d_ + 3072), (void)0, true); } while (0)
#define G1_SEG_W(abuf, slot, hs, dslot, CLOSE) do { const unsigned d_ = dstW + (dslot) * 4096; \
        G1_SEG(aB1, abuf, slot, glds16(P0 + (size_t)(hs) * stepF, voffF, d_), glds16(P1 + (size_t)(hs) * step13, voff13, d_ + 16384), glds16(P2 + (size_t)(hs) * stepF, voffF, d_ + 32768), \
               glds16(P3 + (size_t)(hs) * step13, voff13, d_ + 49152), glds16(P4 + (size_t)(hs) * stepQ, voffQ, d_ + 65536), CLOSE); } while (0)
#define EB 2
#define FIRST_WAIT() asm volatile("s_waitcnt vmcnt(4)" : "+v"(sg), "+v"(cpd) :: "memory")
#define G1_MM(i0, i1) _Pragma("unroll") for (int i = (i0); i < (i1); ++i) { const int b = i >> 3, m = (b & 1) ? 7 - (i & 7) : (i & 7); MFMA16_ACC(acc[m][b], wf[b], af[m]); }
#define G1_MMA(kk, CLOSE) do { __builtin_amdgcn_s_setprio(1); G1_MM(0, 40 - EB); SBAR(); __builtin_amdgcn_s_setprio(2); CLOSE; SBAR(); G1_MM(40 - EB, 40); G1_CSUM(kk); __builtin_amdgcn_s_setprio(0); SBAR(); } while (0)

        STAMP(33);
        WAIT_L0();
        STAMP(34);
        BAR();
        SBAR();
        STAMP(1);
        flags.tag = 0;
        const u64* sgp = flags.p + (F_XC + rb * 32 + (lane & 31)) * 16;
#pragma unroll 1
        for (int t = 0; t < 16; t += 2) {
            const int h1 = (2 * t + 3 + wr < 31) ? 2 * t + 3 + wr : 31, h2 = (2 * t + 5 + wr < 31) ? 2 * t + 5 + wr : 31, t2 = (t < 14) ? t + 2 : 15;
            const int sel = (t == 12) ? 0 : ((t ^ 2) | wid);

            PEEK_AHEAD(sg, sgp, sel);
            if (t == 14) CODE_PF(cpd);
            G1_SEG_A(0, 0, t + 1); G1_MMA(0, { FIRST_WAIT(); BAR(); });
            if (t == 0) {
                const unsigned elo = __builtin_amdgcn_readfirstlane((unsigned)sg), ehi = __builtin_amdgcn_readfirstlane((unsigned)(sg >> 32));
                flags.tag = (((u64)ehi << 32) | elo) * 0x9E3779B97F4A7C15ull + 0x632BE59BD9B4E019ull;
                if (wid == 4) signal(flags, F_XC + bid, lane);
            }
            if (sel == 0) {
                if (t == 12) *(LAS u64*)(lds + G1_STAT + (wid * 32 + (opaque_tid() & 31)) * 8) = sg;
                else wait_all(flags, F_XC + rb * 32 + (opaque_tid() & 31), sg);
            }
            G1_SEG_W(0, 1, h1, (3 + wr) & 3, true); G1_MMA(1, { WAIT_V(5); BAR(); });
            if (t == 4) {
                const int tl = opaque_tid() & 63, k = tl & 15;
                if (k < 3) *(LAS f32x4*)(lds + G1_CS + wc * 384 + (tl >> 4) * 16 + (k == 2 ? 256 + wr * 64 : wr * 128 + k * 64)) = k == 0 ? cs0 : k == 1 ? cs1 : cs2;
                sgp = (const u64*)(ws + WS_STAT) + rb * 256 + wid * 32 + (tl & 31);
            }
            G1_SEG_A(1, 2, t2); G1_MMA(0, { WAIT_V(4); BAR(); });
            G1_SEG_W(1, 3, h2, (1 + wr) & 3, true);
            G1_MMA(1, asm volatile("s_waitcnt vmcnt(5)\n\ts_cmp_eq_u32 %0, 15\n\ts_cbranch_scc1 1f\n\ts_barrier\n1:" :: "s"(t + wr) : "memory", "scc"));
        }
        MFMA_DRAIN(); SBAR();
        STAMP(2);

        const int tz = opaque_tid(), ln = tz & 63;
        fr = tz & 15; fq = (tz >> 4) & 3;
        const int row0 = rb * 256 + wr * 128 + fr;
        f32x4 csv[5]; f32x2 st[8];
#pragma unroll
        for (int b = 0; b < 5; ++b) csv[b] = *(const LAS f32x4*)(lds + G1_CS + wc * 384 + b * 64 + fq * 16);
        csv[4] += *(const LAS f32x4*)(lds + G1_CS + wc * 384 + 320 + fq * 16);
#pragma unroll
        for (int m = 0; m < 8; ++m) st[m] = *(const LAS f32x2*)(lds + G1_STAT + (wr * 128 + m * 16 + fr) * 8);
        auto lnv = [&](int m, int b, float sc) { return (acc[m][b] - st[m].x * csv[b]) * (st[m].y * sc); };
        auto glu = [&](int m, int bg, int bu) {
            const f32x4 g = lnv(m, bg, -LOG2E), u = lnv(m, bu, 1.0f);
            f32x4 h;
#pragma unroll
            for (int j = 0; j < 4; ++j) h[j] = u[j] * __builtin_amdgcn_rcpf(1.0f + __builtin_amdgcn_exp2f(g[j]));
            return h;
        };
        auto pack2 = [&](const f32x4& v0, const f32x4& v1) {
            unsigned p0 = cvtpk(v0[0], v0[1]), p1 = cvtpk(v0[2], v0[3]), q0 = cvtpk(v1[0], v1[1]), q1 = cvtpk(v1[2], v1[3]);
            swap16(p0, q0); swap16(p1, q1);
            return (u32x4){p0, p1, q0, q1};
        };
        const int hd = 4 * xcd + (typeY ? 2 : 0) + (wc >> 1);
        const unsigned qoff = WS_QKV + (unsigned)(hd * 2 + (wc & 1)) * 65536u + (unsigned)(row0 + (fq & 1) * 16) * 32u + (fq >> 1) * 16;
        constexpr unsigned KIND = NH * 2 * 65536u;
        if (!typeY) {
            const float sc4 = (js == 0) ? QSCALE : 1.0f;
#pragma unroll
            for (int m = 0; m < 8; m += 2) ST16(pack2(lnv(m, 4, sc4), lnv(m + 1, 4, sc4)), qoff + js * KIND + m * 512);
            CFENCE();
            hp.base = ws + WS_ACT + (unsigned)(14 * xcd + 4 * js + wc) * GRP + (unsigned)(rb * 256 + wr * 128) * 64u;
            hp.off = fr * 64 + (fq & 1) * 32 + (fq >> 1) * 16; hp.size = 8192;
#pragma unroll
            for (int m = 0; m < 8; ++m) {
                hp.v[m] = pack2(glu(m, 0, 2), glu(m, 1, 3));
                if (m == 5) { PIN(hp.v[5]); WAIT_V(0); arrive(flags, F_QKV + bid, ln, lds + G1_CNT, 7u); }
            }
        } else {
#pragma unroll
            for (int m = 0; m < 8; m += 2) {
                ST16(pack2(lnv(m, 3, 1.0f), lnv(m + 1, 3, 1.0f)), qoff + KIND + m * 512);
                ST16(pack2(lnv(m, 4, 1.0f), lnv(m + 1, 4, 1.0f)), qoff + 2 * KIND + m * 512);
                ST16(pack2(lnv(m, 1, QSCALE), lnv(m + 1, 1, QSCALE)), qoff + m * 512);
            }
            CFENCE();
            hp.base = ws + WS_ACT + (unsigned)(14 * xcd + 12 + (wc >> 1)) * GRP + (wc & 1) * (GRP / 2) + (unsigned)(rb * 256 + wr * 128) * 32u;
            hp.off = (fr + (fq & 1) * 16) * 32 + (fq >> 1) * 16; hp.size = 4096;
#pragma unroll
            for (int m = 0; m < 8; m += 2) { hp.v[m >> 1] = pack2(glu(m, 0, 2), glu(m + 1, 0, 2)); hp.v[4 + (m >> 1)] = hp.v[m >> 1]; }
            PIN(hp.v[3]); WAIT_V(0); arrive(flags, F_QKV + bid, ln, lds + G1_CNT, 7u);
        }
#pragma unroll
        for (int i = 0; i < 8; ++i) PIN(hp.v[i]);
        STAMP(3);
    }

    const int z = xcd & 1, rbk = ((xcd >> 1) & 1) * 8 + (idx & 7), cbk = (xcd >> 2) * 4 + (idx >> 3);
    G2 g2;
    g2.nh = z ? 56 - NH0 : NH0;
    g2.rowb = rbk * 128 * 64;
    g2.split = z ? split_mask(NH0, 56 - NH0) : split_mask(0, NH0);
    g2.Ah = (const char*)ws + WS_ACT + (size_t)(z * 2 * NH0) * GRP;
    g2.Ao = (const char*)ws + WS_ACT + (size_t)(112 + z * 16 - 2 * g2.nh) * GRP;
    g2.Wd = (const char*)(wd + (size_t)z * NH0 * 64 * DM + cbk * 128);
    g2.Wo = (const char*)(wo + (size_t)z * 512 * DM + cbk * 128) - (size_t)g2.nh * 64 * DM * 2;
    {
        const int hl = idx & 3, qb = idx >> 2, h = 4 * xcd + hl;
        const int l0_ = opaque_tid() & 63;
        const int qkv0 = F_QKV + xcd + 8 * (qb * 4 + ((hl >= 2) ? 3 : l0_ % 3));
        const int qkvword = F_QKV + xcd + 8 * ((l0_ & 7) * 4 + ((hl >= 2) ? 3 : 1 + ((l0_ >> 3) & 1)));
        const int odep = (rbk >> 1) * 32 + ((l0_ >> 2) & 3) * 8 + 4 * z + (l0_ & 3);
        const int depword = (l0_ & 16) ? (odep & ~7) + 4 : odep;
        const char* Qg = (const char*)ws + WS_QKV + (size_t)h * 131072 + qb * 256 * 32;
        const char* Kg = (const char*)ws + WS_QKV + (size_t)(NH + h) * 131072;
        const char* Vg = (const char*)ws + WS_QKV + (size_t)(2 * NH + h) * 131072;
        const char* wpd = g2.Wd - (cbk & 3) * 256;
        const char* wpo = g2.Wo - (cbk & 3) * 256;
        AttnOut R;
#define DG , dg_c, dg_r
        attn_fast(lds, lds0, Qg, Kg, Vg, g2, flags, qkv0, qkvword, depword, F_H + bid, qb, bid == 0, cp, hp, wpd, wpo, idx * 2 * (g2.nh + 8), wid, R DG);
        STAMP(6);
        const int tz = opaque_tid(), ln = tz & 63, w4 = wid & 3, fr = ln & 15, fq = ln >> 4;
        lds_ptr po0 = lds + 2 * SLOT + w4 * 4096 + ln * 16, po1 = lds + 2 * SLOT + 16384 + w4 * 2048 + ln * 16;
        PIN(po0); PIN(po1);
        auto po = [&](int i) { return i < 4 ? po0 + i * 1024 : po1 + (i >> 1 & 1) * 8192 + (i & 1) * 1024; };
        const lds_ptr pl = po0 + SLOT;
        volatile LAS unsigned* const badw = (volatile LAS unsigned*)(lds + 3 * SLOT + 1024);
        const int wm = w4 >> 1, wn = w4 & 1;
        const unsigned aO0 = (wm * 64 + fr) * 128 + ((fq ^ (fr >> 1)) * 16), aO1 = aO0 ^ 64;
        const int tq = (ln >> 2) & 3, tp = ln & 3, tg = (tq & 2) | ((fq & 1) << 2);
        unsigned wO[4];
#pragma unroll
        for (int b = 0; b < 4; ++b) wO[b] = 16384 + wn * 8192 + (8 * fq + tq) * 128 + (((2 * b + (tp >> 1)) ^ tg) * 16) + (tp & 1) * 8;
        f32x4 acc[4][4];
        bf16x8 xa[4], ya[4]; s16x4 xl[4], xh[4], yl[4], yh[4];
        lds_ptr pa0, pa1, pw0, pw1, pw2, pw3;
        int so = 4 * SLOT;
        auto handover = [&]() {
            if (wid < 4) {
#pragma unroll
                for (int i = 0; i < 4; ++i) {
                    *(LAS f32x4*)po(i) = (f32x4){R.o0[4 * i], R.o0[4 * i + 1], R.o0[4 * i + 2], R.o0[4 * i + 3]};
                    *(LAS f32x4*)po(4 + i) = (f32x4){R.o1[4 * i], R.o1[4 * i + 1], R.o1[4 * i + 2], R.o1[4 * i + 3]};
                }
                *(LAS f32x4*)pl = (f32x4){R.l0[0], R.m0, R.l1[0], R.m1};
                xl[0] = lds_tr(pw0); xh[0] = lds_tr(pw0 + 512);
#pragma unroll
                for (int m = 0; m < 4; ++m) xa[m] = lds_b128(pa0 + m * 2048);
                xl[1] = lds_tr(pw1); xh[1] = lds_tr(pw1 + 512); xl[2] = lds_tr(pw2); xh[2] = lds_tr(pw2 + 512); xl[3] = lds_tr(pw3); xh[3] = lds_tr(pw3 + 512);
#pragma unroll
                for (int m = 0; m < 4; ++m)
#pragma unroll
                    for (int b = 0; b < 4; ++b) { acc[m][b] = (f32x4){0.f, 0.f, 0.f, 0.f}; PIN(acc[m][b]); }
            }
        };
        G2Off go; g2_voff(ln, go);
        PIN(go.A0); PIN(go.A1); PIN(go.N0); PIN(go.N1); PIN(go.B0); PIN(go.B1);
        pa0 = lds + aO0 + so; pa1 = lds + aO1 + so; pw0 = lds + wO[0] + so; pw1 = lds + wO[1] + so; pw2 = lds + wO[2] + so; pw3 = lds + wO[3] + so;
        PIN(pa0); PIN(pa1); PIN(pw0); PIN(pw1); PIN(pw2); PIN(pw3);
        {
            handover();
            const f32x16 z16 = R.o0 * 0.0f + R.o1 * 0.0f;
            const f32x4 z4 = (z16.s0123 + z16.s4567) + (z16.s89ab + z16.scdef);
            const f32x2 z2 = z4.lo + z4.hi;
            const float zs = z2.x + z2.y;
            const bool ok = (zs == 0.f) && (R.l0[0] > 0.f) && (R.l0[0] < 0x1p96f) && (R.l1[0] > 0.f) && (R.l1[0] < 0x1p96f);
            const bool wave_bad = !__all(ok);
            if (ln == 0) badw[wid] = wave_bad ? 1u : 0u;
            STAMP(16);
            WAIT_L0(); BAR(); CFENCE();
            const unsigned anybad = badw[ln & 7];
            if (__builtin_expect(__any(anybad != 0u), 0)) {
                WAIT_L0(); BAR(); CFENCE();
                attn_safe(lds, lds0, Qg, Kg, Vg, wid, R);
                handover();
                WAIT_L0(); BAR(); CFENCE();
            }
        }
        STAMP(7);

        const int tile = rbk * 8 + cbk, nt = g2.nh + 8;
        if (__builtin_expect(wid >= 4, 0)) {
#define G2_LOAD0(j, sl) do { const unsigned d_ = lds0 + (sl) * SLOT; g2_load_b(g2, j, d_, 2 * w4, go.B0); g2_load_a(g2, j, d_, 2 * w4, go); } while (0)
#define G2_LOAD1(j, sl) do { const unsigned d_ = lds0 + (sl) * SLOT; g2_load_b(g2, j, d_, 2 * w4 + 1, go.B1); g2_load_a(g2, j, d_, 2 * w4 + 1, go); } while (0)
#define G2_LOAD(j, sl) do { G2_LOAD0(j, sl); G2_LOAD1(j, sl); } while (0)
            const f32x4 lm = *(const LAS f32x4*)pl;
            f32x4 t[8];
#pragma unroll
            for (int i = 0; i < 8; ++i) t[i] = *(const LAS f32x4*)po(i);
            WAIT_L0();
            G2_LOAD(3, 2);
            BAR();
            G2_LOAD(4, 3);
            float ck[2], co[2]; unsigned p[16];
            auto coef = [&](int q) {
                const float lk = q ? R.l1[0] : R.l0[0], mk = q ? R.m1 : R.m0, lo = lm[2 * q], mo = lm[2 * q + 1];
                const float mm = __builtin_fmaxf(mk, mo), fk = __builtin_amdgcn_exp2f(mk - mm), fo = __builtin_amdgcn_exp2f(mo - mm);
                const float rl = 1.0f / (lk * fk + lo * fo);
                ck[q] = fk * rl; co[q] = fo * rl; PIN(ck[q]); PIN(co[q]); SBAR();
            };
            auto half = [&](int q, int hh) {
                const f32x16 ok_ = q ? R.o1 : R.o0;
                unsigned* pp = p + 8 * q + 4 * hh;
#pragma unroll
                for (int i = 0; i < 2; ++i) {
                    const int g = 2 * hh + i; const f32x4 u = t[4 * q + g];
                    pp[2 * i] = cvtpk(ok_[4 * g] * ck[q] + u[0] * co[q], ok_[4 * g + 1] * ck[q] + u[1] * co[q]);
                    pp[2 * i + 1] = cvtpk(ok_[4 * g + 2] * ck[q] + u[2] * co[q], ok_[4 * g + 3] * ck[q] + u[3] * co[q]);
                }
                swap32(pp[0], pp[2]); swap32(pp[1], pp[3]);
                PIN(pp[0]); PIN(pp[1]); PIN(pp[2]); PIN(pp[3]); SBAR();
            };
            const unsigned ooff = WS_ACT + (unsigned)(112 + h) * GRP + (unsigned)(qb * 256 + w4 * 64 + fr) * 64u + (fq & 1) * 32 + (fq >> 1) * 16;
            auto ost = [&](int q) {
                unsigned* pp = p + 8 * q;
                swap16(pp[0], pp[4]); swap16(pp[1], pp[5]); swap16(pp[2], pp[6]); swap16(pp[3], pp[7]);
                ST16(((u32x4){pp[0], pp[1], pp[2], pp[3]}), ooff + q * 2048); ST16(((u32x4){pp[4], pp[5], pp[6], pp[7]}), ooff + q * 2048 + 1024); CFENCE();
            };
            SBAR(); coef(0); coef(1);
            WAIT_V(8);  BAR(); G2_LOAD(5, 4); G2_LOAD0(6, 0);
            WAIT_V(12); BAR(); G2_LOAD1(6, 0); G2_LOAD(7, 1);
            WAIT_V(16); BAR(); G2_LOAD0(8, 2);  SBAR(); half(0, 0); G2_LOAD1(8, 2);
            WAIT_V(16); BAR(); G2_LOAD0(9, 3);  SBAR(); half(0, 1); G2_LOAD1(9, 3);  ost(0);
            WAIT_V(18); BAR(); G2_LOAD0(10, 4); SBAR(); half(1, 0); G2_LOAD1(10, 4);
            WAIT_V(18); BAR(); G2_LOAD0(11, 0); SBAR(); half(1, 1); G2_LOAD1(11, 0); ost(1);
            WAIT_V(20); BAR(); G2_LOAD(12, 1);
            WAIT_V(18); BAR(); G2_LOAD(13, 2);
            WAIT_V(18);
            int sl = 3; cpd = 0;
#pragma unroll 1
            for (int j = 11; j < nt - 3; ++j) {
                BAR();
                if (j == 29) CODE_PF(cpd);
                if (j == 12 && wid == 4) signal(flags, F_O + bid, ln);
                G2_LOAD(j + 3, sl);
                sl = (sl == 4) ? 0 : sl + 1;
                WAIT_V(16);
            }
            BAR();
            if (tz == 256) *(LAS unsigned*)(lds + sl * SLOT) = 0u;
            WAIT_V(8); WAIT_L0(); BAR(); WAIT_V(0); BAR();
            asm volatile("" :: "v"(cpd));
            if (wid == 4) wait_all(flags, F_X + tile * 2 + z, peek(flags, F_X + tile * 2 + z));
            CFENCE(); BAR();
            asm volatile(".Lk_end:");
        } else {
#define G2_M(C, m, b) do { MFMA16_ACC(acc[m][b], cat8(C##l[b], C##h[b]), C##a[m]); SBAR(); } while (0)
#define G2_HALF(C, N, AB, WO, RD, TAIL) do { \
            G2_M(C, 0, 0); if (RD) { N##l[0] = lds_tr(pw0 + (WO)); N##h[0] = lds_tr(pw0 + (WO) + 512); } SBAR(); \
            G2_M(C, 1, 0); if (RD) N##a[0] = lds_b128(AB); SBAR(); \
            G2_M(C, 2, 0); if (RD) N##a[1] = lds_b128((AB) + 2048); SBAR(); \
            G2_M(C, 3, 0); if (RD) N##a[2] = lds_b128((AB) + 4096); SBAR(); \
            G2_M(C, 0, 1); if (RD) N##a[3] = lds_b128((AB) + 6144); SBAR(); \
            G2_M(C, 1, 1); if (RD) { N##l[1] = lds_tr(pw1 + (WO)); N##h[1] = lds_tr(pw1 + (WO) + 512); } SBAR(); \
            G2_M(C, 2, 1); if (RD) { N##l[2] = lds_tr(pw2 + (WO)); N##h[2] = lds_tr(pw2 + (WO) + 512); } SBAR(); \
            G2_M(C, 3, 1); if (RD) { N##l[3] = lds_tr(pw3 + (WO)); N##h[3] = lds_tr(pw3 + (WO) + 512); } SBAR(); \
            G2_M(C, 0, 2); TAIL(0); SBAR(); G2_M(C, 1, 2); TAIL(1); SBAR(); G2_M(C, 2, 2); TAIL(2); SBAR(); G2_M(C, 3, 2); TAIL(3); SBAR(); \
            G2_M(C, 0, 3); G2_M(C, 1, 3); G2_M(C, 2, 3); G2_M(C, 3, 3); } while (0)
            const u64* const optr = flags.p + (F_O + odep) * 16;
            u64 ov = 0;
            STAMP(8); STAMP(43);
#define G2_NEXT(i) do { if ((i) == 0) so = (so == 4 * SLOT) ? 0 : so + SLOT; \
                if ((i) == 1) { pa0 = lds + aO0 + so; pa1 = lds + aO1 + so; PIN(pa0); PIN(pa1); } \
                if ((i) == 2) { pw0 = lds + wO[0] + so; pw1 = lds + wO[1] + so; PIN(pw0); PIN(pw1); } \
                if ((i) == 3) { pw2 = lds + wO[2] + so; pw3 = lds + wO[3] + so; PIN(pw2); PIN(pw3); } } while (0)
#define G2_NONE(i) do {} while (0)
            WAIT_L0(); SBAR(); G2_HALF(x, y, pa1, 4096, true, G2_NEXT);
            WAIT_L0(); SBAR(); G2_HALF(y, x, pa0, 0, true, G2_NONE);
            BAR();
            WAIT_L0(); SBAR(); G2_HALF(x, y, pa1, 4096, true, G2_NEXT);
            WAIT_L0(); SBAR(); G2_HALF(y, x, pa0, 0, true, G2_NONE);
            if (NH0 & 1) {
                WAIT_L0(); SBAR(); G2_HALF(x, y, pa1, 4096, true, G2_NEXT);
                WAIT_L0(); BAR(); SBAR(); G2_HALF(y, x, pa0, 0, true, G2_NONE);
            }
#pragma unroll 1
            for (int ph = 0; ph < 3; ++ph) {
                const int n = (ph == 0) ? g2.nh / 2 - 4 : (ph == 1) ? 1 : 5;
#pragma unroll 1
                for (int i = 0; i < n; ++i) {
                    WAIT_L0(); SBAR(); G2_HALF(x, y, pa1, 4096, true, G2_NEXT);
                    WAIT_L0(); BAR(); SBAR(); G2_HALF(y, x, pa0, 0, true, G2_NONE);
                    WAIT_L0(); SBAR(); G2_HALF(x, y, pa1, 4096, true, G2_NEXT);
                    WAIT_L0(); BAR(); SBAR(); G2_HALF(y, x, pa0, 0, true, G2_NONE);

                }
                PEEK_AHEAD(ov, optr, wid | ph);
                if ((wid | (ph ^ 1)) == 0) { PEEK_DONE(ov); wait_all(flags, F_O + odep, ov); }
            }
            const int row = rbk * 128 + wm * 64 + z * 32 + fr, col = cbk * 128 + wn * 64 + (fq & 1) * 16 + (fq >> 1) * 8;
            u32x4 xr[4];
#pragma unroll
            for (int i = 0; i < 4; ++i) xr[i] = *(const u32x4*)(x + (size_t)(row + (i >> 1) * 16) * DM + col + (i & 1) * 32);
            WAIT_L0(); SBAR(); G2_HALF(x, y, pa1, 4096, true, G2_NEXT);
            WAIT_L0(); BAR(); SBAR(); G2_HALF(y, x, pa0, 0, true, G2_NONE);
            WAIT_L0(); SBAR(); G2_HALF(x, y, pa1, 4096, true, G2_NONE);
            WAIT_L0(); SBAR(); G2_HALF(y, x, pa0, 0, false, G2_NONE);
            MFMA_DRAIN(); SBAR();
            STAMP(9);
            f32x4 vk[8];
            const unsigned xo = WS_XCH + (unsigned)(tile * 2 + 1 - z) * 32768u + w4 * 8192 + ln * 16;
            if (z == 0) {
#pragma unroll
                for (int i = 0; i < 8; ++i) { ST16(__builtin_bit_cast(u32x4, acc[2 + (i >> 2)][i & 3]), xo + i * 1024); vk[i] = acc[i >> 2][i & 3]; }
            } else {
#pragma unroll
                for (int i = 0; i < 8; ++i) { ST16(__builtin_bit_cast(u32x4, acc[i >> 2][i & 3]), xo + i * 1024); vk[i] = acc[2 + (i >> 2)][i & 3]; }
            }
            WAIT_V(0);
            arrive(flags, F_X + tile * 2 + 1 - z, ln, lds + ((so == 4 * SLOT) ? 0 : so + SLOT), 3u);
            STAMP(10);
            CFENCE(); BAR(); CFENCE();
            STAMP(11);
            const unsigned xi = WS_XCH + (unsigned)(tile * 2 + z) * 32768u + w4 * 8192 + ln * 16;
#pragma unroll
            for (int i = 0; i < 8; ++i) vk[i] += __builtin_bit_cast(f32x4, __builtin_amdgcn_raw_buffer_load_b128(wsr, xi + i * 1024, 0, 16));
            const __amdgpu_buffer_rsrc_t outr = __builtin_amdgcn_make_buffer_rsrc((void*)out, 0, SEQ * DM * 2, 0x00020000);
            u32x4 w[4];
#pragma unroll
            for (int i = 0; i < 4; ++i) {
                const u32x4 a = __builtin_bit_cast(u32x4, vk[2 * i]), b = __builtin_bit_cast(u32x4, vk[2 * i + 1]);
                unsigned a0 = a[0], a1 = a[1], a2 = a[2], a3 = a[3], b0 = b[0], b1 = b[1], b2 = b[2], b3 = b[3];
                swap16(a0, b0); swap16(a1, b1); swap16(a2, b2); swap16(a3, b3);
                const u32x4 xx = xr[i];
                w[i][0] = cvtpk(u2f(a0) + bf_lo(xx[0]), u2f(a1) + bf_hi(xx[0]));
                w[i][1] = cvtpk(u2f(a2) + bf_lo(xx[1]), u2f(a3) + bf_hi(xx[1]));
                w[i][2] = cvtpk(u2f(b0) + bf_lo(xx[2]), u2f(b1) + bf_hi(xx[2]));
                w[i][3] = cvtpk(u2f(b2) + bf_lo(xx[3]), u2f(b3) + bf_hi(xx[3]));
            }
            const unsigned oo = (unsigned)((row - (fr & 8)) * DM + col + (fr & 8) * 4) * 2u;
#pragma unroll
            for (int m = 0; m < 2; ++m) {
                u32x4 lo, hi;
#pragma unroll
                for (int k = 0; k < 4; ++k) {
                    lo[k] = __builtin_amdgcn_update_dpp(w[2 * m][k], w[2 * m + 1][k], 0x128, 0xf, 0xc, false);
                    hi[k] = __builtin_amdgcn_update_dpp(w[2 * m + 1][k], w[2 * m][k], 0x128, 0xf, 0x3, false);
                }
                __builtin_amdgcn_raw_buffer_store_b128(lo, outr, oo + m * 16 * DM * 2, 0, 16);
                __builtin_amdgcn_raw_buffer_store_b128(hi, outr, oo + (m * 16 + 8) * DM * 2, 0, 16);
            }
        }
        WAIT_V(0);
        STAMP(12);
        if (wid == 0 && blockIdx.x == 0) { const unsigned lim = 12000u + (dg_r - dg_c); while ((unsigned)__builtin_amdgcn_s_memrealtime() - dg_c < lim) __builtin_amdgcn_s_sleep(2); }
    }
}

extern "C" void kernel_launch(void* const* d_in, const int* in_sizes, int n_in, void* d_out, int out_size, void* d_ws, size_t ws_size, hipStream_t stream) {
    const bf16_t* x = (const bf16_t*)d_in[0];
    k_main<<<256, 512, 0, stream>>>(x, (const bf16_t*)d_in[1], (const bf16_t*)d_in[2], (const bf16_t*)d_in[4], (const bf16_t*)d_in[5], (const bf16_t*)d_in[6],
                                    (bf16_t*)d_out, (unsigned char*)d_ws);
}
```

```cpp
#include <hip/hip_runtime.h>
#include <stdint.h>

typedef unsigned short bf16_t;
typedef short bf16x8 __attribute__((ext_vector_type(8)));
typedef short s16x4 __attribute__((ext_vector_type(4)));
typedef float f32x2 __attribute__((ext_vector_type(2)));
typedef float f32x4 __attribute__((ext_vector_type(4)));
typedef float f32x16 __attribute__((ext_vector_type(16)));
typedef unsigned u32x2 __attribute__((ext_vector_type(2)));
typedef unsigned u32x4 __attribute__((ext_vector_type(4)));
typedef __bf16 bf16x2_t __attribute__((ext_vector_type(2)));
#define LAS __attribute__((address_space(3)))
#define GAS __attribute__((address_space(1)))
typedef LAS unsigned char* lds_ptr;

constexpr int SEQ = 2048, DM = 1024, FF = 3584, NQKV = 3072, NH = 32;
constexpr float LN_EPS = 1e-5f;
constexpr float LOG2E = 1.4426950408889634f;
constexpr float QSCALE = 0.17677669529663687f * LOG2E;

constexpr unsigned WS_FLAGS = 0;
constexpr unsigned WS_STAT = 192 * 1024;
constexpr unsigned WS_QKV = 256 * 1024;
constexpr unsigned WS_ACT = WS_QKV + 3u * SEQ * DM * 2;
constexpr unsigned WS_XCH = WS_ACT + 144u * SEQ * 64;
constexpr unsigned WS_XC = WS_XCH + 256u * 32768;
constexpr unsigned WS_END = WS_XC + SEQ * DM * 2;
constexpr int F_EPOCH = 0, F_QKV = 1, F_H = 257, F_O = 513, F_X = 769, F_XC = 1025;
constexpr unsigned GRP = SEQ * 64;

#define SBAR() __builtin_amdgcn_sched_barrier(0)
#define BAR() __builtin_amdgcn_s_barrier()
#define WAIT_V(n) asm volatile("s_waitcnt vmcnt(" #n ")" ::: "memory")
#define WAIT_L0() do { __builtin_amdgcn_s_waitcnt(0xC07F); asm volatile("" ::: "memory"); } while (0)
#define CFENCE() asm volatile("" ::: "memory")
#define PIN(x) asm volatile("" : "+v"(x))
#define MFMA16(a, b, c) __builtin_amdgcn_mfma_f32_16x16x32_bf16(a, b, c, 0, 0, 0)
#define MFMA32(a, b, c) __builtin_amdgcn_mfma_f32_32x32x16_bf16(a, b, c, 0, 0, 0)
#define MFMA16_ACC(acc, a, b) asm volatile("v_mfma_f32_16x16x32_bf16 %0, %1, %2, %0" : "+v"(acc) : "v"(a), "v"(b))
#define MFMA32_ACC(acc, a, b) asm volatile("v_mfma_f32_32x32x16_bf16 %0, %1, %2, %0" : "+v"(acc) : "v"(a), "v"(b))
#define MFMA32_SET(d, a, b, c) asm volatile("v_mfma_f32_32x32x16_bf16 %0, %1, %2, %3" : "=&v"(d) : "v"(a), "v"(b), "v"(c))
#define MFMA_DRAIN() asm volatile("s_nop 15" ::: "memory")
#define RLX_AGENT __ATOMIC_RELAXED, __HIP_MEMORY_SCOPE_AGENT

__device__ __forceinline__ void glds16(const void* sbase, unsigned voff, unsigned ldsdst) {
    asm volatile("s_mov_b32 m0, %2\n\ts_nop 0\n\tglobal_load_lds_dwordx4 %0, %1" ::"v"(voff), "s"(sbase), "s"(ldsdst) : "memory");
}
__device__ __forceinline__ void glds16c(const void* sbase, unsigned voff, unsigned ldsdst) {
    asm volatile("s_mov_b32 m0, %2\n\ts_nop 0\n\tglobal_load_lds_dwordx4 %0, %1 sc1" ::"v"(voff), "s"(sbase), "s"(ldsdst) : "memory");
}
__device__ __forceinline__ bf16x8 lds_b128(lds_ptr p) { return *(const LAS bf16x8*)p; }
__device__ __forceinline__ s16x4 lds_tr(lds_ptr p) { return __builtin_amdgcn_ds_read_tr16_b64_v4i16((LAS s16x4*)p); }
__device__ __forceinline__ bf16x8 cat8(s16x4 a, s16x4 b) { return __builtin_shufflevector(a, b, 0, 1, 2, 3, 4, 5, 6, 7); }
__device__ __forceinline__ unsigned cvtpk(float lo, float hi) {
    f32x2 v = {lo, hi};
    return __builtin_bit_cast(unsigned, __builtin_convertvector(v, bf16x2_t));
}
__device__ __forceinline__ float u2f(unsigned u) { return __builtin_bit_cast(float, u); }
__device__ __forceinline__ unsigned f2u(float f) { return __builtin_bit_cast(unsigned, f); }
__device__ __forceinline__ float bf_lo(unsigned u) { return u2f(u << 16); }
__device__ __forceinline__ float bf_hi(unsigned u) { return u2f(u & 0xffff0000u); }
__device__ __forceinline__ void swap16(unsigned& a, unsigned& b) {
    auto r = __builtin_amdgcn_permlane16_swap(a, b, false, false);
    a = r[0]; b = r[1];
}
__device__ __forceinline__ void swap32(unsigned& a, unsigned& b) {
    auto r = __builtin_amdgcn_permlane32_swap(a, b, false, false);
    a = r[0]; b = r[1];
}
__device__ __forceinline__ int opaque_tid() { int t = threadIdx.x; asm volatile("" : "+v"(t)); return t; }
typedef unsigned long long u64;
struct Flags { u64* p; u64 tag; };
__device__ __forceinline__ void signal(const Flags& f, int word, int lane) {
    if (lane == 0) __hip_atomic_store((GAS u64*)(f.p + word * 16), f.tag, RLX_AGENT);
}
__device__ __forceinline__ u64 peek(const Flags& f, int word) {
    u64 v;
    asm volatile("global_load_dwordx2 %0, %1, off sc1\n\ts_waitcnt vmcnt(0)" : "=&v"(v) : "v"(f.p + word * 16) : "memory");
    return v;
}
__device__ __forceinline__ void wait_all(const Flags& f, int word, u64 v) {
#pragma unroll 1
    for (unsigned spin = 0; spin < (1u << 20) && !__all(v == f.tag); ++spin) { __builtin_amdgcn_s_sleep(1); v = peek(f, word); }
}
__device__ __forceinline__ void arrive(const Flags& f, int word, int lane, lds_ptr cnt, unsigned last) {
    if (lane == 0 && __hip_atomic_fetch_add((LAS unsigned*)cnt, 1u, __ATOMIC_RELAXED, __HIP_MEMORY_SCOPE_WORKGROUP) == last)
        __hip_atomic_store((GAS u64*)(f.p + word * 16), f.tag, RLX_AGENT);
}
#define PEEK_AHEAD(v, ptr, sel) asm volatile("s_cmp_lg_u32 %2, 0\n\ts_cbranch_scc1 1f\n\tglobal_load_dwordx2 %0, %1, off sc1\n1:" : "+v"(v) : "v"(ptr), "s"(sel) : "memory", "scc")
struct CodePf { const char* base; unsigned len; int first; };
#define CODE_PF(d) do { unsigned o_ = (unsigned)(cp.first + (opaque_tid() & 15)) * 128u; o_ = (o_ < cp.len) ? o_ : 0u; \
    asm volatile("global_load_dword %0, %1, %2" : "+v"(d) : "v"(o_), "s"(cp.base) : "memory"); } while (0)
#define PEEK_DONE(v) asm volatile("s_waitcnt vmcnt(0)" : "+v"(v) :: "memory")

template <int CTRL> __device__ __forceinline__ float dpp_add(float v) { return v + u2f(__builtin_amdgcn_update_dpp(0u, f2u(v), CTRL, 0xf, 0xf, true)); }
__device__ __forceinline__ float red4(float v) {
    unsigned a = f2u(v), b = a;
    swap16(a, b);
    v = u2f(a) + u2f(b);
    a = f2u(v); b = a;
    swap32(a, b);
    return u2f(a) + u2f(b);
}
__device__ __forceinline__ float wave_sum(float v) {
    v = dpp_add<0xB1>(v); v = dpp_add<0x4E>(v); v = dpp_add<0x141>(v); v = dpp_add<0x140>(v);
    return red4(v);
}
__device__ __forceinline__ f32x2 row_stats(u32x4& a, u32x4& b) {
    float v[16];
#pragma unroll
    for (int i = 0; i < 4; ++i) { v[2 * i] = bf_lo(a[i]); v[2 * i + 1] = bf_hi(a[i]); v[8 + 2 * i] = bf_lo(b[i]); v[8 + 2 * i + 1] = bf_hi(b[i]); }
    float s = 0.f;
#pragma unroll
    for (int i = 0; i < 16; ++i) s += v[i];
    const float mu = wave_sum(s) * (1.0f / DM);
    float q = 0.f;
#pragma unroll
    for (int i = 0; i < 16; ++i) { v[i] -= mu; q += v[i] * v[i]; }
#pragma unroll
    for (int i = 0; i < 4; ++i) { a[i] = cvtpk(v[2 * i], v[2 * i + 1]); b[i] = cvtpk(v[8 + 2 * i], v[8 + 2 * i + 1]); }
    return (f32x2){mu, 1.0f / __builtin_sqrtf(wave_sum(q) * (1.0f / DM) + LN_EPS)};
}

constexpr int SLOT = 32768, LDS_BYTES = 5 * SLOT;
constexpr int G1_A = 0, G1_W = 65536, G1_STAT = G1_W + 5 * 16384, G1_CS = G1_STAT + 2048, G1_CNT = G1_CS + 4 * 6 * 64;

#define NH0 30
static_assert(NH0 >= 28 && NH0 <= 32, "");
struct G2 { const char* Ah; const char* Ao; const char* Wd; const char* Wo; int nh; unsigned rowb; u64 split; };
__device__ __forceinline__ void g2_load_b(const G2& g, int j, unsigned dst, int vw, unsigned voffB) {
    const char* b = (j >= g.nh ? g.Wo : g.Wd) + (size_t)(j * 64 + vw * 8) * (DM * 2);
    glds16(b, voffB, dst + 16384 + vw * 1024); glds16(b + 128, voffB, dst + 16384 + 8192 + vw * 1024);
}
struct G2Off { unsigned A0, A1, N0, N1, B0, B1; };
__device__ __forceinline__ void g2_load_a(const G2& g, int j, unsigned dst, int vw, const G2Off& o) {
    const unsigned sp = (unsigned)(g.split >> j) & 1u;
    const char* a = (j >= g.nh ? g.Ao : g.Ah) + (size_t)j * (2 * GRP) + ((g.rowb + vw * 1024) >> sp);
    glds16c(a, sp ? o.N0 : o.A0, dst + vw * 2048); glds16c(a, sp ? o.N1 : o.A1, dst + vw * 2048 + 1024);
}
__device__ __forceinline__ void g2_voff(int lane, G2Off& o) {
    const int r8 = lane >> 3, u8 = lane & 7, ua = u8 ^ (r8 >> 1), ub = ua ^ 4;
    o.A0 = (unsigned)(ua >> 2) * GRP + r8 * 64 + (ua & 3) * 16;
    o.A1 = (unsigned)(ub >> 2) * GRP + (r8 + 8) * 64 + (ub & 3) * 16;
    o.N0 = (unsigned)(ua >> 1) * (GRP / 2) + r8 * 32 + (ua & 1) * 16;
    o.N1 = (unsigned)(ub >> 1) * (GRP / 2) + (r8 + 8) * 32 + (ub & 1) * 16;
    o.B0 = (unsigned)(r8 * (DM * 2) + (u8 ^ (r8 & 2)) * 16);
    o.B1 = (unsigned)(r8 * (DM * 2) + (u8 ^ (4 | (r8 & 2))) * 16);
}
constexpr u64 split_mask(int first, int n) { u64 m = 0; for (int j = 0; j < n; ++j) if ((first + j) % 7 == 6) m |= 1ull << j; return m; }
static_assert(((split_mask(0, NH0) | split_mask(NH0, 56 - NH0)) & 7) == 0, "phase B's tail prefetches K-tiles 0..2 as plain groups");

struct AttnOut { f32x16 o0, o1; f32x4 l0, l1; float m0, m1; bf16x8 q00, q01, q10, q11; };
struct HPend { u32x4 v[8]; unsigned off, size; const void* base; };
struct AttnLane {
    unsigned voffK, voffV; lds_ptr kb0, kb1, vb; bf16x8 ones;
    __device__ __forceinline__ void init(lds_ptr lds, int wid, int lane) {
        const int r32 = lane & 31, hi = lane >> 5, kh = wid >> 2, ksw = (r32 >> 2) & 3;
        const int uk = (lane & 3) ^ ((lane >> 4) & 3), uv = lane & 3;
        voffK = (unsigned)((uk >> 1) * 65536 + wid * 1024 + (lane >> 2) * 32 + (uk & 1) * 16);
        voffV = (unsigned)((uv >> 1) * 65536 + wid * 1024 + (lane >> 2) * 32 + (uv & 1) * 16);
        kb0 = lds + kh * 8192 + r32 * 64 + ((hi ^ ksw) * 16); kb1 = lds + kh * 8192 + r32 * 64 + (((2 | hi) ^ ksw) * 16);
        vb = lds + 16384 + kh * 8192 + (4 * hi + ((lane >> 2) & 3)) * 64 + ((lane >> 4) & 1) * 32 + (lane & 3) * 8;
        const short v = (((lane >> 4) & 1) == ((lane >> 2) & 1)) ? (short)0x3f80 : (short)0;
        ones = (bf16x8){v, v, v, v, v, v, v, v};
    }
};
#define EX(v) __builtin_amdgcn_exp2f(v)
#define AT_LOAD_K(c, slot) do { const unsigned d_ = lds0 + wid * 2048 + (slot) * SLOT; const char* k_ = Kg + (c) * 8192; glds16c(k_, A.voffK, d_); glds16c(k_ + 512, A.voffK, d_ + 1024); } while (0)
#define AT_LOAD_V(c, slot) do { const unsigned d_ = lds0 + wid * 2048 + (slot) * SLOT + 16384; const char* v_ = Vg + (c) * 8192; glds16c(v_, A.voffV, d_); glds16c(v_ + 512, A.voffV, d_ + 1024); } while (0)
#define AT_PACK(S, i) __builtin_bit_cast(bf16x8, (u32x4){cvtpk(S[i], S[i + 1]), cvtpk(S[i + 2], S[i + 3]), cvtpk(S[i + 4], S[i + 5]), cvtpk(S[i + 6], S[i + 7])})
__device__ __forceinline__ float rowmax16(const f32x16& s) {
    float a = s[0];
#pragma unroll
    for (int r = 1; r < 16; ++r) a = __builtin_fmaxf(a, s[r]);
    unsigned x = f2u(a), y = x; swap32(x, y);
    return __builtin_fmaxf(u2f(x), u2f(y));
}

__device__ __forceinline__ void attn_safe(lds_ptr lds, unsigned lds0, const char* Kg, const char* Vg, int wid, AttnOut& R) {
    AttnLane A; A.init(lds, wid, opaque_tid() & 63);
    const bf16x8 q00 = R.q00, q01 = R.q01, q10 = R.q10, q11 = R.q11;
    f32x16 o0 = (f32x16){}, o1 = (f32x16){};
    f32x4 l0 = (f32x4){0.f, 0.f, 0.f, 0.f}, l1 = l0;
    float m0 = -__builtin_inff(), m1 = m0;
#pragma unroll 1
    for (int c = 0; c < 8; ++c) {
        AT_LOAD_K(c, 3); AT_LOAD_V(c, 3);
        WAIT_V(0); BAR(); CFENCE();
#pragma unroll 1
        for (int t = 0; t < 4; ++t) {
            const int to = 3 * SLOT + t * 2048;
            const bf16x8 ka = lds_b128(A.kb0 + to), kb = lds_b128(A.kb1 + to);
            const bf16x8 va = cat8(lds_tr(A.vb + to), lds_tr(A.vb + to + 512)), vc = cat8(lds_tr(A.vb + to + 1024), lds_tr(A.vb + to + 1536));
            f32x16 s0 = MFMA32(kb, q01, MFMA32(ka, q00, (f32x16){})), s1 = MFMA32(kb, q11, MFMA32(ka, q10, (f32x16){}));
            const float n0 = __builtin_fmaxf(m0, rowmax16(s0)), n1 = __builtin_fmaxf(m1, rowmax16(s1));
            const float f0 = EX(m0 - n0), f1 = EX(m1 - n1);
            m0 = n0; m1 = n1; o0 *= f0; o1 *= f1; l0 *= f0; l1 *= f1;
#pragma unroll
            for (int r = 0; r < 16; ++r) { s0[r] = EX(s0[r] - n0); s1[r] = EX(s1[r] - n1); }
            const bf16x8 p00 = AT_PACK(s0, 0), p01 = AT_PACK(s0, 8), p10 = AT_PACK(s1, 0), p11 = AT_PACK(s1, 8);
            o0 = MFMA32(vc, p01, MFMA32(va, p00, o0)); l0 = MFMA16(A.ones, p01, MFMA16(A.ones, p00, l0));
            o1 = MFMA32(vc, p11, MFMA32(va, p10, o1)); l1 = MFMA16(A.ones, p11, MFMA16(A.ones, p10, l1));
        }
        WAIT_L0(); BAR(); CFENCE();
    }
    R.o0 = o0; R.o1 = o1; R.l0 = l0; R.l1 = l1; R.m0 = m0; R.m1 = m1;
}

__device__ __forceinline__ void attn_fast(lds_ptr lds, unsigned lds0, const char* Kg, const char* Vg, const G2& g2, const Flags& flags, int qkv1, int qkvword, int depword,
                                          int hsig, int c0, bool bump, const CodePf& cp, const HPend& hp, const char* wpd, const char* wpo, int prow, int wid, AttnOut& R) {
    const int lane = opaque_tid() & 63;
    AttnLane A; A.init(lds, wid, lane);
    lds_ptr kb0 = A.kb0, kb1 = A.kb1, vb = A.vb;
    PIN(kb0); PIN(kb1); PIN(vb);
    lds_ptr kh0 = kb0 + 2 * SLOT, kh1 = kb1 + 2 * SLOT, vh = vb + 2 * SLOT;
    PIN(kh0); PIN(kh1); PIN(vh);
    const bf16x8 ones = A.ones;
    const __amdgpu_buffer_rsrc_t hr = __builtin_amdgcn_make_buffer_rsrc((void*)hp.base, 0, hp.size, 0x00020000);
    f32x16 o0 = (f32x16){}, o1 = (f32x16){}, negm0 = (f32x16){}, negm1 = (f32x16){}, sA0, sA1, sB0, sB1;
    f32x4 l0 = (f32x4){0.f, 0.f, 0.f, 0.f}, l1 = l0;
    bf16x8 kfa, kfb, va, vc = (bf16x8){}, pf = (bf16x8){};
    PIN(o0); PIN(o1); PIN(l0); PIN(l1); PIN(vc); PIN(pf); PIN(A.voffK); PIN(A.voffV);
    G2Off go; g2_voff(lane, go);
    unsigned gA0 = go.A0, gA1 = go.A1, gB = (wid & 1) ? go.B1 : go.B0;
    unsigned pfo = (unsigned)(((lane >> 3) & 1) * (DM * 2) + (lane & 7) * 128);
    const u64* hptr = flags.p + (F_H + depword) * 16;
    const u64* qptr = flags.p + qkvword * 16;
    PIN(gA0); PIN(gA1); PIN(gB); PIN(pfo); PIN(hptr); PIN(qptr);
    u64 hv = 0;
    float m0, m1;
    PEEK_AHEAD(hv, flags.p + qkv1 * 16, 0);
    WAIT_L0(); BAR(); SBAR();

    const int qo = 2 * SLOT + (wid & 3) * 4096 - (wid >> 2) * 8192;
    const bf16x8 q00 = lds_b128(kb0 + qo), q10 = lds_b128(kb0 + qo + 2048), q01 = lds_b128(kb1 + qo), q11 = lds_b128(kb1 + qo + 2048);
    {
        kfa = lds_b128(kb0); kfb = lds_b128(kb1);
        sA0 = MFMA32(kfa, q00, negm0); sA1 = MFMA32(kfa, q10, negm1); sA0 = MFMA32(kfb, q01, sA0); sA1 = MFMA32(kfb, q11, sA1);
        kfa = lds_b128(kb0 + 2048); kfb = lds_b128(kb1 + 2048);
        m0 = rowmax16(sA0); m1 = rowmax16(sA1);
        f32x2 n0 = {-m0, -m0}, n1 = {-m1, -m1};
        PIN(n0); PIN(n1);
        negm0 = __builtin_shufflevector(n0, n0, 0, 1, 0, 1, 0, 1, 0, 1, 0, 1, 0, 1, 0, 1, 0, 1); negm1 = __builtin_shufflevector(n1, n1, 0, 1, 0, 1, 0, 1, 0, 1, 0, 1, 0, 1, 0, 1, 0, 1);
        PIN(negm0); PIN(negm1);
        sA0 += negm0; sA1 += negm1;
        PIN(sA0); PIN(sA1);
    }
    unsigned pfd = 0;
    PEEK_DONE(hv);
    BAR(); SBAR();

    wait_all(flags, qkv1, hv);
    AT_LOAD_K((c0 + 1) & 7, 1); AT_LOAD_V((c0 + 1) & 7, 1);

#define AT_GROUP(QK, RD, S, i, O, L, VF) do { \
        QK; SBAR(); RD; SBAR(); \
        float e0, e1, e2, e3, e4, e5, e6, e7; \
        asm volatile("v_exp_f32 %0, %13\n\tv_exp_f32 %1, %14\n\tv_exp_f32 %2, %15\n\tv_mfma_f32_32x32x16_bf16 %8, %10, %11, %8\n\t" \
                     "v_exp_f32 %3, %16\n\tv_exp_f32 %4, %17\n\tv_exp_f32 %5, %18\n\tv_mfma_f32_16x16x32_bf16 %9, %12, %11, %9\n\tv_exp_f32 %6, %19\n\tv_exp_f32 %7, %20" \
            : "=&v"(e0), "=&v"(e1), "=&v"(e2), "=&v"(e3), "=&v"(e4), "=&v"(e5), "=&v"(e6), "=&v"(e7), "+v"(O), "+v"(L) \
            : "v"(VF), "v"(pf), "v"(ones), "v"(S[i]), "v"(S[i + 1]), "v"(S[i + 2]), "v"(S[i + 3]), "v"(S[i + 4]), "v"(S[i + 5]), "v"(S[i + 6]), "v"(S[i + 7])); \
        pf = __builtin_bit_cast(bf16x8, (u32x4){cvtpk(e0, e1), cvtpk(e2, e3), cvtpk(e4, e5), cvtpk(e6, e7)}); SBAR(); } while (0)
#define LO(lo, hi, off) (((off) >= 2 * SLOT ? hi : lo) + ((off) & (2 * SLOT - 1)))
#define AT_STEP(C0, C1, N0, N1, QK, KR, KO, VO, H0, H1, H2, H3) do { \
        AT_GROUP(if (QK) MFMA32_SET(N0, kfa, q00, negm0), { va = cat8(lds_tr(LO(vb, vh, VO)), lds_tr(LO(vb, vh, VO) + 512)); H0; }, C0, 0, o1, l1, vc); \
        AT_GROUP(if (QK) MFMA32_SET(N1, kfa, q10, negm1), { if (KR) kfa = lds_b128(LO(kb0, kh0, KO)); vc = cat8(lds_tr(LO(vb, vh, VO) + 1024), lds_tr(LO(vb, vh, VO) + 1536)); H1; }, C1, 0, o0, l0, va); \
        AT_GROUP(if (QK) MFMA32_ACC(N0, kfb, q01), H2, C0, 8, o1, l1, va); \
        AT_GROUP(if (QK) MFMA32_ACC(N1, kfb, q11), { if (KR) kfb = lds_b128(LO(kb1, kh1, KO)); H3; }, C1, 8, o0, l0, vc); } while (0)
#define YP(p) asm volatile("s_cmp_lt_u32 %0, 4\n\ts_cbranch_scc1 1f\n\ts_setprio " #p "\n1:" :: "s"(wid) : "scc")
    __builtin_amdgcn_s_setprio(1);
    auto ylow = [&]() { YP(0); };
#define S0(c, ...) AT_STEP(sA0, sA1, sB0, sB1, true, true, ((c) & 3) * SLOT + 4096, ((c) & 3) * SLOT, __VA_ARGS__)
#define S1(c, ...) YP(0); AT_STEP(sB0, sB1, sA0, sA1, true, true, ((c) & 3) * SLOT + 6144, ((c) & 3) * SLOT + 2048, __VA_ARGS__)
#define S2(c, ...) YP(2); AT_STEP(sA0, sA1, sB0, sB1, true, (c) < 7, (((c) + 1) & 3) * SLOT, ((c) & 3) * SLOT + 4096, __VA_ARGS__)
#define S3(c, ...) AT_STEP(sB0, sB1, sA0, sA1, (c) < 7, (c) < 7, (((c) + 1) & 3) * SLOT + 2048, ((c) & 3) * SLOT + 6144, __VA_ARGS__)
#define SY(c, n) do { WAIT_V(n); WAIT_L0(); BAR(); SBAR(); } while (0)
#define NO (void)0
#define KP(c, i) glds16c(Kg + ((c0 + (c)) & 7) * 8192 + (i) * 512, A.voffK, lds0 + wid * 2048 + ((c) & 3) * SLOT + (i) * 1024)
#define VP(c, i) glds16c(Vg + ((c0 + (c)) & 7) * 8192 + (i) * 512, A.voffV, lds0 + wid * 2048 + ((c) & 3) * SLOT + 16384 + (i) * 1024)
#define CH(c) KP(c, 0), KP(c, 1), VP(c, 0), VP(c, 1)
#define HS(i) do { __builtin_amdgcn_raw_buffer_store_b128(hp.v[i], hr, hp.off + (i) * 1024, 0, 16); CFENCE(); } while (0)
#define WP(i) wpf(i)
#define GB(j, slot, i) glds16(g2.Wd + (size_t)((j) * 64 + wid * 8) * (DM * 2) + (i) * 128, gB, lds0 + (slot) * SLOT + 16384 + (i) * 8192 + wid * 1024)
#define GA(j, slot, i) glds16c(g2.Ah + (size_t)(j) * (2 * GRP) + g2.rowb + wid * 1024, (i) ? gA1 : gA0, lds0 + (slot) * SLOT + wid * 2048 + (i) * 1024)
    auto wpf = [&](int i) {
        const int pi = (wid + 8 * i < g2.nh + 8) ? wid + 8 * i : 0, lr = prow + 2 * pi;
        asm volatile("global_load_dword %0, %1, %2" : "+v"(pfd) : "v"(pfo), "s"((lr < g2.nh * 64 ? wpd : wpo) + (size_t)lr * (DM * 2)) : "memory");
    };
    auto cpf = [&]() { CODE_PF(pfd); };
    auto qpeek = [&]() { PEEK_AHEAD(hv, qptr, wid); };
    auto hpeek = [&]() { PEEK_AHEAD(hv, hptr, wid); };
    S0(0, NO, NO, NO, NO);         S1(0, qpeek(), NO, NO, NO);
    PEEK_DONE(hv);
    if (wid == 0) {
        wait_all(flags, qkvword, hv);
        if (bump) signal(flags, F_EPOCH, lane);
    }
                                                             SY(0, 1);  S2(0, CH(2)); S3(0, CH(3));
    S0(1, WP(0), NO, HS(0), NO);   S1(1, NO, NO, HS(1), NO); SY(1, 7);  S2(1, CH(4)); S3(1, WP(1), NO, HS(2), NO);
    S0(2, NO, NO, HS(3), NO);      S1(2, NO, NO, HS(4), NO); SY(2, 11); S2(2, CH(5)); S3(2, WP(2), NO, HS(5), NO);
    S0(3, NO, NO, HS(6), NO);      S1(3, NO, NO, HS(7), NO); SY(3, 12); S2(3, CH(6)); S3(3, WP(3), NO, NO, NO);
    S0(4, NO, NO, NO, NO);         S1(4, NO, NO, NO, NO);    SY(4, 0);
    if (wid == 0) signal(flags, hsig, lane);
                                                                        S2(4, CH(7)); S3(4, WP(4), NO, NO, NO);
    S0(5, GB(0, 4, 0), GB(0, 4, 1), NO, NO); S1(5, NO, NO, NO, NO); SY(5, 2); S2(5, GB(1, 0, 0), GB(1, 0, 1), NO, NO); S3(5, NO, NO, NO, NO);
    S0(6, hpeek(), NO, NO, NO);
    if (wid == 0) { PEEK_DONE(hv); wait_all(flags, F_H + depword, hv); }
                                   S1(6, NO, NO, NO, NO);    SY(6, 0);  S2(6, GA(0, 4, 0), GA(0, 4, 1), GB(2, 1, 0), GB(2, 1, 1)); S3(6, GA(1, 0, 0), GA(1, 0, 1), cpf(), NO);
    S0(7, GA(2, 1, 0), GA(2, 1, 1), NO, NO); S1(7, NO, NO, NO, NO); SY(7, 0); S2(7, NO, NO, NO, NO); S3(7, NO, NO, ylow(), NO);
    __builtin_amdgcn_s_setprio(0);
#undef S0
#undef S1
#undef S2
#undef S3
#undef NO

    asm volatile("s_nop 1" ::: "memory");
    MFMA32_ACC(o1, vc, pf); MFMA16_ACC(l1, ones, pf);
    MFMA_DRAIN(); SBAR();
    asm volatile("" :: "v"(pfd));
    R.o0 = o0; R.o1 = o1; R.l0 = l0; R.l1 = l1; R.m0 = m0; R.m1 = m1; R.q00 = q00; R.q01 = q01; R.q10 = q10; R.q11 = q11;
}

__global__ __launch_bounds__(512, 2) void k_main(const bf16_t* __restrict__ x, const bf16_t* __restrict__ wqkv, const bf16_t* __restrict__ wo, const bf16_t* __restrict__ wg,
                                                 const bf16_t* __restrict__ wu, const bf16_t* __restrict__ wd, bf16_t* __restrict__ out, unsigned char* ws) {
    __shared__ __attribute__((aligned(16))) unsigned char smem[LDS_BYTES];
    const lds_ptr lds = (lds_ptr)smem;
    const unsigned lds0 = (unsigned)(uintptr_t)lds;
    const int wid = __builtin_amdgcn_readfirstlane(threadIdx.x >> 6);
    const int bid = blockIdx.x, xcd = bid & 7, idx = bid >> 3;
    Flags flags; flags.p = (u64*)(ws + WS_FLAGS);
    const __amdgpu_buffer_rsrc_t wsr = __builtin_amdgcn_make_buffer_rsrc((void*)ws, 0, WS_END, 0x00020000);
#define ST16(v, off) __builtin_amdgcn_raw_buffer_store_b128(v, wsr, off, 0, 16)

    unsigned cpd = 0;
    CodePf cp;
    asm volatile("s_getpc_b64 %0\n.Lk_begin:\n\ts_mov_b32 %1, .Lk_end-.Lk_begin" : "=s"(cp.base), "=s"(cp.len));
    cp.first = idx * 16;
    HPend hp;
    {
        const int lane = threadIdx.x & 63, wr = wid >> 2, wc = wid & 3, js = idx & 3, rb = idx >> 2;
        const bool typeY = (js == 3);
        const int f0 = 448 * xcd + 128 * js;
        const char* P0 = (const char*)(wg + f0);
        const char* P2 = (const char*)(wu + f0);
        const char* P1 = typeY ? (const char*)(wqkv + 2048 + 128 * xcd) : (const char*)(wg + f0 + 64);
        const char* P3 = typeY ? (const char*)(wqkv + 2048 + 128 * xcd + 64) : (const char*)(wu + f0 + 64);
        const char* P4 = (const char*)(wqkv + 128 * xcd + 32 * js);
        const unsigned stepF = 32u * FF * 2u, stepQ = 32u * NQKV * 2u, step13 = typeY ? stepQ : stepF;
        const char* XA = (const char*)x + (size_t)rb * 256 * DM * 2;
        const char* XC = (const char*)ws + WS_XC + (size_t)rb * 256 * DM * 2;
        const int r8 = lane >> 3, u8 = lane & 7;
        const int gw = (r8 & 2) | ((wc & 1) << 2);
        const unsigned voffF = (unsigned)(((wc * 8 + r8) * FF + (u8 ^ gw) * 8) * 2);
        const unsigned voffQ = (unsigned)(((wc * 8 + r8) * NQKV + ((u8 ^ gw) >> 2) * 1024 + ((u8 ^ gw) & 3) * 8) * 2);
        const unsigned voff13 = typeY ? (unsigned)(((wc * 8 + r8) * NQKV + (u8 ^ gw) * 8) * 2) : voffF;
        const unsigned voffA0 = (unsigned)(((32 * wid + r8) * DM + (u8 ^ (r8 >> 1)) * 8) * 2);
        const unsigned voffA1 = (unsigned)(((32 * wid + 8 + r8) * DM + (u8 ^ (4 | (r8 >> 1))) * 8) * 2);
        const unsigned dstA = lds0 + G1_A + wid * 4096, dstW = lds0 + G1_W + wc * 1024;
#define G1_LOAD_A(t) do { const char* b_ = XA + (t) * 128; const unsigned d_ = dstA + ((t) & 1) * 32768; \
        glds16(b_, voffA0, d_); glds16(b_, voffA1, d_ + 1024); glds16(b_ + 32768, voffA0, d_ + 2048); glds16(b_ + 32768, voffA1, d_ + 3072); } while (0)
#define G1_LOAD_W(hs) do { const unsigned d_ = dstW + ((hs) & 3) * 4096; \
        glds16(P0 + (size_t)(hs) * stepF, voffF, d_); glds16(P1 + (size_t)(hs) * step13, voff13, d_ + 16384); glds16(P2 + (size_t)(hs) * stepF, voffF, d_ + 32768); \
        glds16(P3 + (size_t)(hs) * step13, voff13, d_ + 49152); glds16(P4 + (size_t)(hs) * stepQ, voffQ, d_ + 65536); } while (0)

        if (wr == 1) G1_LOAD_W(0);
        G1_LOAD_A(0);
        u32x4 xa0, xa1;
        const int srow = rb * 256 + (js * 8 + xcd) * 8 + wid;
        asm volatile("global_load_dwordx4 %0, %2, %3\n\tglobal_load_dwordx4 %1, %2, %3 offset:1024"
                     : "=&v"(xa0), "=&v"(xa1) : "v"((unsigned)(lane * 16)), "s"((const char*)x + (size_t)srow * DM * 2) : "memory");
        CODE_PF(cpd);

        int fr = lane & 15, fq = lane >> 4;
        const lds_ptr aB0 = lds + G1_A + (wr * 128 + fr) * 128 + ((fq ^ (fr >> 1)) * 16);
        const lds_ptr aB1 = lds + G1_A + (wr * 128 + fr) * 128 + (((4 | fq) ^ (fr >> 1)) * 16);
        const int tq = (lane >> 2) & 3, tp = lane & 3, tg = (tq & 2) | ((fq & 1) << 2);
        auto wbase = [&](int panel, int bp) { return lds + G1_W + panel * 16384 + (8 * fq + tq) * 128 + (((2 * bp + (tp >> 1)) ^ tg) * 16) + (tp & 1) * 8; };
        const int pX = wc >> 1, bX = 2 * (wc & 1);
        const lds_ptr wB0 = typeY ? wbase(0, wc) : wbase(pX, bX);
        const lds_ptr wB1 = typeY ? wbase(1, wc) : wbase(pX, bX + 1);
        const lds_ptr wB2 = typeY ? wbase(2, wc) : wbase(2 + pX, bX);
        const lds_ptr wB3 = typeY ? wbase(3, wc) : wbase(2 + pX, bX + 1);
        const lds_ptr wB4 = wbase(4, wc);
        if (threadIdx.x == 0) *(LAS unsigned*)(lds + G1_CNT) = 0u;
        f32x4 acc[8][5];
#pragma unroll
        for (int m = 0; m < 8; ++m)
#pragma unroll
            for (int b = 0; b < 5; ++b) { acc[m][b] = (f32x4){0.f, 0.f, 0.f, 0.f}; PIN(acc[m][b]); }
        if (wr == 1) { WAIT_V(7); BAR(); }
        asm volatile("s_waitcnt vmcnt(1)" : "+v"(xa0), "+v"(xa1) :: "memory");

        u64 sg;
        {
            const f32x2 st = row_stats(xa0, xa1);
            G1_LOAD_W(1 + wr);
            ST16(xa0, (lane >= 32) ? WS_XC + (unsigned)srow * 2048u + lane * 16 : WS_END);
            ST16(xa1, WS_XC + (unsigned)srow * 2048u + 1024 + lane * 16);
            __builtin_amdgcn_raw_buffer_store_b64(__builtin_bit_cast(u32x2, st), wsr, (lane == 0) ? WS_STAT + (unsigned)srow * 8u : WS_END, 0, 16);
            asm volatile("global_load_dwordx2 %0, %1, %2 sc1" : "=&v"(sg) : "v"(0u), "s"(flags.p) : "memory");
        }

        bf16x8 af[8], wf[5];
        f32x4 cs0 = (f32x4){0.f, 0.f, 0.f, 0.f}, cs1 = cs0, cs2 = cs0;
        bf16x8 one8 = (bf16x8){0x3f80, 0x3f80, 0x3f80, 0x3f80, 0x3f80, 0x3f80, 0x3f80, 0x3f80};
        PIN(cs0); PIN(cs1); PIN(cs2); PIN(one8);
#define CS1(c, w) "v_mfma_f32_16x16x32_bf16 %" #c ", %" #w ", %8, %" #c "\n\t"
#define G1_CSUM_(P0, P1) asm volatile("s_cmp_gt_u32 %10, 3\n\ts_cbranch_scc1 2f\n\ts_cmp_lg_u32 %9, 0\n\ts_cbranch_scc1 1f\n\t" CS1(0, 3) CS1(1, 4) P0 "s_branch 2f\n1:\n\t" CS1(0, 5) CS1(1, 6) P1 "\n2:" \
            : "+v"(cs0), "+v"(cs1), "+v"(cs2) : "v"(wf[0]), "v"(wf[1]), "v"(wf[2]), "v"(wf[3]), "v"(wf[4]), "v"(one8), "s"(wr), "s"(t) : "scc")
#define G1_CSUM(kk) do { if ((kk) == 0) G1_CSUM_(CS1(2, 7), ""); else G1_CSUM_("", CS1(2, 7)); } while (0)
#define G1_RW(i, B, slot) wf[i] = cat8(lds_tr(B + (slot) * 4096), lds_tr(B + (slot) * 4096 + 512))
#define G1_RA(m, AB, abuf) af[m] = lds_b128(AB + (abuf) * 32768 + (m) * 2048)
#define G1_SEG(AB, abuf, slot, L0, L1, L2, L3, L4, CLOSE) do { \
        L0; G1_RW(0, wB0, slot); G1_RW(1, wB1, slot); L1; G1_RW(2, wB2, slot); G1_RW(3, wB3, slot); L2; G1_RW(4, wB4, slot); G1_RA(0, AB, abuf); G1_RA(1, AB, abuf); \
        L3; G1_RA(2, AB, abuf); G1_RA(3, AB, abuf); G1_RA(4, AB, abuf); L4; G1_RA(5, AB, abuf); G1_RA(6, AB, abuf); G1_RA(7, AB, abuf); \
        WAIT_L0(); SBAR(); if (CLOSE) { BAR(); SBAR(); } } while (0)
#define G1_SEG_A(abuf, slot, tt, LIVE) do { const char* b_ = ((tt) < 4 ? XA : XC) + (tt) * 128; const unsigned d_ = (LIVE) ? dstA + (1 - (abuf)) * 32768 : lds0 + LDS_BYTES; \
        G1_SEG(aB0, abuf, slot, glds16c(b_, voffA0, d_), glds16c(b_, voffA1, d_ + 1024), glds16c(b_ + 32768, voffA0, d_ + 2048), glds16c(b_ + 32768, voffA1, d_ + 3072), (void)0, true); } while (0)
#define G1_SEG_W(abuf, slot, hs, dslot, LIVE, CLOSE) do { const unsigned d_ = (LIVE) ? dstW + (dslot) * 4096 : lds0 + LDS_BYTES; \
        G1_SEG(aB1, abuf, slot, glds16(P0 + (size_t)(hs) * stepF, voffF, d_), glds16(P1 + (size_t)(hs) * step13, voff13, d_ + 16384), glds16(P2 + (size_t)(hs) * stepF, voffF, d_ + 32768), \
               glds16(P3 + (size_t)(hs) * step13, voff13, d_ + 49152), glds16(P4 + (size_t)(hs) * stepQ, voffQ, d_ + 65536), CLOSE); } while (0)
#define EB 2
#define FIRST_WAIT() asm volatile("s_waitcnt vmcnt(4)" : "+v"(sg), "+v"(cpd) :: "memory")
#define G1_MM(i0, i1) _Pragma("unroll") for (int i = (i0); i < (i1); ++i) { const int b = i >> 3, m = (b & 1) ? 7 - (i & 7) : (i & 7); MFMA16_ACC(acc[m][b], wf[b], af[m]); }
#define G1_MMA(kk, CLOSE) do { __builtin_amdgcn_s_setprio(1); G1_MM(0, 40 - EB); SBAR(); __builtin_amdgcn_s_setprio(2); CLOSE; SBAR(); G1_MM(40 - EB, 40); G1_CSUM(kk); __builtin_amdgcn_s_setprio(0); SBAR(); } while (0)

        WAIT_L0();

        BAR();
        SBAR();

        flags.tag = 0;
        const u64* sgp = flags.p + (F_XC + rb * 32 + (lane & 31)) * 16;
#pragma unroll 1
        for (int t = 0; t < 16; t += 2) {
            const int h1 = (2 * t + 3 + wr < 31) ? 2 * t + 3 + wr : 31, h2 = (2 * t + 5 + wr < 31) ? 2 * t + 5 + wr : 31, t2 = (t < 14) ? t + 2 : 15;
            const int sel = (t == 12) ? 0 : ((t ^ 2) | wid);

            PEEK_AHEAD(sg, sgp, sel);
            if (t == 14) CODE_PF(cpd);
            G1_SEG_A(0, 0, t + 1, true); G1_MMA(0, { FIRST_WAIT(); BAR(); });
            if (t == 0) {
                const unsigned elo = __builtin_amdgcn_readfirstlane((unsigned)sg), ehi = __builtin_amdgcn_readfirstlane((unsigned)(sg >> 32));
                flags.tag = (((u64)ehi << 32) | elo) * 0x9E3779B97F4A7C15ull + 0x632BE59BD9B4E019ull;
                if (wid == 4) signal(flags, F_XC + bid, lane);
            }
            if (sel == 0) {
                if (t == 12) *(LAS u64*)(lds + G1_STAT + (wid * 32 + (opaque_tid() & 31)) * 8) = sg;
                else wait_all(flags, F_XC + rb * 32 + (opaque_tid() & 31), sg);
            }
            G1_SEG_W(0, 1, h1, (3 + wr) & 3, t + wr < 15, true); G1_MMA(1, { WAIT_V(5); BAR(); });
            if (t == 4) {
                const int tl = opaque_tid() & 63, k = tl & 15;
                if (k < 3) *(LAS f32x4*)(lds + G1_CS + wc * 384 + (tl >> 4) * 16 + (k == 2 ? 256 + wr * 64 : wr * 128 + k * 64)) = -(k == 0 ? cs0 : k == 1 ? cs1 : cs2);
                sgp = (const u64*)(ws + WS_STAT) + rb * 256 + wid * 32 + (tl & 31);
            }
            G1_SEG_A(1, 2, t2, t < 14); G1_MMA(0, { WAIT_V(4); BAR(); });
            G1_SEG_W(1, 3, h2, (1 + wr) & 3, t < 14, true);
            G1_MMA(1, asm volatile("s_waitcnt vmcnt(5)\n\ts_cmp_eq_u32 %0, 15\n\ts_cbranch_scc1 1f\n\ts_barrier\n1:" :: "s"(t + wr) : "memory", "scc"));
        }
        MFMA_DRAIN(); SBAR();

        __builtin_amdgcn_s_setprio(1);
        const int tz = opaque_tid(), ln = tz & 63;
        fr = tz & 15; fq = (tz >> 4) & 3;
        const int row0 = rb * 256 + wr * 128 + fr;
        f32x4 csv[5]; f32x2 st[8];
#pragma unroll
        for (int b = 0; b < 5; ++b) csv[b] = *(const LAS f32x4*)(lds + G1_CS + wc * 384 + b * 64 + fq * 16);
        csv[4] += *(const LAS f32x4*)(lds + G1_CS + wc * 384 + 320 + fq * 16);
#pragma unroll
        for (int m = 0; m < 8; ++m) st[m] = *(const LAS f32x2*)(lds + G1_STAT + (wr * 128 + m * 16 + fr) * 8);
        auto cen = [&](int m, int b, int hf) {
            const f32x2 a = hf ? acc[m][b].hi : acc[m][b].lo, c = hf ? csv[b].hi : csv[b].lo;
            return __builtin_elementwise_fma(c, (f32x2){st[m].x, st[m].x}, a);
        };
        auto lnv = [&](int m, int b, float sc) { const float k = st[m].y * sc; const f32x2 lo = cen(m, b, 0) * k, hi = cen(m, b, 1) * k; return (f32x4){lo.x, lo.y, hi.x, hi.y}; };
        auto glu = [&](int m, int bg, int bu) {
            const float k = st[m].y * -LOG2E, sd = __builtin_amdgcn_rcpf(st[m].y);
            f32x2 hh[2];
#pragma unroll
            for (int hf = 0; hf < 2; ++hf) {
                const f32x2 g = cen(m, bg, hf) * k;
                const f32x2 d = __builtin_elementwise_fma((f32x2){__builtin_amdgcn_exp2f(g.x), __builtin_amdgcn_exp2f(g.y)}, (f32x2){sd, sd}, (f32x2){sd, sd});
                hh[hf] = cen(m, bu, hf) * (f32x2){__builtin_amdgcn_rcpf(d.x), __builtin_amdgcn_rcpf(d.y)};
            }
            return (f32x4){hh[0].x, hh[0].y, hh[1].x, hh[1].y};
        };
        auto pack2 = [&](const f32x4& v0, const f32x4& v1) {
            unsigned p0 = cvtpk(v0[0], v0[1]), p1 = cvtpk(v0[2], v0[3]), q0 = cvtpk(v1[0], v1[1]), q1 = cvtpk(v1[2], v1[3]);
            swap16(p0, q0); swap16(p1, q1);
            return (u32x4){p0, p1, q0, q1};
        };
        constexpr unsigned KIND = NH * 2 * 65536u;
        const unsigned roff = WS_QKV + (wc & 1) * 65536u + (unsigned)(row0 + (fq & 1) * 16) * 32u + (fq >> 1) * 16;
        if (typeY) {
            const unsigned voff = roff + 2 * KIND + (unsigned)(4 * xcd + (wc >> 1)) * 131072u;
#pragma unroll
            for (int m = 0; m < 8; m += 2) {
                ST16(pack2(lnv(m, 1, 1.0f), lnv(m + 1, 1, 1.0f)), voff + m * 512);
                ST16(pack2(lnv(m, 3, 1.0f), lnv(m + 1, 3, 1.0f)), voff + 2 * 131072 + m * 512);
            }
            CFENCE();
        }
        {
            const float sc4 = (wc < 2) ? QSCALE : 1.0f;
            const lds_ptr ip = lds + (wc < 2 ? 2 * SLOT : 0) + (wr * 128 + (fq & 1) * 16 + fr) * 64 + ((((wc & 1) * 2 + (fq >> 1)) ^ (fr >> 2)) * 16);
            const unsigned koff = (wc < 2) ? WS_END : roff + KIND + (unsigned)(4 * xcd + js) * 131072u;
#pragma unroll
            for (int m = 0; m < 8; m += 2) {
                const u32x4 v = pack2(lnv(m, 4, sc4), lnv(m + 1, 4, sc4));
                ST16(v, koff + m * 512);
                *(LAS u32x4*)(ip + m * 1024) = v;
            }
            CFENCE(); SBAR();
            asm volatile("s_cmp_lg_u32 %0, 0\n\ts_cbranch_scc1 1f\n\ts_setprio 0\n1:" :: "s"(wr) : "scc");
        }
        constexpr int XM = 2, YM = 0, XS = 6;
        const int vword = F_QKV + xcd + 8 * (rb * 4 + 3);
        const u64* vsp = flags.p + vword * 16;
        u64 vs = 0;
        auto first_v = [&]() {
            PEEK_DONE(vs); wait_all(flags, vword, vs);
            const unsigned vv = (unsigned)(((ln & 3) >> 1) * 65536 + wid * 1024 + (ln >> 2) * 32 + (ln & 1) * 16);
            const char* vg = (const char*)ws + WS_QKV + (size_t)(2 * NH + 4 * xcd + js) * 131072 + rb * 8192;
            glds16c(vg, vv, lds0 + 16384 + wid * 2048); glds16c(vg + 512, vv, lds0 + 16384 + wid * 2048 + 1024);
        };
        if (!typeY) {
            hp.base = ws + WS_ACT + (unsigned)(14 * xcd + 4 * js + wc) * GRP + (unsigned)(rb * 256 + wr * 128) * 64u;
            hp.off = fr * 64 + (fq & 1) * 32 + (fq >> 1) * 16; hp.size = 8192;
#pragma unroll
            for (int m = 0; m < 8; ++m) {
                hp.v[m] = pack2(glu(m, 0, 2), glu(m, 1, 3));
                if (m == XM) { PIN(hp.v[XM]); WAIT_V(0); arrive(flags, F_QKV + bid, ln, lds + G1_CNT, 7u); }
                if (m == 2) { PIN(hp.v[m]); SBAR(); __builtin_amdgcn_s_setprio(0); }
                if (m == XS) { PIN(hp.v[XS]); PEEK_AHEAD(vs, vsp, 0); SBAR(); }
                if (m == XS + 1) { PIN(hp.v[XS + 1]); SBAR(); first_v(); }
            }
        } else {
            hp.base = ws + WS_ACT + (unsigned)(14 * xcd + 12 + (wc >> 1)) * GRP + (wc & 1) * (GRP / 2) + (unsigned)(rb * 256 + wr * 128) * 32u;
            hp.off = (fr + (fq & 1) * 16) * 32 + (fq >> 1) * 16; hp.size = 4096;
#pragma unroll
            for (int m = 0; m < 8; m += 2) {
                hp.v[m >> 1] = pack2(glu(m, 0, 2), glu(m + 1, 0, 2)); hp.v[4 + (m >> 1)] = hp.v[m >> 1];
                if (m == YM) { PIN(hp.v[YM >> 1]); WAIT_V(0); arrive(flags, F_QKV + bid, ln, lds + G1_CNT, 7u); }
            }
            PIN(hp.v[3]); __builtin_amdgcn_s_setprio(0); first_v();
        }
#pragma unroll
        for (int i = 0; i < 8; ++i) PIN(hp.v[i]);

    }

    const int z = xcd & 1, rbk = ((xcd >> 1) & 1) * 8 + (idx & 7), cbk = (xcd >> 2) * 4 + (idx >> 3);
    G2 g2;
    g2.nh = z ? 56 - NH0 : NH0;
    g2.rowb = rbk * 128 * 64;
    g2.split = z ? split_mask(NH0, 56 - NH0) : split_mask(0, NH0);
    g2.Ah = (const char*)ws + WS_ACT + (size_t)(z * 2 * NH0) * GRP;
    g2.Ao = (const char*)ws + WS_ACT + (size_t)(112 + z * 16 - 2 * g2.nh) * GRP;
    g2.Wd = (const char*)(wd + (size_t)z * NH0 * 64 * DM + cbk * 128);
    g2.Wo = (const char*)(wo + (size_t)z * 512 * DM + cbk * 128) - (size_t)g2.nh * 64 * DM * 2;
    {
        const int hl = idx & 3, qb = idx >> 2, h = 4 * xcd + hl;
        const int l0_ = opaque_tid() & 63;
        const int qkv1 = F_QKV + xcd + 8 * (((qb + 1) & 7) * 4 + ((l0_ & 8) ? 3 : hl));
        const int qkvword = F_QKV + xcd + 8 * ((l0_ & 7) * 4 + ((l0_ & 8) ? 3 : hl));
        const int odep = (rbk >> 1) * 32 + ((l0_ >> 2) & 3) * 8 + 4 * z + (l0_ & 3);
        const int depword = (l0_ & 16) ? (odep & ~7) + 4 : odep;
        const char* Kg = (const char*)ws + WS_QKV + (size_t)(NH + h) * 131072;
        const char* Vg = (const char*)ws + WS_QKV + (size_t)(2 * NH + h) * 131072;
        const char* wpd = g2.Wd - (cbk & 3) * 256;
        const char* wpo = g2.Wo - (cbk & 3) * 256;
        AttnOut R;
        attn_fast(lds, lds0, Kg, Vg, g2, flags, qkv1, qkvword, depword, F_H + bid, qb, bid == 0, cp, hp, wpd, wpo, idx * 2 * (g2.nh + 8), wid, R);

        const int tz = opaque_tid(), ln = tz & 63, w4 = wid & 3, fr = ln & 15, fq = ln >> 4;
        lds_ptr po0 = lds + 2 * SLOT + w4 * 4096 + ln * 16, po1 = lds + 2 * SLOT + 16384 + w4 * 2048 + ln * 16;
        PIN(po0); PIN(po1);
        auto po = [&](int i) { return i < 4 ? po0 + i * 1024 : po1 + (i >> 1 & 1) * 8192 + (i & 1) * 1024; };
        const lds_ptr pl = po0 + SLOT;
        volatile LAS unsigned* const badw = (volatile LAS unsigned*)(lds + 3 * SLOT + 1024);
        const int wm = w4 >> 1, wn = w4 & 1;
        const unsigned aO0 = (wm * 64 + fr) * 128 + ((fq ^ (fr >> 1)) * 16), aO1 = aO0 ^ 64;
        const int tq = (ln >> 2) & 3, tp = ln & 3, tg = (tq & 2) | ((fq & 1) << 2);
        unsigned wO[4];
#pragma unroll
        for (int b = 0; b < 4; ++b) wO[b] = 16384 + wn * 8192 + (8 * fq + tq) * 128 + (((2 * b + (tp >> 1)) ^ tg) * 16) + (tp & 1) * 8;
        f32x4 acc[4][4];
        bf16x8 xa[4], ya[4]; s16x4 xl[4], xh[4], yl[4], yh[4];
        lds_ptr pa0, pa1, pw0, pw1, pw2, pw3;
        int so = 4 * SLOT;
        auto handover = [&]() {
            if (wid < 4) {
#pragma unroll
                for (int i = 0; i < 4; ++i) {
                    *(LAS f32x4*)po(i) = (f32x4){R.o0[4 * i], R.o0[4 * i + 1], R.o0[4 * i + 2], R.o0[4 * i + 3]};
                    *(LAS f32x4*)po(4 + i) = (f32x4){R.o1[4 * i], R.o1[4 * i + 1], R.o1[4 * i + 2], R.o1[4 * i + 3]};
                }
                *(LAS f32x4*)pl = (f32x4){R.l0[0], R.m0, R.l1[0], R.m1};
                xl[0] = lds_tr(pw0); xh[0] = lds_tr(pw0 + 512);
#pragma unroll
                for (int m = 0; m < 4; ++m) xa[m] = lds_b128(pa0 + m * 2048);
                xl[1] = lds_tr(pw1); xh[1] = lds_tr(pw1 + 512); xl[2] = lds_tr(pw2); xh[2] = lds_tr(pw2 + 512); xl[3] = lds_tr(pw3); xh[3] = lds_tr(pw3 + 512);
#pragma unroll
                for (int m = 0; m < 4; ++m)
#pragma unroll
                    for (int b = 0; b < 4; ++b) { acc[m][b] = (f32x4){0.f, 0.f, 0.f, 0.f}; PIN(acc[m][b]); }
            }
        };
        G2Off go; g2_voff(ln, go);
        PIN(go.A0); PIN(go.A1); PIN(go.N0); PIN(go.N1); PIN(go.B0); PIN(go.B1);
        pa0 = lds + aO0 + so; pa1 = lds + aO1 + so; pw0 = lds + wO[0] + so; pw1 = lds + wO[1] + so; pw2 = lds + wO[2] + so; pw3 = lds + wO[3] + so;
        PIN(pa0); PIN(pa1); PIN(pw0); PIN(pw1); PIN(pw2); PIN(pw3);
        {
            handover();
            const f32x16 z16 = R.o0 * 0.0f + R.o1 * 0.0f;
            const f32x4 z4 = (z16.s0123 + z16.s4567) + (z16.s89ab + z16.scdef);
            const f32x2 z2 = z4.lo + z4.hi;
            const float zs = z2.x + z2.y;
            const bool ok = (zs == 0.f) && (R.l0[0] > 0.f) && (R.l0[0] < 0x1p96f) && (R.l1[0] > 0.f) && (R.l1[0] < 0x1p96f);
            const bool wave_bad = !__all(ok);
            if (ln == 0) badw[wid] = wave_bad ? 1u : 0u;

            WAIT_L0(); BAR(); CFENCE();
            const unsigned anybad = badw[ln & 7];
            if (__builtin_expect(__any(anybad != 0u), 0)) {
                WAIT_L0(); BAR(); CFENCE();
                attn_safe(lds, lds0, Kg, Vg, wid, R);
                handover();
                WAIT_L0(); BAR(); CFENCE();
            }
        }

        const int tile = rbk * 8 + cbk, nt = g2.nh + 8;
        if (__builtin_expect(wid >= 4, 0)) {
#define G2_LOAD0(j, sl) do { const unsigned d_ = lds0 + (sl) * SLOT; g2_load_b(g2, j, d_, 2 * w4, go.B0); g2_load_a(g2, j, d_, 2 * w4, go); } while (0)
#define G2_LOAD1(j, sl) do { const unsigned d_ = lds0 + (sl) * SLOT; g2_load_b(g2, j, d_, 2 * w4 + 1, go.B1); g2_load_a(g2, j, d_, 2 * w4 + 1, go); } while (0)
#define G2_LOAD(j, sl) do { G2_LOAD0(j, sl); G2_LOAD1(j, sl); } while (0)
            const f32x4 lm = *(const LAS f32x4*)pl;
            f32x4 t[8];
#pragma unroll
            for (int i = 0; i < 8; ++i) t[i] = *(const LAS f32x4*)po(i);
            WAIT_L0();
            G2_LOAD(3, 2);
            BAR();
            G2_LOAD(4, 3);
            float ck[2], co[2]; unsigned p[16];
            auto coef = [&](int q) {
                const float lk = q ? R.l1[0] : R.l0[0], mk = q ? R.m1 : R.m0, lo = lm[2 * q], mo = lm[2 * q + 1];
                const float mm = __builtin_fmaxf(mk, mo), fk = __builtin_amdgcn_exp2f(mk - mm), fo = __builtin_amdgcn_exp2f(mo - mm);
                const float rl = 1.0f / (lk * fk + lo * fo);
                ck[q] = fk * rl; co[q] = fo * rl; PIN(ck[q]); PIN(co[q]); SBAR();
            };
            auto half = [&](int q, int hh) {
                const f32x16 ok_ = q ? R.o1 : R.o0;
                unsigned* pp = p + 8 * q + 4 * hh;
#pragma unroll
                for (int i = 0; i < 2; ++i) {
                    const int g = 2 * hh + i; const f32x4 u = t[4 * q + g];
                    pp[2 * i] = cvtpk(ok_[4 * g] * ck[q] + u[0] * co[q], ok_[4 * g + 1] * ck[q] + u[1] * co[q]);
                    pp[2 * i + 1] = cvtpk(ok_[4 * g + 2] * ck[q] + u[2] * co[q], ok_[4 * g + 3] * ck[q] + u[3] * co[q]);
                }
                swap32(pp[0], pp[2]); swap32(pp[1], pp[3]);
                PIN(pp[0]); PIN(pp[1]); PIN(pp[2]); PIN(pp[3]); SBAR();
            };
            const unsigned ooff = WS_ACT + (unsigned)(112 + h) * GRP + (unsigned)(qb * 256 + w4 * 64 + fr) * 64u + (fq & 1) * 32 + (fq >> 1) * 16;
            auto ost = [&](int q) {
                unsigned* pp = p + 8 * q;
                swap16(pp[0], pp[4]); swap16(pp[1], pp[5]); swap16(pp[2], pp[6]); swap16(pp[3], pp[7]);
                ST16(((u32x4){pp[0], pp[1], pp[2], pp[3]}), ooff + q * 2048); ST16(((u32x4){pp[4], pp[5], pp[6], pp[7]}), ooff + q * 2048 + 1024); CFENCE();
            };
            SBAR(); coef(0); coef(1);
            WAIT_V(8);  BAR(); G2_LOAD(5, 4); G2_LOAD0(6, 0);
            WAIT_V(12); BAR(); G2_LOAD1(6, 0); G2_LOAD(7, 1);
            WAIT_V(16); BAR(); G2_LOAD0(8, 2);  SBAR(); half(0, 0); G2_LOAD1(8, 2);
            WAIT_V(16); BAR(); G2_LOAD0(9, 3);  SBAR(); half(0, 1); G2_LOAD1(9, 3);  ost(0);
            WAIT_V(18); BAR(); G2_LOAD0(10, 4); SBAR(); half(1, 0); G2_LOAD1(10, 4);
            WAIT_V(18); BAR(); G2_LOAD0(11, 0); SBAR(); half(1, 1); G2_LOAD1(11, 0); ost(1);
            WAIT_V(20); BAR(); G2_LOAD(12, 1);
            WAIT_V(18); BAR(); G2_LOAD(13, 2);
            WAIT_V(18);
            int sl = 3; cpd = 0;
#pragma unroll 1
            for (int j = 11; j < nt - 3; ++j) {
                BAR();
                if (j == 29) CODE_PF(cpd);
                if (j == 12 && wid == 4) signal(flags, F_O + bid, ln);
                G2_LOAD(j + 3, sl);
                sl = (sl == 4) ? 0 : sl + 1;
                WAIT_V(16);
            }
            BAR();
            if (tz == 256) *(LAS unsigned*)(lds + sl * SLOT) = 0u;
            WAIT_V(8); WAIT_L0(); BAR(); WAIT_V(0); BAR();
            asm volatile("" :: "v"(cpd));
            if (wid == 4) wait_all(flags, F_X + tile * 2 + z, peek(flags, F_X + tile * 2 + z));
            CFENCE(); BAR();
            asm volatile(".Lk_end:");
        } else {
#define G2_M(C, m, b) do { MFMA16_ACC(acc[m][b], cat8(C##l[b], C##h[b]), C##a[m]); SBAR(); } while (0)
#define G2_HALF(C, N, AB, WO, RD, TAIL) do { \
            G2_M(C, 0, 0); if (RD) { N##l[0] = lds_tr(pw0 + (WO)); N##h[0] = lds_tr(pw0 + (WO) + 512); } SBAR(); \
            G2_M(C, 1, 0); if (RD) N##a[0] = lds_b128(AB); SBAR(); \
            G2_M(C, 2, 0); if (RD) N##a[1] = lds_b128((AB) + 2048); SBAR(); \
            G2_M(C, 3, 0); if (RD) N##a[2] = lds_b128((AB) + 4096); SBAR(); \
            G2_M(C, 0, 1); if (RD) N##a[3] = lds_b128((AB) + 6144); SBAR(); \
            G2_M(C, 1, 1); if (RD) { N##l[1] = lds_tr(pw1 + (WO)); N##h[1] = lds_tr(pw1 + (WO) + 512); } SBAR(); \
            G2_M(C, 2, 1); if (RD) { N##l[2] = lds_tr(pw2 + (WO)); N##h[2] = lds_tr(pw2 + (WO) + 512); } SBAR(); \
            G2_M(C, 3, 1); if (RD) { N##l[3] = lds_tr(pw3 + (WO)); N##h[3] = lds_tr(pw3 + (WO) + 512); } SBAR(); \
            G2_M(C, 0, 2); TAIL(0); SBAR(); G2_M(C, 1, 2); TAIL(1); SBAR(); G2_M(C, 2, 2); TAIL(2); SBAR(); G2_M(C, 3, 2); TAIL(3); SBAR(); \
            G2_M(C, 0, 3); G2_M(C, 1, 3); G2_M(C, 2, 3); G2_M(C, 3, 3); } while (0)
            const u64* const optr = flags.p + (F_O + odep) * 16;
            u64 ov = 0;
#define G2_NEXT(i) do { if ((i) == 0) so = (so == 4 * SLOT) ? 0 : so + SLOT; \
                if ((i) == 1) { pa0 = lds + aO0 + so; pa1 = lds + aO1 + so; PIN(pa0); PIN(pa1); } \
                if ((i) == 2) { pw0 = lds + wO[0] + so; pw1 = lds + wO[1] + so; PIN(pw0); PIN(pw1); } \
                if ((i) == 3) { pw2 = lds + wO[2] + so; pw3 = lds + wO[3] + so; PIN(pw2); PIN(pw3); } } while (0)
#define G2_NONE(i) do {} while (0)
            WAIT_L0(); SBAR(); G2_HALF(x, y, pa1, 4096, true, G2_NEXT);
            WAIT_L0(); SBAR(); G2_HALF(y, x, pa0, 0, true, G2_NONE);
            BAR();
            WAIT_L0(); SBAR(); G2_HALF(x, y, pa1, 4096, true, G2_NEXT);
            WAIT_L0(); SBAR(); G2_HALF(y, x, pa0, 0, true, G2_NONE);
            if (NH0 & 1) {
                WAIT_L0(); SBAR(); G2_HALF(x, y, pa1, 4096, true, G2_NEXT);
                WAIT_L0(); BAR(); SBAR(); G2_HALF(y, x, pa0, 0, true, G2_NONE);
            }
#pragma unroll 1
            for (int ph = 0; ph < 3; ++ph) {
                const int n = (ph == 0) ? g2.nh / 2 - 4 : (ph == 1) ? 1 : 5;
#pragma unroll 1
                for (int i = 0; i < n; ++i) {
                    WAIT_L0(); SBAR(); G2_HALF(x, y, pa1, 4096, true, G2_NEXT);
                    WAIT_L0(); BAR(); SBAR(); G2_HALF(y, x, pa0, 0, true, G2_NONE);
                    WAIT_L0(); SBAR(); G2_HALF(x, y, pa1, 4096, true, G2_NEXT);
                    WAIT_L0(); BAR(); SBAR(); G2_HALF(y, x, pa0, 0, true, G2_NONE);
                }
                PEEK_AHEAD(ov, optr, wid | ph);
                if ((wid | (ph ^ 1)) == 0) { PEEK_DONE(ov); wait_all(flags, F_O + odep, ov); }
            }
            const int row = rbk * 128 + wm * 64 + z * 32 + fr, col = cbk * 128 + wn * 64 + (fq & 1) * 16 + (fq >> 1) * 8;
            u32x4 xr[4];
#pragma unroll
            for (int i = 0; i < 4; ++i) xr[i] = *(const u32x4*)(x + (size_t)(row + (i >> 1) * 16) * DM + col + (i & 1) * 32);
            WAIT_L0(); SBAR(); G2_HALF(x, y, pa1, 4096, true, G2_NEXT);
            WAIT_L0(); BAR(); SBAR(); G2_HALF(y, x, pa0, 0, true, G2_NONE);
            WAIT_L0(); SBAR(); G2_HALF(x, y, pa1, 4096, true, G2_NONE);
            WAIT_L0(); SBAR(); G2_HALF(y, x, pa0, 0, false, G2_NONE);
            MFMA_DRAIN(); SBAR();

            f32x4 vk[8];
            const unsigned xo = WS_XCH + (unsigned)(tile * 2 + 1 - z) * 32768u + w4 * 8192 + ln * 16;
            if (z == 0) {
#pragma unroll
                for (int i = 0; i < 8; ++i) { ST16(__builtin_bit_cast(u32x4, acc[2 + (i >> 2)][i & 3]), xo + i * 1024); vk[i] = acc[i >> 2][i & 3]; }
            } else {
#pragma unroll
                for (int i = 0; i < 8; ++i) { ST16(__builtin_bit_cast(u32x4, acc[i >> 2][i & 3]), xo + i * 1024); vk[i] = acc[2 + (i >> 2)][i & 3]; }
            }
            WAIT_V(0);
            arrive(flags, F_X + tile * 2 + 1 - z, ln, lds + ((so == 4 * SLOT) ? 0 : so + SLOT), 3u);

            CFENCE(); BAR(); CFENCE();

            const unsigned xi = WS_XCH + (unsigned)(tile * 2 + z) * 32768u + w4 * 8192 + ln * 16;
#pragma unroll
            for (int i = 0; i < 8; ++i) vk[i] += __builtin_bit_cast(f32x4, __builtin_amdgcn_raw_buffer_load_b128(wsr, xi + i * 1024, 0, 16));
            const __amdgpu_buffer_rsrc_t outr = __builtin_amdgcn_make_buffer_rsrc((void*)out, 0, SEQ * DM * 2, 0x00020000);
            u32x4 w[4];
#pragma unroll
            for (int i = 0; i < 4; ++i) {
                const u32x4 a = __builtin_bit_cast(u32x4, vk[2 * i]), b = __builtin_bit_cast(u32x4, vk[2 * i + 1]);
                unsigned a0 = a[0], a1 = a[1], a2 = a[2], a3 = a[3], b0 = b[0], b1 = b[1], b2 = b[2], b3 = b[3];
                swap16(a0, b0); swap16(a1, b1); swap16(a2, b2); swap16(a3, b3);
                const u32x4 xx = xr[i];
                w[i][0] = cvtpk(u2f(a0) + bf_lo(xx[0]), u2f(a1) + bf_hi(xx[0]));
                w[i][1] = cvtpk(u2f(a2) + bf_lo(xx[1]), u2f(a3) + bf_hi(xx[1]));
                w[i][2] = cvtpk(u2f(b0) + bf_lo(xx[2]), u2f(b1) + bf_hi(xx[2]));
                w[i][3] = cvtpk(u2f(b2) + bf_lo(xx[3]), u2f(b3) + bf_hi(xx[3]));
            }
            const unsigned oo = (unsigned)((row - (fr & 8)) * DM + col + (fr & 8) * 4) * 2u;
#pragma unroll
            for (int m = 0; m < 2; ++m) {
                u32x4 lo, hi;
#pragma unroll
                for (int k = 0; k < 4; ++k) {
                    lo[k] = __builtin_amdgcn_update_dpp(w[2 * m][k], w[2 * m + 1][k], 0x128, 0xf, 0xc, false);
                    hi[k] = __builtin_amdgcn_update_dpp(w[2 * m + 1][k], w[2 * m][k], 0x128, 0xf, 0x3, false);
                }
                __builtin_amdgcn_raw_buffer_store_b128(lo, outr, oo + m * 16 * DM * 2, 0, 16);
                __builtin_amdgcn_raw_buffer_store_b128(hi, outr, oo + (m * 16 + 8) * DM * 2, 0, 16);
            }
        }
    }
}

extern "C" void kernel_launch(void* const* d_in, const int* in_sizes, int n_in, void* d_out, int out_size, void* d_ws, size_t ws_size, hipStream_t stream) {
    const bf16_t* x = (const bf16_t*)d_in[0];
    k_main<<<256, 512, 0, stream>>>(x, (const bf16_t*)d_in[1], (const bf16_t*)d_in[2], (const bf16_t*)d_in[4], (const bf16_t*)d_in[5], (const bf16_t*)d_in[6],
                                    (bf16_t*)d_out, (unsigned char*)d_ws);
}
```
